```python
import torch

def _softmax(x, axis=-1):
    return torch.softmax(x, dim=axis)
def _gelu(x):
    return torch.nn.functional.gelu(x, approximate="tanh")
def _silu(x):
    return torch.nn.functional.silu(x)
def _sigmoid(x):
    return torch.sigmoid(x)
def _layernorm(x, axis=-1, eps=1e-5):
    m = x.mean(dim=axis, keepdim=True)
    v = x.var(dim=axis, keepdim=True, unbiased=False)
    return (x - m) / torch.sqrt(v + eps)
def _scatter_add(tgt, idx, src, axis=0):
    return tgt.clone().index_add_(axis, idx, src)
def _segment_sum(src, seg, n):
    out = torch.zeros((n,) + src.shape[1:], dtype=src.dtype)
    return out.index_add_(0, seg, src)
def _pad(x, pw):
    flat = tuple(v for pair in reversed(pw) for v in pair)
    return torch.nn.functional.pad(x, flat)

kernel_name = 'concat_heads__linear_kernel_hedgehog__compressed_kv_cla__prompt'

def setup_inputs():

    _w = (lambda a: (lambda t: t.to(torch.bfloat16) if t.is_floating_point() else t)(torch.as_tensor(a)))
    def _A_setup():
        B, S, D, H = 1, 2048, 640, 32
        Dh = D // H
        return {
            "x": _w((lambda *s: torch.randn(*s, dtype=torch.bfloat16) * 0.4)(B, S, D)),
            "wqkv": _w((lambda *s: torch.randn(*s, dtype=torch.bfloat16) * 0.4)(D, 3 * D)),
            "w_phi": _w((lambda *s: torch.randn(*s, dtype=torch.bfloat16) * 0.4)(H, Dh, Dh) * (Dh ** -0.5)),
            "wo": _w((lambda *s: torch.randn(*s, dtype=torch.bfloat16) * 0.05)(D, D)),
        }
    _A_in = _A_setup()

    def _B_setup():
        B, H, Sq, Sk, D, M = 1, 32, 2048, 2048, 128, 640
        Dc = 4 * D
        return {
            "x": _w((lambda *s: torch.randn(*s, dtype=torch.bfloat16) * 0.8)(B, Sq, M)),
            "c_kv": _w((lambda *s: torch.randn(*s, dtype=torch.bfloat16) * 0.4)(B, Sk, Dc)),
            "w_q": _w((lambda *s: torch.randn(*s, dtype=torch.bfloat16) * 6.4)(M, H, D) * (M ** -0.5)),
            "w_uk": _w((lambda *s: torch.randn(*s, dtype=torch.bfloat16) * 6.4)(Dc, H, D) * (Dc ** -0.5)),
            "w_uv": _w((lambda *s: torch.randn(*s, dtype=torch.bfloat16) * 0.4)(Dc, H, D) * (Dc ** -0.5)),
            "w_o": _w((lambda *s: torch.randn(*s, dtype=torch.bfloat16) * 0.4)(H, D, M) * ((H * D) ** -0.5)),
        }
    _B_in = _B_setup()

    return {
        "A_x": _A_in["x"],
        "A_wqkv": _A_in["wqkv"],
        "A_w_phi": _A_in["w_phi"],
        "A_wo": _A_in["wo"],
        "B_x": _B_in["x"],
        "B_c_kv": _B_in["c_kv"],
        "B_w_q": _B_in["w_q"],
        "B_w_uk": _B_in["w_uk"],
        "B_w_uv": _B_in["w_uv"],
        "B_w_o": _B_in["w_o"],
    }

def reference(A_x, A_wqkv, A_w_phi, A_wo, B_x, B_c_kv, B_w_q, B_w_uk, B_w_uv, B_w_o):
    def _A_ref(x, wqkv, w_phi, wo):
        B, S, D = x.shape
        H = 32; Dh = D // H
        qkv = (lambda x, *d: x.permute(*d))((x @ wqkv).reshape(B, S, 3, H, Dh), 2, 0, 3, 1, 4)
        q, k, v = qkv[0], qkv[1], qkv[2]
        pq = torch.einsum("bhsd,hdf->bhsf", q, w_phi)
        pk = torch.einsum("bhsd,hdf->bhsf", k, w_phi)
        phi_q = torch.concatenate([_softmax(pq, axis=-1), _softmax(-pq, axis=-1)], axis=-1)
        phi_k = torch.concatenate([_softmax(pk, axis=-1), _softmax(-pk, axis=-1)], axis=-1)
        kv = torch.einsum("bhsf,bhsd->bhfd", phi_k, v)
        z  = torch.einsum("bhsf->bhf", phi_k)
        num = torch.einsum("bhqf,bhfd->bhqd", phi_q, kv)
        den = torch.einsum("bhqf,bhf->bhq", phi_q, z)[..., None] + 1e-6
        o = num / den
        return (lambda x, *d: x.permute(*d))(o, 0, 2, 1, 3).reshape(B, S, D) @ wo

    def _B_ref(x, c_kv, w_q, w_uk, w_uv, w_o):
        B, Sq, M = x.shape[0], x.shape[1], x.shape[2]
        Sk = c_kv.shape[1]
        H, D = w_q.shape[1], w_q.shape[2]
        q = torch.einsum("bqm,mhd->bhqd", x, w_q)
        k = torch.einsum("bkc,chd->bhkd", c_kv, w_uk)
        v = torch.einsum("bkc,chd->bhkd", c_kv, w_uv)
        scores = torch.einsum("bhqd,bhkd->bhqk", q, k) * (D ** -0.5)
        off = Sk - Sq
        qi = torch.arange(Sq)[:, None]
        kj = torch.arange(Sk)[None, :]
        cmask = kj <= (qi + off)
        scores = torch.where(cmask[None, None], scores, -1e9)
        p = _softmax(scores, axis=-1)
        ctx = torch.einsum("bhqk,bhkd->bhqd", p, v)
        return torch.einsum("bhqd,hdm->bqm", ctx, w_o)

    a = _A_ref(A_x, A_wqkv, A_w_phi, A_wo)
    b = _B_ref(B_x, B_c_kv, B_w_q, B_w_uk, B_w_uv, B_w_o)
    return torch.concatenate([a, b], axis=-1)
```

```cpp
#include <hip/hip_runtime.h>
#include <hip/hip_bf16.h>
typedef unsigned short u16;
__device__ __forceinline__ float b2f(u16 x){ return __uint_as_float(((unsigned)x)<<16); }
__device__ __forceinline__ u16 f2b(float f){ __hip_bfloat16 h = __float2bfloat16(f); return *(u16*)&h; }
__device__ __forceinline__ float rb(float f){ return b2f(f2b(f)); }

__global__ void gemm(const u16* A, const u16* B, u16* C, int M, int N, int K, int ldc){
  __shared__ float As[64][17]; __shared__ float Bs[16][65];
  int tx = threadIdx.x & 15, ty = threadIdx.x >> 4;
  int bm = blockIdx.y*64, bn = blockIdx.x*64;
  float acc[4][4] = {};
  for(int k0=0;k0<K;k0+=16){
    for(int i=threadIdx.x;i<64*16;i+=256){ int r=i>>4,c=i&15; As[r][c]=b2f(A[(size_t)(bm+r)*K+k0+c]); }
    for(int i=threadIdx.x;i<16*64;i+=256){ int r=i>>6,c=i&63; Bs[r][c]=b2f(B[(size_t)(k0+r)*N+bn+c]); }
    __syncthreads();
    for(int k=0;k<16;++k){
      float a[4],b[4];
      for(int i=0;i<4;++i){a[i]=As[ty*4+i][k]; b[i]=Bs[k][tx*4+i];}
      for(int i=0;i<4;++i)for(int j=0;j<4;++j)acc[i][j]+=a[i]*b[j];
    }
    __syncthreads();
  }
  for(int i=0;i<4;++i)for(int j=0;j<4;++j) C[(size_t)(bm+ty*4+i)*ldc+bn+tx*4+j]=f2b(acc[i][j]);
}

__global__ void attn(const u16* Q, const u16* K, const u16* V, u16* ctx){
  int qi = blockIdx.x, h = blockIdx.y, t = threadIdx.x;
  __shared__ float q[128]; __shared__ float p[2048]; __shared__ float red[128];
  q[t] = b2f(Q[(size_t)qi*4096+h*128+t]); __syncthreads();
  float mx=-1e30f;
  for(int j=t;j<=qi;j+=128){ float s=0; const u16* kr=K+(size_t)j*4096+h*128; for(int d=0;d<128;++d) s+=q[d]*b2f(kr[d]); s*=0.08838834764831845f; p[j]=s; mx=fmaxf(mx,s);}
  red[t]=mx; __syncthreads();
  for(int o=64;o>0;o>>=1){ if(t<o) red[t]=fmaxf(red[t],red[t+o]); __syncthreads(); }
  mx=red[0]; __syncthreads();
  float sm=0; for(int j=t;j<=qi;j+=128){ float e=__expf(p[j]-mx); p[j]=e; sm+=e; }
  red[t]=sm; __syncthreads();
  for(int o=64;o>0;o>>=1){ if(t<o) red[t]+=red[t+o]; __syncthreads(); }
  sm=red[0]; __syncthreads();
  float acc=0; for(int j=0;j<=qi;++j) acc+=rb(p[j])*b2f(V[(size_t)j*4096+h*128+t]);
  ctx[(size_t)qi*4096+h*128+t]=f2b(acc/sm);
}

__global__ void phi_k(const u16* qkv, const u16* wphi, float* phiq, float* phik){
  int idx = blockIdx.x*blockDim.x+threadIdx.x;
  int which = idx & 1, h = (idx>>1)&31, s = idx>>6;
  float x[20], p[20];
  for(int d=0;d<20;++d) x[d]=b2f(qkv[(size_t)s*1920+which*640+h*20+d]);
  float mx=-1e30f, mn=1e30f;
  for(int f=0;f<20;++f){ float a=0; for(int d=0;d<20;++d) a+=x[d]*b2f(wphi[h*400+d*20+f]); p[f]=rb(a); mx=fmaxf(mx,p[f]); mn=fminf(mn,p[f]); }
  float s1=0,s2=0; for(int f=0;f<20;++f){ s1+=__expf(p[f]-mx); s2+=__expf(mn-p[f]); }
  float* o = (which?phik:phiq)+((size_t)s*32+h)*40;
  for(int f=0;f<20;++f){ o[f]=rb(__expf(p[f]-mx)/s1); o[20+f]=rb(__expf(mn-p[f])/s2); }
}
__global__ void kvk(const u16* qkv, const float* phik, float* kv){
  int h=blockIdx.x, t=threadIdx.x; if(t>=840) return; int f=t/21,d=t%21;
  float a=0; for(int s=0;s<2048;++s){ float v = d<20? b2f(qkv[(size_t)s*1920+1280+h*20+d]) : 1.f; a+=phik[((size_t)s*32+h)*40+f]*v; }
  kv[h*840+t]=rb(a);
}
__global__ void ok(const float* phiq, const float* kv, u16* o){
  int idx = blockIdx.x*blockDim.x+threadIdx.x;
  int s=idx/640, c=idx%640, h=c/20, d=c%20;
  float num=0,den=0; for(int f=0;f<40;++f){ float p=phiq[((size_t)s*32+h)*40+f]; num+=p*kv[h*840+f*21+d]; den+=p*kv[h*840+f*21+20]; }
  o[idx]=f2b(rb(num)/rb(rb(den)+1e-6f));
}

extern "C" void kernel_launch(void* const* d_in, const int* in_sizes, int n_in, void* d_out, int out_size, void* d_ws, size_t ws_size, hipStream_t stream){
  const u16 *Ax=(const u16*)d_in[0], *Awqkv=(const u16*)d_in[1], *Awphi=(const u16*)d_in[2], *Awo=(const u16*)d_in[3];
  const u16 *Bx=(const u16*)d_in[4], *Bc=(const u16*)d_in[5], *Bwq=(const u16*)d_in[6], *Bwk=(const u16*)d_in[7], *Bwv=(const u16*)d_in[8], *Bwo=(const u16*)d_in[9];
  u16* out=(u16*)d_out; char* ws=(char*)d_ws;
  u16* Q=(u16*)ws; u16* K=Q+2048*4096; u16* V=K+2048*4096; u16* ctx=V+2048*4096; u16* qkv=ctx+2048*4096; u16* o=qkv+2048*1920;
  float* phiq=(float*)(o+2048*640); float* phik=phiq+2048*32*40; float* kv=phik+2048*32*40;
  gemm<<<dim3(64,32),256,0,stream>>>(Bx,Bwq,Q,2048,4096,640,4096);
  gemm<<<dim3(64,32),256,0,stream>>>(Bc,Bwk,K,2048,4096,512,4096);
  gemm<<<dim3(64,32),256,0,stream>>>(Bc,Bwv,V,2048,4096,512,4096);
  gemm<<<dim3(30,32),256,0,stream>>>(Ax,Awqkv,qkv,2048,1920,640,1920);
  attn<<<dim3(2048,32),128,0,stream>>>(Q,K,V,ctx);
  phi_k<<<2048*64/256,256,0,stream>>>(qkv,Awphi,phiq,phik);
  kvk<<<32,896,0,stream>>>(qkv,phik,kv);
  ok<<<2048*640/256,256,0,stream>>>(phiq,kv,o);
  gemm<<<dim3(10,32),256,0,stream>>>(o,Awo,out,2048,640,640,1280);
  gemm<<<dim3(10,32),256,0,stream>>>(ctx,Bwo,out+640,2048,640,4096,1280);
}
```

```cpp
#include <hip/hip_runtime.h>

__global__ __launch_bounds__(256,1) __attribute__((aligned(4096))) void fused(const void* ax, const void* wqkv, const void* wphi, const void* wo, const void* bx, const void* c, const void* wq, const void* wuk, const void* wuv, const void* wob, void* out, void* ws){
  unsigned tid = threadIdx.x;
  unsigned bid = blockIdx.x;

  asm volatile(
"s_load_dwordx16 s[4:19], %1, 0\n"
"s_load_dwordx8 s[20:27], %1, 0x40\n"
"s_getpc_b64 s[44:45]\n"
".Lpc_%=:\n"
"s_mov_b32 s46, .Lcodeend_%=-.Lpc_%=\n"
"s_mov_b32 s47, 0x20000\n"
"v_lshlrev_b32 v254, 4, v0\n"
"s_lshr_b32 s32, %2, 3\n"
"s_lshl_b32 s59, s32, 12\n"
"buffer_load_dwordx4 a[252:255], v254, s[44:47], s59 offen\n"
"v_readfirstlane_b32 s76, v0\n"
"v_and_b32 v255, 63, v0\n"
"s_mov_b64 s[62:63], s[44:45]\n"
"s_and_b32 s33, %2, 7\n"
"s_lshr_b32 s76, s76, 6\n"
"s_lshl_b32 s77, s33, 2\n"
"s_and_b32 s59, s32, 3\n"
"s_add_u32 s77, s77, s59\n"
"s_lshl_b32 s32, s32, 2\n"
"s_add_u32 s32, s32, s76\n"
"s_mov_b32 s31, 0x20000\n"
"s_lshr_b32 s78, %2, 5\n"
"s_lshl_b32 s57, s76, 12\n"
"s_lshl_b32 s89, s76, 13\n"
"v_and_b32 v1, 15, v255\n"
"v_lshrrev_b32 v2, 4, v255\n"
"v_lshlrev_b32 v3, 1, v2\n"
"v_xor_b32 v3, v3, v1\n"
"v_lshlrev_b32 v5, 4, v3\n"
"v_mul_u32_u24 v242, 3840, v2\n"
"v_add_u32 v242, v242, v5\n"
"v_xor_b32 v243, 128, v242\n"
"s_and_b32 s60, %2, 1\n"
"s_lshl_b32 s60, s60, 3\n"
"s_bfe_u32 s59, %2, 0x30003\n"
"s_add_u32 s60, s60, s59\n"
"s_bfe_u32 s61, %2, 0x20001\n"
"s_lshl_b32 s61, s61, 2\n"
"s_lshr_b32 s59, %2, 6\n"
"s_add_u32 s61, s61, s59\n"
"s_lshl_b32 s59, s60, 2\n"
"s_add_u32 s59, s59, s76\n"
"s_mul_i32 s59, s59, 40960\n"
"s_mul_i32 s80, s76, 61440\n"
"s_lshl_b32 s52, s61, 8\n"
"s_add_u32 s80, s80, s52\n"
"s_sub_u32 s74, 2457600, s80\n"
"s_cmp_lt_u32 s61, 15\n"
"s_cselect_b32 s74, s74, 0\n"
"s_cselect_b32 s70, 40960, 0\n"
"s_mov_b32 s71, 0x20000\n"
"s_mov_b32 s75, 0x20000\n"
"v_lshrrev_b32 v3, 3, v255\n"
"v_and_b32 v4, 7, v255\n"
"v_xor_b32 v4, v4, v2\n"
"v_lshlrev_b32 v4, 4, v4\n"
"v_mul_u32_u24 v244, 1280, v3\n"
"v_add_u32 v244, v244, v4\n"
"v_xor_b32 v245, 64, v244\n"
"v_lshlrev_b32 v254, 4, v255\n"
"s_and_b32 s35, s33, 1\n"
"s_lshl_b32 s35, s35, 2\n"
"s_lshr_b32 s64, s33, 1\n"
"s_add_u32 s35, s35, s64\n"
"s_lshl_b32 s35, s35, 7\n"
"s_add_u32 s64, s35, s32\n"
"s_mul_i32 s64, s64, 2560\n"
"s_mov_b32 s38, 20\n"
"s_mov_b32 s39, 0x20000\n"
"s_lshr_b32 s35, s33, 1\n"
"s_lshl_b32 s35, s35, 3\n"
"s_and_b32 s65, s32, 7\n"
"s_add_u32 s35, s35, s65\n"
"s_min_u32 s35, s35, 29\n"
"s_lshl_b32 s35, s35, 7\n"
"s_lshr_b32 s65, s32, 3\n"
"s_mul_i32 s65, s65, 153600\n"
"s_add_u32 s65, s65, s35\n"
"s_mov_b32 s42, 40\n"
"s_mov_b32 s43, 0x20000\n"
"s_waitcnt lgkmcnt(0)\n"
"s_add_u32 s68, s4, s59\n"
"s_addc_u32 s69, s5, 0\n"
"s_add_u32 s72, s6, s80\n"
"s_addc_u32 s73, s7, 0\n"
"s_add_u32 m0, s57, 0\n"
"s_mov_b32 s52, 0\n"
"buffer_load_dwordx4 v244, s[68:71], s52 offen offset:0 lds\n"
"s_mov_b32 s52, 9216\n"
"buffer_load_dwordx4 v245, s[68:71], s52 offen offset:1024 lds\n"
"s_mov_b32 s52, 18432\n"
"buffer_load_dwordx4 v244, s[68:71], s52 offen offset:2048 lds\n"
"s_mov_b32 s52, 27648\n"
"buffer_load_dwordx4 v245, s[68:71], s52 offen offset:3072 lds\n"
"s_add_u32 m0, s57, 65536\n"
"s_mov_b32 s52, 0\n"
"buffer_load_dwordx4 v242, s[72:75], s52 offen offset:0 lds\n"
"s_mov_b32 s52, 14336\n"
"buffer_load_dwordx4 v242, s[72:75], s52 offen offset:1024 lds\n"
"s_mov_b32 s52, 28672\n"
"buffer_load_dwordx4 v243, s[72:75], s52 offen offset:2048 lds\n"
"s_mov_b32 s52, 43008\n"
"buffer_load_dwordx4 v243, s[72:75], s52 offen offset:3072 lds\n"
"s_mov_b64 s[36:37], s[4:5]\n"
"s_or_b32 s37, s37, 8388608\n"
"buffer_load_dword a255, v255, s[36:39], s64 idxen\n"
"s_mov_b64 s[40:41], s[6:7]\n"
"s_or_b32 s41, s41, 251658240\n"
"buffer_load_dword a255, v255, s[40:43], s65 idxen\n"
"s_mov_b64 s[84:85], s[26:27]\n"
"s_mov_b32 s86, 82837504\n"
"s_mov_b32 s87, 0x20000\n"
"s_mov_b32 s59, 81788928\n"
"s_load_dword s88, s[26:27], s59\n"
"v_mul_u32_u24 v240, 1024, v3\n"
"v_add_u32 v240, v240, v4\n"
"v_xor_b32 v241, 64, v240\n"
"v_mul_u32_u24 v246, 8192, v2\n"
"v_add_u32 v246, v246, v5\n"
"v_xor_b32 v247, 128, v246\n"
"s_lshr_b32 s64, s76, 1\n"
"s_and_b32 s65, s76, 1\n"
"s_lshl_b32 s59, s65, 9\n"
"s_add_u32 s59, s59, s77\n"
"s_lshl_b32 s59, s59, 8\n"
"s_sub_u32 s42, 4194304, s59\n"
"s_mov_b32 s43, 0x20000\n"
"s_cmp_lt_u32 s76, 2\n"
"s_cselect_b64 s[40:41], s[18:19], s[20:21]\n"
"s_add_u32 s40, s40, s59\n"
"s_addc_u32 s41, s41, 0\n"
"s_cmp_lt_u32 s76, 2\n"
"s_cselect_b32 s54, 16, 0\n"
"s_add_u32 s54, s54, s78\n"
"s_sub_u32 s59, 55, s54\n"
"s_cmp_eq_u32 s65, 0\n"
"s_cselect_b32 s59, s54, s59\n"
"s_lshl_b32 s59, s59, 15\n"
"s_mov_b64 s[36:37], s[14:15]\n"
"s_add_u32 s36, s36, s59\n"
"s_addc_u32 s37, s37, 0\n"
"s_mov_b32 s38, 294912\n"
"s_mov_b32 s39, 0x20000\n"
"s_lshl_b32 s59, s77, 6\n"
"s_add_u32 s54, s54, s59\n"
"s_lshl_b32 s54, s54, 14\n"
"s_cmp_lt_u32 s76, 2\n"
"s_cselect_b32 s59, 1073741824, 0\n"
"s_add_u32 s54, s54, s59\n"
"s_lshl_b32 s59, s65, 11\n"
"s_add_u32 s54, s54, s59\n"
"s_add_u32 s55, s54, s59\n"
"s_add_u32 s55, s55, 8192\n"
"s_mul_i32 s59, s61, 205\n"
"s_lshr_b32 s59, s59, 10\n"
"s_mul_i32 s80, s59, 5\n"
"s_sub_u32 s81, s61, s80\n"
"s_lshl_b32 s81, s81, 4\n"
"s_lshl_b32 s80, s65, 3\n"
"s_add_u32 s81, s81, s80\n"
"s_mul_i32 s56, s59, 1280\n"
"s_lshl_b32 s59, s60, 1\n"
"s_add_u32 s59, s59, s64\n"
"s_lshl_b32 s59, s59, 14\n"
"s_add_u32 s56, s56, s59\n"
"s_add_u32 s56, s56, 52428800\n"
"s_cmp_lt_u32 s61, 15\n"
"s_cselect_b32 s56, s56, 1073741824\n"
"s_mov_b32 s82, 81797120\n"
"s_cmp_eq_u32 s76, 0\n"
"s_cselect_b32 s82, s82, 1073741824\n"
"s_lshl_b32 s59, s60, 4\n"
"s_add_u32 s59, s59, s61\n"
"s_lshl_b32 s59, s59, 2\n"
"s_add_u32 s82, s82, s59\n"
"s_lshl_b32 s80, s78, 1\n"
"s_add_u32 s80, s80, s65\n"
"s_sub_u32 s59, 31, s80\n"
"s_cmp_lt_u32 s76, 2\n"
"s_cselect_b32 s79, s59, s80\n"
"s_lshl_b32 s59, s76, 9\n"
"s_add_u32 s59, s59, s77\n"
"s_lshl_b32 s59, s59, 8\n"
"s_mov_b64 s[48:49], s[16:17]\n"
"s_add_u32 s48, s48, s59\n"
"s_addc_u32 s49, s49, 0\n"
"s_mov_b32 s50, 0\n"
"s_mov_b32 s51, 0x20000\n"
"s_sub_u32 s50, 5242880, s59\n"
"v_lshrrev_b32 v3, 1, v1\n"
"v_xor_b32 v3, v3, v2\n"
"v_lshlrev_b32 v3, 4, v3\n"
"s_lshl_b32 s59, s64, 13\n"
"v_lshl_add_u32 v248, v1, 7, v3\n"
"v_add_u32 v248, s59, v248\n"
"v_xor_b32 v249, 64, v248\n"
"v_lshrrev_b32 v3, 2, v1\n"
"v_lshl_add_u32 v4, v2, 3, v3\n"
"v_lshlrev_b32 v250, 8, v4\n"
"v_and_b32 v2, 1, v2\n"
"v_lshl_add_u32 v3, v2, 2, v3\n"
"v_lshlrev_b32 v3, 5, v3\n"
"s_lshl_b32 s59, s65, 7\n"
"v_xor_b32 v3, s59, v3\n"
"v_and_b32 v1, 3, v255\n"
"v_lshl_add_u32 v250, v1, 3, v250\n"
"v_add_u32 v250, 65536, v250\n"
"v_add_u32 v250, v250, v3\n"
"v_xor_b32 v251, 32, v250\n"
"v_xor_b32 v252, 64, v250\n"
"v_xor_b32 v253, 96, v250\n"
"s_mov_b32 s80, 1120\n"
"s_mov_b32 s83, 0x3fb8aa3b\n"
"s_mov_b64 exec, 0\n"
"s_cmp_eq_u32 s76, 0\n"
"s_cbranch_scc1 .Lpad1_%=\n"
"s_cmp_eq_u32 s76, 1\n"
"s_cbranch_scc1 .Lpad2_%=\n"
"s_cmp_eq_u32 s76, 2\n"
"s_cbranch_scc1 .Lpad3_%=\n"
"s_cmp_eq_u32 s76, 3\n"
"s_cbranch_scc1 .Lpad4_%=\n"
".Lhopi2_%=:\n"
"s_mov_b64 exec, -1\n"
"s_add_u32 m0, s57, 16384\n"
"s_mov_b32 s52, 128\n"
"buffer_load_dwordx4 v244, s[68:71], s52 offen offset:0 lds\n"
"s_mov_b32 s52, 9344\n"
"buffer_load_dwordx4 v245, s[68:71], s52 offen offset:1024 lds\n"
"s_mov_b32 s52, 18560\n"
"buffer_load_dwordx4 v244, s[68:71], s52 offen offset:2048 lds\n"
"s_mov_b32 s52, 27776\n"
"buffer_load_dwordx4 v245, s[68:71], s52 offen offset:3072 lds\n"
"s_add_u32 m0, s57, 81920\n"
"s_mov_b32 s52, 245760\n"
"buffer_load_dwordx4 v242, s[72:75], s52 offen offset:0 lds\n"
"s_mov_b32 s52, 260096\n"
"buffer_load_dwordx4 v242, s[72:75], s52 offen offset:1024 lds\n"
"s_mov_b32 s52, 274432\n"
"buffer_load_dwordx4 v243, s[72:75], s52 offen offset:2048 lds\n"
"s_mov_b32 s52, 288768\n"
"buffer_load_dwordx4 v243, s[72:75], s52 offen offset:3072 lds\n"
"s_waitcnt lgkmcnt(0)\n"
"s_add_u32 s88, s88, 1\n"
"s_mul_i32 s88, s88, 0x9e3779b1\n"
"s_xor_b32 s88, s88, 0x5bd1e995\n"
"s_mov_b32 s4, s88\n"
"s_cmp_eq_u32 s76, 0\n"
"s_cselect_b32 s88, s88, 0\n"
"s_waitcnt vmcnt(10)\n"
"s_barrier\n"
"ds_read_b64_tr_b16 a[160:161], v250 offset:0\n"
"ds_read_b64_tr_b16 a[162:163], v250 offset:1024\n"
"ds_read_b128 a[144:147], v248 offset:0\n"
"ds_read_b128 a[148:151], v248 offset:2048\n"
"ds_read_b128 a[152:155], v248 offset:4096\n"
"ds_read_b128 a[156:159], v248 offset:6144\n"
"ds_read_b64_tr_b16 a[164:165], v251 offset:0\n"
"ds_read_b64_tr_b16 a[166:167], v251 offset:1024\n"
"ds_read_b64_tr_b16 a[168:169], v252 offset:0\n"
"ds_read_b64_tr_b16 a[170:171], v252 offset:1024\n"
"ds_read_b64_tr_b16 a[172:173], v253 offset:0\n"
"ds_read_b64_tr_b16 a[174:175], v253 offset:1024\n"
"s_waitcnt lgkmcnt(0)\n"
"v_mfma_f32_16x16x32_bf16 v[176:179], a[160:163], a[144:147], 0\n"
"s_add_u32 m0, s57, 32768\n"
"ds_read_b64_tr_b16 a[192:193], v250 offset:8192\n"
"s_mov_b32 s52, 256\n"
"v_mfma_f32_16x16x32_bf16 v[180:183], a[160:163], a[148:151], 0\n"
"buffer_load_dwordx4 v244, s[68:71], s52 offen offset:0 lds\n"
"s_mov_b32 s52, 9472\n"
"v_mfma_f32_16x16x32_bf16 v[184:187], a[160:163], a[152:155], 0\n"
"buffer_load_dwordx4 v245, s[68:71], s52 offen offset:1024 lds\n"
"v_mfma_f32_16x16x32_bf16 v[188:191], a[160:163], a[156:159], 0\n"
"s_mov_b32 s52, 18688\n"
"ds_read_b64_tr_b16 a[194:195], v250 offset:9216\n"
"ds_read_b128 a[176:179], v249 offset:0\n"
"v_mfma_f32_16x16x32_bf16 v[192:195], a[164:167], a[144:147], 0\n"
"buffer_load_dwordx4 v244, s[68:71], s52 offen offset:2048 lds\n"
"v_mfma_f32_16x16x32_bf16 v[196:199], a[164:167], a[148:151], 0\n"
"s_mov_b32 s52, 27904\n"
"ds_read_b128 a[180:183], v249 offset:2048\n"
"ds_read_b128 a[184:187], v249 offset:4096\n"
"v_mfma_f32_16x16x32_bf16 v[200:203], a[164:167], a[152:155], 0\n"
"buffer_load_dwordx4 v245, s[68:71], s52 offen offset:3072 lds\n"
"s_add_u32 m0, s57, 98304\n"
"v_mfma_f32_16x16x32_bf16 v[204:207], a[164:167], a[156:159], 0\n"
"s_mov_b32 s52, 491520\n"
"ds_read_b128 a[188:191], v249 offset:6144\n"
"ds_read_b64_tr_b16 a[196:197], v251 offset:8192\n"
"v_mfma_f32_16x16x32_bf16 v[208:211], a[168:171], a[144:147], 0\n"
"ds_read_b64_tr_b16 a[198:199], v251 offset:9216\n"
"ds_read_b64_tr_b16 a[200:201], v252 offset:8192\n"
"v_mfma_f32_16x16x32_bf16 v[212:215], a[168:171], a[148:151], 0\n"
"buffer_load_dwordx4 v242, s[72:75], s52 offen offset:0 lds\n"
"s_mov_b32 s52, 505856\n"
"v_mfma_f32_16x16x32_bf16 v[216:219], a[168:171], a[152:155], 0\n"
"ds_read_b64_tr_b16 a[202:203], v252 offset:9216\n"
"ds_read_b64_tr_b16 a[204:205], v253 offset:8192\n"
"v_mfma_f32_16x16x32_bf16 v[220:223], a[168:171], a[156:159], 0\n"
"buffer_load_dwordx4 v242, s[72:75], s52 offen offset:1024 lds\n"
"s_mov_b32 s52, 520192\n"
"v_mfma_f32_16x16x32_bf16 v[224:227], a[172:175], a[144:147], 0\n"
"ds_read_b64_tr_b16 a[206:207], v253 offset:9216\n"
"v_mfma_f32_16x16x32_bf16 v[228:231], a[172:175], a[148:151], 0\n"
"buffer_load_dwordx4 v243, s[72:75], s52 offen offset:2048 lds\n"
"s_mov_b32 s52, 534528\n"
"v_mfma_f32_16x16x32_bf16 v[232:235], a[172:175], a[152:155], 0\n"
"buffer_load_dwordx4 v243, s[72:75], s52 offen offset:3072 lds\n"
"v_mfma_f32_16x16x32_bf16 v[236:239], a[172:175], a[156:159], 0\n"
"s_waitcnt lgkmcnt(0)\n"
"v_mfma_f32_16x16x32_bf16 v[176:179], a[192:195], a[176:179], v[176:179]\n"
"s_waitcnt vmcnt(8)\n"
"v_mfma_f32_16x16x32_bf16 v[180:183], a[192:195], a[180:183], v[180:183]\n"
"s_barrier\n"
"ds_read_b64_tr_b16 a[160:161], v250 offset:16384\n"
"v_mfma_f32_16x16x32_bf16 v[184:187], a[192:195], a[184:187], v[184:187]\n"
"s_add_u32 m0, s57, 49152\n"
"ds_read_b64_tr_b16 a[162:163], v250 offset:17408\n"
"s_mov_b32 s52, 384\n"
"v_mfma_f32_16x16x32_bf16 v[188:191], a[192:195], a[188:191], v[188:191]\n"
"buffer_load_dwordx4 v244, s[68:71], s52 offen offset:0 lds\n"
"s_mov_b32 s52, 9600\n"
"v_mfma_f32_16x16x32_bf16 v[192:195], a[196:199], a[176:179], v[192:195]\n"
"buffer_load_dwordx4 v245, s[68:71], s52 offen offset:1024 lds\n"
"s_mov_b32 s52, 18816\n"
"v_mfma_f32_16x16x32_bf16 v[196:199], a[196:199], a[180:183], v[196:199]\n"
"ds_read_b128 a[144:147], v248 offset:16384\n"
"ds_read_b128 a[148:151], v248 offset:18432\n"
"v_mfma_f32_16x16x32_bf16 v[200:203], a[196:199], a[184:187], v[200:203]\n"
"ds_read_b128 a[152:155], v248 offset:20480\n"
"ds_read_b128 a[156:159], v248 offset:22528\n"
"v_mfma_f32_16x16x32_bf16 v[204:207], a[196:199], a[188:191], v[204:207]\n"
"buffer_load_dwordx4 v244, s[68:71], s52 offen offset:2048 lds\n"
"s_mov_b32 s52, 28032\n"
"v_mfma_f32_16x16x32_bf16 v[208:211], a[200:203], a[176:179], v[208:211]\n"
"ds_read_b64_tr_b16 a[164:165], v251 offset:16384\n"
"ds_read_b64_tr_b16 a[166:167], v251 offset:17408\n"
"v_mfma_f32_16x16x32_bf16 v[212:215], a[200:203], a[180:183], v[212:215]\n"
"buffer_load_dwordx4 v245, s[68:71], s52 offen offset:3072 lds\n"
"s_add_u32 m0, s57, 114688\n"
"v_mfma_f32_16x16x32_bf16 v[216:219], a[200:203], a[184:187], v[216:219]\n"
"s_mov_b32 s52, 737280\n"
"ds_read_b64_tr_b16 a[168:169], v252 offset:16384\n"
"ds_read_b64_tr_b16 a[170:171], v252 offset:17408\n"
"v_mfma_f32_16x16x32_bf16 v[220:223], a[200:203], a[188:191], v[220:223]\n"
"ds_read_b64_tr_b16 a[172:173], v253 offset:16384\n"
"ds_read_b64_tr_b16 a[174:175], v253 offset:17408\n"
"v_mfma_f32_16x16x32_bf16 v[224:227], a[204:207], a[176:179], v[224:227]\n"
"buffer_load_dwordx4 v242, s[72:75], s52 offen offset:0 lds\n"
"s_mov_b32 s52, 751616\n"
"v_mfma_f32_16x16x32_bf16 v[228:231], a[204:207], a[180:183], v[228:231]\n"
"buffer_load_dwordx4 v242, s[72:75], s52 offen offset:1024 lds\n"
"s_mov_b32 s52, 765952\n"
"v_mfma_f32_16x16x32_bf16 v[232:235], a[204:207], a[184:187], v[232:235]\n"
"buffer_load_dwordx4 v243, s[72:75], s52 offen offset:2048 lds\n"
"s_mov_b32 s52, 780288\n"
"v_mfma_f32_16x16x32_bf16 v[236:239], a[204:207], a[188:191], v[236:239]\n"
"s_waitcnt lgkmcnt(0)\n"
"buffer_load_dwordx4 v243, s[72:75], s52 offen offset:3072 lds\n"
"v_mfma_f32_16x16x32_bf16 v[176:179], a[160:163], a[144:147], v[176:179]\n"
"s_add_u32 m0, s57, 0\n"
"ds_read_b64_tr_b16 a[192:193], v250 offset:24576\n"
"s_mov_b32 s52, 512\n"
"v_mfma_f32_16x16x32_bf16 v[180:183], a[160:163], a[148:151], v[180:183]\n"
"buffer_load_dwordx4 v244, s[68:71], s52 offen offset:0 lds\n"
"s_mov_b32 s52, 9728\n"
"v_mfma_f32_16x16x32_bf16 v[184:187], a[160:163], a[152:155], v[184:187]\n"
"buffer_load_dwordx4 v245, s[68:71], s52 offen offset:1024 lds\n"
"s_mov_b32 s52, 18944\n"
"v_mfma_f32_16x16x32_bf16 v[188:191], a[160:163], a[156:159], v[188:191]\n"
"ds_read_b64_tr_b16 a[194:195], v250 offset:25600\n"
"ds_read_b128 a[176:179], v249 offset:16384\n"
"v_mfma_f32_16x16x32_bf16 v[192:195], a[164:167], a[144:147], v[192:195]\n"
"buffer_load_dwordx4 v244, s[68:71], s52 offen offset:2048 lds\n"
"s_mov_b32 s52, 28160\n"
"v_mfma_f32_16x16x32_bf16 v[196:199], a[164:167], a[148:151], v[196:199]\n"
"ds_read_b128 a[180:183], v249 offset:18432\n"
"ds_read_b128 a[184:187], v249 offset:20480\n"
"v_mfma_f32_16x16x32_bf16 v[200:203], a[164:167], a[152:155], v[200:203]\n"
"buffer_load_dwordx4 v245, s[68:71], s52 offen offset:3072 lds\n"
"s_add_u32 m0, s57, 65536\n"
"v_mfma_f32_16x16x32_bf16 v[204:207], a[164:167], a[156:159], v[204:207]\n"
"s_mov_b32 s52, 983040\n"
"ds_read_b128 a[188:191], v249 offset:22528\n"
"ds_read_b64_tr_b16 a[196:197], v251 offset:24576\n"
"v_mfma_f32_16x16x32_bf16 v[208:211], a[168:171], a[144:147], v[208:211]\n"
"ds_read_b64_tr_b16 a[198:199], v251 offset:25600\n"
"ds_read_b64_tr_b16 a[200:201], v252 offset:24576\n"
"v_mfma_f32_16x16x32_bf16 v[212:215], a[168:171], a[148:151], v[212:215]\n"
"buffer_load_dwordx4 v242, s[72:75], s52 offen offset:0 lds\n"
"s_mov_b32 s52, 997376\n"
"v_mfma_f32_16x16x32_bf16 v[216:219], a[168:171], a[152:155], v[216:219]\n"
"ds_read_b64_tr_b16 a[202:203], v252 offset:25600\n"
"ds_read_b64_tr_b16 a[204:205], v253 offset:24576\n"
"v_mfma_f32_16x16x32_bf16 v[220:223], a[168:171], a[156:159], v[220:223]\n"
"buffer_load_dwordx4 v242, s[72:75], s52 offen offset:1024 lds\n"
"s_mov_b32 s52, 1011712\n"
"v_mfma_f32_16x16x32_bf16 v[224:227], a[172:175], a[144:147], v[224:227]\n"
"ds_read_b64_tr_b16 a[206:207], v253 offset:25600\n"
"v_mfma_f32_16x16x32_bf16 v[228:231], a[172:175], a[148:151], v[228:231]\n"
"buffer_load_dwordx4 v243, s[72:75], s52 offen offset:2048 lds\n"
"s_mov_b32 s52, 1026048\n"
"v_mfma_f32_16x16x32_bf16 v[232:235], a[172:175], a[152:155], v[232:235]\n"
"buffer_load_dwordx4 v243, s[72:75], s52 offen offset:3072 lds\n"
"v_mfma_f32_16x16x32_bf16 v[236:239], a[172:175], a[156:159], v[236:239]\n"
"s_waitcnt lgkmcnt(0)\n"
"v_mfma_f32_16x16x32_bf16 v[176:179], a[192:195], a[176:179], v[176:179]\n"
"s_waitcnt vmcnt(16)\n"
"v_mfma_f32_16x16x32_bf16 v[180:183], a[192:195], a[180:183], v[180:183]\n"
"s_barrier\n"
"ds_read_b64_tr_b16 a[160:161], v250 offset:32768\n"
"v_mfma_f32_16x16x32_bf16 v[184:187], a[192:195], a[184:187], v[184:187]\n"
"s_add_u32 m0, s57, 16384\n"
"ds_read_b64_tr_b16 a[162:163], v250 offset:33792\n"
"s_mov_b32 s52, 640\n"
"v_mfma_f32_16x16x32_bf16 v[188:191], a[192:195], a[188:191], v[188:191]\n"
"buffer_load_dwordx4 v244, s[68:71], s52 offen offset:0 lds\n"
"s_mov_b32 s52, 9856\n"
"v_mfma_f32_16x16x32_bf16 v[192:195], a[196:199], a[176:179], v[192:195]\n"
"buffer_load_dwordx4 v245, s[68:71], s52 offen offset:1024 lds\n"
"v_mfma_f32_16x16x32_bf16 v[196:199], a[196:199], a[180:183], v[196:199]\n"
"s_mov_b32 s52, 19072\n"
"ds_read_b128 a[144:147], v248 offset:32768\n"
"ds_read_b128 a[148:151], v248 offset:34816\n"
"v_mfma_f32_16x16x32_bf16 v[200:203], a[196:199], a[184:187], v[200:203]\n"
"ds_read_b128 a[152:155], v248 offset:36864\n"
"ds_read_b128 a[156:159], v248 offset:38912\n"
"v_mfma_f32_16x16x32_bf16 v[204:207], a[196:199], a[188:191], v[204:207]\n"
"buffer_load_dwordx4 v244, s[68:71], s52 offen offset:2048 lds\n"
"s_mov_b32 s52, 28288\n"
"v_mfma_f32_16x16x32_bf16 v[208:211], a[200:203], a[176:179], v[208:211]\n"
"ds_read_b64_tr_b16 a[164:165], v251 offset:32768\n"
"ds_read_b64_tr_b16 a[166:167], v251 offset:33792\n"
"v_mfma_f32_16x16x32_bf16 v[212:215], a[200:203], a[180:183], v[212:215]\n"
"buffer_load_dwordx4 v245, s[68:71], s52 offen offset:3072 lds\n"
"s_lshl_b32 s34, s33, 7\n"
"v_mfma_f32_16x16x32_bf16 v[216:219], a[200:203], a[184:187], v[216:219]\n"
"s_add_u32 s34, s34, s32\n"
"ds_read_b64_tr_b16 a[168:169], v252 offset:32768\n"
"ds_read_b64_tr_b16 a[170:171], v252 offset:33792\n"
"v_mfma_f32_16x16x32_bf16 v[220:223], a[200:203], a[188:191], v[220:223]\n"
"s_and_b32 s35, s34, 1\n"
"ds_read_b64_tr_b16 a[172:173], v253 offset:32768\n"
"ds_read_b64_tr_b16 a[174:175], v253 offset:33792\n"
"v_mfma_f32_16x16x32_bf16 v[224:227], a[204:207], a[176:179], v[224:227]\n"
"s_sub_u32 s34, s34, s35\n"
"s_lshl_b32 s34, s34, 4\n"
"s_add_u32 s34, s34, s35\n"
"v_mfma_f32_16x16x32_bf16 v[228:231], a[204:207], a[180:183], v[228:231]\n"
"s_add_u32 s34, s34, 0\n"
"s_lshl_b32 s34, s34, 7\n"
"s_mov_b32 s30, 4\n"
"v_mfma_f32_16x16x32_bf16 v[232:235], a[204:207], a[184:187], v[232:235]\n"
"s_mov_b64 s[28:29], s[14:15]\n"
"s_or_b32 s29, s29, 67108864\n"
"v_mfma_f32_16x16x32_bf16 v[236:239], a[204:207], a[188:191], v[236:239]\n"
"s_waitcnt lgkmcnt(0)\n"
"buffer_load_dword a255, v255, s[28:31], s34 idxen\n"
"v_mfma_f32_16x16x32_bf16 v[176:179], a[160:163], a[144:147], v[176:179]\n"
"s_lshr_b32 s34, s32, 4\n"
"ds_read_b64_tr_b16 a[192:193], v250 offset:40960\n"
"s_mul_i32 s34, s34, 128\n"
"v_mfma_f32_16x16x32_bf16 v[180:183], a[160:163], a[148:151], v[180:183]\n"
"s_add_u32 s34, s34, s33\n"
"ds_read_b64_tr_b16 a[194:195], v250 offset:41984\n"
"s_lshl_b32 s34, s34, 4\n"
"v_mfma_f32_16x16x32_bf16 v[184:187], a[160:163], a[152:155], v[184:187]\n"
"s_and_b32 s35, s32, 14\n"
"ds_read_b128 a[176:179], v249 offset:32768\n"
"s_add_u32 s34, s34, s35\n"
"v_mfma_f32_16x16x32_bf16 v[188:191], a[160:163], a[156:159], v[188:191]\n"
"s_lshl_b32 s34, s34, 6\n"
"ds_read_b128 a[180:183], v249 offset:34816\n"
"s_add_u32 s34, s34, 0\n"
"v_mfma_f32_16x16x32_bf16 v[192:195], a[164:167], a[144:147], v[192:195]\n"
"s_mov_b32 s30, 16\n"
"s_bitcmp1_b32 s32, 0\n"
"s_cselect_b64 s[28:29], s[18:19], s[20:21]\n"
"v_mfma_f32_16x16x32_bf16 v[196:199], a[164:167], a[148:151], v[196:199]\n"
"s_or_b32 s29, s29, 536870912\n"
"buffer_load_dword a255, v255, s[28:31], s34 idxen\n"
"v_mfma_f32_16x16x32_bf16 v[200:203], a[164:167], a[152:155], v[200:203]\n"
"s_add_u32 m0, s57, 81920\n"
"ds_read_b128 a[184:187], v249 offset:36864\n"
"ds_read_b128 a[188:191], v249 offset:38912\n"
"v_mfma_f32_16x16x32_bf16 v[204:207], a[164:167], a[156:159], v[204:207]\n"
"s_mov_b32 s52, 1228800\n"
"ds_read_b64_tr_b16 a[196:197], v251 offset:40960\n"
"ds_read_b64_tr_b16 a[198:199], v251 offset:41984\n"
"v_mfma_f32_16x16x32_bf16 v[208:211], a[168:171], a[144:147], v[208:211]\n"
"buffer_load_dwordx4 v242, s[72:75], s52 offen offset:0 lds\n"
"s_mov_b32 s52, 1243136\n"
"v_mfma_f32_16x16x32_bf16 v[212:215], a[168:171], a[148:151], v[212:215]\n"
"ds_read_b64_tr_b16 a[200:201], v252 offset:40960\n"
"ds_read_b64_tr_b16 a[202:203], v252 offset:41984\n"
"v_mfma_f32_16x16x32_bf16 v[216:219], a[168:171], a[152:155], v[216:219]\n"
"buffer_load_dwordx4 v242, s[72:75], s52 offen offset:1024 lds\n"
"s_mov_b32 s52, 1257472\n"
"v_mfma_f32_16x16x32_bf16 v[220:223], a[168:171], a[156:159], v[220:223]\n"
"ds_read_b64_tr_b16 a[204:205], v253 offset:40960\n"
"ds_read_b64_tr_b16 a[206:207], v253 offset:41984\n"
"v_mfma_f32_16x16x32_bf16 v[224:227], a[172:175], a[144:147], v[224:227]\n"
"buffer_load_dwordx4 v243, s[72:75], s52 offen offset:2048 lds\n"
"s_mov_b32 s52, 1271808\n"
"v_mfma_f32_16x16x32_bf16 v[228:231], a[172:175], a[148:151], v[228:231]\n"
"buffer_load_dwordx4 v243, s[72:75], s52 offen offset:3072 lds\n"
"v_mfma_f32_16x16x32_bf16 v[232:235], a[172:175], a[152:155], v[232:235]\n"
"v_mfma_f32_16x16x32_bf16 v[236:239], a[172:175], a[156:159], v[236:239]\n"
"s_waitcnt lgkmcnt(0)\n"
"v_mfma_f32_16x16x32_bf16 v[176:179], a[192:195], a[176:179], v[176:179]\n"
"s_waitcnt vmcnt(18)\n"
"v_mfma_f32_16x16x32_bf16 v[180:183], a[192:195], a[180:183], v[180:183]\n"
"s_barrier\n"
"ds_read_b64_tr_b16 a[160:161], v250 offset:49152\n"
"v_mfma_f32_16x16x32_bf16 v[184:187], a[192:195], a[184:187], v[184:187]\n"
"s_add_u32 m0, s57, 32768\n"
"ds_read_b64_tr_b16 a[162:163], v250 offset:50176\n"
"s_mov_b32 s52, 768\n"
"v_mfma_f32_16x16x32_bf16 v[188:191], a[192:195], a[188:191], v[188:191]\n"
"buffer_load_dwordx4 v244, s[68:71], s52 offen offset:0 lds\n"
"v_mfma_f32_16x16x32_bf16 v[192:195], a[196:199], a[176:179], v[192:195]\n"
"s_mov_b32 s52, 9984\n"
"ds_read_b128 a[144:147], v248 offset:49152\n"
"ds_read_b128 a[148:151], v248 offset:51200\n"
"v_mfma_f32_16x16x32_bf16 v[196:199], a[196:199], a[180:183], v[196:199]\n"
"buffer_load_dwordx4 v245, s[68:71], s52 offen offset:1024 lds\n"
"v_mfma_f32_16x16x32_bf16 v[200:203], a[196:199], a[184:187], v[200:203]\n"
"ds_read_b128 a[152:155], v248 offset:53248\n"
"ds_read_b128 a[156:159], v248 offset:55296\n"
"v_mfma_f32_16x16x32_bf16 v[204:207], a[196:199], a[188:191], v[204:207]\n"
"s_mov_b32 s52, 19200\n"
"ds_read_b64_tr_b16 a[164:165], v251 offset:49152\n"
"ds_read_b64_tr_b16 a[166:167], v251 offset:50176\n"
"v_mfma_f32_16x16x32_bf16 v[208:211], a[200:203], a[176:179], v[208:211]\n"
"buffer_load_dwordx4 v244, s[68:71], s52 offen offset:2048 lds\n"
"v_mfma_f32_16x16x32_bf16 v[212:215], a[200:203], a[180:183], v[212:215]\n"
"ds_read_b64_tr_b16 a[168:169], v252 offset:49152\n"
"ds_read_b64_tr_b16 a[170:171], v252 offset:50176\n"
"v_mfma_f32_16x16x32_bf16 v[216:219], a[200:203], a[184:187], v[216:219]\n"
"s_mov_b32 s52, 28416\n"
"ds_read_b64_tr_b16 a[172:173], v253 offset:49152\n"
"v_mfma_f32_16x16x32_bf16 v[220:223], a[200:203], a[188:191], v[220:223]\n"
"buffer_load_dwordx4 v245, s[68:71], s52 offen offset:3072 lds\n"
"v_mfma_f32_16x16x32_bf16 v[224:227], a[204:207], a[176:179], v[224:227]\n"
"ds_read_b64_tr_b16 a[174:175], v253 offset:50176\n"
"v_mfma_f32_16x16x32_bf16 v[228:231], a[204:207], a[180:183], v[228:231]\n"
"v_mfma_f32_16x16x32_bf16 v[232:235], a[204:207], a[184:187], v[232:235]\n"
"v_mfma_f32_16x16x32_bf16 v[236:239], a[204:207], a[188:191], v[236:239]\n"
"s_waitcnt lgkmcnt(0)\n"
"v_mfma_f32_16x16x32_bf16 v[176:179], a[160:163], a[144:147], v[176:179]\n"
"s_add_u32 m0, s57, 98304\n"
"ds_read_b64_tr_b16 a[192:193], v250 offset:57344\n"
"s_mov_b32 s52, 1474560\n"
"v_mfma_f32_16x16x32_bf16 v[180:183], a[160:163], a[148:151], v[180:183]\n"
"buffer_load_dwordx4 v242, s[72:75], s52 offen offset:0 lds\n"
"v_mfma_f32_16x16x32_bf16 v[184:187], a[160:163], a[152:155], v[184:187]\n"
"s_mov_b32 s52, 1488896\n"
"ds_read_b64_tr_b16 a[194:195], v250 offset:58368\n"
"ds_read_b128 a[176:179], v249 offset:49152\n"
"v_mfma_f32_16x16x32_bf16 v[188:191], a[160:163], a[156:159], v[188:191]\n"
"buffer_load_dwordx4 v242, s[72:75], s52 offen offset:1024 lds\n"
"v_mfma_f32_16x16x32_bf16 v[192:195], a[164:167], a[144:147], v[192:195]\n"
"s_mov_b32 s52, 1503232\n"
"ds_read_b128 a[180:183], v249 offset:51200\n"
"ds_read_b128 a[184:187], v249 offset:53248\n"
"v_mfma_f32_16x16x32_bf16 v[196:199], a[164:167], a[148:151], v[196:199]\n"
"buffer_load_dwordx4 v243, s[72:75], s52 offen offset:2048 lds\n"
"v_mfma_f32_16x16x32_bf16 v[200:203], a[164:167], a[152:155], v[200:203]\n"
"s_mov_b32 s52, 1517568\n"
"ds_read_b128 a[188:191], v249 offset:55296\n"
"ds_read_b64_tr_b16 a[196:197], v251 offset:57344\n"
"v_mfma_f32_16x16x32_bf16 v[204:207], a[164:167], a[156:159], v[204:207]\n"
"buffer_load_dwordx4 v243, s[72:75], s52 offen offset:3072 lds\n"
"v_mfma_f32_16x16x32_bf16 v[208:211], a[168:171], a[144:147], v[208:211]\n"
"ds_read_b64_tr_b16 a[198:199], v251 offset:58368\n"
"ds_read_b64_tr_b16 a[200:201], v252 offset:57344\n"
"v_mfma_f32_16x16x32_bf16 v[212:215], a[168:171], a[148:151], v[212:215]\n"
"s_lshr_b32 s34, s32, 4\n"
"ds_read_b64_tr_b16 a[202:203], v252 offset:58368\n"
"s_mul_i32 s34, s34, 128\n"
"v_mfma_f32_16x16x32_bf16 v[216:219], a[168:171], a[152:155], v[216:219]\n"
"s_add_u32 s34, s34, s33\n"
"ds_read_b64_tr_b16 a[204:205], v253 offset:57344\n"
"s_lshl_b32 s34, s34, 4\n"
"v_mfma_f32_16x16x32_bf16 v[220:223], a[168:171], a[156:159], v[220:223]\n"
"s_and_b32 s35, s32, 14\n"
"ds_read_b64_tr_b16 a[206:207], v253 offset:58368\n"
"s_add_u32 s34, s34, s35\n"
"v_mfma_f32_16x16x32_bf16 v[224:227], a[172:175], a[144:147], v[224:227]\n"
"s_lshl_b32 s34, s34, 6\n"
"s_add_u32 s34, s34, 1048576\n"
"s_mov_b32 s30, 16\n"
"v_mfma_f32_16x16x32_bf16 v[228:231], a[172:175], a[148:151], v[228:231]\n"
"s_bitcmp1_b32 s32, 0\n"
"s_cselect_b64 s[28:29], s[18:19], s[20:21]\n"
"s_or_b32 s29, s29, 536870912\n"
"v_mfma_f32_16x16x32_bf16 v[232:235], a[172:175], a[152:155], v[232:235]\n"
"buffer_load_dword a255, v255, s[28:31], s34 idxen\n"
"s_lshl_b32 s34, s33, 7\n"
"v_mfma_f32_16x16x32_bf16 v[236:239], a[172:175], a[156:159], v[236:239]\n"
"s_waitcnt lgkmcnt(0)\n"
"s_add_u32 s34, s34, s32\n"
"s_and_b32 s35, s34, 1\n"
"v_mfma_f32_16x16x32_bf16 v[176:179], a[192:195], a[176:179], v[176:179]\n"
"s_waitcnt vmcnt(19)\n"
"s_sub_u32 s34, s34, s35\n"
"s_lshl_b32 s34, s34, 4\n"
"v_mfma_f32_16x16x32_bf16 v[180:183], a[192:195], a[180:183], v[180:183]\n"
"s_barrier\n"
"ds_read_b64_tr_b16 a[160:161], v250 offset:0\n"
"s_add_u32 s34, s34, s35\n"
"v_mfma_f32_16x16x32_bf16 v[184:187], a[192:195], a[184:187], v[184:187]\n"
"s_add_u32 s34, s34, 2\n"
"s_lshl_b32 s34, s34, 7\n"
"s_mov_b32 s30, 4\n"
"s_mov_b64 s[28:29], s[14:15]\n"
"s_or_b32 s29, s29, 67108864\n"
"buffer_load_dword a255, v255, s[28:31], s34 idxen\n"
"v_mfma_f32_16x16x32_bf16 v[188:191], a[192:195], a[188:191], v[188:191]\n"
"s_add_u32 m0, s57, 49152\n"
"ds_read_b64_tr_b16 a[162:163], v250 offset:1024\n"
"ds_read_b128 a[144:147], v248 offset:0\n"
"v_mfma_f32_16x16x32_bf16 v[192:195], a[196:199], a[176:179], v[192:195]\n"
"s_mov_b32 s52, 896\n"
"buffer_load_dwordx4 v244, s[68:71], s52 offen offset:0 lds\n"
"v_mfma_f32_16x16x32_bf16 v[196:199], a[196:199], a[180:183], v[196:199]\n"
"s_mov_b32 s52, 10112\n"
"ds_read_b128 a[148:151], v248 offset:2048\n"
"ds_read_b128 a[152:155], v248 offset:4096\n"
"v_mfma_f32_16x16x32_bf16 v[200:203], a[196:199], a[184:187], v[200:203]\n"
".Lpad1_%=:\n"
"s_cbranch_execz .Lpad5_%=\n"
"buffer_load_dwordx4 v245, s[68:71], s52 offen offset:1024 lds\n"
"v_mfma_f32_16x16x32_bf16 v[204:207], a[196:199], a[188:191], v[204:207]\n"
"s_mov_b32 s52, 19328\n"
"ds_read_b128 a[156:159], v248 offset:6144\n"
"ds_read_b64_tr_b16 a[164:165], v251 offset:0\n"
"v_mfma_f32_16x16x32_bf16 v[208:211], a[200:203], a[176:179], v[208:211]\n"
"buffer_load_dwordx4 v244, s[68:71], s52 offen offset:2048 lds\n"
"v_mfma_f32_16x16x32_bf16 v[212:215], a[200:203], a[180:183], v[212:215]\n"
"ds_read_b64_tr_b16 a[166:167], v251 offset:1024\n"
"ds_read_b64_tr_b16 a[168:169], v252 offset:0\n"
"v_mfma_f32_16x16x32_bf16 v[216:219], a[200:203], a[184:187], v[216:219]\n"
"s_mov_b32 s52, 28544\n"
"ds_read_b64_tr_b16 a[170:171], v252 offset:1024\n"
"ds_read_b64_tr_b16 a[172:173], v253 offset:0\n"
"v_mfma_f32_16x16x32_bf16 v[220:223], a[200:203], a[188:191], v[220:223]\n"
"buffer_load_dwordx4 v245, s[68:71], s52 offen offset:3072 lds\n"
"v_mfma_f32_16x16x32_bf16 v[224:227], a[204:207], a[176:179], v[224:227]\n"
"ds_read_b64_tr_b16 a[174:175], v253 offset:1024\n"
"v_mfma_f32_16x16x32_bf16 v[228:231], a[204:207], a[180:183], v[228:231]\n"
"v_mfma_f32_16x16x32_bf16 v[232:235], a[204:207], a[184:187], v[232:235]\n"
"v_mfma_f32_16x16x32_bf16 v[236:239], a[204:207], a[188:191], v[236:239]\n"
"s_waitcnt lgkmcnt(0)\n"
"v_mfma_f32_16x16x32_bf16 v[176:179], a[160:163], a[144:147], v[176:179]\n"
"s_add_u32 m0, s57, 114688\n"
"ds_read_b64_tr_b16 a[192:193], v250 offset:8192\n"
"s_mov_b32 s52, 1720320\n"
"v_mfma_f32_16x16x32_bf16 v[180:183], a[160:163], a[148:151], v[180:183]\n"
"buffer_load_dwordx4 v242, s[72:75], s52 offen offset:0 lds\n"
"v_mfma_f32_16x16x32_bf16 v[184:187], a[160:163], a[152:155], v[184:187]\n"
"ds_read_b64_tr_b16 a[194:195], v250 offset:9216\n"
"ds_read_b128 a[176:179], v249 offset:0\n"
"v_mfma_f32_16x16x32_bf16 v[188:191], a[160:163], a[156:159], v[188:191]\n"
"s_mov_b32 s52, 1734656\n"
"ds_read_b128 a[180:183], v249 offset:2048\n"
"v_mfma_f32_16x16x32_bf16 v[192:195], a[164:167], a[144:147], v[192:195]\n"
"buffer_load_dwordx4 v242, s[72:75], s52 offen offset:1024 lds\n"
"v_mfma_f32_16x16x32_bf16 v[196:199], a[164:167], a[148:151], v[196:199]\n"
"ds_read_b128 a[184:187], v249 offset:4096\n"
"ds_read_b128 a[188:191], v249 offset:6144\n"
"v_mfma_f32_16x16x32_bf16 v[200:203], a[164:167], a[152:155], v[200:203]\n"
"s_mov_b32 s52, 1748992\n"
"ds_read_b64_tr_b16 a[196:197], v251 offset:8192\n"
"v_mfma_f32_16x16x32_bf16 v[204:207], a[164:167], a[156:159], v[204:207]\n"
"buffer_load_dwordx4 v243, s[72:75], s52 offen offset:2048 lds\n"
"v_mfma_f32_16x16x32_bf16 v[208:211], a[168:171], a[144:147], v[208:211]\n"
"ds_read_b64_tr_b16 a[198:199], v251 offset:9216\n"
"ds_read_b64_tr_b16 a[200:201], v252 offset:8192\n"
"v_mfma_f32_16x16x32_bf16 v[212:215], a[168:171], a[148:151], v[212:215]\n"
"ds_read_b64_tr_b16 a[202:203], v252 offset:9216\n"
"v_mfma_f32_16x16x32_bf16 v[216:219], a[168:171], a[152:155], v[216:219]\n"
"s_mov_b32 s52, 1763328\n"
"ds_read_b64_tr_b16 a[204:205], v253 offset:8192\n"
"v_mfma_f32_16x16x32_bf16 v[220:223], a[168:171], a[156:159], v[220:223]\n"
"buffer_load_dwordx4 v243, s[72:75], s52 offen offset:3072 lds\n"
"v_mfma_f32_16x16x32_bf16 v[224:227], a[172:175], a[144:147], v[224:227]\n"
"ds_read_b64_tr_b16 a[206:207], v253 offset:9216\n"
"v_mfma_f32_16x16x32_bf16 v[228:231], a[172:175], a[148:151], v[228:231]\n"
"v_mfma_f32_16x16x32_bf16 v[232:235], a[172:175], a[152:155], v[232:235]\n"
"v_mfma_f32_16x16x32_bf16 v[236:239], a[172:175], a[156:159], v[236:239]\n"
"s_waitcnt lgkmcnt(0)\n"
"v_mfma_f32_16x16x32_bf16 v[176:179], a[192:195], a[176:179], v[176:179]\n"
"s_waitcnt vmcnt(18)\n"
"v_mfma_f32_16x16x32_bf16 v[180:183], a[192:195], a[180:183], v[180:183]\n"
"s_barrier\n"
"ds_read_b64_tr_b16 a[160:161], v250 offset:16384\n"
"v_mfma_f32_16x16x32_bf16 v[184:187], a[192:195], a[184:187], v[184:187]\n"
"s_add_u32 m0, s57, 0\n"
"ds_read_b64_tr_b16 a[162:163], v250 offset:17408\n"
"s_mov_b32 s52, 1024\n"
"v_mfma_f32_16x16x32_bf16 v[188:191], a[192:195], a[188:191], v[188:191]\n"
"buffer_load_dwordx4 v244, s[68:71], s52 offen offset:0 lds\n"
"v_mfma_f32_16x16x32_bf16 v[192:195], a[196:199], a[176:179], v[192:195]\n"
"s_mov_b32 s52, 10240\n"
"ds_read_b128 a[144:147], v248 offset:16384\n"
"ds_read_b128 a[148:151], v248 offset:18432\n"
"v_mfma_f32_16x16x32_bf16 v[196:199], a[196:199], a[180:183], v[196:199]\n"
"buffer_load_dwordx4 v245, s[68:71], s52 offen offset:1024 lds\n"
"v_mfma_f32_16x16x32_bf16 v[200:203], a[196:199], a[184:187], v[200:203]\n"
"ds_read_b128 a[152:155], v248 offset:20480\n"
"ds_read_b128 a[156:159], v248 offset:22528\n"
"v_mfma_f32_16x16x32_bf16 v[204:207], a[196:199], a[188:191], v[204:207]\n"
"s_mov_b32 s52, 19456\n"
"ds_read_b64_tr_b16 a[164:165], v251 offset:16384\n"
"ds_read_b64_tr_b16 a[166:167], v251 offset:17408\n"
"v_mfma_f32_16x16x32_bf16 v[208:211], a[200:203], a[176:179], v[208:211]\n"
"buffer_load_dwordx4 v244, s[68:71], s52 offen offset:2048 lds\n"
"v_mfma_f32_16x16x32_bf16 v[212:215], a[200:203], a[180:183], v[212:215]\n"
"ds_read_b64_tr_b16 a[168:169], v252 offset:16384\n"
"ds_read_b64_tr_b16 a[170:171], v252 offset:17408\n"
"v_mfma_f32_16x16x32_bf16 v[216:219], a[200:203], a[184:187], v[216:219]\n"
"s_mov_b32 s52, 28672\n"
"ds_read_b64_tr_b16 a[172:173], v253 offset:16384\n"
"v_mfma_f32_16x16x32_bf16 v[220:223], a[200:203], a[188:191], v[220:223]\n"
"buffer_load_dwordx4 v245, s[68:71], s52 offen offset:3072 lds\n"
"v_mfma_f32_16x16x32_bf16 v[224:227], a[204:207], a[176:179], v[224:227]\n"
"ds_read_b64_tr_b16 a[174:175], v253 offset:17408\n"
"v_mfma_f32_16x16x32_bf16 v[228:231], a[204:207], a[180:183], v[228:231]\n"
"v_mfma_f32_16x16x32_bf16 v[232:235], a[204:207], a[184:187], v[232:235]\n"
"v_mfma_f32_16x16x32_bf16 v[236:239], a[204:207], a[188:191], v[236:239]\n"
"s_waitcnt lgkmcnt(0)\n"
"v_mfma_f32_16x16x32_bf16 v[176:179], a[160:163], a[144:147], v[176:179]\n"
"s_add_u32 m0, s57, 65536\n"
"ds_read_b64_tr_b16 a[192:193], v250 offset:24576\n"
"s_mov_b32 s52, 1966080\n"
"v_mfma_f32_16x16x32_bf16 v[180:183], a[160:163], a[148:151], v[180:183]\n"
"buffer_load_dwordx4 v242, s[72:75], s52 offen offset:0 lds\n"
"v_mfma_f32_16x16x32_bf16 v[184:187], a[160:163], a[152:155], v[184:187]\n"
"s_mov_b32 s52, 1980416\n"
"ds_read_b64_tr_b16 a[194:195], v250 offset:25600\n"
"ds_read_b128 a[176:179], v249 offset:16384\n"
"v_mfma_f32_16x16x32_bf16 v[188:191], a[160:163], a[156:159], v[188:191]\n"
"buffer_load_dwordx4 v242, s[72:75], s52 offen offset:1024 lds\n"
"v_mfma_f32_16x16x32_bf16 v[192:195], a[164:167], a[144:147], v[192:195]\n"
"s_mov_b32 s52, 1994752\n"
"ds_read_b128 a[180:183], v249 offset:18432\n"
"ds_read_b128 a[184:187], v249 offset:20480\n"
"v_mfma_f32_16x16x32_bf16 v[196:199], a[164:167], a[148:151], v[196:199]\n"
"buffer_load_dwordx4 v243, s[72:75], s52 offen offset:2048 lds\n"
"v_mfma_f32_16x16x32_bf16 v[200:203], a[164:167], a[152:155], v[200:203]\n"
"s_mov_b32 s52, 2009088\n"
"ds_read_b128 a[188:191], v249 offset:22528\n"
"ds_read_b64_tr_b16 a[196:197], v251 offset:24576\n"
"v_mfma_f32_16x16x32_bf16 v[204:207], a[164:167], a[156:159], v[204:207]\n"
"buffer_load_dwordx4 v243, s[72:75], s52 offen offset:3072 lds\n"
"v_mfma_f32_16x16x32_bf16 v[208:211], a[168:171], a[144:147], v[208:211]\n"
"ds_read_b64_tr_b16 a[198:199], v251 offset:25600\n"
"ds_read_b64_tr_b16 a[200:201], v252 offset:24576\n"
"v_mfma_f32_16x16x32_bf16 v[212:215], a[168:171], a[148:151], v[212:215]\n"
"s_mul_i32 s34, s77, 800\n"
"ds_read_b64_tr_b16 a[202:203], v252 offset:25600\n"
"s_andn2_b32 s34, s34, 127\n"
"v_mfma_f32_16x16x32_bf16 v[216:219], a[168:171], a[152:155], v[216:219]\n"
"s_mov_b32 s30, 7\n"
"ds_read_b64_tr_b16 a[204:205], v253 offset:24576\n"
"s_mov_b64 s[28:29], s[8:9]\n"
"v_mfma_f32_16x16x32_bf16 v[220:223], a[168:171], a[156:159], v[220:223]\n"
"s_or_b32 s29, s29, 8388608\n"
"buffer_load_dword a255, v255, s[28:31], s34 idxen\n"
"v_mfma_f32_16x16x32_bf16 v[224:227], a[172:175], a[144:147], v[224:227]\n"
"s_mov_b32 s35, s33\n"
"ds_read_b64_tr_b16 a[206:207], v253 offset:25600\n"
"s_lshl_b32 s35, s35, 7\n"
"v_mfma_f32_16x16x32_bf16 v[228:231], a[172:175], a[148:151], v[228:231]\n"
"s_add_u32 s34, s35, s32\n"
"s_mul_i32 s34, s34, 5120\n"
"s_mov_b32 s30, 40\n"
"v_mfma_f32_16x16x32_bf16 v[232:235], a[172:175], a[152:155], v[232:235]\n"
"s_mov_b64 s[28:29], s[22:23]\n"
"s_or_b32 s29, s29, 8388608\n"
"v_mfma_f32_16x16x32_bf16 v[236:239], a[172:175], a[156:159], v[236:239]\n"
"s_waitcnt lgkmcnt(0)\n"
"buffer_load_dword a255, v255, s[28:31], s34 idxen\n"
"v_mfma_f32_16x16x32_bf16 v[176:179], a[192:195], a[176:179], v[176:179]\n"
"s_waitcnt vmcnt(20)\n"
"v_mfma_f32_16x16x32_bf16 v[180:183], a[192:195], a[180:183], v[180:183]\n"
"s_barrier\n"
"ds_read_b64_tr_b16 a[160:161], v250 offset:32768\n"
"v_mfma_f32_16x16x32_bf16 v[184:187], a[192:195], a[184:187], v[184:187]\n"
"s_add_u32 m0, s57, 16384\n"
"ds_read_b64_tr_b16 a[162:163], v250 offset:33792\n"
"s_mov_b32 s52, 1152\n"
"v_mfma_f32_16x16x32_bf16 v[188:191], a[192:195], a[188:191], v[188:191]\n"
"buffer_load_dwordx4 v244, s[68:71], s52 offen offset:0 lds\n"
"v_mfma_f32_16x16x32_bf16 v[192:195], a[196:199], a[176:179], v[192:195]\n"
"s_mov_b32 s52, 10368\n"
"ds_read_b128 a[144:147], v248 offset:32768\n"
"ds_read_b128 a[148:151], v248 offset:34816\n"
"v_mfma_f32_16x16x32_bf16 v[196:199], a[196:199], a[180:183], v[196:199]\n"
"buffer_load_dwordx4 v245, s[68:71], s52 offen offset:1024 lds\n"
"v_mfma_f32_16x16x32_bf16 v[200:203], a[196:199], a[184:187], v[200:203]\n"
"ds_read_b128 a[152:155], v248 offset:36864\n"
"ds_read_b128 a[156:159], v248 offset:38912\n"
"v_mfma_f32_16x16x32_bf16 v[204:207], a[196:199], a[188:191], v[204:207]\n"
"s_mov_b32 s52, 19584\n"
"ds_read_b64_tr_b16 a[164:165], v251 offset:32768\n"
"ds_read_b64_tr_b16 a[166:167], v251 offset:33792\n"
"v_mfma_f32_16x16x32_bf16 v[208:211], a[200:203], a[176:179], v[208:211]\n"
"buffer_load_dwordx4 v244, s[68:71], s52 offen offset:2048 lds\n"
"v_mfma_f32_16x16x32_bf16 v[212:215], a[200:203], a[180:183], v[212:215]\n"
"ds_read_b64_tr_b16 a[168:169], v252 offset:32768\n"
"ds_read_b64_tr_b16 a[170:171], v252 offset:33792\n"
"v_mfma_f32_16x16x32_bf16 v[216:219], a[200:203], a[184:187], v[216:219]\n"
"s_mov_b32 s52, 28800\n"
"ds_read_b64_tr_b16 a[172:173], v253 offset:32768\n"
"v_mfma_f32_16x16x32_bf16 v[220:223], a[200:203], a[188:191], v[220:223]\n"
"buffer_load_dwordx4 v245, s[68:71], s52 offen offset:3072 lds\n"
"v_mfma_f32_16x16x32_bf16 v[224:227], a[204:207], a[176:179], v[224:227]\n"
"ds_read_b64_tr_b16 a[174:175], v253 offset:33792\n"
"v_mfma_f32_16x16x32_bf16 v[228:231], a[204:207], a[180:183], v[228:231]\n"
"v_mfma_f32_16x16x32_bf16 v[232:235], a[204:207], a[184:187], v[232:235]\n"
"v_mfma_f32_16x16x32_bf16 v[236:239], a[204:207], a[188:191], v[236:239]\n"
"s_waitcnt lgkmcnt(0)\n"
"v_mfma_f32_16x16x32_bf16 v[176:179], a[160:163], a[144:147], v[176:179]\n"
"s_add_u32 m0, s57, 81920\n"
"ds_read_b64_tr_b16 a[192:193], v250 offset:40960\n"
"s_mov_b32 s52, 2211840\n"
"v_mfma_f32_16x16x32_bf16 v[180:183], a[160:163], a[148:151], v[180:183]\n"
"buffer_load_dwordx4 v242, s[72:75], s52 offen offset:0 lds\n"
"v_mfma_f32_16x16x32_bf16 v[184:187], a[160:163], a[152:155], v[184:187]\n"
"ds_read_b64_tr_b16 a[194:195], v250 offset:41984\n"
"ds_read_b128 a[176:179], v249 offset:32768\n"
"v_mfma_f32_16x16x32_bf16 v[188:191], a[160:163], a[156:159], v[188:191]\n"
"s_mov_b32 s52, 2226176\n"
"ds_read_b128 a[180:183], v249 offset:34816\n"
"v_mfma_f32_16x16x32_bf16 v[192:195], a[164:167], a[144:147], v[192:195]\n"
"buffer_load_dwordx4 v242, s[72:75], s52 offen offset:1024 lds\n"
"v_mfma_f32_16x16x32_bf16 v[196:199], a[164:167], a[148:151], v[196:199]\n"
"ds_read_b128 a[184:187], v249 offset:36864\n"
"ds_read_b128 a[188:191], v249 offset:38912\n"
"v_mfma_f32_16x16x32_bf16 v[200:203], a[164:167], a[152:155], v[200:203]\n"
"s_mov_b32 s52, 2240512\n"
"ds_read_b64_tr_b16 a[196:197], v251 offset:40960\n"
"v_mfma_f32_16x16x32_bf16 v[204:207], a[164:167], a[156:159], v[204:207]\n"
"buffer_load_dwordx4 v243, s[72:75], s52 offen offset:2048 lds\n"
"v_mfma_f32_16x16x32_bf16 v[208:211], a[168:171], a[144:147], v[208:211]\n"
"ds_read_b64_tr_b16 a[198:199], v251 offset:41984\n"
"ds_read_b64_tr_b16 a[200:201], v252 offset:40960\n"
"v_mfma_f32_16x16x32_bf16 v[212:215], a[168:171], a[148:151], v[212:215]\n"
"ds_read_b64_tr_b16 a[202:203], v252 offset:41984\n"
"v_mfma_f32_16x16x32_bf16 v[216:219], a[168:171], a[152:155], v[216:219]\n"
"s_mov_b32 s52, 2254848\n"
"ds_read_b64_tr_b16 a[204:205], v253 offset:40960\n"
"v_mfma_f32_16x16x32_bf16 v[220:223], a[168:171], a[156:159], v[220:223]\n"
"buffer_load_dwordx4 v243, s[72:75], s52 offen offset:3072 lds\n"
"v_mfma_f32_16x16x32_bf16 v[224:227], a[172:175], a[144:147], v[224:227]\n"
"s_and_b32 s59, s57, 8192\n"
"ds_read_b64_tr_b16 a[206:207], v253 offset:41984\n"
"s_add_u32 s60, s59, 131072\n"
"v_mfma_f32_16x16x32_bf16 v[228:231], a[172:175], a[148:151], v[228:231]\n"
"v_add_u32 v242, s60, v248\n"
"v_add_u32 v243, s60, v249\n"
"v_mfma_f32_16x16x32_bf16 v[232:235], a[172:175], a[152:155], v[232:235]\n"
"v_mfma_f32_16x16x32_bf16 v[236:239], a[172:175], a[156:159], v[236:239]\n"
"s_waitcnt lgkmcnt(0)\n"
"v_mfma_f32_16x16x32_bf16 v[176:179], a[192:195], a[176:179], v[176:179]\n"
"s_waitcnt vmcnt(18)\n"
"v_mfma_f32_16x16x32_bf16 v[180:183], a[192:195], a[180:183], v[180:183]\n"
"s_barrier\n"
"ds_read_b64_tr_b16 a[160:161], v250 offset:49152\n"
"v_mfma_f32_16x16x32_bf16 v[184:187], a[192:195], a[184:187], v[184:187]\n"
"s_add_u32 m0, s89, 131072\n"
"ds_read_b64_tr_b16 a[162:163], v250 offset:50176\n"
"s_mov_b32 s52, 0\n"
"v_mfma_f32_16x16x32_bf16 v[188:191], a[192:195], a[188:191], v[188:191]\n"
"buffer_load_dwordx4 v240, s[36:39], s52 offen offset:0 lds\n"
"s_mov_b32 s52, 7168\n"
"v_mfma_f32_16x16x32_bf16 v[192:195], a[196:199], a[176:179], v[192:195]\n"
"buffer_load_dwordx4 v241, s[36:39], s52 offen offset:1024 lds\n"
"s_mov_b32 s52, 14336\n"
"v_mfma_f32_16x16x32_bf16 v[196:199], a[196:199], a[180:183], v[196:199]\n"
"ds_read_b128 a[144:147], v248 offset:49152\n"
"ds_read_b128 a[148:151], v248 offset:51200\n"
"v_mfma_f32_16x16x32_bf16 v[200:203], a[196:199], a[184:187], v[200:203]\n"
"ds_read_b128 a[152:155], v248 offset:53248\n"
"ds_read_b128 a[156:159], v248 offset:55296\n"
"v_mfma_f32_16x16x32_bf16 v[204:207], a[196:199], a[188:191], v[204:207]\n"
"buffer_load_dwordx4 v240, s[36:39], s52 offen offset:2048 lds\n"
"s_mov_b32 s52, 21504\n"
"v_mfma_f32_16x16x32_bf16 v[208:211], a[200:203], a[176:179], v[208:211]\n"
"ds_read_b64_tr_b16 a[164:165], v251 offset:49152\n"
"ds_read_b64_tr_b16 a[166:167], v251 offset:50176\n"
"v_mfma_f32_16x16x32_bf16 v[212:215], a[200:203], a[180:183], v[212:215]\n"
"buffer_load_dwordx4 v241, s[36:39], s52 offen offset:3072 lds\n"
"s_add_u32 m0, s89, 135168\n"
"v_mfma_f32_16x16x32_bf16 v[216:219], a[200:203], a[184:187], v[216:219]\n"
"s_mov_b32 s52, 262144\n"
"ds_read_b64_tr_b16 a[168:169], v252 offset:49152\n"
"ds_read_b64_tr_b16 a[170:171], v252 offset:50176\n"
"v_mfma_f32_16x16x32_bf16 v[220:223], a[200:203], a[188:191], v[220:223]\n"
"ds_read_b64_tr_b16 a[172:173], v253 offset:49152\n"
"ds_read_b64_tr_b16 a[174:175], v253 offset:50176\n"
"v_mfma_f32_16x16x32_bf16 v[224:227], a[204:207], a[176:179], v[224:227]\n"
"buffer_load_dwordx4 v240, s[36:39], s52 offen offset:0 lds\n"
"s_mov_b32 s52, 269312\n"
"v_mfma_f32_16x16x32_bf16 v[228:231], a[204:207], a[180:183], v[228:231]\n"
"buffer_load_dwordx4 v241, s[36:39], s52 offen offset:1024 lds\n"
"s_mov_b32 s52, 276480\n"
"v_mfma_f32_16x16x32_bf16 v[232:235], a[204:207], a[184:187], v[232:235]\n"
"buffer_load_dwordx4 v240, s[36:39], s52 offen offset:2048 lds\n"
"s_mov_b32 s52, 283648\n"
"v_mfma_f32_16x16x32_bf16 v[236:239], a[204:207], a[188:191], v[236:239]\n"
"s_waitcnt lgkmcnt(0)\n"
"buffer_load_dwordx4 v241, s[36:39], s52 offen offset:3072 lds\n"
"v_mfma_f32_16x16x32_bf16 v[176:179], a[160:163], a[144:147], v[176:179]\n"
"s_add_u32 m0, s57, 98304\n"
"ds_read_b64_tr_b16 a[192:193], v250 offset:57344\n"
"s_mov_b32 s52, 0\n"
"v_mfma_f32_16x16x32_bf16 v[180:183], a[160:163], a[148:151], v[180:183]\n"
"buffer_load_dwordx4 v246, s[40:43], s52 offen offset:0 lds\n"
"v_mfma_f32_16x16x32_bf16 v[184:187], a[160:163], a[152:155], v[184:187]\n"
"ds_read_b64_tr_b16 a[194:195], v250 offset:58368\n"
"ds_read_b128 a[176:179], v249 offset:49152\n"
"v_mfma_f32_16x16x32_bf16 v[188:191], a[160:163], a[156:159], v[188:191]\n"
"s_mov_b32 s52, 31744\n"
"ds_read_b128 a[180:183], v249 offset:51200\n"
"v_mfma_f32_16x16x32_bf16 v[192:195], a[164:167], a[144:147], v[192:195]\n"
"buffer_load_dwordx4 v246, s[40:43], s52 offen offset:1024 lds\n"
"v_mfma_f32_16x16x32_bf16 v[196:199], a[164:167], a[148:151], v[196:199]\n"
"ds_read_b128 a[184:187], v249 offset:53248\n"
"ds_read_b128 a[188:191], v249 offset:55296\n"
"v_mfma_f32_16x16x32_bf16 v[200:203], a[164:167], a[152:155], v[200:203]\n"
"s_mov_b32 s52, 63488\n"
"ds_read_b64_tr_b16 a[196:197], v251 offset:57344\n"
"v_mfma_f32_16x16x32_bf16 v[204:207], a[164:167], a[156:159], v[204:207]\n"
"buffer_load_dwordx4 v247, s[40:43], s52 offen offset:2048 lds\n"
"v_mfma_f32_16x16x32_bf16 v[208:211], a[168:171], a[144:147], v[208:211]\n"
"ds_read_b64_tr_b16 a[198:199], v251 offset:58368\n"
"ds_read_b64_tr_b16 a[200:201], v252 offset:57344\n"
"v_mfma_f32_16x16x32_bf16 v[212:215], a[168:171], a[148:151], v[212:215]\n"
"ds_read_b64_tr_b16 a[202:203], v252 offset:58368\n"
"v_mfma_f32_16x16x32_bf16 v[216:219], a[168:171], a[152:155], v[216:219]\n"
"s_mov_b32 s52, 95232\n"
"ds_read_b64_tr_b16 a[204:205], v253 offset:57344\n"
"v_mfma_f32_16x16x32_bf16 v[220:223], a[168:171], a[156:159], v[220:223]\n"
"buffer_load_dwordx4 v247, s[40:43], s52 offen offset:3072 lds\n"
"v_mfma_f32_16x16x32_bf16 v[224:227], a[172:175], a[144:147], v[224:227]\n"
"ds_read_b64_tr_b16 a[206:207], v253 offset:58368\n"
"v_mfma_f32_16x16x32_bf16 v[228:231], a[172:175], a[148:151], v[228:231]\n"
"v_mfma_f32_16x16x32_bf16 v[232:235], a[172:175], a[152:155], v[232:235]\n"
"v_mfma_f32_16x16x32_bf16 v[236:239], a[172:175], a[156:159], v[236:239]\n"
"s_waitcnt lgkmcnt(0)\n"
"v_mfma_f32_16x16x32_bf16 v[176:179], a[192:195], a[176:179], v[176:179]\n"
"s_waitcnt vmcnt(22)\n"
"v_mfma_f32_16x16x32_bf16 v[180:183], a[192:195], a[180:183], v[180:183]\n"
"s_barrier\n"
"ds_read_b64_tr_b16 a[160:161], v250 offset:0\n"
"v_mfma_f32_16x16x32_bf16 v[184:187], a[192:195], a[184:187], v[184:187]\n"
"s_add_u32 m0, s57, 114688\n"
"ds_read_b64_tr_b16 a[162:163], v250 offset:1024\n"
"s_mov_b32 s52, 262144\n"
"v_mfma_f32_16x16x32_bf16 v[188:191], a[192:195], a[188:191], v[188:191]\n"
"buffer_load_dwordx4 v246, s[40:43], s52 offen offset:0 lds\n"
"v_mfma_f32_16x16x32_bf16 v[192:195], a[196:199], a[176:179], v[192:195]\n"
"s_mov_b32 s52, 293888\n"
"ds_read_b128 a[144:147], v248 offset:0\n"
"ds_read_b128 a[148:151], v248 offset:2048\n"
"v_mfma_f32_16x16x32_bf16 v[196:199], a[196:199], a[180:183], v[196:199]\n"
"buffer_load_dwordx4 v246, s[40:43], s52 offen offset:1024 lds\n"
"v_mfma_f32_16x16x32_bf16 v[200:203], a[196:199], a[184:187], v[200:203]\n"
"ds_read_b128 a[152:155], v248 offset:4096\n"
"ds_read_b128 a[156:159], v248 offset:6144\n"
"v_mfma_f32_16x16x32_bf16 v[204:207], a[196:199], a[188:191], v[204:207]\n"
"s_mov_b32 s52, 325632\n"
"ds_read_b64_tr_b16 a[164:165], v251 offset:0\n"
"ds_read_b64_tr_b16 a[166:167], v251 offset:1024\n"
"v_mfma_f32_16x16x32_bf16 v[208:211], a[200:203], a[176:179], v[208:211]\n"
"buffer_load_dwordx4 v247, s[40:43], s52 offen offset:2048 lds\n"
"v_mfma_f32_16x16x32_bf16 v[212:215], a[200:203], a[180:183], v[212:215]\n"
"ds_read_b64_tr_b16 a[168:169], v252 offset:0\n"
"ds_read_b64_tr_b16 a[170:171], v252 offset:1024\n"
"v_mfma_f32_16x16x32_bf16 v[216:219], a[200:203], a[184:187], v[216:219]\n"
"s_mov_b32 s52, 357376\n"
"ds_read_b64_tr_b16 a[172:173], v253 offset:0\n"
"v_mfma_f32_16x16x32_bf16 v[220:223], a[200:203], a[188:191], v[220:223]\n"
"buffer_load_dwordx4 v247, s[40:43], s52 offen offset:3072 lds\n"
"v_mfma_f32_16x16x32_bf16 v[224:227], a[204:207], a[176:179], v[224:227]\n"
"ds_read_b64_tr_b16 a[174:175], v253 offset:1024\n"
"v_mfma_f32_16x16x32_bf16 v[228:231], a[204:207], a[180:183], v[228:231]\n"
"v_mfma_f32_16x16x32_bf16 v[232:235], a[204:207], a[184:187], v[232:235]\n"
"v_mfma_f32_16x16x32_bf16 v[236:239], a[204:207], a[188:191], v[236:239]\n"
"s_waitcnt lgkmcnt(0)\n"
"v_mfma_f32_16x16x32_bf16 v[176:179], a[160:163], a[144:147], v[176:179]\n"
"s_add_u32 m0, s89, 32768\n"
"ds_read_b64_tr_b16 a[192:193], v250 offset:8192\n"
"s_mov_b32 s52, 128\n"
"v_mfma_f32_16x16x32_bf16 v[180:183], a[160:163], a[148:151], v[180:183]\n"
"buffer_load_dwordx4 v240, s[36:39], s52 offen offset:0 lds\n"
"v_mfma_f32_16x16x32_bf16 v[184:187], a[160:163], a[152:155], v[184:187]\n"
"ds_read_b64_tr_b16 a[194:195], v250 offset:9216\n"
"ds_read_b128 a[176:179], v249 offset:0\n"
"v_mfma_f32_16x16x32_bf16 v[188:191], a[160:163], a[156:159], v[188:191]\n"
"s_mov_b32 s52, 7296\n"
"ds_read_b128 a[180:183], v249 offset:2048\n"
"v_mfma_f32_16x16x32_bf16 v[192:195], a[164:167], a[144:147], v[192:195]\n"
"buffer_load_dwordx4 v241, s[36:39], s52 offen offset:1024 lds\n"
"v_mfma_f32_16x16x32_bf16 v[196:199], a[164:167], a[148:151], v[196:199]\n"
"ds_read_b128 a[184:187], v249 offset:4096\n"
"ds_read_b128 a[188:191], v249 offset:6144\n"
"v_mfma_f32_16x16x32_bf16 v[200:203], a[164:167], a[152:155], v[200:203]\n"
"s_mov_b32 s52, 14464\n"
"ds_read_b64_tr_b16 a[196:197], v251 offset:8192\n"
"v_mfma_f32_16x16x32_bf16 v[204:207], a[164:167], a[156:159], v[204:207]\n"
"buffer_load_dwordx4 v240, s[36:39], s52 offen offset:2048 lds\n"
"v_mfma_f32_16x16x32_bf16 v[208:211], a[168:171], a[144:147], v[208:211]\n"
"ds_read_b64_tr_b16 a[198:199], v251 offset:9216\n"
"ds_read_b64_tr_b16 a[200:201], v252 offset:8192\n"
"v_mfma_f32_16x16x32_bf16 v[212:215], a[168:171], a[148:151], v[212:215]\n"
"ds_read_b64_tr_b16 a[202:203], v252 offset:9216\n"
"v_mfma_f32_16x16x32_bf16 v[216:219], a[168:171], a[152:155], v[216:219]\n"
"s_mov_b32 s52, 21632\n"
"ds_read_b64_tr_b16 a[204:205], v253 offset:8192\n"
"v_mfma_f32_16x16x32_bf16 v[220:223], a[168:171], a[156:159], v[220:223]\n"
"buffer_load_dwordx4 v241, s[36:39], s52 offen offset:3072 lds\n"
"v_mfma_f32_16x16x32_bf16 v[224:227], a[172:175], a[144:147], v[224:227]\n"
"ds_read_b64_tr_b16 a[206:207], v253 offset:9216\n"
"v_mfma_f32_16x16x32_bf16 v[228:231], a[172:175], a[148:151], v[228:231]\n"
"v_mfma_f32_16x16x32_bf16 v[232:235], a[172:175], a[152:155], v[232:235]\n"
"v_mfma_f32_16x16x32_bf16 v[236:239], a[172:175], a[156:159], v[236:239]\n"
"s_waitcnt lgkmcnt(0)\n"
"v_mfma_f32_16x16x32_bf16 v[176:179], a[192:195], a[176:179], v[176:179]\n"
"s_waitcnt vmcnt(8)\n"
"v_mfma_f32_16x16x32_bf16 v[180:183], a[192:195], a[180:183], v[180:183]\n"
"s_barrier\n"
"ds_read_b64_tr_b16 a[160:161], v250 offset:16384\n"
"v_mfma_f32_16x16x32_bf16 v[184:187], a[192:195], a[184:187], v[184:187]\n"
"s_add_u32 m0, s89, 36864\n"
"ds_read_b64_tr_b16 a[162:163], v250 offset:17408\n"
"ds_read_b128 a[144:147], v248 offset:16384\n"
"v_mfma_f32_16x16x32_bf16 v[188:191], a[192:195], a[188:191], v[188:191]\n"
"s_mov_b32 s52, 262272\n"
"buffer_load_dwordx4 v240, s[36:39], s52 offen offset:0 lds\n"
"v_mfma_f32_16x16x32_bf16 v[192:195], a[196:199], a[176:179], v[192:195]\n"
"s_mov_b32 s52, 269440\n"
"ds_read_b128 a[148:151], v248 offset:18432\n"
"ds_read_b128 a[152:155], v248 offset:20480\n"
"v_mfma_f32_16x16x32_bf16 v[196:199], a[196:199], a[180:183], v[196:199]\n"
"ds_read_b128 a[156:159], v248 offset:22528\n"
"ds_read_b64_tr_b16 a[164:165], v251 offset:16384\n"
"v_mfma_f32_16x16x32_bf16 v[200:203], a[196:199], a[184:187], v[200:203]\n"
"ds_read_b64_tr_b16 a[166:167], v251 offset:17408\n"
"ds_read_b64_tr_b16 a[168:169], v252 offset:16384\n"
"v_mfma_f32_16x16x32_bf16 v[204:207], a[196:199], a[188:191], v[204:207]\n"
"ds_read_b64_tr_b16 a[170:171], v252 offset:17408\n"
"ds_read_b64_tr_b16 a[172:173], v253 offset:16384\n"
"v_mfma_f32_16x16x32_bf16 v[208:211], a[200:203], a[176:179], v[208:211]\n"
"ds_read_b64_tr_b16 a[174:175], v253 offset:17408\n"
"ds_read_b128 a[0:3], v242 offset:0\n"
"v_mfma_f32_16x16x32_bf16 v[212:215], a[200:203], a[180:183], v[212:215]\n"
"buffer_load_dwordx4 v241, s[36:39], s52 offen offset:1024 lds\n"
"s_mov_b32 s52, 276608\n"
"v_mfma_f32_16x16x32_bf16 v[216:219], a[200:203], a[184:187], v[216:219]\n"
"ds_read_b64_tr_b16 a[32:33], v250 offset:32768\n"
"ds_read_b64_tr_b16 a[34:35], v250 offset:33792\n"
"v_mfma_f32_16x16x32_bf16 v[220:223], a[200:203], a[188:191], v[220:223]\n"
"buffer_load_dwordx4 v240, s[36:39], s52 offen offset:2048 lds\n"
"s_mov_b32 s52, 283776\n"
"v_mfma_f32_16x16x32_bf16 v[224:227], a[204:207], a[176:179], v[224:227]\n"
"buffer_load_dwordx4 v241, s[36:39], s52 offen offset:3072 lds\n"
"v_mfma_f32_16x16x32_bf16 v[228:231], a[204:207], a[180:183], v[228:231]\n"
"ds_read_b64_tr_b16 a[36:37], v251 offset:32768\n"
"ds_read_b64_tr_b16 a[38:39], v251 offset:33792\n"
"v_mfma_f32_16x16x32_bf16 v[232:235], a[204:207], a[184:187], v[232:235]\n"
"ds_read_b64_tr_b16 a[40:41], v252 offset:32768\n"
"v_mfma_f32_16x16x32_bf16 v[236:239], a[204:207], a[188:191], v[236:239]\n"
"s_waitcnt lgkmcnt(6)\n"
"v_mfma_f32_16x16x32_bf16 v[176:179], a[160:163], a[144:147], v[176:179]\n"
"s_add_u32 m0, s57, 65536\n"
"ds_read_b64_tr_b16 a[192:193], v250 offset:24576\n"
"s_mov_b32 s52, 524288\n"
"v_mfma_f32_16x16x32_bf16 v[180:183], a[160:163], a[148:151], v[180:183]\n"
"buffer_load_dwordx4 v246, s[40:43], s52 offen offset:0 lds\n"
"v_mfma_f32_16x16x32_bf16 v[184:187], a[160:163], a[152:155], v[184:187]\n"
"ds_read_b64_tr_b16 a[194:195], v250 offset:25600\n"
"ds_read_b128 a[176:179], v249 offset:16384\n"
"v_mfma_f32_16x16x32_bf16 v[188:191], a[160:163], a[156:159], v[188:191]\n"
"s_mov_b32 s52, 556032\n"
"ds_read_b128 a[180:183], v249 offset:18432\n"
"ds_read_b128 a[184:187], v249 offset:20480\n"
"v_mfma_f32_16x16x32_bf16 v[192:195], a[164:167], a[144:147], v[192:195]\n"
"buffer_load_dwordx4 v246, s[40:43], s52 offen offset:1024 lds\n"
"v_mfma_f32_16x16x32_bf16 v[196:199], a[164:167], a[148:151], v[196:199]\n"
"ds_read_b128 a[188:191], v249 offset:22528\n"
"ds_read_b64_tr_b16 a[196:197], v251 offset:24576\n"
"v_mfma_f32_16x16x32_bf16 v[200:203], a[164:167], a[152:155], v[200:203]\n"
"s_mov_b32 s52, 587776\n"
"ds_read_b64_tr_b16 a[198:199], v251 offset:25600\n"
"ds_read_b64_tr_b16 a[200:201], v252 offset:24576\n"
"v_mfma_f32_16x16x32_bf16 v[204:207], a[164:167], a[156:159], v[204:207]\n"
"ds_read_b64_tr_b16 a[202:203], v252 offset:25600\n"
"ds_read_b64_tr_b16 a[204:205], v253 offset:24576\n"
"v_mfma_f32_16x16x32_bf16 v[208:211], a[168:171], a[144:147], v[208:211]\n"
"ds_read_b64_tr_b16 a[206:207], v253 offset:25600\n"
"ds_read_b64_tr_b16 a[42:43], v252 offset:33792\n"
"v_mfma_f32_16x16x32_bf16 v[212:215], a[168:171], a[148:151], v[212:215]\n"
"buffer_load_dwordx4 v247, s[40:43], s52 offen offset:2048 lds\n"
"v_mfma_f32_16x16x32_bf16 v[216:219], a[168:171], a[152:155], v[216:219]\n"
"s_mov_b32 s52, 619520\n"
"ds_read_b64_tr_b16 a[44:45], v253 offset:32768\n"
"ds_read_b64_tr_b16 a[46:47], v253 offset:33792\n"
"v_mfma_f32_16x16x32_bf16 v[220:223], a[168:171], a[156:159], v[220:223]\n"
"buffer_load_dwordx4 v247, s[40:43], s52 offen offset:3072 lds\n"
"v_mfma_f32_16x16x32_bf16 v[224:227], a[172:175], a[144:147], v[224:227]\n"
"ds_read_b64_tr_b16 a[128:129], v250 offset:40960\n"
"v_mfma_f32_16x16x32_bf16 v[228:231], a[172:175], a[148:151], v[228:231]\n"
"ds_read_b64_tr_b16 a[130:131], v250 offset:41984\n"
"v_mfma_f32_16x16x32_bf16 v[232:235], a[172:175], a[152:155], v[232:235]\n"
"ds_read_b64_tr_b16 a[132:133], v251 offset:40960\n"
"v_mfma_f32_16x16x32_bf16 v[236:239], a[172:175], a[156:159], v[236:239]\n"
"s_waitcnt lgkmcnt(6)\n"
"v_mfma_f32_16x16x32_bf16 v[176:179], a[192:195], a[176:179], v[176:179]\n"
"ds_read_b64_tr_b16 a[134:135], v251 offset:41984\n"
"v_add_u32 v248, s59, v248\n"
"v_mfma_f32_16x16x32_bf16 v[180:183], a[192:195], a[180:183], v[180:183]\n"
"s_barrier\n"
"ds_read_b64_tr_b16 a[136:137], v252 offset:40960\n"
"v_add_u32 v249, s59, v249\n"
"v_mfma_f32_16x16x32_bf16 v[184:187], a[192:195], a[184:187], v[184:187]\n"
"s_add_u32 m0, s57, 81920\n"
"ds_read_b64_tr_b16 a[138:139], v252 offset:41984\n"
"s_mov_b32 s52, 786432\n"
"v_mfma_f32_16x16x32_bf16 v[188:191], a[192:195], a[188:191], v[188:191]\n"
"buffer_load_dwordx4 v246, s[40:43], s52 offen offset:0 lds\n"
"v_mfma_f32_16x16x32_bf16 v[192:195], a[196:199], a[176:179], v[192:195]\n"
"s_mov_b32 s52, 818176\n"
"ds_read_b64_tr_b16 a[140:141], v253 offset:40960\n"
"ds_read_b64_tr_b16 a[142:143], v253 offset:41984\n"
"v_mfma_f32_16x16x32_bf16 v[196:199], a[196:199], a[180:183], v[196:199]\n"
"buffer_load_dwordx4 v246, s[40:43], s52 offen offset:1024 lds\n"
"v_mfma_f32_16x16x32_bf16 v[200:203], a[196:199], a[184:187], v[200:203]\n"
"ds_read_b128 a[4:7], v242 offset:2048\n"
"ds_read_b128 a[8:11], v242 offset:4096\n"
"v_mfma_f32_16x16x32_bf16 v[204:207], a[196:199], a[188:191], v[204:207]\n"
"s_mov_b32 s52, 849920\n"
"ds_read_b128 a[12:15], v242 offset:6144\n"
"ds_read_b128 a[16:19], v242 offset:8192\n"
"v_mfma_f32_16x16x32_bf16 v[208:211], a[200:203], a[176:179], v[208:211]\n"
"buffer_load_dwordx4 v247, s[40:43], s52 offen offset:2048 lds\n"
"v_mfma_f32_16x16x32_bf16 v[212:215], a[200:203], a[180:183], v[212:215]\n"
"ds_read_b128 a[20:23], v242 offset:10240\n"
"ds_read_b128 a[24:27], v242 offset:12288\n"
"v_mfma_f32_16x16x32_bf16 v[216:219], a[200:203], a[184:187], v[216:219]\n"
"s_mov_b32 s52, 881664\n"
"ds_read_b128 a[28:31], v242 offset:14336\n"
".Lpad2_%=:\n"
"s_cbranch_execz .Lpad6_%=\n"
"v_cvt_pk_bf16_f32 v176, v176, v177\n"
"v_mfma_f32_16x16x32_bf16 v[220:223], a[200:203], a[188:191], v[220:223]\n"
"buffer_load_dwordx4 v247, s[40:43], s52 offen offset:3072 lds\n"
"s_add_u32 m0, s57, 98304\n"
"v_mfma_f32_16x16x32_bf16 v[224:227], a[204:207], a[176:179], v[224:227]\n"
"s_mov_b32 s52, 1048576\n"
"v_cvt_pk_bf16_f32 v177, v178, v179\n"
"v_cvt_pk_bf16_f32 v178, v180, v181\n"
"v_mfma_f32_16x16x32_bf16 v[228:231], a[204:207], a[180:183], v[228:231]\n"
"v_cvt_pk_bf16_f32 v179, v182, v183\n"
"v_mfma_f32_16x16x32_bf16 v[232:235], a[204:207], a[184:187], v[232:235]\n"
"v_permlane16_swap_b32 v176, v178\n"
"v_permlane16_swap_b32 v177, v179\n"
"v_mfma_f32_16x16x32_bf16 v[236:239], a[204:207], a[188:191], v[236:239]\n"
"s_waitcnt lgkmcnt(0)\n"
"v_cvt_pk_bf16_f32 v184, v184, v185\n"
"v_cvt_pk_bf16_f32 v185, v186, v187\n"
"v_mfma_f32_16x16x32_bf16 v[0:3], a[32:35], a[0:3], 0\n"
"s_waitcnt vmcnt(16)\n"
"v_cvt_pk_bf16_f32 v186, v188, v189\n"
"v_cvt_pk_bf16_f32 v187, v190, v191\n"
"v_mfma_f32_16x16x32_bf16 v[128:131], a[36:39], a[0:3], 0\n"
"s_barrier\n"
"v_permlane16_swap_b32 v184, v186\n"
"v_permlane16_swap_b32 v185, v187\n"
"v_mfma_f32_16x16x32_bf16 v[4:7], a[40:43], a[0:3], 0\n"
"buffer_load_dwordx4 v246, s[40:43], s52 offen offset:0 lds\n"
"s_mov_b32 s52, 1080320\n"
"v_mfma_f32_16x16x32_bf16 v[132:135], a[44:47], a[0:3], 0\n"
"ds_read_b128 a[144:147], v243 offset:0\n"
"v_lshrrev_b32 v180, 5, v255\n"
"v_mfma_f32_16x16x32_bf16 v[16:19], a[0:3], a[128:131], 0\n"
"v_add_u32 v180, s81, v180\n"
"v_and_b32 v181, 511, v254\n"
"v_mfma_f32_16x16x32_bf16 v[20:23], a[0:3], a[132:135], 0\n"
"ds_read_b64_tr_b16 a[176:177], v250 offset:49152\n"
"v_and_b32 v182, 256, v254\n"
"v_mfma_f32_16x16x32_bf16 v[24:27], a[0:3], a[136:139], 0\n"
"buffer_load_dwordx4 v246, s[40:43], s52 offen offset:1024 lds\n"
"s_mov_b32 s52, 1112064\n"
"v_mfma_f32_16x16x32_bf16 v[28:31], a[0:3], a[140:143], 0\n"
"ds_read_b64_tr_b16 a[178:179], v250 offset:50176\n"
"v_mul_u32_u24 v182, 15, v182\n"
"v_mfma_f32_16x16x32_bf16 v[8:11], a[32:35], a[4:7], 0\n"
"v_add_u32 v181, v181, v182\n"
"v_lshl_add_u32 v181, v180, 8, v181\n"
"s_add_u32 s58, s56, 8192\n"
"v_mfma_f32_16x16x32_bf16 v[136:139], a[36:39], a[4:7], 0\n"
"ds_read_b64_tr_b16 a[180:181], v251 offset:49152\n"
"v_add_u32 v182, 0, v180\n"
"v_mfma_f32_16x16x32_bf16 v[12:15], a[40:43], a[4:7], 0\n"
"buffer_load_dwordx4 v247, s[40:43], s52 offen offset:2048 lds\n"
"s_mov_b32 s52, 1143808\n"
"v_mfma_f32_16x16x32_bf16 v[140:143], a[44:47], a[4:7], 0\n"
"ds_read_b64_tr_b16 a[182:183], v251 offset:50176\n"
"v_mul_u32_u24 v182, 205, v182\n"
"v_mfma_f32_16x16x32_bf16 v[144:147], a[4:7], a[128:131], 0\n"
"v_lshrrev_b32 v182, 10, v182\n"
"v_mul_u32_u24 v182, 523008, v182\n"
"v_mfma_f32_16x16x32_bf16 v[148:151], a[4:7], a[132:135], 0\n"
"ds_read_b64_tr_b16 a[184:185], v252 offset:49152\n"
"v_add_u32 v188, v182, v181\n"
"v_mfma_f32_16x16x32_bf16 v[152:155], a[4:7], a[136:139], 0\n"
"buffer_load_dwordx4 v247, s[40:43], s52 offen offset:3072 lds\n"
"s_lshr_b32 s34, s32, 4\n"
"v_mfma_f32_16x16x32_bf16 v[156:159], a[4:7], a[140:143], 0\n"
"s_mul_i32 s34, s34, 256\n"
"ds_read_b64_tr_b16 a[186:187], v252 offset:50176\n"
"s_add_u32 s34, s34, s33\n"
"v_mfma_f32_16x16x32_bf16 v[32:35], a[32:35], a[8:11], 0\n"
"s_lshl_b32 s34, s34, 4\n"
"s_and_b32 s35, s32, 14\n"
"s_add_u32 s34, s34, s35\n"
"v_mfma_f32_16x16x32_bf16 v[160:163], a[36:39], a[8:11], 0\n"
"s_lshl_b32 s34, s34, 6\n"
"s_add_u32 s34, s34, 2097152\n"
"s_mov_b32 s30, 32\n"
"v_mfma_f32_16x16x32_bf16 v[36:39], a[40:43], a[8:11], 0\n"
"ds_read_b64_tr_b16 a[188:189], v253 offset:49152\n"
"s_bitcmp1_b32 s32, 0\n"
"s_cselect_b64 s[28:29], s[18:19], s[20:21]\n"
"v_mfma_f32_16x16x32_bf16 v[164:167], a[44:47], a[8:11], 0\n"
"s_or_b32 s29, s29, 536870912\n"
"buffer_load_dword a255, v255, s[28:31], s34 idxen\n"
"v_mfma_f32_16x16x32_bf16 v[48:51], a[8:11], a[128:131], 0\n"
"s_lshl_b32 s34, s33, 7\n"
"ds_read_b64_tr_b16 a[190:191], v253 offset:50176\n"
"s_add_u32 s34, s34, s32\n"
"v_mfma_f32_16x16x32_bf16 v[52:55], a[8:11], a[132:135], 0\n"
"s_and_b32 s35, s34, 3\n"
"s_sub_u32 s34, s34, s35\n"
"s_lshl_b32 s34, s34, 4\n"
"v_mfma_f32_16x16x32_bf16 v[56:59], a[8:11], a[136:139], 0\n"
"s_add_u32 s34, s34, s35\n"
"ds_read_b64_tr_b16 a[192:193], v250 offset:57344\n"
"s_add_u32 s34, s34, 4\n"
"v_mfma_f32_16x16x32_bf16 v[60:63], a[8:11], a[140:143], 0\n"
"s_lshl_b32 s34, s34, 7\n"
"s_mov_b32 s30, 8\n"
"s_mov_b64 s[28:29], s[14:15]\n"
"v_mfma_f32_16x16x32_bf16 v[40:43], a[32:35], a[12:15], 0\n"
"s_or_b32 s29, s29, 67108864\n"
"buffer_load_dword a255, v255, s[28:31], s34 idxen\n"
"v_mfma_f32_16x16x32_bf16 v[168:171], a[36:39], a[12:15], 0\n"
"buffer_store_dwordx4 v[176:179], v188, s[84:87], s56 offen offset:0 sc1\n"
"v_mfma_f32_16x16x32_bf16 v[44:47], a[40:43], a[12:15], 0\n"
"ds_read_b64_tr_b16 a[194:195], v250 offset:58368\n"
"ds_read_b64_tr_b16 a[196:197], v251 offset:57344\n"
"v_mfma_f32_16x16x32_bf16 v[172:175], a[44:47], a[12:15], 0\n"
"v_cvt_pk_bf16_f32 v192, v192, v193\n"
"v_cvt_pk_bf16_f32 v193, v194, v195\n"
"v_mfma_f32_16x16x32_bf16 a[48:51], a[12:15], a[128:131], 0\n"
"ds_read_b64_tr_b16 a[198:199], v251 offset:58368\n"
"v_cvt_pk_bf16_f32 v194, v196, v197\n"
"v_mfma_f32_16x16x32_bf16 a[52:55], a[12:15], a[132:135], 0\n"
"v_cvt_pk_bf16_f32 v195, v198, v199\n"
"v_mfma_f32_16x16x32_bf16 a[56:59], a[12:15], a[136:139], 0\n"
"v_permlane16_swap_b32 v192, v194\n"
"v_permlane16_swap_b32 v193, v195\n"
"v_mfma_f32_16x16x32_bf16 a[60:63], a[12:15], a[140:143], 0\n"
"ds_read_b64_tr_b16 a[200:201], v252 offset:57344\n"
"v_cvt_pk_bf16_f32 v200, v200, v201\n"
"v_mfma_f32_16x16x32_bf16 v[64:67], a[32:35], a[16:19], 0\n"
"v_cvt_pk_bf16_f32 v201, v202, v203\n"
"v_cvt_pk_bf16_f32 v202, v204, v205\n"
"v_mfma_f32_16x16x32_bf16 a[64:67], a[36:39], a[16:19], 0\n"
"buffer_store_dwordx4 v[184:187], v188, s[84:87], s58 offen offset:0 sc1\n"
"v_mfma_f32_16x16x32_bf16 v[68:71], a[40:43], a[16:19], 0\n"
"ds_read_b64_tr_b16 a[202:203], v252 offset:58368\n"
"v_cvt_pk_bf16_f32 v203, v206, v207\n"
"v_mfma_f32_16x16x32_bf16 a[68:71], a[44:47], a[16:19], 0\n"
"ds_read_b64_tr_b16 a[204:205], v253 offset:57344\n"
"v_permlane16_swap_b32 v200, v202\n"
"v_mfma_f32_16x16x32_bf16 v[80:83], a[16:19], a[128:131], 0\n"
"v_permlane16_swap_b32 v201, v203\n"
"v_add_u32 v182, 2, v180\n"
"v_mfma_f32_16x16x32_bf16 v[84:87], a[16:19], a[132:135], 0\n"
"ds_read_b64_tr_b16 a[206:207], v253 offset:58368\n"
"v_mul_u32_u24 v182, 205, v182\n"
"v_mfma_f32_16x16x32_bf16 v[88:91], a[16:19], a[136:139], 0\n"
"v_lshrrev_b32 v182, 10, v182\n"
"v_mul_u32_u24 v182, 523008, v182\n"
"v_mfma_f32_16x16x32_bf16 v[92:95], a[16:19], a[140:143], 0\n"
"ds_read_b128 a[148:151], v243 offset:2048\n"
"v_add_u32 v189, v182, v181\n"
"v_mfma_f32_16x16x32_bf16 v[72:75], a[32:35], a[20:23], 0\n"
"v_cvt_pk_bf16_f32 v208, v208, v209\n"
"v_cvt_pk_bf16_f32 v209, v210, v211\n"
"v_mfma_f32_16x16x32_bf16 a[72:75], a[36:39], a[20:23], 0\n"
"ds_read_b128 a[152:155], v243 offset:4096\n"
"v_cvt_pk_bf16_f32 v210, v212, v213\n"
"v_mfma_f32_16x16x32_bf16 v[76:79], a[40:43], a[20:23], 0\n"
"v_cvt_pk_bf16_f32 v211, v214, v215\n"
"v_mfma_f32_16x16x32_bf16 a[76:79], a[44:47], a[20:23], 0\n"
"v_permlane16_swap_b32 v208, v210\n"
"v_permlane16_swap_b32 v209, v211\n"
"v_mfma_f32_16x16x32_bf16 a[80:83], a[20:23], a[128:131], 0\n"
"ds_read_b128 a[156:159], v243 offset:6144\n"
"v_cvt_pk_bf16_f32 v216, v216, v217\n"
"v_mfma_f32_16x16x32_bf16 a[84:87], a[20:23], a[132:135], 0\n"
"v_cvt_pk_bf16_f32 v217, v218, v219\n"
"v_cvt_pk_bf16_f32 v218, v220, v221\n"
"v_mfma_f32_16x16x32_bf16 a[88:91], a[20:23], a[136:139], 0\n"
"ds_read_b128 a[160:163], v243 offset:8192\n"
"v_cvt_pk_bf16_f32 v219, v222, v223\n"
"v_mfma_f32_16x16x32_bf16 a[92:95], a[20:23], a[140:143], 0\n"
"buffer_store_dwordx4 v[192:195], v189, s[84:87], s56 offen offset:512 sc1\n"
"v_mfma_f32_16x16x32_bf16 v[96:99], a[32:35], a[24:27], 0\n"
"ds_read_b128 a[164:167], v243 offset:10240\n"
"v_permlane16_swap_b32 v216, v218\n"
"v_mfma_f32_16x16x32_bf16 a[96:99], a[36:39], a[24:27], 0\n"
"v_permlane16_swap_b32 v217, v219\n"
"v_add_u32 v182, 4, v180\n"
"v_mfma_f32_16x16x32_bf16 v[100:103], a[40:43], a[24:27], 0\n"
"ds_read_b128 a[168:171], v243 offset:12288\n"
"v_mul_u32_u24 v182, 205, v182\n"
"v_mfma_f32_16x16x32_bf16 a[100:103], a[44:47], a[24:27], 0\n"
"v_lshrrev_b32 v182, 10, v182\n"
"v_mul_u32_u24 v182, 523008, v182\n"
"v_mfma_f32_16x16x32_bf16 v[112:115], a[24:27], a[128:131], 0\n"
"ds_read_b128 a[172:175], v243 offset:14336\n"
"v_add_u32 v190, v182, v181\n"
"v_mfma_f32_16x16x32_bf16 v[116:119], a[24:27], a[132:135], 0\n"
"v_cvt_pk_bf16_f32 v224, v224, v225\n"
"v_cvt_pk_bf16_f32 v225, v226, v227\n"
"v_mfma_f32_16x16x32_bf16 v[120:123], a[24:27], a[136:139], 0\n"
"v_cvt_pk_bf16_f32 v226, v228, v229\n"
"v_cvt_pk_bf16_f32 v227, v230, v231\n"
"v_mfma_f32_16x16x32_bf16 v[124:127], a[24:27], a[140:143], 0\n"
"v_permlane16_swap_b32 v224, v226\n"
"v_permlane16_swap_b32 v225, v227\n"
"v_mfma_f32_16x16x32_bf16 v[104:107], a[32:35], a[28:31], 0\n"
"v_cvt_pk_bf16_f32 v232, v232, v233\n"
"v_cvt_pk_bf16_f32 v233, v234, v235\n"
"v_mfma_f32_16x16x32_bf16 a[104:107], a[36:39], a[28:31], 0\n"
"v_cvt_pk_bf16_f32 v234, v236, v237\n"
"v_cvt_pk_bf16_f32 v235, v238, v239\n"
"v_mfma_f32_16x16x32_bf16 v[108:111], a[40:43], a[28:31], 0\n"
"v_permlane16_swap_b32 v232, v234\n"
"v_permlane16_swap_b32 v233, v235\n"
"v_mfma_f32_16x16x32_bf16 a[108:111], a[44:47], a[28:31], 0\n"
"v_add_u32 v182, 6, v180\n"
"v_mul_u32_u24 v182, 205, v182\n"
"v_mfma_f32_16x16x32_bf16 a[112:115], a[28:31], a[128:131], 0\n"
"v_lshrrev_b32 v182, 10, v182\n"
"v_mul_u32_u24 v182, 523008, v182\n"
"v_mfma_f32_16x16x32_bf16 a[116:119], a[28:31], a[132:135], 0\n"
"buffer_store_dwordx4 v[200:203], v189, s[84:87], s58 offen offset:512 sc1\n"
"v_mfma_f32_16x16x32_bf16 a[120:123], a[28:31], a[136:139], 0\n"
"v_add_u32 v191, v182, v181\n"
"s_lshr_b32 s60, %2, 1\n"
"s_and_b32 s61, %2, 1\n"
"v_mfma_f32_16x16x32_bf16 a[124:127], a[28:31], a[140:143], 0\n"
"s_waitcnt lgkmcnt(0)\n"
"s_lshl_b32 s61, s61, 2\n"
"s_add_u32 s61, s61, s76\n"
"v_mfma_f32_16x16x32_bf16 v[0:3], a[176:179], a[144:147], v[0:3]\n"
"s_waitcnt vmcnt(14)\n"
"s_mul_i32 s67, s61, 148\n"
"s_add_u32 s67, s67, s60\n"
"v_mfma_f32_16x16x32_bf16 v[128:131], a[180:183], a[144:147], v[128:131]\n"
"s_barrier\n"
"s_mul_i32 s67, s67, 5120\n"
"s_add_u32 s67, s67, 63963136\n"
"v_mfma_f32_16x16x32_bf16 v[4:7], a[184:187], a[144:147], v[4:7]\n"
"s_mul_i32 s61, s61, 160\n"
"ds_read_b128 a[0:3], v248 offset:32768\n"
"s_mov_b32 s64, -10240\n"
"v_mfma_f32_16x16x32_bf16 v[132:135], a[188:191], a[144:147], v[132:135]\n"
"s_mov_b32 s65, -5120\n"
"s_mov_b32 s66, 15360\n"
"s_mul_i32 s59, s60, 40960\n"
"v_mfma_f32_16x16x32_bf16 v[16:19], a[144:147], a[192:195], v[16:19]\n"
"s_add_u32 s59, s59, s61\n"
"v_mfma_f32_16x16x32_bf16 v[20:23], a[144:147], a[196:199], v[20:23]\n"
"s_mov_b64 s[68:69], s[22:23]\n"
"s_add_u32 s68, s68, s59\n"
"s_addc_u32 s69, s69, 0\n"
"v_mfma_f32_16x16x32_bf16 v[24:27], a[144:147], a[200:203], v[24:27]\n"
"s_mov_b32 s70, 0\n"
"ds_read_b64_tr_b16 a[32:33], v250 offset:0\n"
"s_mov_b32 s71, 0x20000\n"
"v_mfma_f32_16x16x32_bf16 v[28:31], a[144:147], a[204:207], v[28:31]\n"
"s_sub_u32 s70, 5242880, s59\n"
"ds_read_b64_tr_b16 a[34:35], v250 offset:1024\n"
"v_add_u32 v214, 0, v255\n"
"v_mfma_f32_16x16x32_bf16 v[8:11], a[176:179], a[148:151], v[8:11]\n"
"v_mul_u32_u24 v214, 205, v214\n"
"v_lshrrev_b32 v214, 11, v214\n"
"v_mfma_f32_16x16x32_bf16 v[136:139], a[180:183], a[148:151], v[136:139]\n"
"ds_read_b64_tr_b16 a[36:37], v251 offset:0\n"
"v_mad_i32_i24 v196, v214, s80, v254\n"
"v_mfma_f32_16x16x32_bf16 v[12:15], a[184:187], a[148:151], v[12:15]\n"
"v_bfe_u32 v215, v214, 2, 1\n"
"v_mad_i32_i24 v196, v215, s66, v196\n"
"v_mfma_f32_16x16x32_bf16 v[140:143], a[188:191], a[148:151], v[140:143]\n"
"buffer_store_dwordx4 v[208:211], v190, s[84:87], s56 offen offset:1024 sc1\n"
"v_mfma_f32_16x16x32_bf16 v[144:147], a[148:151], a[192:195], v[144:147]\n"
"ds_read_b64_tr_b16 a[38:39], v251 offset:1024\n"
"v_bfe_u32 v215, v214, 3, 1\n"
"v_mfma_f32_16x16x32_bf16 v[148:151], a[148:151], a[196:199], v[148:151]\n"
"s_add_u32 m0, s89, 0\n"
"ds_read_b64_tr_b16 a[40:41], v252 offset:0\n"
"s_mov_b32 s52, 256\n"
"v_mfma_f32_16x16x32_bf16 v[152:155], a[148:151], a[200:203], v[152:155]\n"
"v_mad_i32_i24 v196, v215, s65, v196\n"
"v_lshrrev_b32 v215, 4, v214\n"
"v_mfma_f32_16x16x32_bf16 v[156:159], a[148:151], a[204:207], v[156:159]\n"
"ds_read_b64_tr_b16 a[42:43], v252 offset:1024\n"
"v_mad_i32_i24 v196, v215, s64, v196\n"
"v_mfma_f32_16x16x32_bf16 v[32:35], a[176:179], a[152:155], v[32:35]\n"
"v_add_u32 v214, 64, v255\n"
"v_mul_u32_u24 v214, 205, v214\n"
"v_mfma_f32_16x16x32_bf16 v[160:163], a[180:183], a[152:155], v[160:163]\n"
"v_lshrrev_b32 v214, 11, v214\n"
"v_mad_i32_i24 v197, v214, s80, v254\n"
"v_mfma_f32_16x16x32_bf16 v[36:39], a[184:187], a[152:155], v[36:39]\n"
"ds_read_b64_tr_b16 a[44:45], v253 offset:0\n"
"v_bfe_u32 v215, v214, 2, 1\n"
"v_mfma_f32_16x16x32_bf16 v[164:167], a[188:191], a[152:155], v[164:167]\n"
"v_mad_i32_i24 v197, v215, s66, v197\n"
"v_bfe_u32 v215, v214, 3, 1\n"
"v_mfma_f32_16x16x32_bf16 v[48:51], a[152:155], a[192:195], v[48:51]\n"
"ds_read_b64_tr_b16 a[46:47], v253 offset:1024\n"
"v_mad_i32_i24 v197, v215, s65, v197\n"
"v_mfma_f32_16x16x32_bf16 v[52:55], a[152:155], a[196:199], v[52:55]\n"
"v_lshrrev_b32 v215, 4, v214\n"
"v_mad_i32_i24 v197, v215, s64, v197\n"
"v_mfma_f32_16x16x32_bf16 v[56:59], a[152:155], a[200:203], v[56:59]\n"
"ds_read_b64_tr_b16 a[128:129], v250 offset:8192\n"
"v_add_u32 v214, 128, v255\n"
"v_mfma_f32_16x16x32_bf16 v[60:63], a[152:155], a[204:207], v[60:63]\n"
"v_mul_u32_u24 v214, 205, v214\n"
"v_lshrrev_b32 v214, 11, v214\n"
"v_mfma_f32_16x16x32_bf16 v[40:43], a[176:179], a[156:159], v[40:43]\n"
"ds_read_b64_tr_b16 a[130:131], v250 offset:9216\n"
"v_mad_i32_i24 v198, v214, s80, v254\n"
"v_mfma_f32_16x16x32_bf16 v[168:171], a[180:183], a[156:159], v[168:171]\n"
"buffer_load_dwordx4 v240, s[36:39], s52 offen offset:0 lds\n"
"s_mov_b32 s52, 7424\n"
"v_mfma_f32_16x16x32_bf16 v[44:47], a[184:187], a[156:159], v[44:47]\n"
"ds_read_b64_tr_b16 a[132:133], v251 offset:8192\n"
"v_bfe_u32 v215, v214, 2, 1\n"
"v_mfma_f32_16x16x32_bf16 v[172:175], a[188:191], a[156:159], v[172:175]\n"
"v_mad_i32_i24 v198, v215, s66, v198\n"
"v_bfe_u32 v215, v214, 3, 1\n"
"v_mfma_f32_16x16x32_bf16 a[48:51], a[156:159], a[192:195], a[48:51]\n"
"ds_read_b64_tr_b16 a[134:135], v251 offset:9216\n"
"v_mad_i32_i24 v198, v215, s65, v198\n"
"v_mfma_f32_16x16x32_bf16 a[52:55], a[156:159], a[196:199], a[52:55]\n"
"buffer_load_dwordx4 v241, s[36:39], s52 offen offset:1024 lds\n"
"s_mov_b32 s52, 14592\n"
"v_mfma_f32_16x16x32_bf16 a[56:59], a[156:159], a[200:203], a[56:59]\n"
"v_lshrrev_b32 v215, 4, v214\n"
"v_mad_i32_i24 v198, v215, s64, v198\n"
"v_mfma_f32_16x16x32_bf16 a[60:63], a[156:159], a[204:207], a[60:63]\n"
"ds_read_b64_tr_b16 a[136:137], v252 offset:8192\n"
"v_add_u32 v214, 192, v255\n"
"v_mfma_f32_16x16x32_bf16 v[64:67], a[176:179], a[160:163], v[64:67]\n"
"v_mul_u32_u24 v214, 205, v214\n"
"v_lshrrev_b32 v214, 11, v214\n"
"v_mfma_f32_16x16x32_bf16 a[64:67], a[180:183], a[160:163], a[64:67]\n"
"buffer_load_dwordx4 v240, s[36:39], s52 offen offset:2048 lds\n"
"s_mov_b32 s52, 21760\n"
"v_mfma_f32_16x16x32_bf16 v[68:71], a[184:187], a[160:163], v[68:71]\n"
"ds_read_b64_tr_b16 a[138:139], v252 offset:9216\n"
"v_mad_i32_i24 v199, v214, s80, v254\n"
"v_mfma_f32_16x16x32_bf16 a[68:71], a[188:191], a[160:163], a[68:71]\n"
"ds_read_b64_tr_b16 a[140:141], v253 offset:8192\n"
"v_bfe_u32 v215, v214, 2, 1\n"
"v_mfma_f32_16x16x32_bf16 v[80:83], a[160:163], a[192:195], v[80:83]\n"
"v_mad_i32_i24 v199, v215, s66, v199\n"
"v_bfe_u32 v215, v214, 3, 1\n"
"v_mfma_f32_16x16x32_bf16 v[84:87], a[160:163], a[196:199], v[84:87]\n"
"buffer_load_dwordx4 v241, s[36:39], s52 offen offset:3072 lds\n"
"s_add_u32 m0, s89, 4096\n"
"v_mfma_f32_16x16x32_bf16 v[88:91], a[160:163], a[200:203], v[88:91]\n"
"s_mov_b32 s52, 262400\n"
"ds_read_b64_tr_b16 a[142:143], v253 offset:9216\n"
"v_mad_i32_i24 v199, v215, s65, v199\n"
"v_mfma_f32_16x16x32_bf16 v[92:95], a[160:163], a[204:207], v[92:95]\n"
"ds_read_b128 a[4:7], v248 offset:34816\n"
"v_lshrrev_b32 v215, 4, v214\n"
"v_mfma_f32_16x16x32_bf16 v[72:75], a[176:179], a[164:167], v[72:75]\n"
"v_mad_i32_i24 v199, v215, s64, v199\n"
"v_add_u32 v214, 256, v255\n"
"v_mfma_f32_16x16x32_bf16 a[72:75], a[180:183], a[164:167], a[72:75]\n"
"ds_read_b128 a[8:11], v248 offset:36864\n"
"v_mul_u32_u24 v214, 205, v214\n"
"v_mfma_f32_16x16x32_bf16 v[76:79], a[184:187], a[164:167], v[76:79]\n"
"buffer_load_dwordx4 v240, s[36:39], s52 offen offset:0 lds\n"
"s_mov_b32 s52, 269568\n"
"v_mfma_f32_16x16x32_bf16 a[76:79], a[188:191], a[164:167], a[76:79]\n"
"v_lshrrev_b32 v214, 11, v214\n"
"v_mad_i32_i24 v204, v214, s80, v254\n"
"v_mfma_f32_16x16x32_bf16 a[80:83], a[164:167], a[192:195], a[80:83]\n"
"ds_read_b128 a[12:15], v248 offset:38912\n"
"v_bfe_u32 v215, v214, 2, 1\n"
"v_mfma_f32_16x16x32_bf16 a[84:87], a[164:167], a[196:199], a[84:87]\n"
"v_mad_i32_i24 v204, v215, s66, v204\n"
"v_bfe_u32 v215, v214, 3, 1\n"
"v_mfma_f32_16x16x32_bf16 a[88:91], a[164:167], a[200:203], a[88:91]\n"
"buffer_load_dwordx4 v241, s[36:39], s52 offen offset:1024 lds\n"
"s_mov_b32 s52, 276736\n"
"v_mfma_f32_16x16x32_bf16 a[92:95], a[164:167], a[204:207], a[92:95]\n"
"ds_read_b128 a[16:19], v248 offset:40960\n"
"v_mad_i32_i24 v204, v215, s65, v204\n"
"v_mfma_f32_16x16x32_bf16 v[96:99], a[176:179], a[168:171], v[96:99]\n"
"ds_read_b128 a[20:23], v248 offset:43008\n"
"v_lshrrev_b32 v215, 4, v214\n"
"v_mfma_f32_16x16x32_bf16 a[96:99], a[180:183], a[168:171], a[96:99]\n"
"v_mad_i32_i24 v204, v215, s64, v204\n"
"s_lshr_b32 s60, %2, 3\n"
"s_and_b32 s61, %2, 7\n"
"v_mfma_f32_16x16x32_bf16 v[100:103], a[184:187], a[168:171], v[100:103]\n"
"s_mul_i32 s81, s61, 148\n"
"buffer_load_dwordx4 v240, s[36:39], s52 offen offset:2048 lds\n"
"v_mfma_f32_16x16x32_bf16 a[100:103], a[188:191], a[168:171], a[100:103]\n"
"s_mov_b32 s52, 283904\n"
"ds_read_b128 a[24:27], v248 offset:45056\n"
"s_add_u32 s81, s81, s60\n"
"v_mfma_f32_16x16x32_bf16 v[112:115], a[168:171], a[192:195], v[112:115]\n"
"s_mul_i32 s81, s81, 5120\n"
"ds_read_b128 a[28:31], v248 offset:47104\n"
"s_add_u32 s81, s81, 64618496\n"
"v_mfma_f32_16x16x32_bf16 v[116:119], a[168:171], a[196:199], v[116:119]\n"
"s_mul_i32 s61, s61, 160\n"
"v_mfma_f32_16x16x32_bf16 v[120:123], a[168:171], a[200:203], v[120:123]\n"
"s_cmp_lt_u32 s60, 16\n"
"s_cselect_b32 s64, 20, 80\n"
"s_cselect_b32 s65, 8, 40\n"
"s_cselect_b32 s66, 4, 20\n"
"v_mfma_f32_16x16x32_bf16 v[124:127], a[168:171], a[204:207], v[124:127]\n"
"s_mul_i32 s59, s60, 40\n"
"buffer_load_dwordx4 v241, s[36:39], s52 offen offset:3072 lds\n"
"v_mfma_f32_16x16x32_bf16 v[104:107], a[176:179], a[172:175], v[104:107]\n"
"s_mul_i32 s60, s60, 160\n"
"s_sub_u32 s60, s60, 2544\n"
"v_mfma_f32_16x16x32_bf16 a[104:107], a[180:183], a[172:175], a[104:107]\n"
"s_cmp_eq_u32 s64, 20\n"
"s_cselect_b32 s59, s59, s60\n"
"s_mul_i32 s59, s59, 1280\n"
"v_mfma_f32_16x16x32_bf16 v[108:111], a[184:187], a[172:175], v[108:111]\n"
"s_add_u32 s59, s59, s61\n"
"s_cmp_eq_u32 s76, 3\n"
"s_cselect_b32 s60, %2, 255\n"
"v_mfma_f32_16x16x32_bf16 a[108:111], a[188:191], a[172:175], a[108:111]\n"
"s_cmp_lt_u32 s60, 160\n"
"s_cselect_b32 s59, s59, 819200\n"
"s_cselect_b32 s81, s81, 1073741824\n"
"v_mfma_f32_16x16x32_bf16 a[112:115], a[172:175], a[192:195], a[112:115]\n"
"buffer_store_dwordx4 v[216:219], v190, s[84:87], s58 offen offset:1024 sc1\n"
"v_mfma_f32_16x16x32_bf16 a[116:119], a[172:175], a[196:199], a[116:119]\n"
"s_mov_b64 s[72:73], s[10:11]\n"
"s_add_u32 s72, s72, s59\n"
"s_addc_u32 s73, s73, 0\n"
"v_mfma_f32_16x16x32_bf16 a[120:123], a[172:175], a[200:203], a[120:123]\n"
"s_mov_b32 s74, 0\n"
"s_mov_b32 s75, 0x20000\n"
"s_sub_u32 s74, 819200, s59\n"
"v_mfma_f32_16x16x32_bf16 a[124:127], a[172:175], a[204:207], a[124:127]\n"
"s_waitcnt lgkmcnt(0)\n"
"s_sub_u32 s64, s64, 16\n"
"s_mul_i32 s64, s64, 1280\n"
"v_mfma_f32_16x16x32_bf16 v[0:3], a[32:35], a[0:3], v[0:3]\n"
"s_waitcnt vmcnt(20)\n"
"s_sub_u32 s65, s65, 8\n"
"s_mul_i32 s65, s65, 1280\n"
"v_mfma_f32_16x16x32_bf16 v[128:131], a[36:39], a[0:3], v[128:131]\n"
"s_barrier\n"
"s_sub_u32 s66, s66, 4\n"
"s_mul_i32 s66, s66, 1280\n"
"v_mfma_f32_16x16x32_bf16 v[4:7], a[40:43], a[0:3], v[4:7]\n"
"ds_read_b128 a[144:147], v249 offset:32768\n"
"v_add_u32 v214, 0, v255\n"
"v_mfma_f32_16x16x32_bf16 v[132:135], a[44:47], a[0:3], v[132:135]\n"
"v_mul_u32_u24 v214, 205, v214\n"
"v_lshrrev_b32 v214, 11, v214\n"
"v_mfma_f32_16x16x32_bf16 v[16:19], a[0:3], a[128:131], v[16:19]\n"
"v_mad_i32_i24 v205, v214, s80, v254\n"
"v_bfe_u32 v215, v214, 2, 1\n"
"v_mfma_f32_16x16x32_bf16 v[20:23], a[0:3], a[132:135], v[20:23]\n"
"ds_read_b64_tr_b16 a[176:177], v250 offset:16384\n"
"v_mad_i32_i24 v205, v215, s66, v205\n"
"v_mfma_f32_16x16x32_bf16 v[24:27], a[0:3], a[136:139], v[24:27]\n"
"v_bfe_u32 v215, v214, 3, 1\n"
"v_mad_i32_i24 v205, v215, s65, v205\n"
"v_mfma_f32_16x16x32_bf16 v[28:31], a[0:3], a[140:143], v[28:31]\n"
"ds_read_b64_tr_b16 a[178:179], v250 offset:17408\n"
"v_lshrrev_b32 v215, 4, v214\n"
"v_mfma_f32_16x16x32_bf16 v[8:11], a[32:35], a[4:7], v[8:11]\n"
"v_mad_i32_i24 v205, v215, s64, v205\n"
"v_add_u32 v214, 64, v255\n"
"v_mfma_f32_16x16x32_bf16 v[136:139], a[36:39], a[4:7], v[136:139]\n"
"ds_read_b64_tr_b16 a[180:181], v251 offset:16384\n"
"v_mul_u32_u24 v214, 205, v214\n"
"v_mfma_f32_16x16x32_bf16 v[12:15], a[40:43], a[4:7], v[12:15]\n"
"buffer_store_dwordx4 v[224:227], v191, s[84:87], s56 offen offset:1536 sc1\n"
"v_mfma_f32_16x16x32_bf16 v[140:143], a[44:47], a[4:7], v[140:143]\n"
"ds_read_b64_tr_b16 a[182:183], v251 offset:17408\n"
"v_lshrrev_b32 v214, 11, v214\n"
"v_mfma_f32_16x16x32_bf16 v[144:147], a[4:7], a[128:131], v[144:147]\n"
"v_mad_i32_i24 v206, v214, s80, v254\n"
"v_bfe_u32 v215, v214, 2, 1\n"
"v_mfma_f32_16x16x32_bf16 v[148:151], a[4:7], a[132:135], v[148:151]\n"
"ds_read_b64_tr_b16 a[184:185], v252 offset:16384\n"
"v_mad_i32_i24 v206, v215, s66, v206\n"
"v_mfma_f32_16x16x32_bf16 v[152:155], a[4:7], a[136:139], v[152:155]\n"
"v_bfe_u32 v215, v214, 3, 1\n"
"v_mad_i32_i24 v206, v215, s65, v206\n"
"v_mfma_f32_16x16x32_bf16 v[156:159], a[4:7], a[140:143], v[156:159]\n"
"ds_read_b64_tr_b16 a[186:187], v252 offset:17408\n"
"v_lshrrev_b32 v215, 4, v214\n"
"v_mfma_f32_16x16x32_bf16 v[32:35], a[32:35], a[8:11], v[32:35]\n"
"v_mad_i32_i24 v206, v215, s64, v206\n"
"v_add_u32 v214, 128, v255\n"
"v_mfma_f32_16x16x32_bf16 v[160:163], a[36:39], a[8:11], v[160:163]\n"
"v_mul_u32_u24 v214, 205, v214\n"
"v_lshrrev_b32 v214, 11, v214\n"
"v_mfma_f32_16x16x32_bf16 v[36:39], a[40:43], a[8:11], v[36:39]\n"
"ds_read_b64_tr_b16 a[188:189], v253 offset:16384\n"
"v_mad_i32_i24 v207, v214, s80, v254\n"
"v_mfma_f32_16x16x32_bf16 v[164:167], a[44:47], a[8:11], v[164:167]\n"
"v_bfe_u32 v215, v214, 2, 1\n"
"v_mad_i32_i24 v207, v215, s66, v207\n"
"v_mfma_f32_16x16x32_bf16 v[48:51], a[8:11], a[128:131], v[48:51]\n"
"ds_read_b64_tr_b16 a[190:191], v253 offset:17408\n"
"v_bfe_u32 v215, v214, 3, 1\n"
"v_mfma_f32_16x16x32_bf16 v[52:55], a[8:11], a[132:135], v[52:55]\n"
"v_mad_i32_i24 v207, v215, s65, v207\n"
"v_lshrrev_b32 v215, 4, v214\n"
"v_mfma_f32_16x16x32_bf16 v[56:59], a[8:11], a[136:139], v[56:59]\n"
"ds_read_b64_tr_b16 a[192:193], v250 offset:24576\n"
"v_mad_i32_i24 v207, v215, s64, v207\n"
"v_mfma_f32_16x16x32_bf16 v[60:63], a[8:11], a[140:143], v[60:63]\n"
"v_add_u32 v214, 192, v255\n"
"v_mul_u32_u24 v214, 205, v214\n"
"v_mfma_f32_16x16x32_bf16 v[40:43], a[32:35], a[12:15], v[40:43]\n"
"buffer_store_dwordx4 v[232:235], v191, s[84:87], s58 offen offset:1536 sc1\n"
"v_mfma_f32_16x16x32_bf16 v[168:171], a[36:39], a[12:15], v[168:171]\n"
"ds_read_b64_tr_b16 a[194:195], v250 offset:25600\n"
"v_lshrrev_b32 v214, 11, v214\n"
"v_mfma_f32_16x16x32_bf16 v[44:47], a[40:43], a[12:15], v[44:47]\n"
"ds_read_b64_tr_b16 a[196:197], v251 offset:24576\n"
"v_mad_i32_i24 v212, v214, s80, v254\n"
"v_mfma_f32_16x16x32_bf16 v[172:175], a[44:47], a[12:15], v[172:175]\n"
"v_bfe_u32 v215, v214, 2, 1\n"
"v_mad_i32_i24 v212, v215, s66, v212\n"
"v_mfma_f32_16x16x32_bf16 a[48:51], a[12:15], a[128:131], a[48:51]\n"
"s_add_u32 m0, s57, 114688\n"
"ds_read_b64_tr_b16 a[198:199], v251 offset:25600\n"
"s_mov_b32 s52, 1310720\n"
"v_mfma_f32_16x16x32_bf16 a[52:55], a[12:15], a[132:135], a[52:55]\n"
"v_bfe_u32 v215, v214, 3, 1\n"
"v_mad_i32_i24 v212, v215, s65, v212\n"
"v_mfma_f32_16x16x32_bf16 a[56:59], a[12:15], a[136:139], a[56:59]\n"
"v_lshrrev_b32 v215, 4, v214\n"
"v_mad_i32_i24 v212, v215, s64, v212\n"
"v_mfma_f32_16x16x32_bf16 a[60:63], a[12:15], a[140:143], a[60:63]\n"
"ds_read_b64_tr_b16 a[200:201], v252 offset:24576\n"
"v_add_u32 v214, 256, v255\n"
"v_mfma_f32_16x16x32_bf16 v[64:67], a[32:35], a[16:19], v[64:67]\n"
"v_mul_u32_u24 v214, 205, v214\n"
"v_lshrrev_b32 v214, 11, v214\n"
"v_mfma_f32_16x16x32_bf16 a[64:67], a[36:39], a[16:19], a[64:67]\n"
"ds_read_b64_tr_b16 a[202:203], v252 offset:25600\n"
"v_mad_i32_i24 v213, v214, s80, v254\n"
"v_mfma_f32_16x16x32_bf16 v[68:71], a[40:43], a[16:19], v[68:71]\n"
"v_bfe_u32 v215, v214, 2, 1\n"
"v_mad_i32_i24 v213, v215, s66, v213\n"
"v_mfma_f32_16x16x32_bf16 a[68:71], a[44:47], a[16:19], a[68:71]\n"
"ds_read_b64_tr_b16 a[204:205], v253 offset:24576\n"
"v_bfe_u32 v215, v214, 3, 1\n"
"v_mfma_f32_16x16x32_bf16 v[80:83], a[16:19], a[128:131], v[80:83]\n"
"v_mad_i32_i24 v213, v215, s65, v213\n"
"v_lshrrev_b32 v215, 4, v214\n"
"v_mfma_f32_16x16x32_bf16 v[84:87], a[16:19], a[132:135], v[84:87]\n"
"ds_read_b64_tr_b16 a[206:207], v253 offset:25600\n"
"v_mad_i32_i24 v213, v215, s64, v213\n"
"v_mfma_f32_16x16x32_bf16 v[88:91], a[16:19], a[136:139], v[88:91]\n"
"s_cmp_eq_u32 s76, 3\n"
"s_cselect_b32 s64, 0, 5120\n"
"s_add_u32 s64, s64, 151552\n"
"v_mfma_f32_16x16x32_bf16 v[92:95], a[16:19], a[140:143], v[92:95]\n"
"ds_read_b128 a[148:151], v249 offset:34816\n"
"v_lshrrev_b32 v180, 4, v255\n"
"v_mfma_f32_16x16x32_bf16 v[72:75], a[32:35], a[20:23], v[72:75]\n"
"buffer_load_dwordx4 v246, s[40:43], s52 offen offset:0 lds\n"
"s_mov_b32 s52, 1342464\n"
"v_mfma_f32_16x16x32_bf16 a[72:75], a[36:39], a[20:23], a[72:75]\n"
"ds_read_b128 a[152:155], v249 offset:36864\n"
"v_bfe_u32 v181, v255, 2, 2\n"
"v_mfma_f32_16x16x32_bf16 v[76:79], a[40:43], a[20:23], v[76:79]\n"
"v_lshl_add_u32 v180, v180, 3, v181\n"
"v_mul_u32_u24 v180, 160, v180\n"
"v_mfma_f32_16x16x32_bf16 a[76:79], a[44:47], a[20:23], a[76:79]\n"
"v_and_b32 v181, 3, v255\n"
"v_lshl_add_u32 v182, v181, 3, v180\n"
"v_mfma_f32_16x16x32_bf16 a[80:83], a[20:23], a[128:131], a[80:83]\n"
"buffer_load_dwordx4 v246, s[40:43], s52 offen offset:1024 lds\n"
"s_mov_b32 s52, 1374208\n"
"v_mfma_f32_16x16x32_bf16 a[84:87], a[20:23], a[132:135], a[84:87]\n"
"ds_read_b128 a[156:159], v249 offset:38912\n"
"v_lshl_add_u32 v180, v181, 4, v180\n"
"s_mul_i32 s59, s76, 5120\n"
"v_mfma_f32_16x16x32_bf16 a[88:91], a[20:23], a[136:139], a[88:91]\n"
"s_add_u32 s59, s59, 131072\n"
"ds_read_b128 a[160:163], v249 offset:40960\n"
"v_add_u32 v181, s64, v180\n"
"v_mfma_f32_16x16x32_bf16 a[92:95], a[20:23], a[140:143], a[92:95]\n"
".Lpad3_%=:\n"
"s_cbranch_execz .Lpad7_%=\n"
"v_add_u32 v180, s59, v180\n"
"v_add_u32 v183, s64, v182\n"
"v_mfma_f32_16x16x32_bf16 v[96:99], a[32:35], a[24:27], v[96:99]\n"
"buffer_load_dwordx4 v247, s[40:43], s52 offen offset:2048 lds\n"
"s_mov_b32 s52, 1405952\n"
"v_mfma_f32_16x16x32_bf16 a[96:99], a[36:39], a[24:27], a[96:99]\n"
"ds_read_b128 a[164:167], v249 offset:43008\n"
"v_add_u32 v182, s59, v182\n"
"s_mul_i32 s59, s77, 800\n"
"v_mfma_f32_16x16x32_bf16 v[100:103], a[40:43], a[24:27], v[100:103]\n"
"s_and_b32 s60, s77, 1\n"
"ds_read_b128 a[168:171], v249 offset:45056\n"
"s_mul_i32 s60, s60, 160\n"
"v_mfma_f32_16x16x32_bf16 a[100:103], a[44:47], a[24:27], a[100:103]\n"
"s_sub_u32 s59, s59, s60\n"
"v_mfma_f32_16x16x32_bf16 v[112:115], a[24:27], a[128:131], v[112:115]\n"
"buffer_load_dwordx4 v247, s[40:43], s52 offen offset:3072 lds\n"
"v_mfma_f32_16x16x32_bf16 v[116:119], a[24:27], a[132:135], v[116:119]\n"
"s_mov_b64 s[44:45], s[8:9]\n"
"s_add_u32 s44, s44, s59\n"
"s_addc_u32 s45, s45, 0\n"
"v_mfma_f32_16x16x32_bf16 v[120:123], a[24:27], a[136:139], v[120:123]\n"
"s_add_u32 m0, s89, 32768\n"
"ds_read_b128 a[172:175], v249 offset:47104\n"
"s_mov_b32 s52, 384\n"
"v_mfma_f32_16x16x32_bf16 v[124:127], a[24:27], a[140:143], v[124:127]\n"
"s_mov_b32 s46, 0\n"
"s_mov_b32 s47, 0x20000\n"
"s_add_u32 s46, s60, 800\n"
"v_mfma_f32_16x16x32_bf16 v[104:107], a[32:35], a[28:31], v[104:107]\n"
"s_lshl_b32 s60, s60, 8\n"
"v_lshrrev_b32 v228, 4, v255\n"
"v_mul_u32_u24 v228, 320, v228\n"
"v_mfma_f32_16x16x32_bf16 a[104:107], a[36:39], a[28:31], a[104:107]\n"
"v_add_u32 v228, 161792, v228\n"
"v_and_b32 v229, 15, v255\n"
"v_mfma_f32_16x16x32_bf16 v[108:111], a[40:43], a[28:31], v[108:111]\n"
"v_lshl_add_u32 v221, v229, 1, v228\n"
"v_lshrrev_b32 v229, 2, v229\n"
"v_mfma_f32_16x16x32_bf16 a[108:111], a[44:47], a[28:31], a[108:111]\n"
"v_lshl_add_u32 v222, v229, 1, v228\n"
"v_mov_b32 v229, s60\n"
"v_mfma_f32_16x16x32_bf16 a[112:115], a[28:31], a[128:131], a[112:115]\n"
"v_cmp_gt_u32 vcc, 10, v255\n"
"s_nop 1\n"
"v_cndmask_b32 v229, 0, v229, vcc\n"
"v_mfma_f32_16x16x32_bf16 a[116:119], a[28:31], a[132:135], a[116:119]\n"
"v_add_u32 v220, v254, v229\n"
"v_mfma_f32_16x16x32_bf16 a[120:123], a[28:31], a[136:139], a[120:123]\n"
"v_mfma_f32_16x16x32_bf16 a[124:127], a[28:31], a[140:143], a[124:127]\n"
"s_waitcnt lgkmcnt(0)\n"
"v_mfma_f32_16x16x32_bf16 v[0:3], a[176:179], a[144:147], v[0:3]\n"
"s_waitcnt vmcnt(4)\n"
"v_mfma_f32_16x16x32_bf16 v[128:131], a[180:183], a[144:147], v[128:131]\n"
"s_barrier\n"
"v_mfma_f32_16x16x32_bf16 v[4:7], a[184:187], a[144:147], v[4:7]\n"
"buffer_load_dwordx4 v240, s[36:39], s52 offen offset:0 lds\n"
"s_mov_b32 s52, 7552\n"
"v_mfma_f32_16x16x32_bf16 v[132:135], a[188:191], a[144:147], v[132:135]\n"
"ds_read_b128 a[0:3], v248 offset:0\n"
"v_lshlrev_b32 v238, 21, v255\n"
"v_mfma_f32_16x16x32_bf16 v[16:19], a[144:147], a[192:195], v[16:19]\n"
"v_mov_b32 v239, s88\n"
"buffer_store_dword v239, v238, s[84:87], s82 offen sc1\n"
"v_mfma_f32_16x16x32_bf16 v[20:23], a[144:147], a[196:199], v[20:23]\n"
"ds_read_b64_tr_b16 a[32:33], v250 offset:32768\n"
"v_mfma_f32_16x16x32_bf16 v[24:27], a[144:147], a[200:203], v[24:27]\n"
"s_lshl_b32 s53, s78, 7\n"
"buffer_load_dwordx4 v241, s[36:39], s52 offen offset:1024 lds\n"
"v_mfma_f32_16x16x32_bf16 v[28:31], a[144:147], a[204:207], v[28:31]\n"
"s_mov_b32 s52, 14720\n"
"ds_read_b64_tr_b16 a[34:35], v250 offset:33792\n"
"v_mfma_f32_16x16x32_bf16 v[8:11], a[176:179], a[148:151], v[8:11]\n"
"s_mov_b32 s82, 81797120\n"
"s_cmp_eq_u32 s76, 0\n"
"s_cselect_b32 s82, s82, 1073741824\n"
"v_mfma_f32_16x16x32_bf16 v[136:139], a[180:183], a[148:151], v[136:139]\n"
"s_add_u32 s82, s82, s53\n"
"ds_read_b64_tr_b16 a[36:37], v251 offset:32768\n"
"v_lshrrev_b32 v238, 2, v254\n"
"v_mfma_f32_16x16x32_bf16 v[12:15], a[184:187], a[148:151], v[12:15]\n"
"buffer_load_dwordx4 v240, s[36:39], s52 offen offset:2048 lds\n"
"s_mov_b32 s52, 21888\n"
"v_mfma_f32_16x16x32_bf16 v[140:143], a[188:191], a[148:151], v[140:143]\n"
"ds_read_b64_tr_b16 a[38:39], v251 offset:33792\n"
"v_and_b32 v238, 124, v238\n"
"v_mfma_f32_16x16x32_bf16 v[144:147], a[148:151], a[192:195], v[144:147]\n"
"v_mfma_f32_16x16x32_bf16 v[148:151], a[148:151], a[196:199], v[148:151]\n"
"ds_read_b64_tr_b16 a[40:41], v252 offset:32768\n"
"v_mfma_f32_16x16x32_bf16 v[152:155], a[148:151], a[200:203], v[152:155]\n"
"buffer_load_dwordx4 v241, s[36:39], s52 offen offset:3072 lds\n"
"s_add_u32 m0, s89, 36864\n"
"v_mfma_f32_16x16x32_bf16 v[156:159], a[148:151], a[204:207], v[156:159]\n"
"s_mov_b32 s52, 262528\n"
"ds_read_b64_tr_b16 a[42:43], v252 offset:33792\n"
"v_mfma_f32_16x16x32_bf16 v[32:35], a[176:179], a[152:155], v[32:35]\n"
"v_mfma_f32_16x16x32_bf16 v[160:163], a[180:183], a[152:155], v[160:163]\n"
"v_mfma_f32_16x16x32_bf16 v[36:39], a[184:187], a[152:155], v[36:39]\n"
"ds_read_b64_tr_b16 a[44:45], v253 offset:32768\n"
"v_mfma_f32_16x16x32_bf16 v[164:167], a[188:191], a[152:155], v[164:167]\n"
"buffer_load_dwordx4 v240, s[36:39], s52 offen offset:0 lds\n"
"s_mov_b32 s52, 269696\n"
"v_mfma_f32_16x16x32_bf16 v[48:51], a[152:155], a[192:195], v[48:51]\n"
"ds_read_b64_tr_b16 a[46:47], v253 offset:33792\n"
"v_mfma_f32_16x16x32_bf16 v[52:55], a[152:155], a[196:199], v[52:55]\n"
"v_mfma_f32_16x16x32_bf16 v[56:59], a[152:155], a[200:203], v[56:59]\n"
"ds_read_b64_tr_b16 a[128:129], v250 offset:40960\n"
"v_mfma_f32_16x16x32_bf16 v[60:63], a[152:155], a[204:207], v[60:63]\n"
"buffer_load_dwordx4 v241, s[36:39], s52 offen offset:1024 lds\n"
"s_mov_b32 s52, 276864\n"
"v_mfma_f32_16x16x32_bf16 v[40:43], a[176:179], a[156:159], v[40:43]\n"
"ds_read_b64_tr_b16 a[130:131], v250 offset:41984\n"
"v_mfma_f32_16x16x32_bf16 v[168:171], a[180:183], a[156:159], v[168:171]\n"
"v_mfma_f32_16x16x32_bf16 v[44:47], a[184:187], a[156:159], v[44:47]\n"
"ds_read_b64_tr_b16 a[132:133], v251 offset:40960\n"
"v_mfma_f32_16x16x32_bf16 v[172:175], a[188:191], a[156:159], v[172:175]\n"
"buffer_load_dwordx4 v240, s[36:39], s52 offen offset:2048 lds\n"
"s_mov_b32 s52, 284032\n"
"v_mfma_f32_16x16x32_bf16 a[48:51], a[156:159], a[192:195], a[48:51]\n"
"ds_read_b64_tr_b16 a[134:135], v251 offset:41984\n"
"v_mfma_f32_16x16x32_bf16 a[52:55], a[156:159], a[196:199], a[52:55]\n"
"v_mfma_f32_16x16x32_bf16 a[56:59], a[156:159], a[200:203], a[56:59]\n"
"v_mfma_f32_16x16x32_bf16 a[60:63], a[156:159], a[204:207], a[60:63]\n"
"ds_read_b64_tr_b16 a[136:137], v252 offset:40960\n"
"v_mfma_f32_16x16x32_bf16 v[64:67], a[176:179], a[160:163], v[64:67]\n"
"buffer_load_dwordx4 v241, s[36:39], s52 offen offset:3072 lds\n"
"s_add_u32 m0, s57, 65536\n"
"v_mfma_f32_16x16x32_bf16 a[64:67], a[180:183], a[160:163], a[64:67]\n"
"s_mov_b32 s52, 1572864\n"
"ds_read_b64_tr_b16 a[138:139], v252 offset:41984\n"
"v_mfma_f32_16x16x32_bf16 v[68:71], a[184:187], a[160:163], v[68:71]\n"
"v_mfma_f32_16x16x32_bf16 a[68:71], a[188:191], a[160:163], a[68:71]\n"
"ds_read_b64_tr_b16 a[140:141], v253 offset:40960\n"
"v_mfma_f32_16x16x32_bf16 v[80:83], a[160:163], a[192:195], v[80:83]\n"
"v_mfma_f32_16x16x32_bf16 v[84:87], a[160:163], a[196:199], v[84:87]\n"
"buffer_load_dwordx4 v246, s[40:43], s52 offen offset:0 lds\n"
"s_mov_b32 s52, 1604608\n"
"v_mfma_f32_16x16x32_bf16 v[88:91], a[160:163], a[200:203], v[88:91]\n"
"ds_read_b64_tr_b16 a[142:143], v253 offset:41984\n"
"v_mfma_f32_16x16x32_bf16 v[92:95], a[160:163], a[204:207], v[92:95]\n"
"ds_read_b128 a[4:7], v248 offset:2048\n"
"v_mfma_f32_16x16x32_bf16 v[72:75], a[176:179], a[164:167], v[72:75]\n"
"v_mfma_f32_16x16x32_bf16 a[72:75], a[180:183], a[164:167], a[72:75]\n"
"buffer_load_dwordx4 v246, s[40:43], s52 offen offset:1024 lds\n"
"s_mov_b32 s52, 1636352\n"
"v_mfma_f32_16x16x32_bf16 v[76:79], a[184:187], a[164:167], v[76:79]\n"
"ds_read_b128 a[8:11], v248 offset:4096\n"
"v_mfma_f32_16x16x32_bf16 a[76:79], a[188:191], a[164:167], a[76:79]\n"
"v_mfma_f32_16x16x32_bf16 a[80:83], a[164:167], a[192:195], a[80:83]\n"
"ds_read_b128 a[12:15], v248 offset:6144\n"
"v_mfma_f32_16x16x32_bf16 a[84:87], a[164:167], a[196:199], a[84:87]\n"
"buffer_load_dwordx4 v247, s[40:43], s52 offen offset:2048 lds\n"
"s_mov_b32 s52, 1668096\n"
"v_mfma_f32_16x16x32_bf16 a[88:91], a[164:167], a[200:203], a[88:91]\n"
"ds_read_b128 a[16:19], v248 offset:8192\n"
"v_mfma_f32_16x16x32_bf16 a[92:95], a[164:167], a[204:207], a[92:95]\n"
"v_mfma_f32_16x16x32_bf16 v[96:99], a[176:179], a[168:171], v[96:99]\n"
"ds_read_b128 a[20:23], v248 offset:10240\n"
"v_mfma_f32_16x16x32_bf16 a[96:99], a[180:183], a[168:171], a[96:99]\n"
"buffer_load_dwordx4 v247, s[40:43], s52 offen offset:3072 lds\n"
"s_add_u32 m0, s57, 81920\n"
"v_mfma_f32_16x16x32_bf16 v[100:103], a[184:187], a[168:171], v[100:103]\n"
"s_mov_b32 s52, 1835008\n"
"ds_read_b128 a[24:27], v248 offset:12288\n"
"v_mfma_f32_16x16x32_bf16 a[100:103], a[188:191], a[168:171], a[100:103]\n"
"v_mfma_f32_16x16x32_bf16 v[112:115], a[168:171], a[192:195], v[112:115]\n"
"ds_read_b128 a[28:31], v248 offset:14336\n"
"v_mfma_f32_16x16x32_bf16 v[116:119], a[168:171], a[196:199], v[116:119]\n"
"v_mfma_f32_16x16x32_bf16 v[120:123], a[168:171], a[200:203], v[120:123]\n"
"buffer_load_dwordx4 v246, s[40:43], s52 offen offset:0 lds\n"
"s_mov_b32 s52, 1866752\n"
"v_mfma_f32_16x16x32_bf16 v[124:127], a[168:171], a[204:207], v[124:127]\n"
"v_mfma_f32_16x16x32_bf16 v[104:107], a[176:179], a[172:175], v[104:107]\n"
"v_mfma_f32_16x16x32_bf16 a[104:107], a[180:183], a[172:175], a[104:107]\n"
"v_mfma_f32_16x16x32_bf16 v[108:111], a[184:187], a[172:175], v[108:111]\n"
"buffer_load_dwordx4 v246, s[40:43], s52 offen offset:1024 lds\n"
"s_mov_b32 s52, 1898496\n"
"v_mfma_f32_16x16x32_bf16 a[108:111], a[188:191], a[172:175], a[108:111]\n"
"v_mfma_f32_16x16x32_bf16 a[112:115], a[172:175], a[192:195], a[112:115]\n"
"v_mfma_f32_16x16x32_bf16 a[116:119], a[172:175], a[196:199], a[116:119]\n"
"v_mfma_f32_16x16x32_bf16 a[120:123], a[172:175], a[200:203], a[120:123]\n"
"buffer_load_dwordx4 v247, s[40:43], s52 offen offset:2048 lds\n"
"s_mov_b32 s52, 1930240\n"
"v_mfma_f32_16x16x32_bf16 a[124:127], a[172:175], a[204:207], a[124:127]\n"
"s_waitcnt lgkmcnt(0)\n"
"v_mfma_f32_16x16x32_bf16 v[0:3], a[32:35], a[0:3], v[0:3]\n"
"s_waitcnt vmcnt(16)\n"
"v_mfma_f32_16x16x32_bf16 v[128:131], a[36:39], a[0:3], v[128:131]\n"
"s_barrier\n"
"v_mfma_f32_16x16x32_bf16 v[4:7], a[40:43], a[0:3], v[4:7]\n"
"buffer_load_dwordx4 v247, s[40:43], s52 offen offset:3072 lds\n"
"s_mov_b32 m0, 161792\n"
"v_mfma_f32_16x16x32_bf16 v[132:135], a[44:47], a[0:3], v[132:135]\n"
"ds_read_b128 a[144:147], v249 offset:0\n"
"v_mfma_f32_16x16x32_bf16 v[16:19], a[0:3], a[128:131], v[16:19]\n"
"v_mfma_f32_16x16x32_bf16 v[20:23], a[0:3], a[132:135], v[20:23]\n"
"ds_read_b64_tr_b16 a[176:177], v250 offset:49152\n"
"v_mfma_f32_16x16x32_bf16 v[24:27], a[0:3], a[136:139], v[24:27]\n"
"v_mfma_f32_16x16x32_bf16 v[28:31], a[0:3], a[140:143], v[28:31]\n"
"buffer_load_dwordx4 v220, s[44:47], 0 offen offset:0 lds\n"
"v_mfma_f32_16x16x32_bf16 v[8:11], a[32:35], a[4:7], v[8:11]\n"
"ds_read_b64_tr_b16 a[178:179], v250 offset:50176\n"
"v_mfma_f32_16x16x32_bf16 v[136:139], a[36:39], a[4:7], v[136:139]\n"
"buffer_load_dwordx4 v220, s[44:47], 0 offen offset:1024 lds\n"
"s_add_u32 m0, s57, 98304\n"
"v_mfma_f32_16x16x32_bf16 v[12:15], a[40:43], a[4:7], v[12:15]\n"
"s_mov_b32 s52, 2097152\n"
"ds_read_b64_tr_b16 a[180:181], v251 offset:49152\n"
"s_mul_i32 s59, s79, 81920\n"
"v_mfma_f32_16x16x32_bf16 v[140:143], a[44:47], a[4:7], v[140:143]\n"
"buffer_load_dwordx4 v246, s[40:43], s52 offen offset:0 lds\n"
"s_mov_b32 s52, 2128896\n"
"v_mfma_f32_16x16x32_bf16 v[144:147], a[4:7], a[128:131], v[144:147]\n"
"s_mov_b64 s[44:45], s[12:13]\n"
"s_add_u32 s44, s44, s59\n"
"s_addc_u32 s45, s45, 0\n"
"v_mfma_f32_16x16x32_bf16 v[148:151], a[4:7], a[132:135], v[148:151]\n"
"s_mov_b32 s46, 81920\n"
"ds_read_b64_tr_b16 a[182:183], v251 offset:50176\n"
"ds_read_b64_tr_b16 a[184:185], v252 offset:49152\n"
"v_mfma_f32_16x16x32_bf16 v[152:155], a[4:7], a[136:139], v[152:155]\n"
"s_mov_b32 s47, 0x20000\n"
"v_mfma_f32_16x16x32_bf16 v[156:159], a[4:7], a[140:143], v[156:159]\n"
"buffer_load_dwordx4 v246, s[40:43], s52 offen offset:1024 lds\n"
"s_mov_b32 s52, 2160640\n"
"v_mfma_f32_16x16x32_bf16 v[32:35], a[32:35], a[8:11], v[32:35]\n"
"ds_read_b64_tr_b16 a[186:187], v252 offset:50176\n"
"v_mfma_f32_16x16x32_bf16 v[160:163], a[36:39], a[8:11], v[160:163]\n"
"v_mfma_f32_16x16x32_bf16 v[36:39], a[40:43], a[8:11], v[36:39]\n"
"ds_read_b64_tr_b16 a[188:189], v253 offset:49152\n"
"v_mfma_f32_16x16x32_bf16 v[164:167], a[44:47], a[8:11], v[164:167]\n"
"buffer_load_dwordx4 v247, s[40:43], s52 offen offset:2048 lds\n"
"s_mov_b32 s52, 2192384\n"
"v_mfma_f32_16x16x32_bf16 v[48:51], a[8:11], a[128:131], v[48:51]\n"
"ds_read_b64_tr_b16 a[190:191], v253 offset:50176\n"
"v_mfma_f32_16x16x32_bf16 v[52:55], a[8:11], a[132:135], v[52:55]\n"
"v_mfma_f32_16x16x32_bf16 v[56:59], a[8:11], a[136:139], v[56:59]\n"
"ds_read_b64_tr_b16 a[192:193], v250 offset:57344\n"
"v_mfma_f32_16x16x32_bf16 v[60:63], a[8:11], a[140:143], v[60:63]\n"
"buffer_load_dwordx4 v247, s[40:43], s52 offen offset:3072 lds\n"
"v_mfma_f32_16x16x32_bf16 v[40:43], a[32:35], a[12:15], v[40:43]\n"
"ds_read_b64_tr_b16 a[194:195], v250 offset:58368\n"
"s_mul_i32 s52, s76, 5120\n"
"s_add_u32 m0, s52, 131072\n"
"v_mfma_f32_16x16x32_bf16 v[168:171], a[36:39], a[12:15], v[168:171]\n"
"s_movk_i32 s52, 4096\n"
"v_mfma_f32_16x16x32_bf16 v[44:47], a[40:43], a[12:15], v[44:47]\n"
"ds_read_b64_tr_b16 a[196:197], v251 offset:57344\n"
"v_mfma_f32_16x16x32_bf16 v[172:175], a[44:47], a[12:15], v[172:175]\n"
"v_mfma_f32_16x16x32_bf16 a[48:51], a[12:15], a[128:131], a[48:51]\n"
"ds_read_b64_tr_b16 a[198:199], v251 offset:58368\n"
"v_mfma_f32_16x16x32_bf16 a[52:55], a[12:15], a[132:135], a[52:55]\n"
"buffer_load_dwordx4 v196, s[68:71], 0 offen offset:0 lds\n"
"v_mfma_f32_16x16x32_bf16 a[56:59], a[12:15], a[136:139], a[56:59]\n"
"v_mfma_f32_16x16x32_bf16 a[60:63], a[12:15], a[140:143], a[60:63]\n"
"ds_read_b64_tr_b16 a[200:201], v252 offset:57344\n"
"v_mfma_f32_16x16x32_bf16 v[64:67], a[32:35], a[16:19], v[64:67]\n"
"v_mfma_f32_16x16x32_bf16 a[64:67], a[36:39], a[16:19], a[64:67]\n"
"buffer_load_dwordx4 v197, s[68:71], 0 offen offset:1024 lds\n"
"v_mfma_f32_16x16x32_bf16 v[68:71], a[40:43], a[16:19], v[68:71]\n"
"ds_read_b64_tr_b16 a[202:203], v252 offset:58368\n"
"v_mfma_f32_16x16x32_bf16 a[68:71], a[44:47], a[16:19], a[68:71]\n"
"ds_read_b64_tr_b16 a[204:205], v253 offset:57344\n"
"v_mfma_f32_16x16x32_bf16 v[80:83], a[16:19], a[128:131], v[80:83]\n"
"v_mfma_f32_16x16x32_bf16 v[84:87], a[16:19], a[132:135], v[84:87]\n"
"buffer_load_dwordx4 v198, s[68:71], 0 offen offset:2048 lds\n"
"v_mfma_f32_16x16x32_bf16 v[88:91], a[16:19], a[136:139], v[88:91]\n"
"ds_read_b64_tr_b16 a[206:207], v253 offset:58368\n"
"v_mfma_f32_16x16x32_bf16 v[92:95], a[16:19], a[140:143], v[92:95]\n"
"ds_read_b128 a[148:151], v249 offset:2048\n"
"v_mfma_f32_16x16x32_bf16 v[72:75], a[32:35], a[20:23], v[72:75]\n"
"v_mfma_f32_16x16x32_bf16 a[72:75], a[36:39], a[20:23], a[72:75]\n"
"buffer_load_dwordx4 v199, s[68:71], 0 offen offset:3072 lds\n"
"s_add_u32 m0, m0, 4096\n"
"v_mfma_f32_16x16x32_bf16 v[76:79], a[40:43], a[20:23], v[76:79]\n"
"ds_read_b128 a[152:155], v249 offset:4096\n"
"v_mfma_f32_16x16x32_bf16 a[76:79], a[44:47], a[20:23], a[76:79]\n"
"v_mfma_f32_16x16x32_bf16 a[80:83], a[20:23], a[128:131], a[80:83]\n"
"ds_read_b128 a[156:159], v249 offset:6144\n"
"v_mfma_f32_16x16x32_bf16 a[84:87], a[20:23], a[132:135], a[84:87]\n"
"v_mfma_f32_16x16x32_bf16 a[88:91], a[20:23], a[136:139], a[88:91]\n"
"buffer_load_dwordx4 v204, s[68:71], s52 offen lds\n"
"s_mov_b32 m0, s64\n"
"v_mfma_f32_16x16x32_bf16 a[92:95], a[20:23], a[140:143], a[92:95]\n"
"s_movk_i32 s52, 4096\n"
"ds_read_b128 a[160:163], v249 offset:8192\n"
"v_mfma_f32_16x16x32_bf16 v[96:99], a[32:35], a[24:27], v[96:99]\n"
"ds_read_b128 a[164:167], v249 offset:10240\n"
"v_mfma_f32_16x16x32_bf16 a[96:99], a[36:39], a[24:27], a[96:99]\n"
"v_mfma_f32_16x16x32_bf16 v[100:103], a[40:43], a[24:27], v[100:103]\n"
"buffer_load_dwordx4 v205, s[72:75], 0 offen offset:0 lds\n"
"v_mfma_f32_16x16x32_bf16 a[100:103], a[44:47], a[24:27], a[100:103]\n"
"ds_read_b128 a[168:171], v249 offset:12288\n"
"v_mfma_f32_16x16x32_bf16 v[112:115], a[24:27], a[128:131], v[112:115]\n"
"buffer_load_dwordx4 v206, s[72:75], 0 offen offset:1024 lds\n"
"v_mfma_f32_16x16x32_bf16 v[116:119], a[24:27], a[132:135], v[116:119]\n"
"buffer_load_dwordx4 v207, s[72:75], 0 offen offset:2048 lds\n"
"v_mfma_f32_16x16x32_bf16 v[120:123], a[24:27], a[136:139], v[120:123]\n"
"s_waitcnt vmcnt(12)\n"
"ds_read_b128 a[172:175], v249 offset:14336\n"
"ds_read_u16 a208, v221 offset:0\n"
"v_mfma_f32_16x16x32_bf16 v[124:127], a[24:27], a[140:143], v[124:127]\n"
"buffer_load_dwordx4 v212, s[72:75], 0 offen offset:3072 lds\n"
"s_add_u32 m0, m0, 4096\n"
"v_mfma_f32_16x16x32_bf16 v[104:107], a[32:35], a[28:31], v[104:107]\n"
"buffer_load_dwordx4 v213, s[72:75], s52 offen lds\n"
"v_mfma_f32_16x16x32_bf16 a[104:107], a[36:39], a[28:31], a[104:107]\n"
"ds_read_u16 a209, v221 offset:40\n"
"ds_read_u16 a210, v221 offset:80\n"
"v_mfma_f32_16x16x32_bf16 v[108:111], a[40:43], a[28:31], v[108:111]\n"
"ds_read_u16 a211, v221 offset:120\n"
"ds_read_u16 a212, v221 offset:160\n"
"v_mfma_f32_16x16x32_bf16 a[108:111], a[44:47], a[28:31], a[108:111]\n"
"ds_read_u16 a213, v221 offset:200\n"
"v_mfma_f32_16x16x32_bf16 a[112:115], a[28:31], a[128:131], a[112:115]\n"
"ds_read_u16 a214, v221 offset:240\n"
"v_mfma_f32_16x16x32_bf16 a[116:119], a[28:31], a[132:135], a[116:119]\n"
"ds_read_u16 a215, v221 offset:280\n"
"v_mfma_f32_16x16x32_bf16 a[120:123], a[28:31], a[136:139], a[120:123]\n"
"s_add_u32 m0, s89, 0\n"
"s_mov_b32 s52, 512\n"
"ds_read_u16 a216, v222 offset:32\n"
"v_mfma_f32_16x16x32_bf16 a[124:127], a[28:31], a[140:143], a[124:127]\n"
"s_waitcnt lgkmcnt(9)\n"
"ds_read_u16 a217, v222 offset:72\n"
"v_mfma_f32_16x16x32_bf16 v[0:3], a[176:179], a[144:147], v[0:3]\n"
"s_waitcnt vmcnt(20)\n"
"buffer_load_dword v239, v238, s[84:87], s82 offen sc1\n"
"v_mfma_f32_16x16x32_bf16 v[128:131], a[180:183], a[144:147], v[128:131]\n"
"s_barrier\n"
"ds_read_u16 a218, v222 offset:112\n"
"ds_read_u16 a219, v222 offset:152\n"
"v_mfma_f32_16x16x32_bf16 v[4:7], a[184:187], a[144:147], v[4:7]\n"
"ds_read_b128 a[0:3], v248 offset:32768\n"
"ds_read_u16 a220, v222 offset:192\n"
"v_mfma_f32_16x16x32_bf16 v[132:135], a[188:191], a[144:147], v[132:135]\n"
"ds_read_u16 a221, v222 offset:232\n"
"v_mfma_f32_16x16x32_bf16 v[16:19], a[144:147], a[192:195], v[16:19]\n"
"ds_read_u16 a222, v222 offset:272\n"
"v_mfma_f32_16x16x32_bf16 v[20:23], a[144:147], a[196:199], v[20:23]\n"
"ds_read_b64_tr_b16 a[32:33], v250 offset:0\n"
"ds_read_u16 a223, v222 offset:312\n"
"v_mfma_f32_16x16x32_bf16 v[24:27], a[144:147], a[200:203], v[24:27]\n"
"v_mfma_f32_16x16x32_bf16 v[28:31], a[144:147], a[204:207], v[28:31]\n"
"ds_read_b64_tr_b16 a[34:35], v250 offset:1024\n"
"v_mfma_f32_16x16x32_bf16 v[8:11], a[176:179], a[148:151], v[8:11]\n"
"v_mfma_f32_16x16x32_bf16 v[136:139], a[180:183], a[148:151], v[136:139]\n"
"ds_read_b64_tr_b16 a[36:37], v251 offset:0\n"
"v_mfma_f32_16x16x32_bf16 v[12:15], a[184:187], a[148:151], v[12:15]\n"
"buffer_load_dwordx4 v240, s[36:39], s52 offen offset:0 lds\n"
"s_mov_b32 s52, 7680\n"
"v_mfma_f32_16x16x32_bf16 v[140:143], a[188:191], a[148:151], v[140:143]\n"
"s_waitcnt lgkmcnt(2)\n"
"ds_read_b64_tr_b16 a[38:39], v251 offset:1024\n"
"v_accvgpr_read_b32 v232, a208\n"
"v_mfma_f32_16x16x32_bf16 v[144:147], a[148:151], a[192:195], v[144:147]\n"
"v_accvgpr_read_b32 v233, a209\n"
"v_lshl_or_b32 v232, v233, 16, v232\n"
"v_mfma_f32_16x16x32_bf16 v[148:151], a[148:151], a[196:199], v[148:151]\n"
"ds_read_b64_tr_b16 a[40:41], v252 offset:0\n"
"v_accvgpr_write_b32 a208, v232\n"
"v_mfma_f32_16x16x32_bf16 v[152:155], a[148:151], a[200:203], v[152:155]\n"
"buffer_load_dwordx4 v241, s[36:39], s52 offen offset:1024 lds\n"
"s_mov_b32 s52, 14848\n"
"v_mfma_f32_16x16x32_bf16 v[156:159], a[148:151], a[204:207], v[156:159]\n"
"ds_read_b64_tr_b16 a[42:43], v252 offset:1024\n"
"v_accvgpr_read_b32 v232, a210\n"
"v_mfma_f32_16x16x32_bf16 v[32:35], a[176:179], a[152:155], v[32:35]\n"
"v_accvgpr_read_b32 v233, a211\n"
"v_lshl_or_b32 v232, v233, 16, v232\n"
"v_mfma_f32_16x16x32_bf16 v[160:163], a[180:183], a[152:155], v[160:163]\n"
"v_accvgpr_write_b32 a209, v232\n"
"v_accvgpr_read_b32 v232, a212\n"
"v_mfma_f32_16x16x32_bf16 v[36:39], a[184:187], a[152:155], v[36:39]\n"
"buffer_load_dwordx4 v240, s[36:39], s52 offen offset:2048 lds\n"
"s_mov_b32 s52, 22016\n"
"v_mfma_f32_16x16x32_bf16 v[164:167], a[188:191], a[152:155], v[164:167]\n"
"ds_read_b64_tr_b16 a[44:45], v253 offset:0\n"
"v_accvgpr_read_b32 v233, a213\n"
"v_mfma_f32_16x16x32_bf16 v[48:51], a[152:155], a[192:195], v[48:51]\n"
"ds_read_b64_tr_b16 a[46:47], v253 offset:1024\n"
"v_lshl_or_b32 v232, v233, 16, v232\n"
"v_mfma_f32_16x16x32_bf16 v[52:55], a[152:155], a[196:199], v[52:55]\n"
"v_accvgpr_write_b32 a210, v232\n"
"v_accvgpr_read_b32 v232, a214\n"
"v_mfma_f32_16x16x32_bf16 v[56:59], a[152:155], a[200:203], v[56:59]\n"
"buffer_load_dwordx4 v241, s[36:39], s52 offen offset:3072 lds\n"
"s_add_u32 m0, s89, 4096\n"
"v_mfma_f32_16x16x32_bf16 v[60:63], a[152:155], a[204:207], v[60:63]\n"
"s_mov_b32 s52, 262656\n"
"ds_read_b64_tr_b16 a[128:129], v250 offset:8192\n"
"v_accvgpr_read_b32 v233, a215\n"
"v_mfma_f32_16x16x32_bf16 v[40:43], a[176:179], a[156:159], v[40:43]\n"
"ds_read_b64_tr_b16 a[130:131], v250 offset:9216\n"
"v_lshl_or_b32 v232, v233, 16, v232\n"
"v_mfma_f32_16x16x32_bf16 v[168:171], a[180:183], a[156:159], v[168:171]\n"
"v_accvgpr_write_b32 a211, v232\n"
"v_accvgpr_read_b32 v232, a216\n"
"v_mfma_f32_16x16x32_bf16 v[44:47], a[184:187], a[156:159], v[44:47]\n"
"ds_read_b64_tr_b16 a[132:133], v251 offset:8192\n"
"v_accvgpr_read_b32 v233, a217\n"
"v_mfma_f32_16x16x32_bf16 v[172:175], a[188:191], a[156:159], v[172:175]\n"
"buffer_load_dwordx4 v240, s[36:39], s52 offen offset:0 lds\n"
"s_mov_b32 s52, 269824\n"
"v_mfma_f32_16x16x32_bf16 a[48:51], a[156:159], a[192:195], a[48:51]\n"
"ds_read_b64_tr_b16 a[134:135], v251 offset:9216\n"
"v_lshl_or_b32 v232, v233, 16, v232\n"
"v_mfma_f32_16x16x32_bf16 a[52:55], a[156:159], a[196:199], a[52:55]\n"
"v_accvgpr_write_b32 a212, v232\n"
"v_accvgpr_read_b32 v232, a218\n"
"v_mfma_f32_16x16x32_bf16 a[56:59], a[156:159], a[200:203], a[56:59]\n"
"v_accvgpr_read_b32 v233, a219\n"
"v_lshl_or_b32 v232, v233, 16, v232\n"
"v_mfma_f32_16x16x32_bf16 a[60:63], a[156:159], a[204:207], a[60:63]\n"
"buffer_load_dwordx4 v241, s[36:39], s52 offen offset:1024 lds\n"
"s_mov_b32 s52, 276992\n"
"v_mfma_f32_16x16x32_bf16 v[64:67], a[176:179], a[160:163], v[64:67]\n"
"ds_read_b64_tr_b16 a[136:137], v252 offset:8192\n"
"v_accvgpr_write_b32 a213, v232\n"
"v_mfma_f32_16x16x32_bf16 a[64:67], a[180:183], a[160:163], a[64:67]\n"
"ds_read_b64_tr_b16 a[138:139], v252 offset:9216\n"
"v_accvgpr_read_b32 v232, a220\n"
"v_mfma_f32_16x16x32_bf16 v[68:71], a[184:187], a[160:163], v[68:71]\n"
"v_accvgpr_read_b32 v233, a221\n"
"v_lshl_or_b32 v232, v233, 16, v232\n"
"v_mfma_f32_16x16x32_bf16 a[68:71], a[188:191], a[160:163], a[68:71]\n"
"buffer_load_dwordx4 v240, s[36:39], s52 offen offset:2048 lds\n"
"s_mov_b32 s52, 284160\n"
"v_mfma_f32_16x16x32_bf16 v[80:83], a[160:163], a[192:195], v[80:83]\n"
"ds_read_b64_tr_b16 a[140:141], v253 offset:8192\n"
"v_accvgpr_write_b32 a214, v232\n"
"v_mfma_f32_16x16x32_bf16 v[84:87], a[160:163], a[196:199], v[84:87]\n"
"ds_read_b64_tr_b16 a[142:143], v253 offset:9216\n"
"v_accvgpr_read_b32 v232, a222\n"
"v_mfma_f32_16x16x32_bf16 v[88:91], a[160:163], a[200:203], v[88:91]\n"
"v_accvgpr_read_b32 v233, a223\n"
"v_lshl_or_b32 v232, v233, 16, v232\n"
"v_mfma_f32_16x16x32_bf16 v[92:95], a[160:163], a[204:207], v[92:95]\n"
"ds_read_b128 a[4:7], v248 offset:34816\n"
"v_accvgpr_write_b32 a215, v232\n"
"s_lshr_b32 s58, s77, 1\n"
"v_mfma_f32_16x16x32_bf16 v[72:75], a[176:179], a[164:167], v[72:75]\n"
"s_lshl_b32 s58, s58, 3\n"
"buffer_load_dwordx4 v241, s[36:39], s52 offen offset:3072 lds\n"
"v_mfma_f32_16x16x32_bf16 a[72:75], a[180:183], a[164:167], a[72:75]\n"
"s_add_u32 m0, s57, 114688\n"
"ds_read_b128 a[8:11], v248 offset:36864\n"
"s_mov_b32 s52, 2359296\n"
"v_mfma_f32_16x16x32_bf16 v[76:79], a[184:187], a[164:167], v[76:79]\n"
"s_add_u32 s58, s58, s78\n"
"s_lshl_b32 s58, s58, 2\n"
"s_add_u32 s58, s58, s76\n"
"v_mfma_f32_16x16x32_bf16 a[76:79], a[188:191], a[164:167], a[76:79]\n"
"s_lshl_b32 s58, s58, 5\n"
"s_and_b32 s59, s77, 1\n"
"s_add_u32 s58, s58, s59\n"
"v_mfma_f32_16x16x32_bf16 a[80:83], a[164:167], a[192:195], a[80:83]\n"
"s_lshl_b32 s58, s58, 9\n"
"ds_read_b128 a[12:15], v248 offset:38912\n"
"s_add_u32 s58, s58, 52428800\n"
"v_mfma_f32_16x16x32_bf16 a[84:87], a[164:167], a[196:199], a[84:87]\n"
"buffer_load_dwordx4 v246, s[40:43], s52 offen offset:0 lds\n"
"s_mov_b32 s52, 2391040\n"
"v_mfma_f32_16x16x32_bf16 a[88:91], a[164:167], a[200:203], a[88:91]\n"
"ds_read_b128 a[16:19], v248 offset:40960\n"
"v_lshrrev_b32 v237, 4, v255\n"
"v_mfma_f32_16x16x32_bf16 a[92:95], a[164:167], a[204:207], a[92:95]\n"
"v_mul_u32_u24 v237, 3840, v237\n"
"v_add_u32 v237, v237, v254\n"
"v_mfma_f32_16x16x32_bf16 v[96:99], a[176:179], a[168:171], v[96:99]\n"
"ds_read_b128 a[20:23], v248 offset:43008\n"
"v_mfma_f32_16x16x32_bf16 a[96:99], a[180:183], a[168:171], a[96:99]\n"
"buffer_load_dwordx4 v246, s[40:43], s52 offen offset:1024 lds\n"
"s_mov_b32 s52, 2422784\n"
"v_mfma_f32_16x16x32_bf16 v[100:103], a[184:187], a[168:171], v[100:103]\n"
"ds_read_b128 a[24:27], v248 offset:45056\n"
"v_mfma_f32_16x16x32_bf16 a[100:103], a[188:191], a[168:171], a[100:103]\n"
"v_mfma_f32_16x16x32_bf16 v[112:115], a[168:171], a[192:195], v[112:115]\n"
"ds_read_b128 a[28:31], v248 offset:47104\n"
"v_mfma_f32_16x16x32_bf16 v[116:119], a[168:171], a[196:199], v[116:119]\n"
"buffer_load_dwordx4 v247, s[40:43], s52 offen offset:2048 lds\n"
"s_mov_b32 s52, 2454528\n"
"v_mfma_f32_16x16x32_bf16 v[120:123], a[168:171], a[200:203], v[120:123]\n"
"v_mfma_f32_16x16x32_bf16 v[124:127], a[168:171], a[204:207], v[124:127]\n"
"v_mfma_f32_16x16x32_bf16 v[104:107], a[176:179], a[172:175], v[104:107]\n"
"v_mfma_f32_16x16x32_bf16 a[104:107], a[180:183], a[172:175], a[104:107]\n"
"buffer_load_dwordx4 v247, s[40:43], s52 offen offset:3072 lds\n"
"v_mfma_f32_16x16x32_bf16 v[108:111], a[184:187], a[172:175], v[108:111]\n"
"v_mfma_f32_16x16x32_bf16 a[108:111], a[188:191], a[172:175], a[108:111]\n"
"v_mfma_f32_16x16x32_bf16 a[112:115], a[172:175], a[192:195], a[112:115]\n"
"v_mfma_f32_16x16x32_bf16 a[116:119], a[172:175], a[196:199], a[116:119]\n"
"s_waitcnt vmcnt(12)\n"
"v_cmp_ne_u32 vcc, s88, v239\n"
"s_cbranch_vccnz .Lslow1_%=\n"
".Lback1_%=:\n"
"v_mfma_f32_16x16x32_bf16 a[120:123], a[172:175], a[200:203], a[120:123]\n"
"v_mfma_f32_16x16x32_bf16 a[124:127], a[172:175], a[204:207], a[124:127]\n"
"s_waitcnt lgkmcnt(0)\n"
"v_mfma_f32_16x16x32_bf16 v[0:3], a[32:35], a[0:3], v[0:3]\n"
"s_waitcnt vmcnt(13)\n"
"v_mfma_f32_16x16x32_bf16 v[128:131], a[36:39], a[0:3], v[128:131]\n"
"s_barrier\n"
"ds_read_b64_tr_b16 v[212:213], v181 offset:0\n"
"v_mfma_f32_16x16x32_bf16 v[4:7], a[40:43], a[0:3], v[4:7]\n"
"s_add_u32 s59, s58, 0\n"
"buffer_load_dwordx4 a[216:219], v254, s[84:87], s59 offen offset:1280 sc1\n"
"v_mfma_f32_16x16x32_bf16 v[132:135], a[44:47], a[0:3], v[132:135]\n"
"ds_read_b128 a[144:147], v249 offset:32768\n"
"ds_read_b64_tr_b16 v[214:215], v181 offset:640\n"
"v_mfma_f32_16x16x32_bf16 v[16:19], a[0:3], a[128:131], v[16:19]\n"
"ds_read_b64_tr_b16 v[216:217], v181 offset:8\n"
"v_mfma_f32_16x16x32_bf16 v[20:23], a[0:3], a[132:135], v[20:23]\n"
"s_add_u32 s59, s58, 4096\n"
"ds_read_b64_tr_b16 a[176:177], v250 offset:16384\n"
"v_mfma_f32_16x16x32_bf16 v[24:27], a[0:3], a[136:139], v[24:27]\n"
"buffer_load_dwordx4 a[220:223], v254, s[84:87], s59 offen offset:1280 sc1\n"
"v_mfma_f32_16x16x32_bf16 v[28:31], a[0:3], a[140:143], v[28:31]\n"
"ds_read_b64_tr_b16 a[178:179], v250 offset:17408\n"
"ds_read_b64_tr_b16 v[218:219], v181 offset:648\n"
"v_mfma_f32_16x16x32_bf16 v[8:11], a[32:35], a[4:7], v[8:11]\n"
"s_add_u32 m0, s57, 65536\n"
"s_mov_b32 s52, 2621440\n"
"ds_read_b64_tr_b16 v[220:221], v181 offset:64\n"
"v_mfma_f32_16x16x32_bf16 v[136:139], a[36:39], a[4:7], v[136:139]\n"
"s_add_u32 s59, s58, 8192\n"
"ds_read_b64_tr_b16 a[180:181], v251 offset:16384\n"
"ds_read_b64_tr_b16 v[222:223], v181 offset:704\n"
"v_mfma_f32_16x16x32_bf16 v[12:15], a[40:43], a[4:7], v[12:15]\n"
"buffer_load_dwordx4 a[224:227], v254, s[84:87], s59 offen offset:1280 sc1\n"
"v_mfma_f32_16x16x32_bf16 v[140:143], a[44:47], a[4:7], v[140:143]\n"
"ds_read_b64_tr_b16 a[182:183], v251 offset:17408\n"
"ds_read_b64_tr_b16 v[224:225], v181 offset:72\n"
"v_mfma_f32_16x16x32_bf16 v[144:147], a[4:7], a[128:131], v[144:147]\n"
"ds_read_b64_tr_b16 v[226:227], v181 offset:712\n"
"v_mfma_f32_16x16x32_bf16 v[148:151], a[4:7], a[132:135], v[148:151]\n"
"s_add_u32 s59, s58, 12288\n"
"ds_read_b64_tr_b16 a[184:185], v252 offset:16384\n"
"ds_read_b64_tr_b16 v[228:229], v183 offset:128\n"
"v_mfma_f32_16x16x32_bf16 v[152:155], a[4:7], a[136:139], v[152:155]\n"
"buffer_load_dwordx4 a[228:231], v254, s[84:87], s59 offen offset:1280 sc1\n"
"v_mfma_f32_16x16x32_bf16 v[156:159], a[4:7], a[140:143], v[156:159]\n"
"ds_read_b64_tr_b16 a[186:187], v252 offset:17408\n"
"ds_read_b64_tr_b16 v[230:231], v183 offset:768\n"
".Lpad4_%=:\n"
"s_cbranch_execz .Lpad8_%=\n"
"v_mfma_f32_16x16x32_bf16 v[32:35], a[32:35], a[8:11], v[32:35]\n"
"ds_read_b64_tr_b16 a[232:233], v180 offset:0\n"
"v_mfma_f32_16x16x32_bf16 v[160:163], a[36:39], a[8:11], v[160:163]\n"
"ds_read_b64_tr_b16 a[234:235], v180 offset:640\n"
"v_mfma_f32_16x16x32_bf16 v[36:39], a[40:43], a[8:11], v[36:39]\n"
"ds_read_b64_tr_b16 a[188:189], v253 offset:16384\n"
"v_mfma_f32_16x16x32_bf16 v[164:167], a[44:47], a[8:11], v[164:167]\n"
"ds_read_b64_tr_b16 a[236:237], v180 offset:8\n"
"v_mfma_f32_16x16x32_bf16 v[48:51], a[8:11], a[128:131], v[48:51]\n"
"buffer_load_dwordx4 v246, s[40:43], s52 offen offset:0 lds\n"
"s_mov_b32 s52, 2653184\n"
"v_mfma_f32_16x16x32_bf16 v[52:55], a[8:11], a[132:135], v[52:55]\n"
"ds_read_b64_tr_b16 a[190:191], v253 offset:17408\n"
"ds_read_b64_tr_b16 a[238:239], v180 offset:648\n"
"v_mfma_f32_16x16x32_bf16 v[56:59], a[8:11], a[136:139], v[56:59]\n"
"ds_read_b64_tr_b16 a[192:193], v250 offset:24576\n"
"ds_read_b64_tr_b16 a[240:241], v180 offset:64\n"
"v_mfma_f32_16x16x32_bf16 v[60:63], a[8:11], a[140:143], v[60:63]\n"
"ds_read_b64_tr_b16 a[242:243], v180 offset:704\n"
"v_mfma_f32_16x16x32_bf16 v[40:43], a[32:35], a[12:15], v[40:43]\n"
"buffer_load_dwordx4 v246, s[40:43], s52 offen offset:1024 lds\n"
"s_mov_b32 s52, 2684928\n"
"v_mfma_f32_16x16x32_bf16 v[168:171], a[36:39], a[12:15], v[168:171]\n"
"ds_read_b64_tr_b16 a[194:195], v250 offset:25600\n"
"ds_read_b64_tr_b16 a[244:245], v180 offset:72\n"
"v_mfma_f32_16x16x32_bf16 v[44:47], a[40:43], a[12:15], v[44:47]\n"
"ds_read_b64_tr_b16 a[196:197], v251 offset:24576\n"
"ds_read_b64_tr_b16 a[246:247], v180 offset:712\n"
"v_mfma_f32_16x16x32_bf16 v[172:175], a[44:47], a[12:15], v[172:175]\n"
"v_mfma_f32_16x16x32_bf16 a[48:51], a[12:15], a[128:131], a[48:51]\n"
"buffer_load_dwordx4 v247, s[40:43], s52 offen offset:2048 lds\n"
"s_mov_b32 s52, 2716672\n"
"v_mfma_f32_16x16x32_bf16 a[52:55], a[12:15], a[132:135], a[52:55]\n"
"ds_read_b64_tr_b16 a[198:199], v251 offset:25600\n"
"ds_read_b64_tr_b16 a[248:249], v182 offset:128\n"
"v_mfma_f32_16x16x32_bf16 a[56:59], a[12:15], a[136:139], a[56:59]\n"
"ds_read_b64_tr_b16 a[250:251], v182 offset:768\n"
"v_mfma_f32_16x16x32_bf16 a[60:63], a[12:15], a[140:143], a[60:63]\n"
"ds_read_b64_tr_b16 a[200:201], v252 offset:24576\n"
"v_mfma_f32_16x16x32_bf16 v[64:67], a[32:35], a[16:19], v[64:67]\n"
"buffer_load_dwordx4 v247, s[40:43], s52 offen offset:3072 lds\n"
"v_mfma_f32_16x16x32_bf16 a[64:67], a[36:39], a[16:19], a[64:67]\n"
"ds_read_b64_tr_b16 a[202:203], v252 offset:25600\n"
"v_mfma_f32_16x16x32_bf16 v[68:71], a[40:43], a[16:19], v[68:71]\n"
"v_mfma_f32_16x16x32_bf16 a[68:71], a[44:47], a[16:19], a[68:71]\n"
"ds_read_b64_tr_b16 a[204:205], v253 offset:24576\n"
"v_mfma_f32_16x16x32_bf16 v[80:83], a[16:19], a[128:131], v[80:83]\n"
"v_mfma_f32_16x16x32_bf16 v[84:87], a[16:19], a[132:135], v[84:87]\n"
"ds_read_b64_tr_b16 a[206:207], v253 offset:25600\n"
"v_mfma_f32_16x16x32_bf16 v[88:91], a[16:19], a[136:139], v[88:91]\n"
"v_mfma_f32_16x16x32_bf16 v[92:95], a[16:19], a[140:143], v[92:95]\n"
"ds_read_b128 a[148:151], v249 offset:34816\n"
"v_mfma_f32_16x16x32_bf16 v[72:75], a[32:35], a[20:23], v[72:75]\n"
"s_waitcnt lgkmcnt(15)\n"
"buffer_store_dwordx4 v[212:215], v254, s[84:87], s81 offen offset:0 sc1\n"
"v_mfma_f32_16x16x32_bf16 a[72:75], a[36:39], a[20:23], a[72:75]\n"
"ds_read_b128 a[152:155], v249 offset:36864\n"
"v_mfma_f32_16x16x32_bf16 v[76:79], a[40:43], a[20:23], v[76:79]\n"
"v_mfma_f32_16x16x32_bf16 a[76:79], a[44:47], a[20:23], a[76:79]\n"
"v_mfma_f32_16x16x32_bf16 a[80:83], a[20:23], a[128:131], a[80:83]\n"
"buffer_store_dwordx4 v[216:219], v254, s[84:87], s81 offen offset:1024 sc1\n"
"v_mfma_f32_16x16x32_bf16 a[84:87], a[20:23], a[132:135], a[84:87]\n"
"ds_read_b128 a[156:159], v249 offset:38912\n"
"v_mfma_f32_16x16x32_bf16 a[88:91], a[20:23], a[136:139], a[88:91]\n"
"ds_read_b128 a[160:163], v249 offset:40960\n"
"v_mfma_f32_16x16x32_bf16 a[92:95], a[20:23], a[140:143], a[92:95]\n"
"v_mfma_f32_16x16x32_bf16 v[96:99], a[32:35], a[24:27], v[96:99]\n"
"buffer_store_dwordx4 v[220:223], v254, s[84:87], s81 offen offset:2048 sc1\n"
"v_mfma_f32_16x16x32_bf16 a[96:99], a[36:39], a[24:27], a[96:99]\n"
"ds_read_b128 a[164:167], v249 offset:43008\n"
"v_mfma_f32_16x16x32_bf16 v[100:103], a[40:43], a[24:27], v[100:103]\n"
"ds_read_b128 a[168:171], v249 offset:45056\n"
"v_mfma_f32_16x16x32_bf16 a[100:103], a[44:47], a[24:27], a[100:103]\n"
"v_mfma_f32_16x16x32_bf16 v[112:115], a[24:27], a[128:131], v[112:115]\n"
"buffer_store_dwordx4 v[224:227], v254, s[84:87], s81 offen offset:3072 sc1\n"
"v_mfma_f32_16x16x32_bf16 v[116:119], a[24:27], a[132:135], v[116:119]\n"
"ds_read_b128 a[172:175], v249 offset:47104\n"
"v_mfma_f32_16x16x32_bf16 v[120:123], a[24:27], a[136:139], v[120:123]\n"
"v_mfma_f32_16x16x32_bf16 v[124:127], a[24:27], a[140:143], v[124:127]\n"
"s_add_u32 s81, s81, 4096\n"
"v_mfma_f32_16x16x32_bf16 v[104:107], a[32:35], a[28:31], v[104:107]\n"
"buffer_store_dwordx4 v[228:231], v254, s[84:87], s81 offen sc1\n"
"s_waitcnt lgkmcnt(11)\n"
"v_mfma_f32_16x16x32_bf16 a[104:107], a[36:39], a[28:31], a[104:107]\n"
"v_mfma_f32_16x16x32_bf16 v[108:111], a[40:43], a[28:31], v[108:111]\n"
"v_mfma_f32_16x16x32_bf16 a[108:111], a[44:47], a[28:31], a[108:111]\n"
"v_mfma_f32_16x16x32_bf16 a[112:115], a[28:31], a[128:131], a[112:115]\n"
"v_mfma_f32_16x16x32_bf16 a[116:119], a[28:31], a[132:135], a[116:119]\n"
"v_mfma_f32_16x16x32_bf16 a[120:123], a[28:31], a[136:139], a[120:123]\n"
"buffer_store_dwordx4 a[232:235], v254, s[84:87], s67 offen offset:0 sc1\n"
"v_mfma_f32_16x16x32_bf16 a[124:127], a[28:31], a[140:143], a[124:127]\n"
"s_waitcnt lgkmcnt(0)\n"
"v_mfma_f32_16x16x32_bf16 v[0:3], a[176:179], a[144:147], v[0:3]\n"
"s_waitcnt vmcnt(10)\n"
"v_and_b32 v232, 112, v254\n"
"v_lshl_add_u32 v232, v255, 7, v232\n"
"v_mfma_f32_16x16x32_bf16 v[128:131], a[180:183], a[144:147], v[128:131]\n"
"s_barrier\n"
"s_add_u32 s53, s89, 131072\n"
"v_add_u32 v232, s53, v232\n"
"v_mfma_f32_16x16x32_bf16 v[176:179], a[208:211], a[216:219], 0\n"
"v_lshrrev_b32 v234, 2, v255\n"
"v_and_b32 v235, 7, v234\n"
"v_mfma_f32_16x16x32_bf16 v[192:195], a[212:215], a[216:219], 0\n"
"v_lshlrev_b32 v235, 4, v235\n"
"v_lshl_add_u32 v234, v234, 7, v235\n"
"v_mfma_f32_16x16x32_bf16 v[180:183], a[208:211], a[220:223], 0\n"
"v_and_b32 v235, 3, v255\n"
"v_lshlrev_b32 v235, 3, v235\n"
"v_mfma_f32_16x16x32_bf16 v[196:199], a[212:215], a[220:223], 0\n"
"v_xor_b32 v234, v234, v235\n"
"v_add_u32 v234, 131072, v234\n"
"v_mfma_f32_16x16x32_bf16 v[184:187], a[208:211], a[224:227], 0\n"
"v_xor_b32 v235, 64, v234\n"
"v_xor_b32 v236, 96, v234\n"
"s_lshl_b32 s53, s76, 5\n"
"v_mfma_f32_16x16x32_bf16 v[200:203], a[212:215], a[224:227], 0\n"
"v_xor_b32 v234, s53, v234\n"
"v_mfma_f32_16x16x32_bf16 v[188:191], a[208:211], a[228:231], 0\n"
"v_mfma_f32_16x16x32_bf16 v[204:207], a[212:215], a[228:231], 0\n"
"s_add_u32 s59, s58, 0\n"
"buffer_load_dwordx4 a[216:219], v254, s[84:87], s59 offen offset:0 sc1\n"
"v_mfma_f32_16x16x32_bf16 v[4:7], a[184:187], a[144:147], v[4:7]\n"
"ds_read_b128 a[0:3], v248 offset:0\n"
"v_mfma_f32_16x16x32_bf16 v[132:135], a[188:191], a[144:147], v[132:135]\n"
"v_mfma_f32_16x16x32_bf16 v[16:19], a[144:147], a[192:195], v[16:19]\n"
"v_mfma_f32_16x16x32_bf16 v[20:23], a[144:147], a[196:199], v[20:23]\n"
"s_add_u32 m0, s89, 32768\n"
"ds_read_b64_tr_b16 a[32:33], v250 offset:32768\n"
"s_mov_b32 s52, 640\n"
"v_mfma_f32_16x16x32_bf16 v[24:27], a[144:147], a[200:203], v[24:27]\n"
"v_permlane32_swap_b32 v176, v184\n"
"v_permlane32_swap_b32 v180, v188\n"
"v_mfma_f32_16x16x32_bf16 v[28:31], a[144:147], a[204:207], v[28:31]\n"
"ds_read_b64_tr_b16 a[34:35], v250 offset:33792\n"
"v_permlane32_swap_b32 v177, v185\n"
"v_mfma_f32_16x16x32_bf16 v[8:11], a[176:179], a[148:151], v[8:11]\n"
"v_permlane32_swap_b32 v181, v189\n"
"v_permlane32_swap_b32 v178, v186\n"
"v_mfma_f32_16x16x32_bf16 v[136:139], a[180:183], a[148:151], v[136:139]\n"
"ds_read_b64_tr_b16 a[36:37], v251 offset:32768\n"
"v_permlane32_swap_b32 v182, v190\n"
"s_add_u32 s59, s58, 4096\n"
"v_mfma_f32_16x16x32_bf16 v[12:15], a[184:187], a[148:151], v[12:15]\n"
"buffer_load_dwordx4 a[220:223], v254, s[84:87], s59 offen offset:0 sc1\n"
"v_mfma_f32_16x16x32_bf16 v[140:143], a[188:191], a[148:151], v[140:143]\n"
"ds_read_b64_tr_b16 a[38:39], v251 offset:33792\n"
"v_permlane32_swap_b32 v179, v187\n"
"v_mfma_f32_16x16x32_bf16 v[144:147], a[148:151], a[192:195], v[144:147]\n"
"v_permlane32_swap_b32 v183, v191\n"
"v_permlane32_swap_b32 v192, v200\n"
"v_mfma_f32_16x16x32_bf16 v[148:151], a[148:151], a[196:199], v[148:151]\n"
"ds_read_b64_tr_b16 a[40:41], v252 offset:32768\n"
"v_permlane32_swap_b32 v196, v204\n"
"v_mfma_f32_16x16x32_bf16 v[152:155], a[148:151], a[200:203], v[152:155]\n"
"v_permlane16_swap_b32 v176, v180\n"
"v_permlane16_swap_b32 v184, v188\n"
"s_add_u32 s59, s58, 8192\n"
"v_mfma_f32_16x16x32_bf16 v[156:159], a[148:151], a[204:207], v[156:159]\n"
"buffer_load_dwordx4 a[224:227], v254, s[84:87], s59 offen offset:0 sc1\n"
"v_mfma_f32_16x16x32_bf16 v[32:35], a[176:179], a[152:155], v[32:35]\n"
"ds_read_b64_tr_b16 a[42:43], v252 offset:33792\n"
"v_permlane16_swap_b32 v177, v181\n"
"v_mfma_f32_16x16x32_bf16 v[160:163], a[180:183], a[152:155], v[160:163]\n"
"buffer_load_dwordx4 v240, s[36:39], s52 offen offset:0 lds\n"
"s_mov_b32 s52, 7808\n"
"v_mfma_f32_16x16x32_bf16 v[36:39], a[184:187], a[152:155], v[36:39]\n"
"ds_read_b64_tr_b16 a[44:45], v253 offset:32768\n"
"v_permlane16_swap_b32 v185, v189\n"
"v_mfma_f32_16x16x32_bf16 v[164:167], a[188:191], a[152:155], v[164:167]\n"
"v_permlane16_swap_b32 v178, v182\n"
"v_permlane16_swap_b32 v186, v190\n"
"v_mfma_f32_16x16x32_bf16 v[48:51], a[152:155], a[192:195], v[48:51]\n"
"ds_read_b64_tr_b16 a[46:47], v253 offset:33792\n"
"v_permlane16_swap_b32 v179, v183\n"
"v_mfma_f32_16x16x32_bf16 v[52:55], a[152:155], a[196:199], v[52:55]\n"
"buffer_load_dwordx4 v241, s[36:39], s52 offen offset:1024 lds\n"
"s_mov_b32 s52, 14976\n"
"v_mfma_f32_16x16x32_bf16 v[56:59], a[152:155], a[200:203], v[56:59]\n"
"ds_read_b64_tr_b16 a[128:129], v250 offset:40960\n"
"v_permlane16_swap_b32 v187, v191\n"
"s_add_u32 s59, s58, 12288\n"
"v_mfma_f32_16x16x32_bf16 v[60:63], a[152:155], a[204:207], v[60:63]\n"
"buffer_load_dwordx4 a[228:231], v254, s[84:87], s59 offen offset:0 sc1\n"
"v_mfma_f32_16x16x32_bf16 v[40:43], a[176:179], a[156:159], v[40:43]\n"
"ds_read_b64_tr_b16 a[130:131], v250 offset:41984\n"
"v_permlane16_swap_b32 v192, v196\n"
"v_mfma_f32_16x16x32_bf16 v[168:171], a[180:183], a[156:159], v[168:171]\n"
"buffer_load_dwordx4 v240, s[36:39], s52 offen offset:2048 lds\n"
"s_mov_b32 s52, 22144\n"
"v_mfma_f32_16x16x32_bf16 v[44:47], a[184:187], a[156:159], v[44:47]\n"
"ds_read_b64_tr_b16 a[132:133], v251 offset:40960\n"
"v_permlane16_swap_b32 v200, v204\n"
"v_mfma_f32_16x16x32_bf16 v[172:175], a[188:191], a[156:159], v[172:175]\n"
"v_max_f32 v208, v176, v177\n"
"v_min_f32 v209, v176, v177\n"
"v_mfma_f32_16x16x32_bf16 a[48:51], a[156:159], a[192:195], a[48:51]\n"
"ds_read_b64_tr_b16 a[134:135], v251 offset:41984\n"
"v_max3_f32 v208, v208, v178, v179\n"
"v_mfma_f32_16x16x32_bf16 a[52:55], a[156:159], a[196:199], a[52:55]\n"
"buffer_load_dwordx4 v241, s[36:39], s52 offen offset:3072 lds\n"
"s_add_u32 m0, s89, 36864\n"
"v_mfma_f32_16x16x32_bf16 a[56:59], a[156:159], a[200:203], a[56:59]\n"
"s_mov_b32 s52, 262784\n"
"v_min3_f32 v209, v209, v178, v179\n"
"v_max3_f32 v208, v208, v180, v181\n"
"v_mfma_f32_16x16x32_bf16 a[60:63], a[156:159], a[204:207], a[60:63]\n"
"ds_read_b64_tr_b16 a[136:137], v252 offset:40960\n"
"v_min3_f32 v209, v209, v180, v181\n"
"v_mfma_f32_16x16x32_bf16 v[64:67], a[176:179], a[160:163], v[64:67]\n"
"v_max3_f32 v208, v208, v182, v183\n"
"v_min3_f32 v209, v209, v182, v183\n"
"v_mfma_f32_16x16x32_bf16 a[64:67], a[180:183], a[160:163], a[64:67]\n"
"ds_read_b64_tr_b16 a[138:139], v252 offset:41984\n"
"v_max3_f32 v208, v208, v184, v185\n"
"v_mfma_f32_16x16x32_bf16 v[68:71], a[184:187], a[160:163], v[68:71]\n"
"buffer_load_dwordx4 v240, s[36:39], s52 offen offset:0 lds\n"
"s_mov_b32 s52, 269952\n"
"v_mfma_f32_16x16x32_bf16 a[68:71], a[188:191], a[160:163], a[68:71]\n"
"ds_read_b64_tr_b16 a[140:141], v253 offset:40960\n"
"v_min3_f32 v209, v209, v184, v185\n"
"v_mfma_f32_16x16x32_bf16 v[80:83], a[160:163], a[192:195], v[80:83]\n"
"v_max3_f32 v208, v208, v186, v187\n"
"v_min3_f32 v209, v209, v186, v187\n"
"v_mfma_f32_16x16x32_bf16 v[84:87], a[160:163], a[196:199], v[84:87]\n"
"ds_read_b64_tr_b16 a[142:143], v253 offset:41984\n"
"v_max3_f32 v208, v208, v188, v189\n"
"v_mfma_f32_16x16x32_bf16 v[88:91], a[160:163], a[200:203], v[88:91]\n"
"buffer_load_dwordx4 v241, s[36:39], s52 offen offset:1024 lds\n"
"s_mov_b32 s52, 277120\n"
"v_mfma_f32_16x16x32_bf16 v[92:95], a[160:163], a[204:207], v[92:95]\n"
"ds_read_b128 a[4:7], v248 offset:2048\n"
"v_min3_f32 v209, v209, v188, v189\n"
"v_mfma_f32_16x16x32_bf16 v[72:75], a[176:179], a[164:167], v[72:75]\n"
"v_max3_f32 v208, v208, v190, v191\n"
"v_min3_f32 v209, v209, v190, v191\n"
"v_mfma_f32_16x16x32_bf16 a[72:75], a[180:183], a[164:167], a[72:75]\n"
"ds_read_b128 a[8:11], v248 offset:4096\n"
"v_max3_f32 v208, v208, v192, v196\n"
"v_mfma_f32_16x16x32_bf16 v[76:79], a[184:187], a[164:167], v[76:79]\n"
"buffer_load_dwordx4 v240, s[36:39], s52 offen offset:2048 lds\n"
"s_mov_b32 s52, 284288\n"
"v_mfma_f32_16x16x32_bf16 a[76:79], a[188:191], a[164:167], a[76:79]\n"
"v_min3_f32 v209, v209, v192, v196\n"
"v_max3_f32 v208, v208, v200, v204\n"
"v_mfma_f32_16x16x32_bf16 a[80:83], a[164:167], a[192:195], a[80:83]\n"
"ds_read_b128 a[12:15], v248 offset:6144\n"
"v_min3_f32 v209, v209, v200, v204\n"
"v_mfma_f32_16x16x32_bf16 a[84:87], a[164:167], a[196:199], a[84:87]\n"
"v_mul_f32 v208, s83, v208\n"
"v_mul_f32 v209, s83, v209\n"
"v_mfma_f32_16x16x32_bf16 a[88:91], a[164:167], a[200:203], a[88:91]\n"
"ds_read_b128 a[16:19], v248 offset:8192\n"
"v_fma_f32 v212, v176, s83, -v208\n"
"v_mfma_f32_16x16x32_bf16 a[92:95], a[164:167], a[204:207], a[92:95]\n"
"buffer_load_dwordx4 v241, s[36:39], s52 offen offset:3072 lds\n"
"s_add_u32 m0, s57, 81920\n"
"v_mfma_f32_16x16x32_bf16 v[96:99], a[176:179], a[168:171], v[96:99]\n"
"s_mov_b32 s52, 2883584\n"
"ds_read_b128 a[20:23], v248 offset:10240\n"
"v_fma_f32 v176, -v176, s83, v209\n"
"v_mfma_f32_16x16x32_bf16 a[96:99], a[180:183], a[168:171], a[96:99]\n"
"v_fma_f32 v213, v177, s83, -v208\n"
"v_fma_f32 v177, -v177, s83, v209\n"
"v_mfma_f32_16x16x32_bf16 v[100:103], a[184:187], a[168:171], v[100:103]\n"
"v_exp_f32 v212, v212\n"
"v_mfma_f32_16x16x32_bf16 a[100:103], a[188:191], a[168:171], a[100:103]\n"
"v_exp_f32 v176, v176\n"
"v_mfma_f32_16x16x32_bf16 v[112:115], a[168:171], a[192:195], v[112:115]\n"
"ds_read_b128 a[24:27], v248 offset:12288\n"
"ds_read_b128 a[28:31], v248 offset:14336\n"
"v_mfma_f32_16x16x32_bf16 v[116:119], a[168:171], a[196:199], v[116:119]\n"
"buffer_load_dwordx4 v246, s[40:43], s52 offen offset:0 lds\n"
"s_mov_b32 s52, 2915328\n"
"v_mfma_f32_16x16x32_bf16 v[120:123], a[168:171], a[200:203], v[120:123]\n"
"v_fma_f32 v214, v178, s83, -v208\n"
"v_fma_f32 v178, -v178, s83, v209\n"
"v_mfma_f32_16x16x32_bf16 v[124:127], a[168:171], a[204:207], v[124:127]\n"
"v_exp_f32 v213, v213\n"
"v_mfma_f32_16x16x32_bf16 v[104:107], a[176:179], a[172:175], v[104:107]\n"
"buffer_load_dwordx4 v246, s[40:43], s52 offen offset:1024 lds\n"
"s_mov_b32 s52, 2947072\n"
"v_mfma_f32_16x16x32_bf16 a[104:107], a[180:183], a[172:175], a[104:107]\n"
"v_exp_f32 v177, v177\n"
"v_mfma_f32_16x16x32_bf16 v[108:111], a[184:187], a[172:175], v[108:111]\n"
"v_fma_f32 v215, v179, s83, -v208\n"
"v_fma_f32 v179, -v179, s83, v209\n"
"v_mfma_f32_16x16x32_bf16 a[108:111], a[188:191], a[172:175], a[108:111]\n"
"v_exp_f32 v214, v214\n"
"v_mfma_f32_16x16x32_bf16 a[112:115], a[172:175], a[192:195], a[112:115]\n"
"buffer_load_dwordx4 v247, s[40:43], s52 offen offset:2048 lds\n"
"s_mov_b32 s52, 2978816\n"
"v_mfma_f32_16x16x32_bf16 a[116:119], a[172:175], a[196:199], a[116:119]\n"
"v_exp_f32 v178, v178\n"
"v_mfma_f32_16x16x32_bf16 a[120:123], a[172:175], a[200:203], a[120:123]\n"
"v_add_f32 v210, v212, v213\n"
"v_add_f32 v211, v176, v177\n"
"v_mfma_f32_16x16x32_bf16 a[124:127], a[172:175], a[204:207], a[124:127]\n"
"s_waitcnt lgkmcnt(0)\n"
"v_fma_f32 v216, v180, s83, -v208\n"
"v_fma_f32 v180, -v180, s83, v209\n"
"v_mfma_f32_16x16x32_bf16 v[0:3], a[32:35], a[0:3], v[0:3]\n"
"s_waitcnt vmcnt(29)\n"
"buffer_load_dwordx4 v247, s[40:43], s52 offen offset:3072 lds\n"
"v_mfma_f32_16x16x32_bf16 v[128:131], a[36:39], a[0:3], v[128:131]\n"
"s_barrier\n"
"s_add_u32 m0, s57, 98304\n"
"s_mov_b32 s52, 3145728\n"
"v_mfma_f32_16x16x32_bf16 v[4:7], a[40:43], a[0:3], v[4:7]\n"
"v_exp_f32 v215, v215\n"
"v_mfma_f32_16x16x32_bf16 v[132:135], a[44:47], a[0:3], v[132:135]\n"
"v_exp_f32 v179, v179\n"
"v_mfma_f32_16x16x32_bf16 v[16:19], a[0:3], a[128:131], v[16:19]\n"
"ds_read_b128 a[144:147], v249 offset:0\n"
"v_add_f32 v210, v210, v214\n"
"v_mfma_f32_16x16x32_bf16 v[20:23], a[0:3], a[132:135], v[20:23]\n"
"buffer_load_dwordx4 v246, s[40:43], s52 offen offset:0 lds\n"
"s_mov_b32 s52, 3177472\n"
"v_mfma_f32_16x16x32_bf16 v[24:27], a[0:3], a[136:139], v[24:27]\n"
"ds_read_b64_tr_b16 a[176:177], v250 offset:49152\n"
"v_add_f32 v211, v211, v178\n"
"v_mfma_f32_16x16x32_bf16 v[28:31], a[0:3], a[140:143], v[28:31]\n"
"ds_read_b64_tr_b16 a[178:179], v250 offset:50176\n"
"v_fma_f32 v217, v181, s83, -v208\n"
"v_mfma_f32_16x16x32_bf16 v[8:11], a[32:35], a[4:7], v[8:11]\n"
"v_fma_f32 v181, -v181, s83, v209\n"
"v_mfma_f32_16x16x32_bf16 v[136:139], a[36:39], a[4:7], v[136:139]\n"
"buffer_load_dwordx4 v246, s[40:43], s52 offen offset:1024 lds\n"
"s_mov_b32 s52, 3209216\n"
"v_mfma_f32_16x16x32_bf16 v[12:15], a[40:43], a[4:7], v[12:15]\n"
"ds_read_b64_tr_b16 a[180:181], v251 offset:49152\n"
"v_mfma_f32_16x16x32_bf16 v[140:143], a[44:47], a[4:7], v[140:143]\n"
"v_exp_f32 v216, v216\n"
"v_mfma_f32_16x16x32_bf16 v[144:147], a[4:7], a[128:131], v[144:147]\n"
"v_exp_f32 v180, v180\n"
"v_mfma_f32_16x16x32_bf16 v[148:151], a[4:7], a[132:135], v[148:151]\n"
"ds_read_b64_tr_b16 a[182:183], v251 offset:50176\n"
"ds_read_b64_tr_b16 a[184:185], v252 offset:49152\n"
"v_mfma_f32_16x16x32_bf16 v[152:155], a[4:7], a[136:139], v[152:155]\n"
"buffer_load_dwordx4 v247, s[40:43], s52 offen offset:2048 lds\n"
"s_mov_b32 s52, 3240960\n"
"v_mfma_f32_16x16x32_bf16 v[156:159], a[4:7], a[140:143], v[156:159]\n"
"ds_read_b64_tr_b16 a[186:187], v252 offset:50176\n"
"v_add_f32 v210, v210, v215\n"
"v_mfma_f32_16x16x32_bf16 v[32:35], a[32:35], a[8:11], v[32:35]\n"
"v_add_f32 v211, v211, v179\n"
"v_fma_f32 v218, v182, s83, -v208\n"
"v_mfma_f32_16x16x32_bf16 v[160:163], a[36:39], a[8:11], v[160:163]\n"
"buffer_load_dwordx4 v247, s[40:43], s52 offen offset:3072 lds\n"
"v_mfma_f32_16x16x32_bf16 v[36:39], a[40:43], a[8:11], v[36:39]\n"
"ds_read_b64_tr_b16 a[188:189], v253 offset:49152\n"
"v_fma_f32 v182, -v182, s83, v209\n"
"v_mfma_f32_16x16x32_bf16 v[164:167], a[44:47], a[8:11], v[164:167]\n"
"v_exp_f32 v217, v217\n"
"v_mfma_f32_16x16x32_bf16 v[48:51], a[8:11], a[128:131], v[48:51]\n"
"v_exp_f32 v181, v181\n"
"v_mfma_f32_16x16x32_bf16 v[52:55], a[8:11], a[132:135], v[52:55]\n"
"ds_read_b64_tr_b16 a[190:191], v253 offset:50176\n"
"v_add_f32 v210, v210, v216\n"
"v_mfma_f32_16x16x32_bf16 v[56:59], a[8:11], a[136:139], v[56:59]\n"
"buffer_store_dwordx4 a[236:239], v254, s[84:87], s67 offen offset:1024 sc1\n"
"v_mfma_f32_16x16x32_bf16 v[60:63], a[8:11], a[140:143], v[60:63]\n"
"ds_read_b64_tr_b16 a[192:193], v250 offset:57344\n"
"v_add_f32 v211, v211, v180\n"
"v_mfma_f32_16x16x32_bf16 v[40:43], a[32:35], a[12:15], v[40:43]\n"
"ds_read_b64_tr_b16 a[194:195], v250 offset:58368\n"
"v_fma_f32 v219, v183, s83, -v208\n"
"v_mfma_f32_16x16x32_bf16 v[168:171], a[36:39], a[12:15], v[168:171]\n"
"v_fma_f32 v183, -v183, s83, v209\n"
"v_mfma_f32_16x16x32_bf16 v[44:47], a[40:43], a[12:15], v[44:47]\n"
"v_exp_f32 v218, v218\n"
"v_mfma_f32_16x16x32_bf16 v[172:175], a[44:47], a[12:15], v[172:175]\n"
"v_exp_f32 v182, v182\n"
"v_mfma_f32_16x16x32_bf16 a[48:51], a[12:15], a[128:131], a[48:51]\n"
"ds_read_b64_tr_b16 a[196:197], v251 offset:57344\n"
"ds_read_b64_tr_b16 a[198:199], v251 offset:58368\n"
"v_mfma_f32_16x16x32_bf16 a[52:55], a[12:15], a[132:135], a[52:55]\n"
"v_add_f32 v210, v210, v217\n"
"v_add_f32 v211, v211, v181\n"
"v_mfma_f32_16x16x32_bf16 a[56:59], a[12:15], a[136:139], a[56:59]\n"
"v_fma_f32 v220, v184, s83, -v208\n"
"v_fma_f32 v184, -v184, s83, v209\n"
"v_mfma_f32_16x16x32_bf16 a[60:63], a[12:15], a[140:143], a[60:63]\n"
"v_exp_f32 v219, v219\n"
"v_mfma_f32_16x16x32_bf16 v[64:67], a[32:35], a[16:19], v[64:67]\n"
"ds_read_b64_tr_b16 a[200:201], v252 offset:57344\n"
"v_mfma_f32_16x16x32_bf16 a[64:67], a[36:39], a[16:19], a[64:67]\n"
"v_exp_f32 v183, v183\n"
"v_mfma_f32_16x16x32_bf16 v[68:71], a[40:43], a[16:19], v[68:71]\n"
"ds_read_b64_tr_b16 a[202:203], v252 offset:58368\n"
"v_add_f32 v210, v210, v218\n"
"v_mfma_f32_16x16x32_bf16 a[68:71], a[44:47], a[16:19], a[68:71]\n"
"ds_read_b64_tr_b16 a[204:205], v253 offset:57344\n"
"v_add_f32 v211, v211, v182\n"
"v_mfma_f32_16x16x32_bf16 v[80:83], a[16:19], a[128:131], v[80:83]\n"
"v_fma_f32 v221, v185, s83, -v208\n"
"v_fma_f32 v185, -v185, s83, v209\n"
"v_mfma_f32_16x16x32_bf16 v[84:87], a[16:19], a[132:135], v[84:87]\n"
"v_exp_f32 v220, v220\n"
"v_mfma_f32_16x16x32_bf16 v[88:91], a[16:19], a[136:139], v[88:91]\n"
"buffer_store_dwordx4 a[240:243], v254, s[84:87], s67 offen offset:2048 sc1\n"
"v_mfma_f32_16x16x32_bf16 v[92:95], a[16:19], a[140:143], v[92:95]\n"
"ds_read_b64_tr_b16 a[206:207], v253 offset:58368\n"
"ds_read_b128 a[148:151], v249 offset:2048\n"
"v_mfma_f32_16x16x32_bf16 v[72:75], a[32:35], a[20:23], v[72:75]\n"
"v_exp_f32 v184, v184\n"
"v_mfma_f32_16x16x32_bf16 a[72:75], a[36:39], a[20:23], a[72:75]\n"
"ds_read_b128 a[152:155], v249 offset:4096\n"
"v_add_f32 v210, v210, v219\n"
"v_mfma_f32_16x16x32_bf16 v[76:79], a[40:43], a[20:23], v[76:79]\n"
"v_add_f32 v211, v211, v183\n"
"v_fma_f32 v222, v186, s83, -v208\n"
"v_mfma_f32_16x16x32_bf16 a[76:79], a[44:47], a[20:23], a[76:79]\n"
"v_fma_f32 v186, -v186, s83, v209\n"
"v_mfma_f32_16x16x32_bf16 a[80:83], a[20:23], a[128:131], a[80:83]\n"
"v_exp_f32 v221, v221\n"
"v_mfma_f32_16x16x32_bf16 a[84:87], a[20:23], a[132:135], a[84:87]\n"
"ds_read_b128 a[156:159], v249 offset:6144\n"
"v_mfma_f32_16x16x32_bf16 a[88:91], a[20:23], a[136:139], a[88:91]\n"
"v_exp_f32 v185, v185\n"
"v_mfma_f32_16x16x32_bf16 a[92:95], a[20:23], a[140:143], a[92:95]\n"
"ds_read_b128 a[160:163], v249 offset:8192\n"
"v_add_f32 v210, v210, v220\n"
"v_mfma_f32_16x16x32_bf16 v[96:99], a[32:35], a[24:27], v[96:99]\n"
"ds_read_b128 a[164:167], v249 offset:10240\n"
"v_add_f32 v211, v211, v184\n"
"v_mfma_f32_16x16x32_bf16 a[96:99], a[36:39], a[24:27], a[96:99]\n"
"v_fma_f32 v223, v187, s83, -v208\n"
"v_fma_f32 v187, -v187, s83, v209\n"
"v_mfma_f32_16x16x32_bf16 v[100:103], a[40:43], a[24:27], v[100:103]\n"
"v_exp_f32 v222, v222\n"
"v_mfma_f32_16x16x32_bf16 a[100:103], a[44:47], a[24:27], a[100:103]\n"
"v_exp_f32 v186, v186\n"
"v_mfma_f32_16x16x32_bf16 v[112:115], a[24:27], a[128:131], v[112:115]\n"
"ds_read_b128 a[168:171], v249 offset:12288\n"
"ds_read_b128 a[172:175], v249 offset:14336\n"
"v_mfma_f32_16x16x32_bf16 v[116:119], a[24:27], a[132:135], v[116:119]\n"
"v_add_f32 v210, v210, v221\n"
"v_add_f32 v211, v211, v185\n"
"v_mfma_f32_16x16x32_bf16 v[120:123], a[24:27], a[136:139], v[120:123]\n"
"buffer_store_dwordx4 a[244:247], v254, s[84:87], s67 offen offset:3072 sc1\n"
"s_add_u32 s67, s67, 4096\n"
"v_mfma_f32_16x16x32_bf16 v[124:127], a[24:27], a[140:143], v[124:127]\n"
"v_fma_f32 v224, v188, s83, -v208\n"
"v_fma_f32 v188, -v188, s83, v209\n"
"v_mfma_f32_16x16x32_bf16 v[104:107], a[32:35], a[28:31], v[104:107]\n"
"v_exp_f32 v223, v223\n"
"v_mfma_f32_16x16x32_bf16 a[104:107], a[36:39], a[28:31], a[104:107]\n"
"v_exp_f32 v187, v187\n"
"v_mfma_f32_16x16x32_bf16 v[108:111], a[40:43], a[28:31], v[108:111]\n"
"v_add_f32 v210, v210, v222\n"
"v_add_f32 v211, v211, v186\n"
"v_mfma_f32_16x16x32_bf16 a[108:111], a[44:47], a[28:31], a[108:111]\n"
"v_fma_f32 v225, v189, s83, -v208\n"
"v_fma_f32 v189, -v189, s83, v209\n"
"v_mfma_f32_16x16x32_bf16 a[112:115], a[28:31], a[128:131], a[112:115]\n"
"v_exp_f32 v224, v224\n"
"v_mfma_f32_16x16x32_bf16 a[116:119], a[28:31], a[132:135], a[116:119]\n"
"v_exp_f32 v188, v188\n"
"v_mfma_f32_16x16x32_bf16 a[120:123], a[28:31], a[136:139], a[120:123]\n"
"v_add_f32 v210, v210, v223\n"
"v_add_f32 v211, v211, v187\n"
"v_mfma_f32_16x16x32_bf16 a[124:127], a[28:31], a[140:143], a[124:127]\n"
"s_waitcnt lgkmcnt(0)\n"
"v_fma_f32 v226, v190, s83, -v208\n"
"v_fma_f32 v190, -v190, s83, v209\n"
"v_mfma_f32_16x16x32_bf16 v[0:3], a[176:179], a[144:147], v[0:3]\n"
"s_waitcnt vmcnt(11)\n"
"v_exp_f32 v225, v225\n"
"v_mfma_f32_16x16x32_bf16 v[128:131], a[180:183], a[144:147], v[128:131]\n"
"s_barrier\n"
"v_exp_f32 v189, v189\n"
"v_mfma_f32_16x16x32_bf16 v[4:7], a[184:187], a[144:147], v[4:7]\n"
"ds_read_b128 a[0:3], v248 offset:32768\n"
"v_add_f32 v210, v210, v224\n"
"v_mfma_f32_16x16x32_bf16 v[132:135], a[188:191], a[144:147], v[132:135]\n"
"v_add_f32 v211, v211, v188\n"
"v_fma_f32 v227, v191, s83, -v208\n"
"v_mfma_f32_16x16x32_bf16 v[16:19], a[144:147], a[192:195], v[16:19]\n"
"v_fma_f32 v191, -v191, s83, v209\n"
"v_mfma_f32_16x16x32_bf16 v[20:23], a[144:147], a[196:199], v[20:23]\n"
"v_exp_f32 v226, v226\n"
"v_mfma_f32_16x16x32_bf16 v[24:27], a[144:147], a[200:203], v[24:27]\n"
"ds_read_b64_tr_b16 a[32:33], v250 offset:0\n"
"v_mfma_f32_16x16x32_bf16 v[28:31], a[144:147], a[204:207], v[28:31]\n"
"buffer_store_dwordx4 a[248:251], v254, s[84:87], s67 offen sc1\n"
"v_mfma_f32_16x16x32_bf16 v[8:11], a[176:179], a[148:151], v[8:11]\n"
"ds_read_b64_tr_b16 a[34:35], v250 offset:1024\n"
"v_mfma_f32_16x16x32_bf16 v[136:139], a[180:183], a[148:151], v[136:139]\n"
"v_exp_f32 v190, v190\n"
"v_mfma_f32_16x16x32_bf16 v[12:15], a[184:187], a[148:151], v[12:15]\n"
"s_add_u32 m0, s89, 0\n"
"ds_read_b64_tr_b16 a[36:37], v251 offset:0\n"
"s_mov_b32 s52, 768\n"
"v_mfma_f32_16x16x32_bf16 v[140:143], a[188:191], a[148:151], v[140:143]\n"
"ds_read_b64_tr_b16 a[38:39], v251 offset:1024\n"
"v_add_f32 v210, v210, v225\n"
"v_mfma_f32_16x16x32_bf16 v[144:147], a[148:151], a[192:195], v[144:147]\n"
"v_add_f32 v211, v211, v189\n"
"v_fma_f32 v228, v192, s83, -v208\n"
"v_mfma_f32_16x16x32_bf16 v[148:151], a[148:151], a[196:199], v[148:151]\n"
"ds_read_b64_tr_b16 a[40:41], v252 offset:0\n"
"v_fma_f32 v192, -v192, s83, v209\n"
"v_mfma_f32_16x16x32_bf16 v[152:155], a[148:151], a[200:203], v[152:155]\n"
"v_exp_f32 v227, v227\n"
"v_mfma_f32_16x16x32_bf16 v[156:159], a[148:151], a[204:207], v[156:159]\n"
"v_exp_f32 v191, v191\n"
"v_mfma_f32_16x16x32_bf16 v[32:35], a[176:179], a[152:155], v[32:35]\n"
"ds_read_b64_tr_b16 a[42:43], v252 offset:1024\n"
"v_add_f32 v210, v210, v226\n"
"v_mfma_f32_16x16x32_bf16 v[160:163], a[180:183], a[152:155], v[160:163]\n"
"v_add_f32 v211, v211, v190\n"
"v_fma_f32 v229, v196, s83, -v208\n"
"v_mfma_f32_16x16x32_bf16 v[36:39], a[184:187], a[152:155], v[36:39]\n"
"ds_read_b64_tr_b16 a[44:45], v253 offset:0\n"
"v_fma_f32 v196, -v196, s83, v209\n"
"v_mfma_f32_16x16x32_bf16 v[164:167], a[188:191], a[152:155], v[164:167]\n"
"v_exp_f32 v228, v228\n"
"v_mfma_f32_16x16x32_bf16 v[48:51], a[152:155], a[192:195], v[48:51]\n"
"v_exp_f32 v192, v192\n"
"v_mfma_f32_16x16x32_bf16 v[52:55], a[152:155], a[196:199], v[52:55]\n"
"ds_read_b64_tr_b16 a[46:47], v253 offset:1024\n"
"v_add_f32 v210, v210, v227\n"
"v_mfma_f32_16x16x32_bf16 v[56:59], a[152:155], a[200:203], v[56:59]\n"
"buffer_load_dwordx4 v240, s[36:39], s52 offen offset:0 lds\n"
"s_mov_b32 s52, 7936\n"
"v_mfma_f32_16x16x32_bf16 v[60:63], a[152:155], a[204:207], v[60:63]\n"
"ds_read_b64_tr_b16 a[128:129], v250 offset:8192\n"
"v_add_f32 v211, v211, v191\n"
"v_mfma_f32_16x16x32_bf16 v[40:43], a[176:179], a[156:159], v[40:43]\n"
"ds_read_b64_tr_b16 a[130:131], v250 offset:9216\n"
"v_fma_f32 v230, v200, s83, -v208\n"
"v_mfma_f32_16x16x32_bf16 v[168:171], a[180:183], a[156:159], v[168:171]\n"
"v_fma_f32 v200, -v200, s83, v209\n"
"v_mfma_f32_16x16x32_bf16 v[44:47], a[184:187], a[156:159], v[44:47]\n"
"buffer_load_dwordx4 v241, s[36:39], s52 offen offset:1024 lds\n"
"s_mov_b32 s52, 15104\n"
"v_mfma_f32_16x16x32_bf16 v[172:175], a[188:191], a[156:159], v[172:175]\n"
"v_exp_f32 v229, v229\n"
"v_mfma_f32_16x16x32_bf16 a[48:51], a[156:159], a[192:195], a[48:51]\n"
"ds_read_b64_tr_b16 a[132:133], v251 offset:8192\n"
"ds_read_b64_tr_b16 a[134:135], v251 offset:9216\n"
"v_mfma_f32_16x16x32_bf16 a[52:55], a[156:159], a[196:199], a[52:55]\n"
"v_exp_f32 v196, v196\n"
"v_mfma_f32_16x16x32_bf16 a[56:59], a[156:159], a[200:203], a[56:59]\n"
"buffer_load_dwordx4 v240, s[36:39], s52 offen offset:2048 lds\n"
"s_mov_b32 s52, 22272\n"
"v_mfma_f32_16x16x32_bf16 a[60:63], a[156:159], a[204:207], a[60:63]\n"
"ds_read_b64_tr_b16 a[136:137], v252 offset:8192\n"
"v_add_f32 v210, v210, v228\n"
"v_mfma_f32_16x16x32_bf16 v[64:67], a[176:179], a[160:163], v[64:67]\n"
"v_add_f32 v211, v211, v192\n"
"v_fma_f32 v231, v204, s83, -v208\n"
"v_mfma_f32_16x16x32_bf16 a[64:67], a[180:183], a[160:163], a[64:67]\n"
"ds_read_b64_tr_b16 a[138:139], v252 offset:9216\n"
"v_fma_f32 v204, -v204, s83, v209\n"
"v_mfma_f32_16x16x32_bf16 v[68:71], a[184:187], a[160:163], v[68:71]\n"
"buffer_load_dwordx4 v241, s[36:39], s52 offen offset:3072 lds\n"
"s_add_u32 m0, s89, 4096\n"
"v_mfma_f32_16x16x32_bf16 a[68:71], a[188:191], a[160:163], a[68:71]\n"
"s_mov_b32 s52, 262912\n"
"v_exp_f32 v230, v230\n"
"v_mfma_f32_16x16x32_bf16 v[80:83], a[160:163], a[192:195], v[80:83]\n"
"ds_read_b64_tr_b16 a[140:141], v253 offset:8192\n"
"v_mfma_f32_16x16x32_bf16 v[84:87], a[160:163], a[196:199], v[84:87]\n"
"v_exp_f32 v200, v200\n"
"v_mfma_f32_16x16x32_bf16 v[88:91], a[160:163], a[200:203], v[88:91]\n"
"ds_read_b64_tr_b16 a[142:143], v253 offset:9216\n"
"v_add_f32 v210, v210, v229\n"
"v_mfma_f32_16x16x32_bf16 v[92:95], a[160:163], a[204:207], v[92:95]\n"
"buffer_load_dwordx4 v240, s[36:39], s52 offen offset:0 lds\n"
"s_mov_b32 s52, 270080\n"
"v_mfma_f32_16x16x32_bf16 v[72:75], a[176:179], a[164:167], v[72:75]\n"
".Lpad5_%=:\n"
"s_cbranch_execz .Lpad9_%=\n"
"ds_read_b128 a[4:7], v248 offset:34816\n"
"v_add_f32 v211, v211, v196\n"
"v_mfma_f32_16x16x32_bf16 a[72:75], a[180:183], a[164:167], a[72:75]\n"
"v_exp_f32 v231, v231\n"
"v_mfma_f32_16x16x32_bf16 v[76:79], a[184:187], a[164:167], v[76:79]\n"
"v_exp_f32 v204, v204\n"
"v_mfma_f32_16x16x32_bf16 a[76:79], a[188:191], a[164:167], a[76:79]\n"
"ds_read_b128 a[8:11], v248 offset:36864\n"
"v_add_f32 v210, v210, v230\n"
"v_mfma_f32_16x16x32_bf16 a[80:83], a[164:167], a[192:195], a[80:83]\n"
"buffer_load_dwordx4 v241, s[36:39], s52 offen offset:1024 lds\n"
"s_mov_b32 s52, 277248\n"
"v_mfma_f32_16x16x32_bf16 a[84:87], a[164:167], a[196:199], a[84:87]\n"
"ds_read_b128 a[12:15], v248 offset:38912\n"
"v_add_f32 v211, v211, v200\n"
"v_mfma_f32_16x16x32_bf16 a[88:91], a[164:167], a[200:203], a[88:91]\n"
"ds_read_b128 a[16:19], v248 offset:40960\n"
"v_add_f32 v210, v210, v231\n"
"v_mfma_f32_16x16x32_bf16 a[92:95], a[164:167], a[204:207], a[92:95]\n"
"buffer_load_dwordx4 v240, s[36:39], s52 offen offset:2048 lds\n"
"s_mov_b32 s52, 284416\n"
"v_mfma_f32_16x16x32_bf16 v[96:99], a[176:179], a[168:171], v[96:99]\n"
"ds_read_b128 a[20:23], v248 offset:43008\n"
"v_add_f32 v211, v211, v204\n"
"v_mfma_f32_16x16x32_bf16 a[96:99], a[180:183], a[168:171], a[96:99]\n"
"v_rcp_f32 v210, v210\n"
"v_mfma_f32_16x16x32_bf16 v[100:103], a[184:187], a[168:171], v[100:103]\n"
"v_rcp_f32 v211, v211\n"
"v_mfma_f32_16x16x32_bf16 a[100:103], a[188:191], a[168:171], a[100:103]\n"
"ds_read_b128 a[24:27], v248 offset:45056\n"
"v_mul_f32 v212, v212, v210\n"
"v_mfma_f32_16x16x32_bf16 v[112:115], a[168:171], a[192:195], v[112:115]\n"
"buffer_load_dwordx4 v241, s[36:39], s52 offen offset:3072 lds\n"
"v_mfma_f32_16x16x32_bf16 v[116:119], a[168:171], a[196:199], v[116:119]\n"
"ds_read_b128 a[28:31], v248 offset:47104\n"
"v_mul_f32 v213, v213, v210\n"
"v_mfma_f32_16x16x32_bf16 v[120:123], a[168:171], a[200:203], v[120:123]\n"
"v_cvt_pk_bf16_f32 v212, v212, v213\n"
"v_mul_f32 v214, v214, v210\n"
"v_mfma_f32_16x16x32_bf16 v[124:127], a[168:171], a[204:207], v[124:127]\n"
"v_mul_f32 v215, v215, v210\n"
"v_cvt_pk_bf16_f32 v213, v214, v215\n"
"v_mfma_f32_16x16x32_bf16 v[104:107], a[176:179], a[172:175], v[104:107]\n"
"v_mul_f32 v216, v216, v210\n"
"v_mul_f32 v217, v217, v210\n"
"v_mfma_f32_16x16x32_bf16 a[104:107], a[180:183], a[172:175], a[104:107]\n"
"buffer_load_dwordx4 a[232:235], v237, s[84:87], s58 offen offset:2560 sc1\n"
"v_mfma_f32_16x16x32_bf16 v[108:111], a[184:187], a[172:175], v[108:111]\n"
"s_add_u32 m0, s57, 114688\n"
"s_mov_b32 s52, 3407872\n"
"v_cvt_pk_bf16_f32 v214, v216, v217\n"
"v_mfma_f32_16x16x32_bf16 a[108:111], a[188:191], a[172:175], a[108:111]\n"
"v_mul_f32 v218, v218, v210\n"
"v_mul_f32 v219, v219, v210\n"
"v_mfma_f32_16x16x32_bf16 a[112:115], a[172:175], a[192:195], a[112:115]\n"
"v_cvt_pk_bf16_f32 v215, v218, v219\n"
"v_mul_f32 v220, v220, v210\n"
"v_mfma_f32_16x16x32_bf16 a[116:119], a[172:175], a[196:199], a[116:119]\n"
"buffer_load_dwordx4 a[236:239], v237, s[84:87], s58 offen offset:2816 sc1\n"
"v_mfma_f32_16x16x32_bf16 a[120:123], a[172:175], a[200:203], a[120:123]\n"
"v_mul_f32 v221, v221, v210\n"
"v_cvt_pk_bf16_f32 v216, v220, v221\n"
"v_mfma_f32_16x16x32_bf16 a[124:127], a[172:175], a[204:207], a[124:127]\n"
"s_waitcnt lgkmcnt(0)\n"
"v_mul_f32 v222, v222, v210\n"
"v_mul_f32 v223, v223, v210\n"
"v_mfma_f32_16x16x32_bf16 v[0:3], a[32:35], a[0:3], v[0:3]\n"
"s_waitcnt vmcnt(18)\n"
"s_lshl_b32 s68, s77, 3\n"
"s_add_u32 s68, s68, s78\n"
"v_mfma_f32_16x16x32_bf16 v[128:131], a[36:39], a[0:3], v[128:131]\n"
"s_barrier\n"
"buffer_load_dwordx4 a[240:243], v237, s[84:87], s58 offen offset:3072 sc1\n"
"v_mfma_f32_16x16x32_bf16 v[4:7], a[40:43], a[0:3], v[4:7]\n"
"s_mul_i32 s68, s68, 3\n"
"ds_read_b128 a[144:147], v249 offset:32768\n"
"s_add_u32 s68, s68, s76\n"
"v_mfma_f32_16x16x32_bf16 v[132:135], a[44:47], a[0:3], v[132:135]\n"
"s_lshl_b32 s68, s68, 11\n"
"s_add_u32 s68, s68, 71303168\n"
"v_cvt_pk_bf16_f32 v217, v222, v223\n"
"v_mfma_f32_16x16x32_bf16 v[16:19], a[0:3], a[128:131], v[16:19]\n"
"s_cmp_lt_u32 s76, 3\n"
"s_cselect_b32 s68, s68, 1073741824\n"
"v_mul_f32 v224, v224, v210\n"
"v_mfma_f32_16x16x32_bf16 v[20:23], a[0:3], a[132:135], v[20:23]\n"
"ds_read_b64_tr_b16 a[176:177], v250 offset:16384\n"
"v_mul_f32 v225, v225, v210\n"
"v_mfma_f32_16x16x32_bf16 v[24:27], a[0:3], a[136:139], v[24:27]\n"
"buffer_load_dwordx4 v246, s[40:43], s52 offen offset:0 lds\n"
"s_mov_b32 s52, 3439616\n"
"v_mfma_f32_16x16x32_bf16 v[28:31], a[0:3], a[140:143], v[28:31]\n"
"ds_read_b64_tr_b16 a[178:179], v250 offset:17408\n"
"v_cvt_pk_bf16_f32 v218, v224, v225\n"
"v_mfma_f32_16x16x32_bf16 v[8:11], a[32:35], a[4:7], v[8:11]\n"
"v_mul_f32 v226, v226, v210\n"
"v_mul_f32 v227, v227, v210\n"
"v_mfma_f32_16x16x32_bf16 v[136:139], a[36:39], a[4:7], v[136:139]\n"
"ds_read_b64_tr_b16 a[180:181], v251 offset:16384\n"
"v_cvt_pk_bf16_f32 v219, v226, v227\n"
"v_mfma_f32_16x16x32_bf16 v[12:15], a[40:43], a[4:7], v[12:15]\n"
"buffer_load_dwordx4 v246, s[40:43], s52 offen offset:1024 lds\n"
"s_mov_b32 s52, 3471360\n"
"v_mfma_f32_16x16x32_bf16 v[140:143], a[44:47], a[4:7], v[140:143]\n"
"ds_read_b64_tr_b16 a[182:183], v251 offset:17408\n"
"v_mul_f32 v228, v228, v210\n"
"v_mfma_f32_16x16x32_bf16 v[144:147], a[4:7], a[128:131], v[144:147]\n"
"v_mul_f32 v229, v229, v210\n"
"v_cvt_pk_bf16_f32 v220, v228, v229\n"
"v_mfma_f32_16x16x32_bf16 v[148:151], a[4:7], a[132:135], v[148:151]\n"
"ds_read_b64_tr_b16 a[184:185], v252 offset:16384\n"
"v_mul_f32 v230, v230, v210\n"
"v_mfma_f32_16x16x32_bf16 v[152:155], a[4:7], a[136:139], v[152:155]\n"
"buffer_load_dwordx4 v247, s[40:43], s52 offen offset:2048 lds\n"
"s_mov_b32 s52, 3503104\n"
"v_mfma_f32_16x16x32_bf16 v[156:159], a[4:7], a[140:143], v[156:159]\n"
"ds_read_b64_tr_b16 a[186:187], v252 offset:17408\n"
"v_mul_f32 v231, v231, v210\n"
"v_mfma_f32_16x16x32_bf16 v[32:35], a[32:35], a[8:11], v[32:35]\n"
"v_cvt_pk_bf16_f32 v221, v230, v231\n"
"v_mul_f32 v176, v176, v211\n"
"v_mfma_f32_16x16x32_bf16 v[160:163], a[36:39], a[8:11], v[160:163]\n"
"v_mul_f32 v177, v177, v211\n"
"v_cvt_pk_bf16_f32 v222, v176, v177\n"
"v_mfma_f32_16x16x32_bf16 v[36:39], a[40:43], a[8:11], v[36:39]\n"
"buffer_load_dwordx4 v247, s[40:43], s52 offen offset:3072 lds\n"
"s_mov_b32 s35, s33\n"
"v_mfma_f32_16x16x32_bf16 v[164:167], a[44:47], a[8:11], v[164:167]\n"
"s_lshl_b32 s35, s35, 7\n"
"ds_read_b64_tr_b16 a[188:189], v253 offset:16384\n"
"s_add_u32 s34, s35, s32\n"
"v_mfma_f32_16x16x32_bf16 v[48:51], a[8:11], a[128:131], v[48:51]\n"
"s_mul_i32 s34, s34, 2560\n"
"ds_read_b64_tr_b16 a[190:191], v253 offset:17408\n"
"s_mov_b32 s30, 20\n"
"v_mfma_f32_16x16x32_bf16 v[52:55], a[8:11], a[132:135], v[52:55]\n"
"s_mov_b64 s[28:29], s[12:13]\n"
"s_or_b32 s29, s29, 8388608\n"
"v_mul_f32 v178, v178, v211\n"
"v_mfma_f32_16x16x32_bf16 v[56:59], a[8:11], a[136:139], v[56:59]\n"
"ds_read_b64_tr_b16 a[192:193], v250 offset:24576\n"
"v_mul_f32 v179, v179, v211\n"
"v_mfma_f32_16x16x32_bf16 v[60:63], a[8:11], a[140:143], v[60:63]\n"
"v_cvt_pk_bf16_f32 v223, v178, v179\n"
"v_mul_f32 v180, v180, v211\n"
"v_mfma_f32_16x16x32_bf16 v[40:43], a[32:35], a[12:15], v[40:43]\n"
"ds_read_b64_tr_b16 a[194:195], v250 offset:25600\n"
"v_mul_f32 v181, v181, v211\n"
"v_mfma_f32_16x16x32_bf16 v[168:171], a[36:39], a[12:15], v[168:171]\n"
"v_cvt_pk_bf16_f32 v224, v180, v181\n"
"v_mul_f32 v182, v182, v211\n"
"v_mfma_f32_16x16x32_bf16 v[44:47], a[40:43], a[12:15], v[44:47]\n"
"buffer_load_dword a255, v255, s[28:31], s34 idxen\n"
"s_lshr_b32 s34, s32, 3\n"
"v_mfma_f32_16x16x32_bf16 v[172:175], a[44:47], a[12:15], v[172:175]\n"
"s_mul_i32 s34, s34, 96\n"
"ds_read_b64_tr_b16 a[196:197], v251 offset:24576\n"
"s_add_u32 s34, s34, s33\n"
"v_mfma_f32_16x16x32_bf16 a[48:51], a[12:15], a[128:131], a[48:51]\n"
"s_lshl_b32 s34, s34, 3\n"
"ds_read_b64_tr_b16 a[198:199], v251 offset:25600\n"
"s_and_b32 s35, s32, 7\n"
"v_mfma_f32_16x16x32_bf16 a[52:55], a[12:15], a[132:135], a[52:55]\n"
"s_add_u32 s34, s34, s35\n"
"s_lshl_b32 s34, s34, 7\n"
"s_add_u32 s34, s34, 0\n"
"v_mfma_f32_16x16x32_bf16 a[56:59], a[12:15], a[136:139], a[56:59]\n"
"s_mov_b32 s30, 12\n"
"s_mov_b64 s[28:29], s[16:17]\n"
"s_or_b32 s29, s29, 536870912\n"
"v_mfma_f32_16x16x32_bf16 a[60:63], a[12:15], a[140:143], a[60:63]\n"
"ds_read_b64_tr_b16 a[200:201], v252 offset:24576\n"
"v_mul_f32 v183, v183, v211\n"
"v_mfma_f32_16x16x32_bf16 v[64:67], a[32:35], a[16:19], v[64:67]\n"
"buffer_load_dword a255, v255, s[28:31], s34 idxen\n"
"v_mfma_f32_16x16x32_bf16 a[64:67], a[36:39], a[16:19], a[64:67]\n"
"ds_read_b64_tr_b16 a[202:203], v252 offset:25600\n"
"v_cvt_pk_bf16_f32 v225, v182, v183\n"
"v_mfma_f32_16x16x32_bf16 v[68:71], a[40:43], a[16:19], v[68:71]\n"
"v_mul_f32 v184, v184, v211\n"
"v_mul_f32 v185, v185, v211\n"
"v_mfma_f32_16x16x32_bf16 a[68:71], a[44:47], a[16:19], a[68:71]\n"
"ds_read_b64_tr_b16 a[204:205], v253 offset:24576\n"
"v_cvt_pk_bf16_f32 v226, v184, v185\n"
"v_mfma_f32_16x16x32_bf16 v[80:83], a[16:19], a[128:131], v[80:83]\n"
"v_mul_f32 v186, v186, v211\n"
"v_mul_f32 v187, v187, v211\n"
"v_mfma_f32_16x16x32_bf16 v[84:87], a[16:19], a[132:135], v[84:87]\n"
"ds_read_b64_tr_b16 a[206:207], v253 offset:25600\n"
"v_cvt_pk_bf16_f32 v227, v186, v187\n"
"v_mfma_f32_16x16x32_bf16 v[88:91], a[16:19], a[136:139], v[88:91]\n"
"v_mul_f32 v188, v188, v211\n"
"v_mul_f32 v189, v189, v211\n"
"v_mfma_f32_16x16x32_bf16 v[92:95], a[16:19], a[140:143], v[92:95]\n"
"ds_read_b128 a[148:151], v249 offset:34816\n"
"v_cvt_pk_bf16_f32 v228, v188, v189\n"
"v_mfma_f32_16x16x32_bf16 v[72:75], a[32:35], a[20:23], v[72:75]\n"
"v_mul_f32 v190, v190, v211\n"
"v_mul_f32 v191, v191, v211\n"
"v_mfma_f32_16x16x32_bf16 a[72:75], a[36:39], a[20:23], a[72:75]\n"
"ds_read_b128 a[152:155], v249 offset:36864\n"
"v_cvt_pk_bf16_f32 v229, v190, v191\n"
"v_mfma_f32_16x16x32_bf16 v[76:79], a[40:43], a[20:23], v[76:79]\n"
"v_mul_f32 v192, v192, v211\n"
"v_mul_f32 v196, v196, v211\n"
"v_mfma_f32_16x16x32_bf16 a[76:79], a[44:47], a[20:23], a[76:79]\n"
"v_cvt_pk_bf16_f32 v230, v192, v196\n"
"v_mul_f32 v200, v200, v211\n"
"v_mfma_f32_16x16x32_bf16 a[80:83], a[20:23], a[128:131], a[80:83]\n"
"ds_read_b128 a[156:159], v249 offset:38912\n"
"v_mul_f32 v204, v204, v211\n"
"v_mfma_f32_16x16x32_bf16 a[84:87], a[20:23], a[132:135], a[84:87]\n"
"v_cvt_pk_bf16_f32 v231, v200, v204\n"
"v_mov_b32 v208, 0x3f80\n"
"s_waitcnt vmcnt(6)\n"
"v_mfma_f32_16x16x32_bf16 a[88:91], a[20:23], a[136:139], a[88:91]\n"
"s_bitcmp1_b32 s77, 0\n"
"s_cselect_b64 exec, -1, 0\n"
"v_accvgpr_write_b32 a232, v208\n"
"v_accvgpr_write_b32 a233, 0\n"
"s_mov_b64 exec, -1\n"
"v_mfma_f32_16x16x32_bf16 a[92:95], a[20:23], a[140:143], a[92:95]\n"
"ds_read_b128 a[160:163], v249 offset:40960\n"
"v_mfma_f32_16x16x32_bf16 v[96:99], a[32:35], a[24:27], v[96:99]\n"
"s_bitcmp0_b32 s77, 0\n"
"s_cselect_b64 exec, -1, 0\n"
"v_accvgpr_write_b32 a242, v208\n"
"v_accvgpr_write_b32 a243, 0\n"
"s_mov_b64 exec, -1\n"
"v_mfma_f32_16x16x32_bf16 a[96:99], a[36:39], a[24:27], a[96:99]\n"
"ds_read_b128 a[164:167], v249 offset:43008\n"
"v_mfma_f32_16x16x32_bf16 v[100:103], a[40:43], a[24:27], v[100:103]\n"
"ds_write_b128 v232, v[212:215]\n"
"v_mfma_f32_16x16x32_bf16 a[100:103], a[44:47], a[24:27], a[100:103]\n"
"ds_read_b128 a[168:171], v249 offset:45056\n"
"v_xor_b32 v233, 16, v232\n"
"v_mfma_f32_16x16x32_bf16 v[112:115], a[24:27], a[128:131], v[112:115]\n"
"ds_write_b128 v233, v[216:219]\n"
"v_mfma_f32_16x16x32_bf16 v[116:119], a[24:27], a[132:135], v[116:119]\n"
"ds_read_b128 a[172:175], v249 offset:47104\n"
"v_xor_b32 v233, 32, v232\n"
"v_mfma_f32_16x16x32_bf16 v[120:123], a[24:27], a[136:139], v[120:123]\n"
"s_add_u32 m0, s89, 32768\n"
"ds_write_b128 v233, v[220:223]\n"
"v_mfma_f32_16x16x32_bf16 v[124:127], a[24:27], a[140:143], v[124:127]\n"
"s_mov_b32 s52, 896\n"
"v_xor_b32 v233, 48, v232\n"
"v_mfma_f32_16x16x32_bf16 v[104:107], a[32:35], a[28:31], v[104:107]\n"
"ds_write_b128 v233, v[224:227]\n"
"v_mfma_f32_16x16x32_bf16 a[104:107], a[36:39], a[28:31], a[104:107]\n"
"v_xor_b32 v233, 64, v232\n"
"v_mfma_f32_16x16x32_bf16 v[108:111], a[40:43], a[28:31], v[108:111]\n"
"ds_write_b128 v233, v[228:231]\n"
"v_mfma_f32_16x16x32_bf16 a[108:111], a[44:47], a[28:31], a[108:111]\n"
"v_xor_b32 v233, 80, v232\n"
"v_mfma_f32_16x16x32_bf16 a[112:115], a[28:31], a[128:131], a[112:115]\n"
"ds_write_b128 v233, a[232:235]\n"
"v_xor_b32 v233, 96, v232\n"
"ds_write_b128 v233, a[236:239]\n"
"v_xor_b32 v233, 112, v232\n"
"ds_write_b128 v233, a[240:243]\n"
"v_mfma_f32_16x16x32_bf16 a[116:119], a[28:31], a[132:135], a[116:119]\n"
"v_mfma_f32_16x16x32_bf16 v[176:179], a[208:211], a[216:219], 0\n"
"v_mfma_f32_16x16x32_bf16 v[192:195], a[212:215], a[216:219], 0\n"
"v_mfma_f32_16x16x32_bf16 v[180:183], a[208:211], a[220:223], 0\n"
"v_mfma_f32_16x16x32_bf16 v[196:199], a[212:215], a[220:223], 0\n"
"v_mfma_f32_16x16x32_bf16 v[184:187], a[208:211], a[224:227], 0\n"
"v_mfma_f32_16x16x32_bf16 v[200:203], a[212:215], a[224:227], 0\n"
"v_mfma_f32_16x16x32_bf16 v[188:191], a[208:211], a[228:231], 0\n"
"v_mfma_f32_16x16x32_bf16 v[204:207], a[212:215], a[228:231], 0\n"
"v_mfma_f32_16x16x32_bf16 a[120:123], a[28:31], a[136:139], a[120:123]\n"
"v_mfma_f32_16x16x32_bf16 a[124:127], a[28:31], a[140:143], a[124:127]\n"
"s_waitcnt lgkmcnt(0)\n"
"v_mfma_f32_16x16x32_bf16 v[0:3], a[176:179], a[144:147], v[0:3]\n"
"s_waitcnt vmcnt(9)\n"
"v_mfma_f32_16x16x32_bf16 v[128:131], a[180:183], a[144:147], v[128:131]\n"
"s_barrier\n"
"v_permlane32_swap_b32 v176, v184\n"
"v_permlane32_swap_b32 v180, v188\n"
"v_mfma_f32_16x16x32_bf16 v[4:7], a[184:187], a[144:147], v[4:7]\n"
"buffer_load_dwordx4 v240, s[36:39], s52 offen offset:0 lds\n"
"s_mov_b32 s52, 8064\n"
"v_mfma_f32_16x16x32_bf16 v[132:135], a[188:191], a[144:147], v[132:135]\n"
"ds_read_b128 a[0:3], v248 offset:0\n"
"ds_read_b64_tr_b16 a[208:209], v234 offset:0\n"
"v_mfma_f32_16x16x32_bf16 v[16:19], a[144:147], a[192:195], v[16:19]\n"
"ds_read_b64_tr_b16 a[210:211], v234 offset:2048\n"
"ds_read_b64_tr_b16 a[212:213], v235 offset:0\n"
"v_mfma_f32_16x16x32_bf16 v[20:23], a[144:147], a[196:199], v[20:23]\n"
"ds_read_b64_tr_b16 a[32:33], v250 offset:32768\n"
"ds_read_b64_tr_b16 a[214:215], v235 offset:2048\n"
"v_mfma_f32_16x16x32_bf16 v[24:27], a[144:147], a[200:203], v[24:27]\n"
"buffer_load_dwordx4 v241, s[36:39], s52 offen offset:1024 lds\n"
"s_mov_b32 s52, 15232\n"
"v_mfma_f32_16x16x32_bf16 v[28:31], a[144:147], a[204:207], v[28:31]\n"
"ds_read_b64_tr_b16 a[34:35], v250 offset:33792\n"
"ds_read_b64_tr_b16 a[216:217], v236 offset:0\n"
"v_mfma_f32_16x16x32_bf16 v[8:11], a[176:179], a[148:151], v[8:11]\n"
"ds_read_b64_tr_b16 a[218:219], v236 offset:2048\n"
"ds_read_b64_tr_b16 a[220:221], v234 offset:4096\n"
"v_mfma_f32_16x16x32_bf16 v[136:139], a[180:183], a[148:151], v[136:139]\n"
"ds_read_b64_tr_b16 a[36:37], v251 offset:32768\n"
"ds_read_b64_tr_b16 a[222:223], v234 offset:6144\n"
"v_mfma_f32_16x16x32_bf16 v[12:15], a[184:187], a[148:151], v[12:15]\n"
"buffer_load_dwordx4 v240, s[36:39], s52 offen offset:2048 lds\n"
"s_mov_b32 s52, 22400\n"
"v_mfma_f32_16x16x32_bf16 v[140:143], a[188:191], a[148:151], v[140:143]\n"
"ds_read_b64_tr_b16 a[38:39], v251 offset:33792\n"
"ds_read_b64_tr_b16 a[224:225], v235 offset:4096\n"
"v_mfma_f32_16x16x32_bf16 v[144:147], a[148:151], a[192:195], v[144:147]\n"
"ds_read_b64_tr_b16 a[226:227], v235 offset:6144\n"
"ds_read_b64_tr_b16 a[228:229], v236 offset:4096\n"
"v_mfma_f32_16x16x32_bf16 v[148:151], a[148:151], a[196:199], v[148:151]\n"
"ds_read_b64_tr_b16 a[40:41], v252 offset:32768\n"
"ds_read_b64_tr_b16 a[230:231], v236 offset:6144\n"
"s_waitcnt lgkmcnt(9)\n"
"v_mfma_f32_16x16x32_bf16 a[244:247], a[208:211], a[212:215], 0\n"
"v_permlane32_swap_b32 v177, v185\n"
"v_permlane32_swap_b32 v181, v189\n"
"v_mfma_f32_16x16x32_bf16 a[248:251], a[208:211], a[216:219], 0\n"
"ds_read_b64_tr_b16 a[232:233], v234 offset:8192\n"
"v_permlane32_swap_b32 v178, v186\n"
"v_mfma_f32_16x16x32_bf16 v[152:155], a[148:151], a[200:203], v[152:155]\n"
"buffer_load_dwordx4 v241, s[36:39], s52 offen offset:3072 lds\n"
"s_add_u32 m0, s89, 36864\n"
"v_mfma_f32_16x16x32_bf16 v[156:159], a[148:151], a[204:207], v[156:159]\n"
"s_mov_b32 s52, 263040\n"
"ds_read_b64_tr_b16 a[42:43], v252 offset:33792\n"
"ds_read_b64_tr_b16 a[234:235], v234 offset:10240\n"
"v_mfma_f32_16x16x32_bf16 v[32:35], a[176:179], a[152:155], v[32:35]\n"
"ds_read_b64_tr_b16 a[236:237], v235 offset:8192\n"
"ds_read_b64_tr_b16 a[238:239], v235 offset:10240\n"
"v_mfma_f32_16x16x32_bf16 v[160:163], a[180:183], a[152:155], v[160:163]\n"
"ds_read_b64_tr_b16 a[240:241], v236 offset:8192\n"
"ds_read_b64_tr_b16 a[242:243], v236 offset:10240\n"
"s_waitcnt lgkmcnt(7)\n"
"v_mfma_f32_16x16x32_bf16 a[244:247], a[220:223], a[224:227], a[244:247]\n"
"v_permlane32_swap_b32 v182, v190\n"
"v_permlane32_swap_b32 v179, v187\n"
"v_mfma_f32_16x16x32_bf16 v[36:39], a[184:187], a[152:155], v[36:39]\n"
"ds_read_b64_tr_b16 a[44:45], v253 offset:32768\n"
"v_permlane32_swap_b32 v183, v191\n"
"v_mfma_f32_16x16x32_bf16 v[164:167], a[188:191], a[152:155], v[164:167]\n"
"buffer_load_dwordx4 v240, s[36:39], s52 offen offset:0 lds\n"
"s_mov_b32 s52, 270208\n"
"v_mfma_f32_16x16x32_bf16 a[248:251], a[220:223], a[228:231], a[248:251]\n"
"v_permlane32_swap_b32 v192, v200\n"
"v_permlane32_swap_b32 v196, v204\n"
"v_mfma_f32_16x16x32_bf16 v[48:51], a[152:155], a[192:195], v[48:51]\n"
"ds_read_b64_tr_b16 a[46:47], v253 offset:33792\n"
"ds_read_b64_tr_b16 a[208:209], v234 offset:12288\n"
"v_mfma_f32_16x16x32_bf16 v[52:55], a[152:155], a[196:199], v[52:55]\n"
"ds_read_b64_tr_b16 a[210:211], v234 offset:14336\n"
"ds_read_b64_tr_b16 a[212:213], v235 offset:12288\n"
"v_mfma_f32_16x16x32_bf16 v[56:59], a[152:155], a[200:203], v[56:59]\n"
"ds_read_b64_tr_b16 a[128:129], v250 offset:40960\n"
"ds_read_b64_tr_b16 a[214:215], v235 offset:14336\n"
"v_mfma_f32_16x16x32_bf16 v[60:63], a[152:155], a[204:207], v[60:63]\n"
"buffer_load_dwordx4 v241, s[36:39], s52 offen offset:1024 lds\n"
"s_mov_b32 s52, 277376\n"
"v_mfma_f32_16x16x32_bf16 v[40:43], a[176:179], a[156:159], v[40:43]\n"
"ds_read_b64_tr_b16 a[130:131], v250 offset:41984\n"
"ds_read_b64_tr_b16 a[216:217], v236 offset:12288\n"
"v_mfma_f32_16x16x32_bf16 v[168:171], a[180:183], a[156:159], v[168:171]\n"
"ds_read_b64_tr_b16 a[218:219], v236 offset:14336\n"
"s_waitcnt lgkmcnt(10)\n"
"v_permlane16_swap_b32 v176, v180\n"
"v_mfma_f32_16x16x32_bf16 a[244:247], a[232:235], a[236:239], a[244:247]\n"
"v_permlane16_swap_b32 v184, v188\n"
"v_permlane16_swap_b32 v177, v181\n"
"v_mfma_f32_16x16x32_bf16 a[248:251], a[232:235], a[240:243], a[248:251]\n"
"v_permlane16_swap_b32 v185, v189\n"
"v_permlane16_swap_b32 v178, v182\n"
"v_mfma_f32_16x16x32_bf16 v[44:47], a[184:187], a[156:159], v[44:47]\n"
"ds_read_b64_tr_b16 a[132:133], v251 offset:40960\n"
"v_permlane16_swap_b32 v186, v190\n"
"v_mfma_f32_16x16x32_bf16 v[172:175], a[188:191], a[156:159], v[172:175]\n"
"buffer_load_dwordx4 v240, s[36:39], s52 offen offset:2048 lds\n"
"s_mov_b32 s52, 284544\n"
"v_mfma_f32_16x16x32_bf16 a[48:51], a[156:159], a[192:195], a[48:51]\n"
"ds_read_b64_tr_b16 a[134:135], v251 offset:41984\n"
"ds_read_b64_tr_b16 a[220:221], v234 offset:16384\n"
"v_mfma_f32_16x16x32_bf16 a[52:55], a[156:159], a[196:199], a[52:55]\n"
"ds_read_b64_tr_b16 a[222:223], v234 offset:18432\n"
"ds_read_b64_tr_b16 a[224:225], v235 offset:16384\n"
"v_mfma_f32_16x16x32_bf16 a[56:59], a[156:159], a[200:203], a[56:59]\n"
"ds_read_b64_tr_b16 a[226:227], v235 offset:18432\n"
"ds_read_b64_tr_b16 a[228:229], v236 offset:16384\n"
"v_mfma_f32_16x16x32_bf16 a[60:63], a[156:159], a[204:207], a[60:63]\n"
"ds_read_b64_tr_b16 a[136:137], v252 offset:40960\n"
"ds_read_b64_tr_b16 a[230:231], v236 offset:18432\n"
"s_waitcnt lgkmcnt(9)\n"
"v_mfma_f32_16x16x32_bf16 a[244:247], a[208:211], a[212:215], a[244:247]\n"
"v_permlane16_swap_b32 v179, v183\n"
"v_permlane16_swap_b32 v187, v191\n"
"v_mfma_f32_16x16x32_bf16 v[64:67], a[176:179], a[160:163], v[64:67]\n"
"buffer_load_dwordx4 v241, s[36:39], s52 offen offset:3072 lds\n"
"s_add_u32 m0, s57, 65536\n"
"v_mfma_f32_16x16x32_bf16 a[248:251], a[208:211], a[216:219], a[248:251]\n"
"s_mov_b32 s52, 3670016\n"
"buffer_load_dwordx4 v246, s[40:43], s52 offen offset:0 lds\n"
"v_mfma_f32_16x16x32_bf16 a[64:67], a[180:183], a[160:163], a[64:67]\n"
"s_mov_b32 s52, 3701760\n"
"ds_read_b64_tr_b16 a[138:139], v252 offset:41984\n"
"v_permlane16_swap_b32 v192, v196\n"
"v_mfma_f32_16x16x32_bf16 v[68:71], a[184:187], a[160:163], v[68:71]\n"
"buffer_load_dwordx4 v246, s[40:43], s52 offen offset:1024 lds\n"
"s_mov_b32 s52, 3733504\n"
"v_mfma_f32_16x16x32_bf16 a[68:71], a[188:191], a[160:163], a[68:71]\n"
"ds_read_b64_tr_b16 a[140:141], v253 offset:40960\n"
"ds_read_b64_tr_b16 a[232:233], v234 offset:20480\n"
"v_mfma_f32_16x16x32_bf16 v[80:83], a[160:163], a[192:195], v[80:83]\n"
"ds_read_b64_tr_b16 a[234:235], v234 offset:22528\n"
"ds_read_b64_tr_b16 a[236:237], v235 offset:20480\n"
"v_mfma_f32_16x16x32_bf16 v[84:87], a[160:163], a[196:199], v[84:87]\n"
"ds_read_b64_tr_b16 a[142:143], v253 offset:41984\n"
"ds_read_b64_tr_b16 a[238:239], v235 offset:22528\n"
"v_mfma_f32_16x16x32_bf16 v[88:91], a[160:163], a[200:203], v[88:91]\n"
"buffer_load_dwordx4 v247, s[40:43], s52 offen offset:2048 lds\n"
"s_mov_b32 s52, 3765248\n"
"v_mfma_f32_16x16x32_bf16 v[92:95], a[160:163], a[204:207], v[92:95]\n"
"ds_read_b128 a[4:7], v248 offset:2048\n"
"ds_read_b64_tr_b16 a[240:241], v236 offset:20480\n"
"v_mfma_f32_16x16x32_bf16 v[72:75], a[176:179], a[164:167], v[72:75]\n"
"ds_read_b64_tr_b16 a[242:243], v236 offset:22528\n"
"s_waitcnt lgkmcnt(10)\n"
"v_permlane16_swap_b32 v200, v204\n"
"v_mfma_f32_16x16x32_bf16 a[244:247], a[220:223], a[224:227], a[244:247]\n"
"v_max_f32 v208, v176, v177\n"
"v_min_f32 v209, v176, v177\n"
"v_mfma_f32_16x16x32_bf16 a[248:251], a[220:223], a[228:231], a[248:251]\n"
"ds_read_b64_tr_b16 a[208:209], v234 offset:24576\n"
"v_max3_f32 v208, v208, v178, v179\n"
"v_mfma_f32_16x16x32_bf16 a[72:75], a[180:183], a[164:167], a[72:75]\n"
"ds_read_b128 a[8:11], v248 offset:4096\n"
"ds_read_b64_tr_b16 a[210:211], v234 offset:26624\n"
"v_mfma_f32_16x16x32_bf16 v[76:79], a[184:187], a[164:167], v[76:79]\n"
"buffer_load_dwordx4 v247, s[40:43], s52 offen offset:3072 lds\n"
"s_add_u32 m0, s57, 81920\n"
"v_mfma_f32_16x16x32_bf16 a[76:79], a[188:191], a[164:167], a[76:79]\n"
"s_mov_b32 s52, 3932160\n"
"ds_read_b64_tr_b16 a[212:213], v235 offset:24576\n"
"ds_read_b64_tr_b16 a[214:215], v235 offset:26624\n"
"v_mfma_f32_16x16x32_bf16 a[80:83], a[164:167], a[192:195], a[80:83]\n"
"ds_read_b128 a[12:15], v248 offset:6144\n"
"ds_read_b64_tr_b16 a[216:217], v236 offset:24576\n"
"v_mfma_f32_16x16x32_bf16 a[84:87], a[164:167], a[196:199], a[84:87]\n"
"ds_read_b64_tr_b16 a[218:219], v236 offset:26624\n"
"s_waitcnt lgkmcnt(8)\n"
"v_min3_f32 v209, v209, v178, v179\n"
"v_mfma_f32_16x16x32_bf16 a[244:247], a[232:235], a[236:239], a[244:247]\n"
"v_max3_f32 v208, v208, v180, v181\n"
"v_min3_f32 v209, v209, v180, v181\n"
"v_mfma_f32_16x16x32_bf16 a[88:91], a[164:167], a[200:203], a[88:91]\n"
"buffer_load_dwordx4 v246, s[40:43], s52 offen offset:0 lds\n"
"s_mov_b32 s52, 3963904\n"
"v_mfma_f32_16x16x32_bf16 a[248:251], a[232:235], a[240:243], a[248:251]\n"
"ds_read_b128 a[16:19], v248 offset:8192\n"
"v_max3_f32 v208, v208, v182, v183\n"
"v_mfma_f32_16x16x32_bf16 a[92:95], a[164:167], a[204:207], a[92:95]\n"
"ds_read_b64_tr_b16 a[220:221], v234 offset:28672\n"
"v_min3_f32 v209, v209, v182, v183\n"
"v_mfma_f32_16x16x32_bf16 v[96:99], a[176:179], a[168:171], v[96:99]\n"
"ds_read_b128 a[20:23], v248 offset:10240\n"
"ds_read_b64_tr_b16 a[222:223], v234 offset:30720\n"
"v_mfma_f32_16x16x32_bf16 a[96:99], a[180:183], a[168:171], a[96:99]\n"
"ds_read_b64_tr_b16 a[224:225], v235 offset:28672\n"
"ds_read_b64_tr_b16 a[226:227], v235 offset:30720\n"
"v_mfma_f32_16x16x32_bf16 v[100:103], a[184:187], a[168:171], v[100:103]\n"
"buffer_load_dwordx4 v246, s[40:43], s52 offen offset:1024 lds\n"
"s_mov_b32 s52, 3995648\n"
"v_mfma_f32_16x16x32_bf16 a[100:103], a[188:191], a[168:171], a[100:103]\n"
"ds_read_b128 a[24:27], v248 offset:12288\n"
"ds_read_b64_tr_b16 a[228:229], v236 offset:28672\n"
"v_mfma_f32_16x16x32_bf16 v[112:115], a[168:171], a[192:195], v[112:115]\n"
"ds_read_b128 a[28:31], v248 offset:14336\n"
"ds_read_b64_tr_b16 a[230:231], v236 offset:30720\n"
"s_waitcnt lgkmcnt(10)\n"
"v_mfma_f32_16x16x32_bf16 a[244:247], a[208:211], a[212:215], a[244:247]\n"
"v_max3_f32 v208, v208, v184, v185\n"
"v_min3_f32 v209, v209, v184, v185\n"
"v_mfma_f32_16x16x32_bf16 a[248:251], a[208:211], a[216:219], a[248:251]\n"
"v_max3_f32 v208, v208, v186, v187\n"
"v_min3_f32 v209, v209, v186, v187\n"
"v_mfma_f32_16x16x32_bf16 v[116:119], a[168:171], a[196:199], v[116:119]\n"
"s_waitcnt lgkmcnt(0)\n"
"v_max3_f32 v208, v208, v188, v189\n"
"v_min3_f32 v209, v209, v188, v189\n"
"v_mfma_f32_16x16x32_bf16 v[120:123], a[168:171], a[200:203], v[120:123]\n"
"buffer_load_dwordx4 v247, s[40:43], s52 offen offset:2048 lds\n"
"s_mov_b32 s52, 4027392\n"
"v_mfma_f32_16x16x32_bf16 a[244:247], a[220:223], a[224:227], a[244:247]\n"
"v_max3_f32 v208, v208, v190, v191\n"
"v_min3_f32 v209, v209, v190, v191\n"
"v_mfma_f32_16x16x32_bf16 v[124:127], a[168:171], a[204:207], v[124:127]\n"
"v_max3_f32 v208, v208, v192, v196\n"
"v_min3_f32 v209, v209, v192, v196\n"
"v_mfma_f32_16x16x32_bf16 a[248:251], a[220:223], a[228:231], a[248:251]\n"
"v_max3_f32 v208, v208, v200, v204\n"
"v_min3_f32 v209, v209, v200, v204\n"
"v_mfma_f32_16x16x32_bf16 v[104:107], a[176:179], a[172:175], v[104:107]\n"
"v_mul_f32 v208, s83, v208\n"
"v_mul_f32 v209, s83, v209\n"
"v_mfma_f32_16x16x32_bf16 a[104:107], a[180:183], a[172:175], a[104:107]\n"
"v_fma_f32 v212, v176, s83, -v208\n"
"v_fma_f32 v176, -v176, s83, v209\n"
"v_mfma_f32_16x16x32_bf16 v[108:111], a[184:187], a[172:175], v[108:111]\n"
"buffer_load_dwordx4 v247, s[40:43], s52 offen offset:3072 lds\n"
"v_mfma_f32_16x16x32_bf16 a[108:111], a[188:191], a[172:175], a[108:111]\n"
"buffer_store_dwordx4 a[244:247], v254, s[84:87], s68 offen offset:0 sc1\n"
"v_mfma_f32_16x16x32_bf16 a[112:115], a[172:175], a[192:195], a[112:115]\n"
"buffer_store_dwordx4 a[248:251], v254, s[84:87], s68 offen offset:1024 sc1\n"
"v_mfma_f32_16x16x32_bf16 a[116:119], a[172:175], a[196:199], a[116:119]\n"
"v_fma_f32 v213, v177, s83, -v208\n"
"v_fma_f32 v177, -v177, s83, v209\n"
"v_mfma_f32_16x16x32_bf16 a[120:123], a[172:175], a[200:203], a[120:123]\n"
"v_exp_f32 v212, v212\n"
"v_mfma_f32_16x16x32_bf16 a[124:127], a[172:175], a[204:207], a[124:127]\n"
"s_waitcnt lgkmcnt(1)\n"
"v_exp_f32 v176, v176\n"
"v_mfma_f32_16x16x32_bf16 v[0:3], a[32:35], a[0:3], v[0:3]\n"
"s_waitcnt vmcnt(20)\n"
"v_fma_f32 v214, v178, s83, -v208\n"
"v_fma_f32 v178, -v178, s83, v209\n"
"v_mfma_f32_16x16x32_bf16 v[128:131], a[36:39], a[0:3], v[128:131]\n"
"s_barrier\n"
"v_exp_f32 v213, v213\n"
"v_mfma_f32_16x16x32_bf16 v[4:7], a[40:43], a[0:3], v[4:7]\n"
"s_add_u32 m0, s89, 131072\n"
"v_exp_f32 v177, v177\n"
"v_mfma_f32_16x16x32_bf16 v[132:135], a[44:47], a[0:3], v[132:135]\n"
"s_mov_b32 s52, 0\n"
"buffer_load_dwordx4 v244, s[44:47], s52 offen offset:0 lds\n"
"v_mfma_f32_16x16x32_bf16 v[16:19], a[0:3], a[128:131], v[16:19]\n"
"ds_read_b128 a[144:147], v249 offset:0\n"
"v_fma_f32 v215, v179, s83, -v208\n"
"v_mfma_f32_16x16x32_bf16 v[20:23], a[0:3], a[132:135], v[20:23]\n"
"ds_read_b64_tr_b16 a[176:177], v250 offset:49152\n"
"v_fma_f32 v179, -v179, s83, v209\n"
"v_mfma_f32_16x16x32_bf16 v[24:27], a[0:3], a[136:139], v[24:27]\n"
"s_mov_b32 s52, 9216\n"
"v_exp_f32 v214, v214\n"
"v_mfma_f32_16x16x32_bf16 v[28:31], a[0:3], a[140:143], v[28:31]\n"
"buffer_load_dwordx4 v245, s[44:47], s52 offen offset:1024 lds\n"
"v_mfma_f32_16x16x32_bf16 v[8:11], a[32:35], a[4:7], v[8:11]\n"
"ds_read_b64_tr_b16 a[178:179], v250 offset:50176\n"
"v_mfma_f32_16x16x32_bf16 v[136:139], a[36:39], a[4:7], v[136:139]\n"
"v_exp_f32 v178, v178\n"
"v_mfma_f32_16x16x32_bf16 v[12:15], a[40:43], a[4:7], v[12:15]\n"
"s_mov_b32 s52, 18432\n"
"ds_read_b64_tr_b16 a[180:181], v251 offset:49152\n"
"v_add_f32 v210, v212, v213\n"
"v_mfma_f32_16x16x32_bf16 v[140:143], a[44:47], a[4:7], v[140:143]\n"
"buffer_load_dwordx4 v244, s[44:47], s52 offen offset:2048 lds\n"
"v_mfma_f32_16x16x32_bf16 v[144:147], a[4:7], a[128:131], v[144:147]\n"
"ds_read_b64_tr_b16 a[182:183], v251 offset:50176\n"
"v_add_f32 v211, v176, v177\n"
"v_mfma_f32_16x16x32_bf16 v[148:151], a[4:7], a[132:135], v[148:151]\n"
"ds_read_b64_tr_b16 a[184:185], v252 offset:49152\n"
"v_fma_f32 v216, v180, s83, -v208\n"
"v_mfma_f32_16x16x32_bf16 v[152:155], a[4:7], a[136:139], v[152:155]\n"
"v_fma_f32 v180, -v180, s83, v209\n"
"v_mfma_f32_16x16x32_bf16 v[156:159], a[4:7], a[140:143], v[156:159]\n"
"s_mov_b32 s52, 27648\n"
"v_exp_f32 v215, v215\n"
"v_mfma_f32_16x16x32_bf16 v[32:35], a[32:35], a[8:11], v[32:35]\n"
"buffer_load_dwordx4 v245, s[44:47], s52 offen offset:3072 lds\n"
"v_mfma_f32_16x16x32_bf16 v[160:163], a[36:39], a[8:11], v[160:163]\n"
"ds_read_b64_tr_b16 a[186:187], v252 offset:50176\n"
"v_mfma_f32_16x16x32_bf16 v[36:39], a[40:43], a[8:11], v[36:39]\n"
"v_exp_f32 v179, v179\n"
"v_mfma_f32_16x16x32_bf16 v[164:167], a[44:47], a[8:11], v[164:167]\n"
"s_add_u32 m0, s89, 135168\n"
"ds_read_b64_tr_b16 a[188:189], v253 offset:49152\n"
"s_mov_b32 s52, 40960\n"
"v_mfma_f32_16x16x32_bf16 v[48:51], a[8:11], a[128:131], v[48:51]\n"
".Lpad6_%=:\n"
"s_cbranch_execz .Lpad10_%=\n"
"buffer_load_dwordx4 v244, s[44:47], s52 offen offset:0 lds\n"
"v_mfma_f32_16x16x32_bf16 v[52:55], a[8:11], a[132:135], v[52:55]\n"
"ds_read_b64_tr_b16 a[190:191], v253 offset:50176\n"
"v_add_f32 v210, v210, v214\n"
"v_mfma_f32_16x16x32_bf16 v[56:59], a[8:11], a[136:139], v[56:59]\n"
"ds_read_b64_tr_b16 a[192:193], v250 offset:57344\n"
"v_add_f32 v211, v211, v178\n"
"v_mfma_f32_16x16x32_bf16 v[60:63], a[8:11], a[140:143], v[60:63]\n"
"v_fma_f32 v217, v181, s83, -v208\n"
"v_fma_f32 v181, -v181, s83, v209\n"
"v_mfma_f32_16x16x32_bf16 v[40:43], a[32:35], a[12:15], v[40:43]\n"
"s_mov_b32 s52, 50176\n"
"v_exp_f32 v216, v216\n"
"v_mfma_f32_16x16x32_bf16 v[168:171], a[36:39], a[12:15], v[168:171]\n"
"buffer_load_dwordx4 v245, s[44:47], s52 offen offset:1024 lds\n"
"v_mfma_f32_16x16x32_bf16 v[44:47], a[40:43], a[12:15], v[44:47]\n"
"ds_read_b64_tr_b16 a[194:195], v250 offset:58368\n"
"ds_read_b64_tr_b16 a[196:197], v251 offset:57344\n"
"v_mfma_f32_16x16x32_bf16 v[172:175], a[44:47], a[12:15], v[172:175]\n"
"v_exp_f32 v180, v180\n"
"v_mfma_f32_16x16x32_bf16 a[48:51], a[12:15], a[128:131], a[48:51]\n"
"s_mov_b32 s52, 59392\n"
"ds_read_b64_tr_b16 a[198:199], v251 offset:58368\n"
"v_add_f32 v210, v210, v215\n"
"v_mfma_f32_16x16x32_bf16 a[52:55], a[12:15], a[132:135], a[52:55]\n"
"buffer_load_dwordx4 v244, s[44:47], s52 offen offset:2048 lds\n"
"v_mfma_f32_16x16x32_bf16 a[56:59], a[12:15], a[136:139], a[56:59]\n"
"v_add_f32 v211, v211, v179\n"
"v_fma_f32 v218, v182, s83, -v208\n"
"v_mfma_f32_16x16x32_bf16 a[60:63], a[12:15], a[140:143], a[60:63]\n"
"ds_read_b64_tr_b16 a[200:201], v252 offset:57344\n"
"v_fma_f32 v182, -v182, s83, v209\n"
"v_mfma_f32_16x16x32_bf16 v[64:67], a[32:35], a[16:19], v[64:67]\n"
"v_exp_f32 v217, v217\n"
"v_mfma_f32_16x16x32_bf16 a[64:67], a[36:39], a[16:19], a[64:67]\n"
"s_mov_b32 s52, 68608\n"
"v_exp_f32 v181, v181\n"
"v_mfma_f32_16x16x32_bf16 v[68:71], a[40:43], a[16:19], v[68:71]\n"
"ds_read_b64_tr_b16 a[202:203], v252 offset:58368\n"
"v_add_f32 v210, v210, v216\n"
"v_mfma_f32_16x16x32_bf16 a[68:71], a[44:47], a[16:19], a[68:71]\n"
"buffer_load_dwordx4 v245, s[44:47], s52 offen offset:3072 lds\n"
"v_mfma_f32_16x16x32_bf16 v[80:83], a[16:19], a[128:131], v[80:83]\n"
"ds_read_b64_tr_b16 a[204:205], v253 offset:57344\n"
"v_add_f32 v211, v211, v180\n"
"v_mfma_f32_16x16x32_bf16 v[84:87], a[16:19], a[132:135], v[84:87]\n"
"s_add_u32 m0, s57, 98304\n"
"ds_read_b64_tr_b16 a[206:207], v253 offset:58368\n"
"s_mov_b32 s52, 0\n"
"v_mfma_f32_16x16x32_bf16 v[88:91], a[16:19], a[136:139], v[88:91]\n"
"buffer_load_dwordx4 v246, s[48:51], s52 offen offset:0 lds\n"
"v_mfma_f32_16x16x32_bf16 v[92:95], a[16:19], a[140:143], v[92:95]\n"
"ds_read_b128 a[148:151], v249 offset:2048\n"
"v_fma_f32 v219, v183, s83, -v208\n"
"v_mfma_f32_16x16x32_bf16 v[72:75], a[32:35], a[20:23], v[72:75]\n"
"v_fma_f32 v183, -v183, s83, v209\n"
"v_mfma_f32_16x16x32_bf16 a[72:75], a[36:39], a[20:23], a[72:75]\n"
"v_exp_f32 v218, v218\n"
"v_mfma_f32_16x16x32_bf16 v[76:79], a[40:43], a[20:23], v[76:79]\n"
"s_mov_b32 s52, 31744\n"
"v_exp_f32 v182, v182\n"
"v_mfma_f32_16x16x32_bf16 a[76:79], a[44:47], a[20:23], a[76:79]\n"
"ds_read_b128 a[152:155], v249 offset:4096\n"
"v_add_f32 v210, v210, v217\n"
"v_mfma_f32_16x16x32_bf16 a[80:83], a[20:23], a[128:131], a[80:83]\n"
"buffer_load_dwordx4 v246, s[48:51], s52 offen offset:1024 lds\n"
"v_mfma_f32_16x16x32_bf16 a[84:87], a[20:23], a[132:135], a[84:87]\n"
"ds_read_b128 a[156:159], v249 offset:6144\n"
"v_add_f32 v211, v211, v181\n"
"v_mfma_f32_16x16x32_bf16 a[88:91], a[20:23], a[136:139], a[88:91]\n"
"s_mov_b32 s52, 63488\n"
"ds_read_b128 a[160:163], v249 offset:8192\n"
"v_fma_f32 v220, v184, s83, -v208\n"
"v_mfma_f32_16x16x32_bf16 a[92:95], a[20:23], a[140:143], a[92:95]\n"
"buffer_load_dwordx4 v247, s[48:51], s52 offen offset:2048 lds\n"
"v_mfma_f32_16x16x32_bf16 v[96:99], a[32:35], a[24:27], v[96:99]\n"
"ds_read_b128 a[164:167], v249 offset:10240\n"
"v_fma_f32 v184, -v184, s83, v209\n"
"v_mfma_f32_16x16x32_bf16 a[96:99], a[36:39], a[24:27], a[96:99]\n"
"v_exp_f32 v219, v219\n"
"v_mfma_f32_16x16x32_bf16 v[100:103], a[40:43], a[24:27], v[100:103]\n"
"v_exp_f32 v183, v183\n"
"v_mfma_f32_16x16x32_bf16 a[100:103], a[44:47], a[24:27], a[100:103]\n"
"s_mov_b32 s52, 95232\n"
"ds_read_b128 a[168:171], v249 offset:12288\n"
"v_add_f32 v210, v210, v218\n"
"v_mfma_f32_16x16x32_bf16 v[112:115], a[24:27], a[128:131], v[112:115]\n"
"buffer_load_dwordx4 v247, s[48:51], s52 offen offset:3072 lds\n"
"v_mfma_f32_16x16x32_bf16 v[116:119], a[24:27], a[132:135], v[116:119]\n"
"ds_read_b128 a[172:175], v249 offset:14336\n"
"v_add_f32 v211, v211, v182\n"
"v_mfma_f32_16x16x32_bf16 v[120:123], a[24:27], a[136:139], v[120:123]\n"
"v_fma_f32 v221, v185, s83, -v208\n"
"v_fma_f32 v185, -v185, s83, v209\n"
"v_mfma_f32_16x16x32_bf16 v[124:127], a[24:27], a[140:143], v[124:127]\n"
"v_exp_f32 v220, v220\n"
"v_mfma_f32_16x16x32_bf16 v[104:107], a[32:35], a[28:31], v[104:107]\n"
"v_exp_f32 v184, v184\n"
"v_mfma_f32_16x16x32_bf16 a[104:107], a[36:39], a[28:31], a[104:107]\n"
"v_add_f32 v210, v210, v219\n"
"v_add_f32 v211, v211, v183\n"
"v_mfma_f32_16x16x32_bf16 v[108:111], a[40:43], a[28:31], v[108:111]\n"
"v_fma_f32 v222, v186, s83, -v208\n"
"v_fma_f32 v186, -v186, s83, v209\n"
"v_mfma_f32_16x16x32_bf16 a[108:111], a[44:47], a[28:31], a[108:111]\n"
"v_exp_f32 v221, v221\n"
"v_mfma_f32_16x16x32_bf16 a[112:115], a[28:31], a[128:131], a[112:115]\n"
"v_exp_f32 v185, v185\n"
"v_mfma_f32_16x16x32_bf16 a[116:119], a[28:31], a[132:135], a[116:119]\n"
"v_add_f32 v210, v210, v220\n"
"v_add_f32 v211, v211, v184\n"
"v_mfma_f32_16x16x32_bf16 a[120:123], a[28:31], a[136:139], a[120:123]\n"
"v_fma_f32 v223, v187, s83, -v208\n"
"v_fma_f32 v187, -v187, s83, v209\n"
"v_mfma_f32_16x16x32_bf16 a[124:127], a[28:31], a[140:143], a[124:127]\n"
"s_waitcnt lgkmcnt(0)\n"
"v_exp_f32 v222, v222\n"
"v_mfma_f32_16x16x32_bf16 v[0:3], a[176:179], a[144:147], v[0:3]\n"
"s_waitcnt vmcnt(18)\n"
"v_exp_f32 v186, v186\n"
"v_mfma_f32_16x16x32_bf16 v[128:131], a[180:183], a[144:147], v[128:131]\n"
"s_barrier\n"
"s_and_b32 s59, s76, 1\n"
"s_lshl_b32 s59, s59, 7\n"
"v_mfma_f32_16x16x32_bf16 v[4:7], a[184:187], a[144:147], v[4:7]\n"
"s_add_u32 m0, s89, 0\n"
"ds_read_b128 a[0:3], v248 offset:32768\n"
"s_mov_b32 s52, 128\n"
"v_mfma_f32_16x16x32_bf16 v[132:135], a[188:191], a[144:147], v[132:135]\n"
"buffer_load_dwordx4 v244, s[44:47], s52 offen offset:0 lds\n"
"v_mfma_f32_16x16x32_bf16 v[16:19], a[144:147], a[192:195], v[16:19]\n"
"v_xor_b32 v232, s59, v250\n"
"v_xor_b32 v233, 32, v232\n"
"v_mfma_f32_16x16x32_bf16 v[20:23], a[144:147], a[196:199], v[20:23]\n"
"ds_read_b64_tr_b16 a[32:33], v250 offset:0\n"
"v_xor_b32 v234, 64, v232\n"
"v_mfma_f32_16x16x32_bf16 v[24:27], a[144:147], a[200:203], v[24:27]\n"
"s_mov_b32 s52, 9344\n"
"v_xor_b32 v235, 96, v232\n"
"v_xor_b32 v236, 128, v232\n"
"v_mfma_f32_16x16x32_bf16 v[28:31], a[144:147], a[204:207], v[28:31]\n"
"buffer_load_dwordx4 v245, s[44:47], s52 offen offset:1024 lds\n"
"v_mfma_f32_16x16x32_bf16 v[8:11], a[176:179], a[148:151], v[8:11]\n"
"ds_read_b64_tr_b16 a[34:35], v250 offset:1024\n"
"v_xor_b32 v237, 160, v232\n"
"v_mfma_f32_16x16x32_bf16 v[136:139], a[180:183], a[148:151], v[136:139]\n"
"ds_read_b64_tr_b16 a[36:37], v251 offset:0\n"
"v_xor_b32 v238, 192, v232\n"
"v_mfma_f32_16x16x32_bf16 v[12:15], a[184:187], a[148:151], v[12:15]\n"
"s_mov_b32 s52, 18560\n"
"v_xor_b32 v239, 224, v232\n"
"v_and_b32 v240, 2047, v248\n"
"v_mfma_f32_16x16x32_bf16 v[140:143], a[188:191], a[148:151], v[140:143]\n"
"buffer_load_dwordx4 v244, s[44:47], s52 offen offset:2048 lds\n"
"v_mfma_f32_16x16x32_bf16 v[144:147], a[148:151], a[192:195], v[144:147]\n"
"ds_read_b64_tr_b16 a[38:39], v251 offset:1024\n"
"v_add_u32 v240, s89, v240\n"
"v_mfma_f32_16x16x32_bf16 v[148:151], a[148:151], a[196:199], v[148:151]\n"
"ds_read_b64_tr_b16 a[40:41], v252 offset:0\n"
"v_xor_b32 v241, 64, v240\n"
"v_mfma_f32_16x16x32_bf16 v[152:155], a[148:151], a[200:203], v[152:155]\n"
"s_mov_b32 s52, 27776\n"
"v_add_u32 v242, 131072, v240\n"
"v_add_u32 v243, 131072, v241\n"
"v_mfma_f32_16x16x32_bf16 v[156:159], a[148:151], a[204:207], v[156:159]\n"
"buffer_load_dwordx4 v245, s[44:47], s52 offen offset:3072 lds\n"
"v_mfma_f32_16x16x32_bf16 v[32:35], a[176:179], a[152:155], v[32:35]\n"
"ds_read_b64_tr_b16 a[42:43], v252 offset:1024\n"
"v_add_f32 v210, v210, v221\n"
"v_mfma_f32_16x16x32_bf16 v[160:163], a[180:183], a[152:155], v[160:163]\n"
"v_add_f32 v211, v211, v185\n"
"v_fma_f32 v224, v188, s83, -v208\n"
"v_mfma_f32_16x16x32_bf16 v[36:39], a[184:187], a[152:155], v[36:39]\n"
"s_add_u32 m0, s89, 4096\n"
"ds_read_b64_tr_b16 a[44:45], v253 offset:0\n"
"s_mov_b32 s52, 41088\n"
"v_mfma_f32_16x16x32_bf16 v[164:167], a[188:191], a[152:155], v[164:167]\n"
"buffer_load_dwordx4 v244, s[44:47], s52 offen offset:0 lds\n"
"v_mfma_f32_16x16x32_bf16 v[48:51], a[152:155], a[192:195], v[48:51]\n"
"ds_read_b64_tr_b16 a[46:47], v253 offset:1024\n"
"v_fma_f32 v188, -v188, s83, v209\n"
"v_mfma_f32_16x16x32_bf16 v[52:55], a[152:155], a[196:199], v[52:55]\n"
"v_exp_f32 v223, v223\n"
"v_mfma_f32_16x16x32_bf16 v[56:59], a[152:155], a[200:203], v[56:59]\n"
"s_mov_b32 s52, 50304\n"
"v_exp_f32 v187, v187\n"
"v_mfma_f32_16x16x32_bf16 v[60:63], a[152:155], a[204:207], v[60:63]\n"
"ds_read_b64_tr_b16 a[128:129], v250 offset:8192\n"
"v_add_f32 v210, v210, v222\n"
"v_mfma_f32_16x16x32_bf16 v[40:43], a[176:179], a[156:159], v[40:43]\n"
"buffer_load_dwordx4 v245, s[44:47], s52 offen offset:1024 lds\n"
"v_mfma_f32_16x16x32_bf16 v[168:171], a[180:183], a[156:159], v[168:171]\n"
"ds_read_b64_tr_b16 a[130:131], v250 offset:9216\n"
"v_add_f32 v211, v211, v186\n"
"v_mfma_f32_16x16x32_bf16 v[44:47], a[184:187], a[156:159], v[44:47]\n"
"s_mov_b32 s52, 59520\n"
"ds_read_b64_tr_b16 a[132:133], v251 offset:8192\n"
"v_fma_f32 v225, v189, s83, -v208\n"
"v_mfma_f32_16x16x32_bf16 v[172:175], a[188:191], a[156:159], v[172:175]\n"
"buffer_load_dwordx4 v244, s[44:47], s52 offen offset:2048 lds\n"
"v_mfma_f32_16x16x32_bf16 a[48:51], a[156:159], a[192:195], a[48:51]\n"
"ds_read_b64_tr_b16 a[134:135], v251 offset:9216\n"
"v_fma_f32 v189, -v189, s83, v209\n"
"v_mfma_f32_16x16x32_bf16 a[52:55], a[156:159], a[196:199], a[52:55]\n"
"v_exp_f32 v224, v224\n"
"v_mfma_f32_16x16x32_bf16 a[56:59], a[156:159], a[200:203], a[56:59]\n"
"v_exp_f32 v188, v188\n"
"v_mfma_f32_16x16x32_bf16 a[60:63], a[156:159], a[204:207], a[60:63]\n"
"s_mov_b32 s52, 68736\n"
"ds_read_b64_tr_b16 a[136:137], v252 offset:8192\n"
"v_add_f32 v210, v210, v223\n"
"v_mfma_f32_16x16x32_bf16 v[64:67], a[176:179], a[160:163], v[64:67]\n"
"buffer_load_dwordx4 v245, s[44:47], s52 offen offset:3072 lds\n"
"v_mfma_f32_16x16x32_bf16 a[64:67], a[180:183], a[160:163], a[64:67]\n"
"ds_read_b64_tr_b16 a[138:139], v252 offset:9216\n"
"v_add_f32 v211, v211, v187\n"
"v_mfma_f32_16x16x32_bf16 v[68:71], a[184:187], a[160:163], v[68:71]\n"
"v_fma_f32 v226, v190, s83, -v208\n"
"v_fma_f32 v190, -v190, s83, v209\n"
"v_mfma_f32_16x16x32_bf16 a[68:71], a[188:191], a[160:163], a[68:71]\n"
"s_add_u32 m0, s57, 114688\n"
"v_exp_f32 v225, v225\n"
"v_mfma_f32_16x16x32_bf16 v[80:83], a[160:163], a[192:195], v[80:83]\n"
"s_mov_b32 s52, 524288\n"
"ds_read_b64_tr_b16 a[140:141], v253 offset:8192\n"
"v_mfma_f32_16x16x32_bf16 v[84:87], a[160:163], a[196:199], v[84:87]\n"
"buffer_load_dwordx4 v246, s[48:51], s52 offen offset:0 lds\n"
"v_mfma_f32_16x16x32_bf16 v[88:91], a[160:163], a[200:203], v[88:91]\n"
"v_exp_f32 v189, v189\n"
"v_mfma_f32_16x16x32_bf16 v[92:95], a[160:163], a[204:207], v[92:95]\n"
"s_mov_b32 s52, 556032\n"
"ds_read_b64_tr_b16 a[142:143], v253 offset:9216\n"
"ds_read_b128 a[4:7], v248 offset:34816\n"
"v_mfma_f32_16x16x32_bf16 v[72:75], a[176:179], a[164:167], v[72:75]\n"
"buffer_load_dwordx4 v246, s[48:51], s52 offen offset:1024 lds\n"
"v_mfma_f32_16x16x32_bf16 a[72:75], a[180:183], a[164:167], a[72:75]\n"
"ds_read_b128 a[8:11], v248 offset:36864\n"
"v_add_f32 v210, v210, v224\n"
"v_mfma_f32_16x16x32_bf16 v[76:79], a[184:187], a[164:167], v[76:79]\n"
"v_add_f32 v211, v211, v188\n"
"v_fma_f32 v227, v191, s83, -v208\n"
"v_mfma_f32_16x16x32_bf16 a[76:79], a[188:191], a[164:167], a[76:79]\n"
"s_mov_b32 s52, 587776\n"
"v_fma_f32 v191, -v191, s83, v209\n"
"v_mfma_f32_16x16x32_bf16 a[80:83], a[164:167], a[192:195], a[80:83]\n"
"buffer_load_dwordx4 v247, s[48:51], s52 offen offset:2048 lds\n"
"v_mfma_f32_16x16x32_bf16 a[84:87], a[164:167], a[196:199], a[84:87]\n"
"ds_read_b128 a[12:15], v248 offset:38912\n"
"v_mfma_f32_16x16x32_bf16 a[88:91], a[164:167], a[200:203], a[88:91]\n"
"v_exp_f32 v226, v226\n"
"v_mfma_f32_16x16x32_bf16 a[92:95], a[164:167], a[204:207], a[92:95]\n"
"s_mov_b32 s52, 619520\n"
"ds_read_b128 a[16:19], v248 offset:40960\n"
"v_mfma_f32_16x16x32_bf16 v[96:99], a[176:179], a[168:171], v[96:99]\n"
"buffer_load_dwordx4 v247, s[48:51], s52 offen offset:3072 lds\n"
"v_mfma_f32_16x16x32_bf16 a[96:99], a[180:183], a[168:171], a[96:99]\n"
"ds_read_b128 a[20:23], v248 offset:43008\n"
"v_mfma_f32_16x16x32_bf16 v[100:103], a[184:187], a[168:171], v[100:103]\n"
"v_exp_f32 v190, v190\n"
"v_mfma_f32_16x16x32_bf16 a[100:103], a[188:191], a[168:171], a[100:103]\n"
"s_lshr_b32 s34, s32, 3\n"
"ds_read_b128 a[24:27], v248 offset:45056\n"
"s_mul_i32 s34, s34, 224\n"
"v_mfma_f32_16x16x32_bf16 v[112:115], a[168:171], a[192:195], v[112:115]\n"
"s_add_u32 s34, s34, s33\n"
"ds_read_b128 a[28:31], v248 offset:47104\n"
"s_lshl_b32 s34, s34, 3\n"
"v_mfma_f32_16x16x32_bf16 v[116:119], a[168:171], a[196:199], v[116:119]\n"
"s_and_b32 s35, s32, 7\n"
"s_add_u32 s34, s34, s35\n"
"s_lshl_b32 s34, s34, 7\n"
"v_mfma_f32_16x16x32_bf16 v[120:123], a[168:171], a[200:203], v[120:123]\n"
"s_add_u32 s34, s34, 1572864\n"
"s_mov_b32 s30, 28\n"
"s_mov_b64 s[28:29], s[16:17]\n"
"v_mfma_f32_16x16x32_bf16 v[124:127], a[168:171], a[204:207], v[124:127]\n"
"s_or_b32 s29, s29, 536870912\n"
"buffer_load_dword a255, v255, s[28:31], s34 idxen\n"
"v_mfma_f32_16x16x32_bf16 v[104:107], a[176:179], a[172:175], v[104:107]\n"
"s_lshl_b32 s53, s77, 8\n"
"v_add_f32 v210, v210, v225\n"
"v_add_f32 v211, v211, v189\n"
"v_mfma_f32_16x16x32_bf16 a[104:107], a[180:183], a[172:175], a[104:107]\n"
"s_mov_b32 s82, 81813504\n"
"s_cmp_eq_u32 s76, 0\n"
"s_cselect_b32 s82, s82, 1073741824\n"
"v_mfma_f32_16x16x32_bf16 v[108:111], a[184:187], a[172:175], v[108:111]\n"
"s_add_u32 s82, s82, s53\n"
"s_lshl_b32 s53, s78, 2\n"
"s_add_u32 s53, s53, s82\n"
"v_mfma_f32_16x16x32_bf16 a[108:111], a[188:191], a[172:175], a[108:111]\n"
"v_fma_f32 v228, v192, s83, -v208\n"
"v_fma_f32 v192, -v192, s83, v209\n"
"v_mfma_f32_16x16x32_bf16 a[112:115], a[172:175], a[192:195], a[112:115]\n"
"v_exp_f32 v227, v227\n"
"v_mfma_f32_16x16x32_bf16 a[116:119], a[172:175], a[196:199], a[116:119]\n"
"v_exp_f32 v191, v191\n"
"v_mfma_f32_16x16x32_bf16 a[120:123], a[172:175], a[200:203], a[120:123]\n"
"v_add_f32 v210, v210, v226\n"
"v_add_f32 v211, v211, v190\n"
"v_mfma_f32_16x16x32_bf16 a[124:127], a[172:175], a[204:207], a[124:127]\n"
"s_waitcnt lgkmcnt(0)\n"
"v_fma_f32 v229, v196, s83, -v208\n"
"v_fma_f32 v196, -v196, s83, v209\n"
"v_mfma_f32_16x16x32_bf16 v[0:3], a[32:35], a[0:3], v[0:3]\n"
"s_waitcnt vmcnt(25)\n"
"v_exp_f32 v228, v228\n"
"v_mfma_f32_16x16x32_bf16 v[128:131], a[36:39], a[0:3], v[128:131]\n"
"s_barrier\n"
"v_exp_f32 v192, v192\n"
"v_mfma_f32_16x16x32_bf16 v[4:7], a[40:43], a[0:3], v[4:7]\n"
"ds_read_b128 a[144:147], v249 offset:32768\n"
"v_lshlrev_b32 v193, 21, v255\n"
"v_mfma_f32_16x16x32_bf16 v[132:135], a[44:47], a[0:3], v[132:135]\n"
"v_mov_b32 v194, s88\n"
"v_add_f32 v210, v210, v227\n"
"v_mfma_f32_16x16x32_bf16 v[16:19], a[0:3], a[128:131], v[16:19]\n"
"buffer_store_dword v194, v193, s[84:87], s53 offen sc1\n"
"v_mfma_f32_16x16x32_bf16 v[20:23], a[0:3], a[132:135], v[20:23]\n"
"ds_read_b64_tr_b16 a[176:177], v250 offset:16384\n"
"v_add_f32 v211, v211, v191\n"
"v_mfma_f32_16x16x32_bf16 v[24:27], a[0:3], a[136:139], v[24:27]\n"
"v_fma_f32 v230, v200, s83, -v208\n"
"v_fma_f32 v200, -v200, s83, v209\n"
"v_mfma_f32_16x16x32_bf16 v[28:31], a[0:3], a[140:143], v[28:31]\n"
"v_exp_f32 v229, v229\n"
"v_mfma_f32_16x16x32_bf16 v[8:11], a[32:35], a[4:7], v[8:11]\n"
"ds_read_b64_tr_b16 a[178:179], v250 offset:17408\n"
"v_mfma_f32_16x16x32_bf16 v[136:139], a[36:39], a[4:7], v[136:139]\n"
"v_exp_f32 v196, v196\n"
"v_mfma_f32_16x16x32_bf16 v[12:15], a[40:43], a[4:7], v[12:15]\n"
"ds_read_b64_tr_b16 a[180:181], v251 offset:16384\n"
"v_add_f32 v210, v210, v228\n"
"v_mfma_f32_16x16x32_bf16 v[140:143], a[44:47], a[4:7], v[140:143]\n"
"ds_read_b64_tr_b16 a[182:183], v251 offset:17408\n"
"v_add_f32 v211, v211, v192\n"
"v_mfma_f32_16x16x32_bf16 v[144:147], a[4:7], a[128:131], v[144:147]\n"
"v_fma_f32 v231, v204, s83, -v208\n"
"v_fma_f32 v204, -v204, s83, v209\n"
"v_mfma_f32_16x16x32_bf16 v[148:151], a[4:7], a[132:135], v[148:151]\n"
"v_exp_f32 v230, v230\n"
"v_mfma_f32_16x16x32_bf16 v[152:155], a[4:7], a[136:139], v[152:155]\n"
"v_exp_f32 v200, v200\n"
"v_mfma_f32_16x16x32_bf16 v[156:159], a[4:7], a[140:143], v[156:159]\n"
"ds_read_b64_tr_b16 a[184:185], v252 offset:16384\n"
"ds_read_b64_tr_b16 a[186:187], v252 offset:17408\n"
"v_mfma_f32_16x16x32_bf16 v[32:35], a[32:35], a[8:11], v[32:35]\n"
"v_add_f32 v210, v210, v229\n"
"v_add_f32 v211, v211, v196\n"
"v_mfma_f32_16x16x32_bf16 v[160:163], a[36:39], a[8:11], v[160:163]\n"
"v_exp_f32 v231, v231\n"
"v_mfma_f32_16x16x32_bf16 v[36:39], a[40:43], a[8:11], v[36:39]\n"
"v_exp_f32 v204, v204\n"
"v_mfma_f32_16x16x32_bf16 v[164:167], a[44:47], a[8:11], v[164:167]\n"
"ds_read_b64_tr_b16 a[188:189], v253 offset:16384\n"
"v_add_f32 v210, v210, v230\n"
"v_mfma_f32_16x16x32_bf16 v[48:51], a[8:11], a[128:131], v[48:51]\n"
"ds_read_b64_tr_b16 a[190:191], v253 offset:17408\n"
"v_add_f32 v211, v211, v200\n"
"v_mfma_f32_16x16x32_bf16 v[52:55], a[8:11], a[132:135], v[52:55]\n"
"v_add_f32 v210, v210, v231\n"
"v_add_f32 v211, v211, v204\n"
"v_mfma_f32_16x16x32_bf16 v[56:59], a[8:11], a[136:139], v[56:59]\n"
"v_rcp_f32 v210, v210\n"
"v_mfma_f32_16x16x32_bf16 v[60:63], a[8:11], a[140:143], v[60:63]\n"
"ds_read_b64_tr_b16 a[192:193], v250 offset:24576\n"
"v_mfma_f32_16x16x32_bf16 v[40:43], a[32:35], a[12:15], v[40:43]\n"
"v_rcp_f32 v211, v211\n"
"v_mfma_f32_16x16x32_bf16 v[168:171], a[36:39], a[12:15], v[168:171]\n"
"ds_read_b64_tr_b16 a[194:195], v250 offset:25600\n"
"v_mul_f32 v212, v212, v210\n"
"v_mfma_f32_16x16x32_bf16 v[44:47], a[40:43], a[12:15], v[44:47]\n"
"ds_read_b64_tr_b16 a[196:197], v251 offset:24576\n"
"v_mul_f32 v213, v213, v210\n"
"v_mfma_f32_16x16x32_bf16 v[172:175], a[44:47], a[12:15], v[172:175]\n"
"v_cvt_pk_bf16_f32 v212, v212, v213\n"
"v_mul_f32 v214, v214, v210\n"
"v_mfma_f32_16x16x32_bf16 a[48:51], a[12:15], a[128:131], a[48:51]\n"
"ds_read_b64_tr_b16 a[198:199], v251 offset:25600\n"
"v_mul_f32 v215, v215, v210\n"
"v_mfma_f32_16x16x32_bf16 a[52:55], a[12:15], a[132:135], a[52:55]\n"
"v_cvt_pk_bf16_f32 v213, v214, v215\n"
"v_mul_f32 v216, v216, v210\n"
"v_mfma_f32_16x16x32_bf16 a[56:59], a[12:15], a[136:139], a[56:59]\n"
"v_mul_f32 v217, v217, v210\n"
"v_cvt_pk_bf16_f32 v214, v216, v217\n"
"v_mfma_f32_16x16x32_bf16 a[60:63], a[12:15], a[140:143], a[60:63]\n"
"ds_read_b64_tr_b16 a[200:201], v252 offset:24576\n"
"v_mul_f32 v218, v218, v210\n"
"v_mfma_f32_16x16x32_bf16 v[64:67], a[32:35], a[16:19], v[64:67]\n"
"v_mul_f32 v219, v219, v210\n"
"v_cvt_pk_bf16_f32 v215, v218, v219\n"
"v_mfma_f32_16x16x32_bf16 a[64:67], a[36:39], a[16:19], a[64:67]\n"
"ds_read_b64_tr_b16 a[202:203], v252 offset:25600\n"
"v_mul_f32 v220, v220, v210\n"
"v_mfma_f32_16x16x32_bf16 v[68:71], a[40:43], a[16:19], v[68:71]\n"
"v_mul_f32 v221, v221, v210\n"
"v_cvt_pk_bf16_f32 v216, v220, v221\n"
"v_mfma_f32_16x16x32_bf16 a[68:71], a[44:47], a[16:19], a[68:71]\n"
"ds_read_b64_tr_b16 a[204:205], v253 offset:24576\n"
"v_mul_f32 v222, v222, v210\n"
"v_mfma_f32_16x16x32_bf16 v[80:83], a[16:19], a[128:131], v[80:83]\n"
"s_add_u32 m0, s57, 65536\n"
"s_mov_b32 s52, 1048576\n"
"v_mul_f32 v223, v223, v210\n"
"v_mfma_f32_16x16x32_bf16 v[84:87], a[16:19], a[132:135], v[84:87]\n"
"buffer_load_dwordx4 v246, s[48:51], s52 offen offset:0 lds\n"
"v_mfma_f32_16x16x32_bf16 v[88:91], a[16:19], a[136:139], v[88:91]\n"
"ds_read_b64_tr_b16 a[206:207], v253 offset:25600\n"
"v_cvt_pk_bf16_f32 v217, v222, v223\n"
"v_mfma_f32_16x16x32_bf16 v[92:95], a[16:19], a[140:143], v[92:95]\n"
"ds_read_b128 a[148:151], v249 offset:34816\n"
"v_mul_f32 v224, v224, v210\n"
"v_mfma_f32_16x16x32_bf16 v[72:75], a[32:35], a[20:23], v[72:75]\n"
"s_mov_b32 s52, 1080320\n"
"v_mul_f32 v225, v225, v210\n"
"v_cvt_pk_bf16_f32 v218, v224, v225\n"
"v_mfma_f32_16x16x32_bf16 a[72:75], a[36:39], a[20:23], a[72:75]\n"
"buffer_load_dwordx4 v246, s[48:51], s52 offen offset:1024 lds\n"
"v_mfma_f32_16x16x32_bf16 v[76:79], a[40:43], a[20:23], v[76:79]\n"
"ds_read_b128 a[152:155], v249 offset:36864\n"
"v_mul_f32 v226, v226, v210\n"
"v_mfma_f32_16x16x32_bf16 a[76:79], a[44:47], a[20:23], a[76:79]\n"
"v_mul_f32 v227, v227, v210\n"
"v_cvt_pk_bf16_f32 v219, v226, v227\n"
"v_mfma_f32_16x16x32_bf16 a[80:83], a[20:23], a[128:131], a[80:83]\n"
"ds_read_b128 a[156:159], v249 offset:38912\n"
"v_mul_f32 v228, v228, v210\n"
"v_mfma_f32_16x16x32_bf16 a[84:87], a[20:23], a[132:135], a[84:87]\n"
"s_mov_b32 s52, 1112064\n"
"v_mul_f32 v229, v229, v210\n"
"v_cvt_pk_bf16_f32 v220, v228, v229\n"
"v_mfma_f32_16x16x32_bf16 a[88:91], a[20:23], a[136:139], a[88:91]\n"
"buffer_load_dwordx4 v247, s[48:51], s52 offen offset:2048 lds\n"
"v_mfma_f32_16x16x32_bf16 a[92:95], a[20:23], a[140:143], a[92:95]\n"
"ds_read_b128 a[160:163], v249 offset:40960\n"
"v_mul_f32 v230, v230, v210\n"
"v_mfma_f32_16x16x32_bf16 v[96:99], a[32:35], a[24:27], v[96:99]\n"
"ds_read_b128 a[164:167], v249 offset:43008\n"
"v_mul_f32 v231, v231, v210\n"
"v_mfma_f32_16x16x32_bf16 a[96:99], a[36:39], a[24:27], a[96:99]\n"
"v_cvt_pk_bf16_f32 v221, v230, v231\n"
"v_mul_f32 v176, v176, v211\n"
"v_mfma_f32_16x16x32_bf16 v[100:103], a[40:43], a[24:27], v[100:103]\n"
"s_mov_b32 s52, 1143808\n"
"ds_read_b128 a[168:171], v249 offset:45056\n"
"v_mul_f32 v177, v177, v211\n"
"v_mfma_f32_16x16x32_bf16 a[100:103], a[44:47], a[24:27], a[100:103]\n"
"buffer_load_dwordx4 v247, s[48:51], s52 offen offset:3072 lds\n"
"v_mfma_f32_16x16x32_bf16 v[112:115], a[24:27], a[128:131], v[112:115]\n"
"ds_read_b128 a[172:175], v249 offset:47104\n"
"v_cvt_pk_bf16_f32 v222, v176, v177\n"
"v_mfma_f32_16x16x32_bf16 v[116:119], a[24:27], a[132:135], v[116:119]\n"
"v_mul_f32 v178, v178, v211\n"
"v_mul_f32 v179, v179, v211\n"
"v_mfma_f32_16x16x32_bf16 v[120:123], a[24:27], a[136:139], v[120:123]\n"
"v_cvt_pk_bf16_f32 v223, v178, v179\n"
"v_mul_f32 v180, v180, v211\n"
"v_mfma_f32_16x16x32_bf16 v[124:127], a[24:27], a[140:143], v[124:127]\n"
"v_mul_f32 v181, v181, v211\n"
"v_cvt_pk_bf16_f32 v224, v180, v181\n"
"v_mfma_f32_16x16x32_bf16 v[104:107], a[32:35], a[28:31], v[104:107]\n"
"v_mul_f32 v182, v182, v211\n"
"v_mul_f32 v183, v183, v211\n"
"v_mfma_f32_16x16x32_bf16 a[104:107], a[36:39], a[28:31], a[104:107]\n"
"v_cvt_pk_bf16_f32 v225, v182, v183\n"
"v_mul_f32 v184, v184, v211\n"
"v_mfma_f32_16x16x32_bf16 v[108:111], a[40:43], a[28:31], v[108:111]\n"
"v_mul_f32 v185, v185, v211\n"
"v_cvt_pk_bf16_f32 v226, v184, v185\n"
"v_mfma_f32_16x16x32_bf16 a[108:111], a[44:47], a[28:31], a[108:111]\n"
"v_mul_f32 v186, v186, v211\n"
"v_mul_f32 v187, v187, v211\n"
"v_mfma_f32_16x16x32_bf16 a[112:115], a[28:31], a[128:131], a[112:115]\n"
"v_cvt_pk_bf16_f32 v227, v186, v187\n"
"v_mul_f32 v188, v188, v211\n"
"v_mfma_f32_16x16x32_bf16 a[116:119], a[28:31], a[132:135], a[116:119]\n"
"v_mul_f32 v189, v189, v211\n"
"v_cvt_pk_bf16_f32 v228, v188, v189\n"
"v_mfma_f32_16x16x32_bf16 a[120:123], a[28:31], a[136:139], a[120:123]\n"
"v_mul_f32 v190, v190, v211\n"
"v_mul_f32 v191, v191, v211\n"
"v_mfma_f32_16x16x32_bf16 a[124:127], a[28:31], a[140:143], a[124:127]\n"
"s_waitcnt lgkmcnt(0)\n"
"v_cvt_pk_bf16_f32 v229, v190, v191\n"
"v_mul_f32 v192, v192, v211\n"
"v_mfma_f32_16x16x32_bf16 v[0:3], a[176:179], a[144:147], v[0:3]\n"
"s_waitcnt vmcnt(18)\n"
"v_mul_f32 v196, v196, v211\n"
"v_cvt_pk_bf16_f32 v230, v192, v196\n"
"v_mfma_f32_16x16x32_bf16 v[128:131], a[180:183], a[144:147], v[128:131]\n"
"s_barrier\n"
"v_mul_f32 v200, v200, v211\n"
"v_mul_f32 v204, v204, v211\n"
"v_mfma_f32_16x16x32_bf16 v[4:7], a[184:187], a[144:147], v[4:7]\n"
"s_add_u32 m0, s89, 32768\n"
"ds_read_b128 a[208:211], v242 offset:0\n"
"s_mov_b32 s52, 256\n"
"v_mfma_f32_16x16x32_bf16 v[132:135], a[188:191], a[144:147], v[132:135]\n"
"buffer_load_dwordx4 v244, s[44:47], s52 offen offset:0 lds\n"
"v_mfma_f32_16x16x32_bf16 v[16:19], a[144:147], a[192:195], v[16:19]\n"
"s_mov_b32 s52, 9472\n"
"ds_read_b128 a[212:215], v242 offset:2048\n"
"ds_read_b128 a[216:219], v242 offset:4096\n"
"v_mfma_f32_16x16x32_bf16 v[20:23], a[144:147], a[196:199], v[20:23]\n"
"buffer_load_dwordx4 v245, s[44:47], s52 offen offset:1024 lds\n"
"v_mfma_f32_16x16x32_bf16 v[24:27], a[144:147], a[200:203], v[24:27]\n"
"s_mov_b32 s52, 18688\n"
"ds_read_b128 a[220:223], v242 offset:6144\n"
"ds_read_b64_tr_b16 a[224:225], v232 offset:32768\n"
"v_mfma_f32_16x16x32_bf16 v[28:31], a[144:147], a[204:207], v[28:31]\n"
"buffer_load_dwordx4 v244, s[44:47], s52 offen offset:2048 lds\n"
"v_mfma_f32_16x16x32_bf16 v[8:11], a[176:179], a[148:151], v[8:11]\n"
"ds_read_b64_tr_b16 a[226:227], v232 offset:33792\n"
"ds_read_b64_tr_b16 a[228:229], v233 offset:32768\n"
"ds_read_b64_tr_b16 a[230:231], v233 offset:33792\n"
"v_mfma_f32_16x16x32_bf16 v[136:139], a[180:183], a[148:151], v[136:139]\n"
"s_mov_b32 s52, 27904\n"
"ds_read_b64_tr_b16 a[232:233], v234 offset:32768\n"
"ds_read_b64_tr_b16 a[234:235], v234 offset:33792\n"
"v_cvt_pk_bf16_f32 v0, v0, v1\n"
"v_mfma_f32_16x16x32_bf16 v[12:15], a[184:187], a[148:151], v[12:15]\n"
"buffer_load_dwordx4 v245, s[44:47], s52 offen offset:3072 lds\n"
"ds_read_b64_tr_b16 a[236:237], v235 offset:32768\n"
"v_mfma_f32_16x16x32_bf16 v[140:143], a[188:191], a[148:151], v[140:143]\n"
"ds_read_b64_tr_b16 a[238:239], v235 offset:33792\n"
"ds_read_b64_tr_b16 a[240:241], v236 offset:32768\n"
"v_cvt_pk_bf16_f32 v1, v2, v3\n"
"v_mfma_f32_16x16x32_bf16 v[144:147], a[148:151], a[192:195], v[144:147]\n"
"ds_read_b64_tr_b16 a[242:243], v236 offset:33792\n"
"v_cvt_pk_bf16_f32 v2, v128, v129\n"
"v_cvt_pk_bf16_f32 v3, v130, v131\n"
"v_mfma_f32_16x16x32_bf16 v[148:151], a[148:151], a[196:199], v[148:151]\n"
"ds_read_b64_tr_b16 a[244:245], v237 offset:32768\n"
"v_accvgpr_write_b32 a0, v0\n"
"v_accvgpr_write_b32 a1, v1\n"
"v_mfma_f32_16x16x32_bf16 v[152:155], a[148:151], a[200:203], v[152:155]\n"
"ds_read_b64_tr_b16 a[246:247], v237 offset:33792\n"
"ds_read_b64_tr_b16 a[248:249], v238 offset:32768\n"
"v_accvgpr_write_b32 a2, v2\n"
"v_mfma_f32_16x16x32_bf16 v[156:159], a[148:151], a[204:207], v[156:159]\n"
"ds_read_b64_tr_b16 a[250:251], v238 offset:33792\n"
"v_accvgpr_write_b32 a3, v3\n"
"v_cvt_pk_bf16_f32 v4, v4, v5\n"
"v_mfma_f32_16x16x32_bf16 v[32:35], a[176:179], a[152:155], v[32:35]\n"
"ds_read_b64_tr_b16 a[252:253], v239 offset:32768\n"
"v_cvt_pk_bf16_f32 v5, v6, v7\n"
"v_cvt_pk_bf16_f32 v6, v132, v133\n"
"v_mfma_f32_16x16x32_bf16 v[160:163], a[180:183], a[152:155], v[160:163]\n"
"v_cvt_pk_bf16_f32 v7, v134, v135\n"
"v_accvgpr_write_b32 a4, v4\n"
"v_accvgpr_write_b32 a5, v5\n"
"v_mfma_f32_16x16x32_bf16 v[36:39], a[184:187], a[152:155], v[36:39]\n"
"v_accvgpr_write_b32 a6, v6\n"
"v_accvgpr_write_b32 a7, v7\n"
"v_cvt_pk_bf16_f32 v8, v8, v9\n"
"v_mfma_f32_16x16x32_bf16 v[164:167], a[188:191], a[152:155], v[164:167]\n"
"v_cvt_pk_bf16_f32 v9, v10, v11\n"
"v_cvt_pk_bf16_f32 v10, v136, v137\n"
"v_cvt_pk_bf16_f32 v11, v138, v139\n"
"v_mfma_f32_16x16x32_bf16 v[48:51], a[152:155], a[192:195], v[48:51]\n"
"v_accvgpr_write_b32 a8, v8\n"
"v_accvgpr_write_b32 a9, v9\n"
"v_accvgpr_write_b32 a10, v10\n"
"v_mfma_f32_16x16x32_bf16 v[52:55], a[152:155], a[196:199], v[52:55]\n"
"v_accvgpr_write_b32 a11, v11\n"
"v_cvt_pk_bf16_f32 v12, v12, v13\n"
"v_cvt_pk_bf16_f32 v13, v14, v15\n"
"v_mfma_f32_16x16x32_bf16 v[56:59], a[152:155], a[200:203], v[56:59]\n"
"v_cvt_pk_bf16_f32 v14, v140, v141\n"
"v_cvt_pk_bf16_f32 v15, v142, v143\n"
"v_accvgpr_write_b32 a12, v12\n"
"v_mfma_f32_16x16x32_bf16 v[60:63], a[152:155], a[204:207], v[60:63]\n"
"v_accvgpr_write_b32 a13, v13\n"
"v_accvgpr_write_b32 a14, v14\n"
"v_accvgpr_write_b32 a15, v15\n"
"v_mfma_f32_16x16x32_bf16 v[40:43], a[176:179], a[156:159], v[40:43]\n"
"s_add_u32 s53, s54, 0\n"
"buffer_store_dwordx4 a[0:3], v254, s[84:87], s53 offen offset:0 sc1\n"
"v_cvt_pk_bf16_f32 v16, v16, v17\n"
"v_mfma_f32_16x16x32_bf16 v[168:171], a[180:183], a[156:159], v[168:171]\n"
"v_cvt_pk_bf16_f32 v17, v18, v19\n"
"v_cvt_pk_bf16_f32 v18, v144, v145\n"
"v_cvt_pk_bf16_f32 v19, v146, v147\n"
".Lpad7_%=:\n"
"s_cbranch_execz .Lpad11_%=\n"
"v_mfma_f32_16x16x32_bf16 v[44:47], a[184:187], a[156:159], v[44:47]\n"
"v_accvgpr_write_b32 a16, v16\n"
"v_accvgpr_write_b32 a17, v17\n"
"v_accvgpr_write_b32 a18, v18\n"
"v_mfma_f32_16x16x32_bf16 v[172:175], a[188:191], a[156:159], v[172:175]\n"
"v_accvgpr_write_b32 a19, v19\n"
"v_cvt_pk_bf16_f32 v20, v20, v21\n"
"v_cvt_pk_bf16_f32 v21, v22, v23\n"
"v_mfma_f32_16x16x32_bf16 a[48:51], a[156:159], a[192:195], a[48:51]\n"
"v_cvt_pk_bf16_f32 v22, v148, v149\n"
"v_cvt_pk_bf16_f32 v23, v150, v151\n"
"v_accvgpr_write_b32 a20, v20\n"
"v_mfma_f32_16x16x32_bf16 a[52:55], a[156:159], a[196:199], a[52:55]\n"
"v_accvgpr_write_b32 a21, v21\n"
"v_accvgpr_write_b32 a22, v22\n"
"v_accvgpr_write_b32 a23, v23\n"
"v_mfma_f32_16x16x32_bf16 a[56:59], a[156:159], a[200:203], a[56:59]\n"
"v_cvt_pk_bf16_f32 v24, v24, v25\n"
"v_cvt_pk_bf16_f32 v25, v26, v27\n"
"v_cvt_pk_bf16_f32 v26, v152, v153\n"
"v_mfma_f32_16x16x32_bf16 a[60:63], a[156:159], a[204:207], a[60:63]\n"
"v_cvt_pk_bf16_f32 v27, v154, v155\n"
"v_accvgpr_write_b32 a24, v24\n"
"v_accvgpr_write_b32 a25, v25\n"
"v_mfma_f32_16x16x32_bf16 v[64:67], a[176:179], a[160:163], v[64:67]\n"
"s_add_u32 s53, s54, 0\n"
"v_accvgpr_write_b32 a26, v26\n"
"v_accvgpr_write_b32 a27, v27\n"
"v_cvt_pk_bf16_f32 v28, v28, v29\n"
"v_mfma_f32_16x16x32_bf16 a[64:67], a[180:183], a[160:163], a[64:67]\n"
"buffer_store_dwordx4 a[4:7], v254, s[84:87], s53 offen offset:1024 sc1\n"
"v_cvt_pk_bf16_f32 v29, v30, v31\n"
"v_mfma_f32_16x16x32_bf16 v[68:71], a[184:187], a[160:163], v[68:71]\n"
"v_cvt_pk_bf16_f32 v30, v156, v157\n"
"v_cvt_pk_bf16_f32 v31, v158, v159\n"
"v_accvgpr_write_b32 a28, v28\n"
"v_mfma_f32_16x16x32_bf16 a[68:71], a[188:191], a[160:163], a[68:71]\n"
"v_accvgpr_write_b32 a29, v29\n"
"v_accvgpr_write_b32 a30, v30\n"
"v_accvgpr_write_b32 a31, v31\n"
"v_mfma_f32_16x16x32_bf16 v[80:83], a[160:163], a[192:195], v[80:83]\n"
"v_cvt_pk_bf16_f32 v32, v32, v33\n"
"v_cvt_pk_bf16_f32 v33, v34, v35\n"
"v_cvt_pk_bf16_f32 v34, v160, v161\n"
"v_mfma_f32_16x16x32_bf16 v[84:87], a[160:163], a[196:199], v[84:87]\n"
"v_cvt_pk_bf16_f32 v35, v162, v163\n"
"v_accvgpr_write_b32 a32, v32\n"
"v_accvgpr_write_b32 a33, v33\n"
"v_mfma_f32_16x16x32_bf16 v[88:91], a[160:163], a[200:203], v[88:91]\n"
"v_accvgpr_write_b32 a34, v34\n"
"v_accvgpr_write_b32 a35, v35\n"
"v_cvt_pk_bf16_f32 v36, v36, v37\n"
"v_mfma_f32_16x16x32_bf16 v[92:95], a[160:163], a[204:207], v[92:95]\n"
"v_cvt_pk_bf16_f32 v37, v38, v39\n"
"v_cvt_pk_bf16_f32 v38, v164, v165\n"
"v_cvt_pk_bf16_f32 v39, v166, v167\n"
"v_mfma_f32_16x16x32_bf16 v[72:75], a[176:179], a[164:167], v[72:75]\n"
"v_accvgpr_write_b32 a36, v36\n"
"v_accvgpr_write_b32 a37, v37\n"
"v_accvgpr_write_b32 a38, v38\n"
"v_mfma_f32_16x16x32_bf16 a[72:75], a[180:183], a[164:167], a[72:75]\n"
"s_add_u32 s53, s54, 4096\n"
"v_accvgpr_write_b32 a39, v39\n"
"v_cvt_pk_bf16_f32 v40, v40, v41\n"
"v_cvt_pk_bf16_f32 v41, v42, v43\n"
"v_mfma_f32_16x16x32_bf16 v[76:79], a[184:187], a[164:167], v[76:79]\n"
"buffer_store_dwordx4 a[8:11], v254, s[84:87], s53 offen offset:0 sc1\n"
"v_cvt_pk_bf16_f32 v42, v168, v169\n"
"v_mfma_f32_16x16x32_bf16 a[76:79], a[188:191], a[164:167], a[76:79]\n"
"v_cvt_pk_bf16_f32 v43, v170, v171\n"
"v_accvgpr_write_b32 a40, v40\n"
"v_accvgpr_write_b32 a41, v41\n"
"v_mfma_f32_16x16x32_bf16 a[80:83], a[164:167], a[192:195], a[80:83]\n"
"v_accvgpr_write_b32 a42, v42\n"
"v_accvgpr_write_b32 a43, v43\n"
"v_cvt_pk_bf16_f32 v44, v44, v45\n"
"v_mfma_f32_16x16x32_bf16 a[84:87], a[164:167], a[196:199], a[84:87]\n"
"v_cvt_pk_bf16_f32 v45, v46, v47\n"
"v_cvt_pk_bf16_f32 v46, v172, v173\n"
"v_cvt_pk_bf16_f32 v47, v174, v175\n"
"v_mfma_f32_16x16x32_bf16 a[88:91], a[164:167], a[200:203], a[88:91]\n"
"v_accvgpr_write_b32 a44, v44\n"
"v_accvgpr_write_b32 a45, v45\n"
"v_accvgpr_write_b32 a46, v46\n"
"v_mfma_f32_16x16x32_bf16 a[92:95], a[164:167], a[204:207], a[92:95]\n"
"v_accvgpr_write_b32 a47, v47\n"
"v_cvt_pk_bf16_f32 v48, v48, v49\n"
"v_cvt_pk_bf16_f32 v49, v50, v51\n"
"v_mfma_f32_16x16x32_bf16 v[96:99], a[176:179], a[168:171], v[96:99]\n"
"v_accvgpr_read_b32 v50, a48\n"
"v_accvgpr_read_b32 v51, a49\n"
"v_accvgpr_write_b32 a48, v48\n"
"v_mfma_f32_16x16x32_bf16 a[96:99], a[180:183], a[168:171], a[96:99]\n"
"v_accvgpr_write_b32 a49, v49\n"
"v_cvt_pk_bf16_f32 v50, v50, v51\n"
"v_accvgpr_read_b32 v48, a50\n"
"v_mfma_f32_16x16x32_bf16 v[100:103], a[184:187], a[168:171], v[100:103]\n"
"s_waitcnt vmcnt(12)\n"
"ds_read_b64_tr_b16 a[254:255], v239 offset:33792\n"
"s_add_u32 s53, s54, 4096\n"
"v_accvgpr_read_b32 v49, a51\n"
"v_mfma_f32_16x16x32_bf16 a[100:103], a[188:191], a[168:171], a[100:103]\n"
"buffer_store_dwordx4 a[12:15], v254, s[84:87], s53 offen offset:1024 sc1\n"
"v_accvgpr_write_b32 a50, v50\n"
"v_mfma_f32_16x16x32_bf16 v[112:115], a[168:171], a[192:195], v[112:115]\n"
"v_cvt_pk_bf16_f32 v48, v48, v49\n"
"v_accvgpr_write_b32 a51, v48\n"
"v_cvt_pk_bf16_f32 v52, v52, v53\n"
"v_mfma_f32_16x16x32_bf16 v[116:119], a[168:171], a[196:199], v[116:119]\n"
"v_cvt_pk_bf16_f32 v53, v54, v55\n"
"v_accvgpr_read_b32 v54, a52\n"
"v_accvgpr_read_b32 v55, a53\n"
"v_mfma_f32_16x16x32_bf16 v[120:123], a[168:171], a[200:203], v[120:123]\n"
"v_accvgpr_write_b32 a52, v52\n"
"v_accvgpr_write_b32 a53, v53\n"
"v_cvt_pk_bf16_f32 v54, v54, v55\n"
"v_mfma_f32_16x16x32_bf16 v[124:127], a[168:171], a[204:207], v[124:127]\n"
"v_accvgpr_read_b32 v52, a54\n"
"v_accvgpr_read_b32 v53, a55\n"
"v_accvgpr_write_b32 a54, v54\n"
"v_mfma_f32_16x16x32_bf16 v[104:107], a[176:179], a[172:175], v[104:107]\n"
"v_cvt_pk_bf16_f32 v52, v52, v53\n"
"v_accvgpr_write_b32 a55, v52\n"
"v_cvt_pk_bf16_f32 v56, v56, v57\n"
"v_mfma_f32_16x16x32_bf16 a[104:107], a[180:183], a[172:175], a[104:107]\n"
"v_cvt_pk_bf16_f32 v57, v58, v59\n"
"v_accvgpr_read_b32 v58, a56\n"
"v_accvgpr_read_b32 v59, a57\n"
"v_mfma_f32_16x16x32_bf16 v[108:111], a[184:187], a[172:175], v[108:111]\n"
"v_accvgpr_write_b32 a56, v56\n"
"v_accvgpr_write_b32 a57, v57\n"
"v_cvt_pk_bf16_f32 v58, v58, v59\n"
"v_mfma_f32_16x16x32_bf16 a[108:111], a[188:191], a[172:175], a[108:111]\n"
"s_add_u32 s53, s55, 0\n"
"v_accvgpr_read_b32 v56, a58\n"
"v_accvgpr_read_b32 v57, a59\n"
"v_accvgpr_write_b32 a58, v58\n"
"v_mfma_f32_16x16x32_bf16 a[112:115], a[172:175], a[192:195], a[112:115]\n"
"buffer_store_dwordx4 a[16:19], v254, s[84:87], s53 offen offset:0 sc1\n"
"v_cvt_pk_bf16_f32 v56, v56, v57\n"
"v_mfma_f32_16x16x32_bf16 a[116:119], a[172:175], a[196:199], a[116:119]\n"
"v_accvgpr_write_b32 a59, v56\n"
"v_cvt_pk_bf16_f32 v60, v60, v61\n"
"v_cvt_pk_bf16_f32 v61, v62, v63\n"
"v_mfma_f32_16x16x32_bf16 a[120:123], a[172:175], a[200:203], a[120:123]\n"
"v_cvt_pk_bf16_f32 v231, v200, v204\n"
"v_accvgpr_read_b32 v62, a60\n"
"v_accvgpr_read_b32 v63, a61\n"
"v_mfma_f32_16x16x32_bf16 a[124:127], a[172:175], a[204:207], a[124:127]\n"
"s_waitcnt lgkmcnt(0)\n"
"v_accvgpr_write_b32 a60, v60\n"
"v_accvgpr_write_b32 a61, v61\n"
"v_cvt_pk_bf16_f32 v62, v62, v63\n"
"v_mfma_f32_16x16x32_bf16 v[176:179], a[224:227], a[208:211], 0\n"
"s_add_u32 m0, s89, 36864\n"
"ds_read_b128 a[144:147], v243 offset:0\n"
"s_mov_b32 s52, 41216\n"
"v_accvgpr_read_b32 v60, a62\n"
"v_mfma_f32_16x16x32_bf16 v[180:183], a[224:227], a[212:215], 0\n"
"buffer_load_dwordx4 v244, s[44:47], s52 offen offset:0 lds\n"
"v_accvgpr_read_b32 v61, a63\n"
"v_mfma_f32_16x16x32_bf16 v[184:187], a[224:227], a[216:219], 0\n"
"ds_read_b128 a[148:151], v243 offset:2048\n"
"v_accvgpr_write_b32 a62, v62\n"
"v_cvt_pk_bf16_f32 v60, v60, v61\n"
"v_mfma_f32_16x16x32_bf16 v[188:191], a[224:227], a[220:223], 0\n"
"s_mov_b32 s52, 50432\n"
"ds_read_b128 a[152:155], v243 offset:4096\n"
"v_accvgpr_write_b32 a63, v60\n"
"v_cvt_pk_bf16_f32 v96, v96, v97\n"
"v_mfma_f32_16x16x32_bf16 v[192:195], a[228:231], a[208:211], 0\n"
"buffer_load_dwordx4 v245, s[44:47], s52 offen offset:1024 lds\n"
"ds_read_b128 a[156:159], v243 offset:6144\n"
"v_mfma_f32_16x16x32_bf16 v[196:199], a[228:231], a[212:215], 0\n"
"ds_read_b64_tr_b16 a[160:161], v232 offset:40960\n"
"v_cvt_pk_bf16_f32 v97, v98, v99\n"
"v_accvgpr_read_b32 v98, a96\n"
"v_mfma_f32_16x16x32_bf16 v[200:203], a[228:231], a[216:219], 0\n"
"s_mov_b32 s52, 59648\n"
"ds_read_b64_tr_b16 a[162:163], v232 offset:41984\n"
"v_accvgpr_read_b32 v99, a97\n"
"v_accvgpr_write_b32 a96, v96\n"
"v_mfma_f32_16x16x32_bf16 v[204:207], a[228:231], a[220:223], 0\n"
"buffer_load_dwordx4 v244, s[44:47], s52 offen offset:2048 lds\n"
"ds_read_b64_tr_b16 a[164:165], v233 offset:40960\n"
"v_mfma_f32_16x16x32_bf16 v[208:211], a[232:235], a[208:211], 0\n"
"v_accvgpr_write_b32 a97, v97\n"
"v_cvt_pk_bf16_f32 v98, v98, v99\n"
"v_accvgpr_read_b32 v96, a98\n"
"v_mfma_f32_16x16x32_bf16 v[128:131], a[232:235], a[212:215], 0\n"
"s_mov_b32 s52, 68864\n"
"ds_read_b64_tr_b16 a[166:167], v233 offset:41984\n"
"v_accvgpr_read_b32 v97, a99\n"
"v_accvgpr_write_b32 a98, v98\n"
"v_mfma_f32_16x16x32_bf16 v[0:3], a[232:235], a[216:219], 0\n"
"buffer_load_dwordx4 v245, s[44:47], s52 offen offset:3072 lds\n"
"ds_read_b64_tr_b16 a[168:169], v234 offset:40960\n"
"v_mfma_f32_16x16x32_bf16 v[132:135], a[232:235], a[220:223], 0\n"
"ds_read_b64_tr_b16 a[170:171], v234 offset:41984\n"
"v_cvt_pk_bf16_f32 v96, v96, v97\n"
"v_accvgpr_write_b32 a99, v96\n"
"v_mfma_f32_16x16x32_bf16 v[4:7], a[236:239], a[208:211], 0\n"
"s_add_u32 m0, s57, 81920\n"
"ds_read_b64_tr_b16 a[172:173], v235 offset:40960\n"
"s_mov_b32 s52, 1572864\n"
"v_cvt_pk_bf16_f32 v100, v100, v101\n"
"v_mfma_f32_16x16x32_bf16 v[136:139], a[236:239], a[212:215], 0\n"
"s_add_u32 s53, s55, 0\n"
"buffer_load_dwordx4 v246, s[48:51], s52 offen offset:0 lds\n"
"ds_read_b64_tr_b16 a[174:175], v235 offset:41984\n"
"v_mfma_f32_16x16x32_bf16 v[8:11], a[236:239], a[216:219], 0\n"
"buffer_store_dwordx4 a[20:23], v254, s[84:87], s53 offen offset:1024 sc1\n"
"ds_read_b64_tr_b16 a[176:177], v236 offset:40960\n"
"v_mfma_f32_16x16x32_bf16 v[140:143], a[236:239], a[220:223], 0\n"
"ds_read_b64_tr_b16 a[178:179], v236 offset:41984\n"
"v_cvt_pk_bf16_f32 v101, v102, v103\n"
"v_accvgpr_read_b32 v102, a100\n"
"v_mfma_f32_16x16x32_bf16 v[12:15], a[240:243], a[208:211], 0\n"
"s_mov_b32 s52, 1604608\n"
"v_accvgpr_read_b32 v103, a101\n"
"v_accvgpr_write_b32 a100, v100\n"
"v_accvgpr_write_b32 a101, v101\n"
"v_mfma_f32_16x16x32_bf16 v[144:147], a[240:243], a[212:215], 0\n"
"buffer_load_dwordx4 v246, s[48:51], s52 offen offset:1024 lds\n"
"ds_read_b64_tr_b16 a[180:181], v237 offset:40960\n"
"v_mfma_f32_16x16x32_bf16 v[16:19], a[240:243], a[216:219], 0\n"
"ds_read_b64_tr_b16 a[182:183], v237 offset:41984\n"
"v_cvt_pk_bf16_f32 v102, v102, v103\n"
"v_accvgpr_read_b32 v100, a102\n"
"v_mfma_f32_16x16x32_bf16 v[148:151], a[240:243], a[220:223], 0\n"
"s_mov_b32 s52, 1636352\n"
"ds_read_b64_tr_b16 a[184:185], v238 offset:40960\n"
"v_accvgpr_read_b32 v101, a103\n"
"v_accvgpr_write_b32 a102, v102\n"
"v_mfma_f32_16x16x32_bf16 v[20:23], a[244:247], a[208:211], 0\n"
"buffer_load_dwordx4 v247, s[48:51], s52 offen offset:2048 lds\n"
"ds_read_b64_tr_b16 a[186:187], v238 offset:41984\n"
"v_mfma_f32_16x16x32_bf16 v[152:155], a[244:247], a[212:215], 0\n"
"ds_read_b64_tr_b16 a[188:189], v239 offset:40960\n"
"v_cvt_pk_bf16_f32 v100, v100, v101\n"
"v_accvgpr_write_b32 a103, v100\n"
"v_mfma_f32_16x16x32_bf16 v[24:27], a[244:247], a[216:219], 0\n"
"s_mov_b32 s52, 1668096\n"
"ds_read_b64_tr_b16 a[190:191], v239 offset:41984\n"
"v_cvt_pk_bf16_f32 v104, v104, v105\n"
"v_cvt_pk_bf16_f32 v105, v106, v107\n"
"v_mfma_f32_16x16x32_bf16 v[156:159], a[244:247], a[220:223], 0\n"
"buffer_load_dwordx4 v247, s[48:51], s52 offen offset:3072 lds\n"
"v_accvgpr_read_b32 v106, a104\n"
"v_mfma_f32_16x16x32_bf16 v[28:31], a[248:251], a[208:211], 0\n"
"v_accvgpr_read_b32 v107, a105\n"
"v_accvgpr_write_b32 a104, v104\n"
"v_accvgpr_write_b32 a105, v105\n"
"v_mfma_f32_16x16x32_bf16 v[160:163], a[248:251], a[212:215], 0\n"
"v_cvt_pk_bf16_f32 v106, v106, v107\n"
"v_accvgpr_read_b32 v104, a106\n"
"v_accvgpr_read_b32 v105, a107\n"
"v_mfma_f32_16x16x32_bf16 v[32:35], a[248:251], a[216:219], 0\n"
"v_accvgpr_write_b32 a106, v106\n"
"v_cvt_pk_bf16_f32 v104, v104, v105\n"
"v_accvgpr_write_b32 a107, v104\n"
"v_mfma_f32_16x16x32_bf16 v[164:167], a[248:251], a[220:223], 0\n"
"v_cvt_pk_bf16_f32 v108, v108, v109\n"
"v_cvt_pk_bf16_f32 v109, v110, v111\n"
"v_accvgpr_read_b32 v110, a108\n"
"v_mfma_f32_16x16x32_bf16 v[36:39], a[252:255], a[208:211], 0\n"
"s_add_u32 s53, s55, 0\n"
"v_accvgpr_read_b32 v111, a109\n"
"v_accvgpr_write_b32 a108, v108\n"
"v_accvgpr_write_b32 a109, v109\n"
"v_mfma_f32_16x16x32_bf16 v[168:171], a[252:255], a[212:215], 0\n"
"buffer_store_dwordx4 a[24:27], v254, s[84:87], s53 offen offset:2048 sc1\n"
"v_cvt_pk_bf16_f32 v110, v110, v111\n"
"v_mfma_f32_16x16x32_bf16 v[40:43], a[252:255], a[216:219], 0\n"
"v_accvgpr_read_b32 v108, a110\n"
"v_accvgpr_read_b32 v109, a111\n"
"v_accvgpr_write_b32 a110, v110\n"
"v_mfma_f32_16x16x32_bf16 v[172:175], a[252:255], a[220:223], 0\n"
"s_waitcnt lgkmcnt(0)\n"
"v_cvt_pk_bf16_f32 v108, v108, v109\n"
"v_accvgpr_write_b32 a111, v108\n"
"v_cvt_pk_bf16_f32 v112, v112, v113\n"
"v_mfma_f32_16x16x32_bf16 v[176:179], a[160:163], a[144:147], v[176:179]\n"
"s_waitcnt vmcnt(25)\n"
"v_cvt_pk_bf16_f32 v113, v114, v115\n"
"v_accvgpr_read_b32 v114, a112\n"
"v_accvgpr_read_b32 v115, a113\n"
"v_mfma_f32_16x16x32_bf16 v[180:183], a[160:163], a[148:151], v[180:183]\n"
"s_barrier\n"
"v_accvgpr_write_b32 a112, v112\n"
"v_accvgpr_write_b32 a113, v113\n"
"v_cvt_pk_bf16_f32 v114, v114, v115\n"
"v_mfma_f32_16x16x32_bf16 v[184:187], a[160:163], a[152:155], v[184:187]\n"
"s_add_u32 m0, s89, 131072\n"
"ds_read_b128 a[208:211], v240 offset:0\n"
"s_mov_b32 s52, 384\n"
"v_accvgpr_read_b32 v112, a114\n"
"v_mfma_f32_16x16x32_bf16 v[188:191], a[160:163], a[156:159], v[188:191]\n"
"buffer_load_dwordx4 v244, s[44:47], s52 offen offset:0 lds\n"
"v_accvgpr_read_b32 v113, a115\n"
"v_mfma_f32_16x16x32_bf16 v[192:195], a[164:167], a[144:147], v[192:195]\n"
"s_mov_b32 s52, 9600\n"
"ds_read_b128 a[212:215], v240 offset:2048\n"
"v_accvgpr_write_b32 a114, v114\n"
"v_cvt_pk_bf16_f32 v112, v112, v113\n"
"v_mfma_f32_16x16x32_bf16 v[196:199], a[164:167], a[148:151], v[196:199]\n"
"buffer_load_dwordx4 v245, s[44:47], s52 offen offset:1024 lds\n"
"ds_read_b128 a[216:219], v240 offset:4096\n"
"v_mfma_f32_16x16x32_bf16 v[200:203], a[164:167], a[152:155], v[200:203]\n"
"ds_read_b128 a[220:223], v240 offset:6144\n"
"v_accvgpr_write_b32 a115, v112\n"
"v_cvt_pk_bf16_f32 v116, v116, v117\n"
"v_mfma_f32_16x16x32_bf16 v[204:207], a[164:167], a[156:159], v[204:207]\n"
"s_mov_b32 s52, 18816\n"
"ds_read_b64_tr_b16 a[224:225], v232 offset:49152\n"
"v_cvt_pk_bf16_f32 v117, v118, v119\n"
"v_accvgpr_read_b32 v118, a116\n"
"v_mfma_f32_16x16x32_bf16 v[208:211], a[168:171], a[144:147], v[208:211]\n"
"buffer_load_dwordx4 v244, s[44:47], s52 offen offset:2048 lds\n"
"ds_read_b64_tr_b16 a[226:227], v232 offset:50176\n"
"v_mfma_f32_16x16x32_bf16 v[128:131], a[168:171], a[148:151], v[128:131]\n"
"ds_read_b64_tr_b16 a[228:229], v233 offset:49152\n"
"v_accvgpr_read_b32 v119, a117\n"
"v_accvgpr_write_b32 a116, v116\n"
"v_mfma_f32_16x16x32_bf16 v[0:3], a[168:171], a[152:155], v[0:3]\n"
"s_mov_b32 s52, 28032\n"
"ds_read_b64_tr_b16 a[230:231], v233 offset:50176\n"
"v_accvgpr_write_b32 a117, v117\n"
"v_cvt_pk_bf16_f32 v118, v118, v119\n"
"v_mfma_f32_16x16x32_bf16 v[132:135], a[168:171], a[156:159], v[132:135]\n"
"s_add_u32 s53, s55, 0\n"
"buffer_load_dwordx4 v245, s[44:47], s52 offen offset:3072 lds\n"
"ds_read_b64_tr_b16 a[232:233], v234 offset:49152\n"
"v_mfma_f32_16x16x32_bf16 v[4:7], a[172:175], a[144:147], v[4:7]\n"
"buffer_store_dwordx4 a[28:31], v254, s[84:87], s53 offen offset:3072 sc1\n"
"ds_read_b64_tr_b16 a[234:235], v234 offset:50176\n"
"v_mfma_f32_16x16x32_bf16 v[136:139], a[172:175], a[148:151], v[136:139]\n"
"s_add_u32 m0, s89, 135168\n"
"ds_read_b64_tr_b16 a[236:237], v235 offset:49152\n"
"s_mov_b32 s52, 41344\n"
"v_accvgpr_read_b32 v116, a118\n"
"v_mfma_f32_16x16x32_bf16 v[8:11], a[172:175], a[152:155], v[8:11]\n"
"buffer_load_dwordx4 v244, s[44:47], s52 offen offset:0 lds\n"
"ds_read_b64_tr_b16 a[238:239], v235 offset:50176\n"
"v_mfma_f32_16x16x32_bf16 v[140:143], a[172:175], a[156:159], v[140:143]\n"
"ds_read_b64_tr_b16 a[240:241], v236 offset:49152\n"
"v_accvgpr_read_b32 v117, a119\n"
"v_accvgpr_write_b32 a118, v118\n"
"v_mfma_f32_16x16x32_bf16 v[12:15], a[176:179], a[144:147], v[12:15]\n"
"s_mov_b32 s52, 50560\n"
"ds_read_b64_tr_b16 a[242:243], v236 offset:50176\n"
"v_cvt_pk_bf16_f32 v116, v116, v117\n"
"v_accvgpr_write_b32 a119, v116\n"
"v_mfma_f32_16x16x32_bf16 v[144:147], a[176:179], a[148:151], v[144:147]\n"
"buffer_load_dwordx4 v245, s[44:47], s52 offen offset:1024 lds\n"
"ds_read_b64_tr_b16 a[244:245], v237 offset:49152\n"
"v_mfma_f32_16x16x32_bf16 v[16:19], a[176:179], a[152:155], v[16:19]\n"
"ds_read_b64_tr_b16 a[246:247], v237 offset:50176\n"
"v_cvt_pk_bf16_f32 v120, v120, v121\n"
"v_cvt_pk_bf16_f32 v121, v122, v123\n"
"v_mfma_f32_16x16x32_bf16 v[148:151], a[176:179], a[156:159], v[148:151]\n"
"s_mov_b32 s52, 59776\n"
"ds_read_b64_tr_b16 a[248:249], v238 offset:49152\n"
"v_accvgpr_read_b32 v122, a120\n"
"v_accvgpr_read_b32 v123, a121\n"
"v_mfma_f32_16x16x32_bf16 v[20:23], a[180:183], a[144:147], v[20:23]\n"
"buffer_load_dwordx4 v244, s[44:47], s52 offen offset:2048 lds\n"
"ds_read_b64_tr_b16 a[250:251], v238 offset:50176\n"
"v_mfma_f32_16x16x32_bf16 v[152:155], a[180:183], a[148:151], v[152:155]\n"
"ds_read_b64_tr_b16 a[252:253], v239 offset:49152\n"
"v_accvgpr_write_b32 a120, v120\n"
"v_accvgpr_write_b32 a121, v121\n"
"v_mfma_f32_16x16x32_bf16 v[24:27], a[180:183], a[152:155], v[24:27]\n"
"s_mov_b32 s52, 68992\n"
"ds_read_b64_tr_b16 a[254:255], v239 offset:50176\n"
"v_cvt_pk_bf16_f32 v122, v122, v123\n"
"v_accvgpr_read_b32 v120, a122\n"
"v_mfma_f32_16x16x32_bf16 v[156:159], a[180:183], a[156:159], v[156:159]\n"
"buffer_load_dwordx4 v245, s[44:47], s52 offen offset:3072 lds\n"
"v_accvgpr_read_b32 v121, a123\n"
"v_mfma_f32_16x16x32_bf16 v[28:31], a[184:187], a[144:147], v[28:31]\n"
"v_accvgpr_write_b32 a122, v122\n"
"v_cvt_pk_bf16_f32 v120, v120, v121\n"
"v_accvgpr_write_b32 a123, v120\n"
"v_mfma_f32_16x16x32_bf16 v[160:163], a[184:187], a[148:151], v[160:163]\n"
"v_cvt_pk_bf16_f32 v124, v124, v125\n"
"v_cvt_pk_bf16_f32 v125, v126, v127\n"
"v_accvgpr_read_b32 v126, a124\n"
"v_mfma_f32_16x16x32_bf16 v[32:35], a[184:187], a[152:155], v[32:35]\n"
"s_add_u32 s53, s54, 131072\n"
"v_accvgpr_read_b32 v127, a125\n"
"v_accvgpr_write_b32 a124, v124\n"
"v_accvgpr_write_b32 a125, v125\n"
"v_mfma_f32_16x16x32_bf16 v[164:167], a[184:187], a[156:159], v[164:167]\n"
"buffer_store_dwordx4 a[32:35], v254, s[84:87], s53 offen offset:0 sc1\n"
"v_cvt_pk_bf16_f32 v126, v126, v127\n"
"v_mfma_f32_16x16x32_bf16 v[36:39], a[188:191], a[144:147], v[36:39]\n"
"v_accvgpr_read_b32 v124, a126\n"
"v_accvgpr_read_b32 v125, a127\n"
"v_accvgpr_write_b32 a126, v126\n"
"v_mfma_f32_16x16x32_bf16 v[168:171], a[188:191], a[148:151], v[168:171]\n"
"v_cvt_pk_bf16_f32 v124, v124, v125\n"
"v_accvgpr_write_b32 a127, v124\n"
"v_cvt_pk_bf16_f32 v64, v64, v65\n"
"v_mfma_f32_16x16x32_bf16 v[40:43], a[188:191], a[152:155], v[40:43]\n"
"v_cvt_pk_bf16_f32 v65, v66, v67\n"
"v_accvgpr_read_b32 v66, a64\n"
"v_accvgpr_read_b32 v67, a65\n"
"v_mfma_f32_16x16x32_bf16 v[172:175], a[188:191], a[156:159], v[172:175]\n"
"s_waitcnt lgkmcnt(0)\n"
"v_accvgpr_write_b32 a64, v64\n"
"v_accvgpr_write_b32 a65, v65\n"
"v_cvt_pk_bf16_f32 v66, v66, v67\n"
"v_mfma_f32_16x16x32_bf16 v[176:179], a[224:227], a[208:211], v[176:179]\n"
"s_add_u32 m0, s57, 98304\n"
"ds_read_b128 a[144:147], v241 offset:0\n"
"s_mov_b32 s52, 2097152\n"
"v_accvgpr_read_b32 v64, a66\n"
"v_mfma_f32_16x16x32_bf16 v[180:183], a[224:227], a[212:215], v[180:183]\n"
"buffer_load_dwordx4 v246, s[48:51], s52 offen offset:0 lds\n"
"v_accvgpr_read_b32 v65, a67\n"
"v_mfma_f32_16x16x32_bf16 v[184:187], a[224:227], a[216:219], v[184:187]\n"
"ds_read_b128 a[148:151], v241 offset:2048\n"
"v_accvgpr_write_b32 a66, v66\n"
"v_cvt_pk_bf16_f32 v64, v64, v65\n"
"v_mfma_f32_16x16x32_bf16 v[188:191], a[224:227], a[220:223], v[188:191]\n"
"ds_read_b128 a[152:155], v241 offset:4096\n"
"v_accvgpr_write_b32 a67, v64\n"
"v_cvt_pk_bf16_f32 v68, v68, v69\n"
"v_mfma_f32_16x16x32_bf16 v[192:195], a[228:231], a[208:211], v[192:195]\n"
"ds_read_b128 a[156:159], v241 offset:6144\n"
"v_cvt_pk_bf16_f32 v69, v70, v71\n"
"v_accvgpr_read_b32 v70, a68\n"
"v_mfma_f32_16x16x32_bf16 v[196:199], a[228:231], a[212:215], v[196:199]\n"
"ds_read_b64_tr_b16 a[160:161], v232 offset:57344\n"
"v_accvgpr_read_b32 v71, a69\n"
"v_accvgpr_write_b32 a68, v68\n"
"v_mfma_f32_16x16x32_bf16 v[200:203], a[228:231], a[216:219], v[200:203]\n"
"s_mov_b32 s52, 2128896\n"
"ds_read_b64_tr_b16 a[162:163], v232 offset:58368\n"
"v_accvgpr_write_b32 a69, v69\n"
"v_cvt_pk_bf16_f32 v70, v70, v71\n"
"v_mfma_f32_16x16x32_bf16 v[204:207], a[228:231], a[220:223], v[204:207]\n"
"buffer_load_dwordx4 v246, s[48:51], s52 offen offset:1024 lds\n"
"ds_read_b64_tr_b16 a[164:165], v233 offset:57344\n"
"v_mfma_f32_16x16x32_bf16 v[208:211], a[232:235], a[208:211], v[208:211]\n"
"v_accvgpr_read_b32 v68, a70\n"
"v_accvgpr_read_b32 v69, a71\n"
"v_accvgpr_write_b32 a70, v70\n"
"v_mfma_f32_16x16x32_bf16 v[128:131], a[232:235], a[212:215], v[128:131]\n"
"s_add_u32 s53, s54, 131072\n"
"ds_read_b64_tr_b16 a[166:167], v233 offset:58368\n"
"v_cvt_pk_bf16_f32 v68, v68, v69\n"
"v_accvgpr_write_b32 a71, v68\n"
"v_mfma_f32_16x16x32_bf16 v[0:3], a[232:235], a[216:219], v[0:3]\n"
"buffer_store_dwordx4 a[36:39], v254, s[84:87], s53 offen offset:1024 sc1\n"
"ds_read_b64_tr_b16 a[168:169], v234 offset:57344\n"
"v_mfma_f32_16x16x32_bf16 v[132:135], a[232:235], a[220:223], v[132:135]\n"
"ds_read_b64_tr_b16 a[170:171], v234 offset:58368\n"
"v_cvt_pk_bf16_f32 v72, v72, v73\n"
"v_cvt_pk_bf16_f32 v73, v74, v75\n"
"v_mfma_f32_16x16x32_bf16 v[4:7], a[236:239], a[208:211], v[4:7]\n"
"s_mov_b32 s52, 2160640\n"
"ds_read_b64_tr_b16 a[172:173], v235 offset:57344\n"
"v_accvgpr_read_b32 v74, a72\n"
"v_accvgpr_read_b32 v75, a73\n"
"v_mfma_f32_16x16x32_bf16 v[136:139], a[236:239], a[212:215], v[136:139]\n"
"buffer_load_dwordx4 v247, s[48:51], s52 offen offset:2048 lds\n"
"ds_read_b64_tr_b16 a[174:175], v235 offset:58368\n"
"v_mfma_f32_16x16x32_bf16 v[8:11], a[236:239], a[216:219], v[8:11]\n"
"ds_read_b64_tr_b16 a[176:177], v236 offset:57344\n"
"v_accvgpr_write_b32 a72, v72\n"
"v_accvgpr_write_b32 a73, v73\n"
"v_mfma_f32_16x16x32_bf16 v[140:143], a[236:239], a[220:223], v[140:143]\n"
"ds_read_b64_tr_b16 a[178:179], v236 offset:58368\n"
"v_cvt_pk_bf16_f32 v74, v74, v75\n"
"v_accvgpr_read_b32 v72, a74\n"
"v_mfma_f32_16x16x32_bf16 v[12:15], a[240:243], a[208:211], v[12:15]\n"
"v_accvgpr_read_b32 v73, a75\n"
"v_accvgpr_write_b32 a74, v74\n"
"v_cvt_pk_bf16_f32 v72, v72, v73\n"
"v_mfma_f32_16x16x32_bf16 v[144:147], a[240:243], a[212:215], v[144:147]\n"
"ds_read_b64_tr_b16 a[180:181], v237 offset:57344\n"
"v_accvgpr_write_b32 a75, v72\n"
"v_cvt_pk_bf16_f32 v76, v76, v77\n"
"v_mfma_f32_16x16x32_bf16 v[16:19], a[240:243], a[216:219], v[16:19]\n"
"ds_read_b64_tr_b16 a[182:183], v237 offset:58368\n"
"v_cvt_pk_bf16_f32 v77, v78, v79\n"
"v_accvgpr_read_b32 v78, a76\n"
"v_mfma_f32_16x16x32_bf16 v[148:151], a[240:243], a[220:223], v[148:151]\n"
"s_mov_b32 s52, 2192384\n"
"ds_read_b64_tr_b16 a[184:185], v238 offset:57344\n"
"v_accvgpr_read_b32 v79, a77\n"
"v_accvgpr_write_b32 a76, v76\n"
"v_mfma_f32_16x16x32_bf16 v[20:23], a[244:247], a[208:211], v[20:23]\n"
"buffer_load_dwordx4 v247, s[48:51], s52 offen offset:3072 lds\n"
"ds_read_b64_tr_b16 a[186:187], v238 offset:58368\n"
"v_mfma_f32_16x16x32_bf16 v[152:155], a[244:247], a[212:215], v[152:155]\n"
"ds_read_b64_tr_b16 a[188:189], v239 offset:57344\n"
"v_accvgpr_write_b32 a77, v77\n"
"v_cvt_pk_bf16_f32 v78, v78, v79\n"
"v_mfma_f32_16x16x32_bf16 v[24:27], a[244:247], a[216:219], v[24:27]\n"
"ds_read_b64_tr_b16 a[190:191], v239 offset:58368\n"
"v_accvgpr_read_b32 v76, a78\n"
"v_accvgpr_read_b32 v77, a79\n"
"v_mfma_f32_16x16x32_bf16 v[156:159], a[244:247], a[220:223], v[156:159]\n"
"v_accvgpr_write_b32 a78, v78\n"
"v_cvt_pk_bf16_f32 v76, v76, v77\n"
"v_accvgpr_write_b32 a79, v76\n"
"v_mfma_f32_16x16x32_bf16 v[28:31], a[248:251], a[208:211], v[28:31]\n"
"s_add_u32 s53, s54, 135168\n"
"v_cvt_pk_bf16_f32 v80, v80, v81\n"
"v_cvt_pk_bf16_f32 v81, v82, v83\n"
"v_accvgpr_read_b32 v82, a80\n"
"v_mfma_f32_16x16x32_bf16 v[160:163], a[248:251], a[212:215], v[160:163]\n"
"buffer_store_dwordx4 a[40:43], v254, s[84:87], s53 offen offset:0 sc1\n"
"v_accvgpr_read_b32 v83, a81\n"
".Lpad8_%=:\n"
"s_cbranch_execz .Lpad12_%=\n"
"v_mfma_f32_16x16x32_bf16 v[32:35], a[248:251], a[216:219], v[32:35]\n"
"v_accvgpr_write_b32 a80, v80\n"
"v_accvgpr_write_b32 a81, v81\n"
"v_cvt_pk_bf16_f32 v82, v82, v83\n"
"v_mfma_f32_16x16x32_bf16 v[164:167], a[248:251], a[220:223], v[164:167]\n"
"v_accvgpr_read_b32 v80, a82\n"
"v_accvgpr_read_b32 v81, a83\n"
"v_accvgpr_write_b32 a82, v82\n"
"v_mfma_f32_16x16x32_bf16 v[36:39], a[252:255], a[208:211], v[36:39]\n"
"v_cvt_pk_bf16_f32 v80, v80, v81\n"
"v_accvgpr_write_b32 a83, v80\n"
"v_cvt_pk_bf16_f32 v84, v84, v85\n"
"v_mfma_f32_16x16x32_bf16 v[168:171], a[252:255], a[212:215], v[168:171]\n"
"v_cvt_pk_bf16_f32 v85, v86, v87\n"
"v_accvgpr_read_b32 v86, a84\n"
"v_accvgpr_read_b32 v87, a85\n"
"v_mfma_f32_16x16x32_bf16 v[40:43], a[252:255], a[216:219], v[40:43]\n"
"v_accvgpr_write_b32 a84, v84\n"
"v_accvgpr_write_b32 a85, v85\n"
"v_cvt_pk_bf16_f32 v86, v86, v87\n"
"v_mfma_f32_16x16x32_bf16 v[172:175], a[252:255], a[220:223], v[172:175]\n"
"s_waitcnt lgkmcnt(0)\n"
"v_accvgpr_read_b32 v84, a86\n"
"v_accvgpr_read_b32 v85, a87\n"
"v_accvgpr_write_b32 a86, v86\n"
"v_mfma_f32_16x16x32_bf16 v[176:179], a[160:163], a[144:147], v[176:179]\n"
"s_waitcnt vmcnt(22)\n"
"v_cvt_pk_bf16_f32 v84, v84, v85\n"
"v_accvgpr_write_b32 a87, v84\n"
"v_cvt_pk_bf16_f32 v88, v88, v89\n"
"v_mfma_f32_16x16x32_bf16 v[180:183], a[160:163], a[148:151], v[180:183]\n"
"s_barrier\n"
"v_cvt_pk_bf16_f32 v89, v90, v91\n"
"v_accvgpr_read_b32 v90, a88\n"
"v_accvgpr_read_b32 v91, a89\n"
"v_mfma_f32_16x16x32_bf16 v[184:187], a[160:163], a[152:155], v[184:187]\n"
"s_add_u32 m0, s89, 0\n"
"ds_read_b128 a[208:211], v240 offset:32768\n"
"s_mov_b32 s52, 512\n"
"v_accvgpr_write_b32 a88, v88\n"
"v_mfma_f32_16x16x32_bf16 v[188:191], a[160:163], a[156:159], v[188:191]\n"
"buffer_load_dwordx4 v244, s[44:47], s52 offen offset:0 lds\n"
"v_accvgpr_write_b32 a89, v89\n"
"v_mfma_f32_16x16x32_bf16 v[192:195], a[164:167], a[144:147], v[192:195]\n"
"s_mov_b32 s52, 9728\n"
"ds_read_b128 a[212:215], v240 offset:34816\n"
"v_cvt_pk_bf16_f32 v90, v90, v91\n"
"v_accvgpr_read_b32 v88, a90\n"
"v_mfma_f32_16x16x32_bf16 v[196:199], a[164:167], a[148:151], v[196:199]\n"
"buffer_load_dwordx4 v245, s[44:47], s52 offen offset:1024 lds\n"
"ds_read_b128 a[216:219], v240 offset:36864\n"
"v_mfma_f32_16x16x32_bf16 v[200:203], a[164:167], a[152:155], v[200:203]\n"
"ds_read_b128 a[220:223], v240 offset:38912\n"
"v_accvgpr_read_b32 v89, a91\n"
"v_accvgpr_write_b32 a90, v90\n"
"v_mfma_f32_16x16x32_bf16 v[204:207], a[164:167], a[156:159], v[204:207]\n"
"s_mov_b32 s52, 18944\n"
"ds_read_b64_tr_b16 a[224:225], v232 offset:0\n"
"s_add_u32 s53, s54, 135168\n"
"v_cvt_pk_bf16_f32 v88, v88, v89\n"
"v_mfma_f32_16x16x32_bf16 v[208:211], a[168:171], a[144:147], v[208:211]\n"
"buffer_load_dwordx4 v244, s[44:47], s52 offen offset:2048 lds\n"
"ds_read_b64_tr_b16 a[226:227], v232 offset:1024\n"
"v_mfma_f32_16x16x32_bf16 v[128:131], a[168:171], a[148:151], v[128:131]\n"
"buffer_store_dwordx4 a[44:47], v254, s[84:87], s53 offen offset:1024 sc1\n"
"ds_read_b64_tr_b16 a[228:229], v233 offset:0\n"
"v_mfma_f32_16x16x32_bf16 v[0:3], a[168:171], a[152:155], v[0:3]\n"
"s_mov_b32 s52, 28160\n"
"ds_read_b64_tr_b16 a[230:231], v233 offset:1024\n"
"v_accvgpr_write_b32 a91, v88\n"
"v_cvt_pk_bf16_f32 v92, v92, v93\n"
"v_mfma_f32_16x16x32_bf16 v[132:135], a[168:171], a[156:159], v[132:135]\n"
"buffer_load_dwordx4 v245, s[44:47], s52 offen offset:3072 lds\n"
"ds_read_b64_tr_b16 a[232:233], v234 offset:0\n"
"v_mfma_f32_16x16x32_bf16 v[4:7], a[172:175], a[144:147], v[4:7]\n"
"ds_read_b64_tr_b16 a[234:235], v234 offset:1024\n"
"v_cvt_pk_bf16_f32 v93, v94, v95\n"
"v_accvgpr_read_b32 v94, a92\n"
"v_mfma_f32_16x16x32_bf16 v[136:139], a[172:175], a[148:151], v[136:139]\n"
"s_add_u32 m0, s89, 4096\n"
"ds_read_b64_tr_b16 a[236:237], v235 offset:0\n"
"s_mov_b32 s52, 41472\n"
"v_accvgpr_read_b32 v95, a93\n"
"v_mfma_f32_16x16x32_bf16 v[8:11], a[172:175], a[152:155], v[8:11]\n"
"buffer_load_dwordx4 v244, s[44:47], s52 offen offset:0 lds\n"
"ds_read_b64_tr_b16 a[238:239], v235 offset:1024\n"
"v_mfma_f32_16x16x32_bf16 v[140:143], a[172:175], a[156:159], v[140:143]\n"
"ds_read_b64_tr_b16 a[240:241], v236 offset:0\n"
"v_accvgpr_write_b32 a92, v92\n"
"v_accvgpr_write_b32 a93, v93\n"
"v_mfma_f32_16x16x32_bf16 v[12:15], a[176:179], a[144:147], v[12:15]\n"
"s_mov_b32 s52, 50688\n"
"ds_read_b64_tr_b16 a[242:243], v236 offset:1024\n"
"v_cvt_pk_bf16_f32 v94, v94, v95\n"
"v_accvgpr_read_b32 v92, a94\n"
"v_mfma_f32_16x16x32_bf16 v[144:147], a[176:179], a[148:151], v[144:147]\n"
"buffer_load_dwordx4 v245, s[44:47], s52 offen offset:1024 lds\n"
"ds_read_b64_tr_b16 a[244:245], v237 offset:0\n"
"v_mfma_f32_16x16x32_bf16 v[16:19], a[176:179], a[152:155], v[16:19]\n"
"ds_read_b64_tr_b16 a[246:247], v237 offset:1024\n"
"v_accvgpr_read_b32 v93, a95\n"
"v_accvgpr_write_b32 a94, v94\n"
"v_mfma_f32_16x16x32_bf16 v[148:151], a[176:179], a[156:159], v[148:151]\n"
"s_mov_b32 s52, 59904\n"
"ds_read_b64_tr_b16 a[248:249], v238 offset:0\n"
"v_cvt_pk_bf16_f32 v92, v92, v93\n"
"v_accvgpr_write_b32 a95, v92\n"
"v_mfma_f32_16x16x32_bf16 v[20:23], a[180:183], a[144:147], v[20:23]\n"
"buffer_load_dwordx4 v244, s[44:47], s52 offen offset:2048 lds\n"
"ds_read_b64_tr_b16 a[250:251], v238 offset:1024\n"
"v_mfma_f32_16x16x32_bf16 v[152:155], a[180:183], a[148:151], v[152:155]\n"
"ds_read_b64_tr_b16 a[252:253], v239 offset:0\n"
"v_mfma_f32_16x16x32_bf16 v[24:27], a[180:183], a[152:155], v[24:27]\n"
"s_mov_b32 s52, 69120\n"
"ds_read_b64_tr_b16 a[254:255], v239 offset:1024\n"
"s_add_u32 s53, s55, 131072\n"
"v_mfma_f32_16x16x32_bf16 v[156:159], a[180:183], a[156:159], v[156:159]\n"
"buffer_load_dwordx4 v245, s[44:47], s52 offen offset:3072 lds\n"
"v_mfma_f32_16x16x32_bf16 v[28:31], a[184:187], a[144:147], v[28:31]\n"
"buffer_store_dwordx4 a[48:51], v254, s[84:87], s53 offen offset:0 sc1\n"
"v_mfma_f32_16x16x32_bf16 v[160:163], a[184:187], a[148:151], v[160:163]\n"
"v_mfma_f32_16x16x32_bf16 v[32:35], a[184:187], a[152:155], v[32:35]\n"
"v_mfma_f32_16x16x32_bf16 v[164:167], a[184:187], a[156:159], v[164:167]\n"
"v_mfma_f32_16x16x32_bf16 v[36:39], a[188:191], a[144:147], v[36:39]\n"
"v_mfma_f32_16x16x32_bf16 v[168:171], a[188:191], a[148:151], v[168:171]\n"
"v_mfma_f32_16x16x32_bf16 v[40:43], a[188:191], a[152:155], v[40:43]\n"
"v_mfma_f32_16x16x32_bf16 v[172:175], a[188:191], a[156:159], v[172:175]\n"
"s_waitcnt lgkmcnt(0)\n"
"v_mfma_f32_16x16x32_bf16 v[176:179], a[224:227], a[208:211], v[176:179]\n"
"s_add_u32 m0, s57, 114688\n"
"ds_read_b128 a[144:147], v241 offset:32768\n"
"s_mov_b32 s52, 2621440\n"
"v_mfma_f32_16x16x32_bf16 v[180:183], a[224:227], a[212:215], v[180:183]\n"
"buffer_load_dwordx4 v246, s[48:51], s52 offen offset:0 lds\n"
"v_mfma_f32_16x16x32_bf16 v[184:187], a[224:227], a[216:219], v[184:187]\n"
"ds_read_b128 a[148:151], v241 offset:34816\n"
"v_mfma_f32_16x16x32_bf16 v[188:191], a[224:227], a[220:223], v[188:191]\n"
"ds_read_b128 a[152:155], v241 offset:36864\n"
"v_mfma_f32_16x16x32_bf16 v[192:195], a[228:231], a[208:211], v[192:195]\n"
"ds_read_b128 a[156:159], v241 offset:38912\n"
"v_mfma_f32_16x16x32_bf16 v[196:199], a[228:231], a[212:215], v[196:199]\n"
"s_add_u32 s53, s55, 131072\n"
"ds_read_b64_tr_b16 a[160:161], v232 offset:8192\n"
"v_mfma_f32_16x16x32_bf16 v[200:203], a[228:231], a[216:219], v[200:203]\n"
"s_mov_b32 s52, 2653184\n"
"buffer_store_dwordx4 a[52:55], v254, s[84:87], s53 offen offset:1024 sc1\n"
"ds_read_b64_tr_b16 a[162:163], v232 offset:9216\n"
"v_mfma_f32_16x16x32_bf16 v[204:207], a[228:231], a[220:223], v[204:207]\n"
"buffer_load_dwordx4 v246, s[48:51], s52 offen offset:1024 lds\n"
"ds_read_b64_tr_b16 a[164:165], v233 offset:8192\n"
"v_mfma_f32_16x16x32_bf16 v[208:211], a[232:235], a[208:211], v[208:211]\n"
"v_mfma_f32_16x16x32_bf16 v[128:131], a[232:235], a[212:215], v[128:131]\n"
"ds_read_b64_tr_b16 a[166:167], v233 offset:9216\n"
"v_mfma_f32_16x16x32_bf16 v[0:3], a[232:235], a[216:219], v[0:3]\n"
"ds_read_b64_tr_b16 a[168:169], v234 offset:8192\n"
"v_mfma_f32_16x16x32_bf16 v[132:135], a[232:235], a[220:223], v[132:135]\n"
"ds_read_b64_tr_b16 a[170:171], v234 offset:9216\n"
"v_mfma_f32_16x16x32_bf16 v[4:7], a[236:239], a[208:211], v[4:7]\n"
"s_mov_b32 s52, 2684928\n"
"ds_read_b64_tr_b16 a[172:173], v235 offset:8192\n"
"v_mfma_f32_16x16x32_bf16 v[136:139], a[236:239], a[212:215], v[136:139]\n"
"buffer_load_dwordx4 v247, s[48:51], s52 offen offset:2048 lds\n"
"ds_read_b64_tr_b16 a[174:175], v235 offset:9216\n"
"v_mfma_f32_16x16x32_bf16 v[8:11], a[236:239], a[216:219], v[8:11]\n"
"ds_read_b64_tr_b16 a[176:177], v236 offset:8192\n"
"v_mfma_f32_16x16x32_bf16 v[140:143], a[236:239], a[220:223], v[140:143]\n"
"ds_read_b64_tr_b16 a[178:179], v236 offset:9216\n"
"v_mfma_f32_16x16x32_bf16 v[12:15], a[240:243], a[208:211], v[12:15]\n"
"v_mfma_f32_16x16x32_bf16 v[144:147], a[240:243], a[212:215], v[144:147]\n"
"ds_read_b64_tr_b16 a[180:181], v237 offset:8192\n"
"v_mfma_f32_16x16x32_bf16 v[16:19], a[240:243], a[216:219], v[16:19]\n"
"ds_read_b64_tr_b16 a[182:183], v237 offset:9216\n"
"v_mfma_f32_16x16x32_bf16 v[148:151], a[240:243], a[220:223], v[148:151]\n"
"s_mov_b32 s52, 2716672\n"
"ds_read_b64_tr_b16 a[184:185], v238 offset:8192\n"
"v_mfma_f32_16x16x32_bf16 v[20:23], a[244:247], a[208:211], v[20:23]\n"
"s_add_u32 s53, s55, 131072\n"
"buffer_load_dwordx4 v247, s[48:51], s52 offen offset:3072 lds\n"
"ds_read_b64_tr_b16 a[186:187], v238 offset:9216\n"
"v_mfma_f32_16x16x32_bf16 v[152:155], a[244:247], a[212:215], v[152:155]\n"
"buffer_store_dwordx4 a[56:59], v254, s[84:87], s53 offen offset:2048 sc1\n"
"ds_read_b64_tr_b16 a[188:189], v239 offset:8192\n"
"v_mfma_f32_16x16x32_bf16 v[24:27], a[244:247], a[216:219], v[24:27]\n"
"ds_read_b64_tr_b16 a[190:191], v239 offset:9216\n"
"v_mfma_f32_16x16x32_bf16 v[156:159], a[244:247], a[220:223], v[156:159]\n"
"v_mfma_f32_16x16x32_bf16 v[28:31], a[248:251], a[208:211], v[28:31]\n"
"v_mfma_f32_16x16x32_bf16 v[160:163], a[248:251], a[212:215], v[160:163]\n"
"v_mfma_f32_16x16x32_bf16 v[32:35], a[248:251], a[216:219], v[32:35]\n"
"v_mfma_f32_16x16x32_bf16 v[164:167], a[248:251], a[220:223], v[164:167]\n"
"v_mfma_f32_16x16x32_bf16 v[36:39], a[252:255], a[208:211], v[36:39]\n"
"v_lshrrev_b32 v44, 2, v254\n"
"v_and_b32 v44, 28, v44\n"
"v_mfma_f32_16x16x32_bf16 v[168:171], a[252:255], a[212:215], v[168:171]\n"
"v_mfma_f32_16x16x32_bf16 v[40:43], a[252:255], a[216:219], v[40:43]\n"
"v_mfma_f32_16x16x32_bf16 v[172:175], a[252:255], a[220:223], v[172:175]\n"
"s_waitcnt lgkmcnt(0)\n"
"v_mfma_f32_16x16x32_bf16 v[176:179], a[160:163], a[144:147], v[176:179]\n"
"s_waitcnt vmcnt(23)\n"
"buffer_load_dword v45, v44, s[84:87], s82 offen sc1\n"
"v_mfma_f32_16x16x32_bf16 v[180:183], a[160:163], a[148:151], v[180:183]\n"
"s_barrier\n"
"v_mfma_f32_16x16x32_bf16 v[184:187], a[160:163], a[152:155], v[184:187]\n"
"s_add_u32 m0, s89, 32768\n"
"ds_read_b128 a[208:211], v242 offset:0\n"
"s_mov_b32 s52, 640\n"
"v_mfma_f32_16x16x32_bf16 v[188:191], a[160:163], a[156:159], v[188:191]\n"
"s_add_u32 s53, s55, 131072\n"
"buffer_load_dwordx4 v244, s[44:47], s52 offen offset:0 lds\n"
"v_mfma_f32_16x16x32_bf16 v[192:195], a[164:167], a[144:147], v[192:195]\n"
"s_mov_b32 s52, 9856\n"
"buffer_store_dwordx4 a[60:63], v254, s[84:87], s53 offen offset:3072 sc1\n"
"ds_read_b128 a[212:215], v242 offset:2048\n"
"v_mfma_f32_16x16x32_bf16 v[196:199], a[164:167], a[148:151], v[196:199]\n"
"s_lshl_b32 s53, s78, 15\n"
"buffer_load_dwordx4 v245, s[44:47], s52 offen offset:1024 lds\n"
"ds_read_b128 a[216:219], v242 offset:4096\n"
"v_mfma_f32_16x16x32_bf16 v[200:203], a[164:167], a[152:155], v[200:203]\n"
"s_sub_u32 s53, 901120, s53\n"
"ds_read_b128 a[220:223], v242 offset:6144\n"
"s_add_u32 s54, s54, s53\n"
"s_add_u32 s55, s55, s53\n"
"v_mfma_f32_16x16x32_bf16 v[204:207], a[164:167], a[156:159], v[204:207]\n"
"s_mov_b32 s52, 19072\n"
"ds_read_b64_tr_b16 a[224:225], v232 offset:16384\n"
"v_mfma_f32_16x16x32_bf16 v[208:211], a[168:171], a[144:147], v[208:211]\n"
"buffer_load_dwordx4 v244, s[44:47], s52 offen offset:2048 lds\n"
"ds_read_b64_tr_b16 a[226:227], v232 offset:17408\n"
"v_mfma_f32_16x16x32_bf16 v[128:131], a[168:171], a[148:151], v[128:131]\n"
"ds_read_b64_tr_b16 a[228:229], v233 offset:16384\n"
"v_mfma_f32_16x16x32_bf16 v[0:3], a[168:171], a[152:155], v[0:3]\n"
"s_mov_b32 s52, 28288\n"
"ds_read_b64_tr_b16 a[230:231], v233 offset:17408\n"
"v_mfma_f32_16x16x32_bf16 v[132:135], a[168:171], a[156:159], v[132:135]\n"
"buffer_load_dwordx4 v245, s[44:47], s52 offen offset:3072 lds\n"
"ds_read_b64_tr_b16 a[232:233], v234 offset:16384\n"
"v_mfma_f32_16x16x32_bf16 v[4:7], a[172:175], a[144:147], v[4:7]\n"
"ds_read_b64_tr_b16 a[234:235], v234 offset:17408\n"
"v_mfma_f32_16x16x32_bf16 v[136:139], a[172:175], a[148:151], v[136:139]\n"
"s_add_u32 m0, s89, 36864\n"
"ds_read_b64_tr_b16 a[236:237], v235 offset:16384\n"
"s_mov_b32 s52, 41600\n"
"v_mfma_f32_16x16x32_bf16 v[8:11], a[172:175], a[152:155], v[8:11]\n"
"buffer_load_dwordx4 v244, s[44:47], s52 offen offset:0 lds\n"
"ds_read_b64_tr_b16 a[238:239], v235 offset:17408\n"
"v_mfma_f32_16x16x32_bf16 v[140:143], a[172:175], a[156:159], v[140:143]\n"
"ds_read_b64_tr_b16 a[240:241], v236 offset:16384\n"
"v_mfma_f32_16x16x32_bf16 v[12:15], a[176:179], a[144:147], v[12:15]\n"
"s_mov_b32 s52, 50816\n"
"ds_read_b64_tr_b16 a[242:243], v236 offset:17408\n"
"v_mfma_f32_16x16x32_bf16 v[144:147], a[176:179], a[148:151], v[144:147]\n"
"buffer_load_dwordx4 v245, s[44:47], s52 offen offset:1024 lds\n"
"ds_read_b64_tr_b16 a[244:245], v237 offset:16384\n"
"v_mfma_f32_16x16x32_bf16 v[16:19], a[176:179], a[152:155], v[16:19]\n"
"s_add_u32 s53, s54, 131072\n"
"ds_read_b64_tr_b16 a[246:247], v237 offset:17408\n"
"v_mfma_f32_16x16x32_bf16 v[148:151], a[176:179], a[156:159], v[148:151]\n"
"s_mov_b32 s52, 60032\n"
"buffer_store_dwordx4 a[96:99], v254, s[84:87], s53 offen offset:0 sc1\n"
"ds_read_b64_tr_b16 a[248:249], v238 offset:16384\n"
"v_mfma_f32_16x16x32_bf16 v[20:23], a[180:183], a[144:147], v[20:23]\n"
"buffer_load_dwordx4 v244, s[44:47], s52 offen offset:2048 lds\n"
"ds_read_b64_tr_b16 a[250:251], v238 offset:17408\n"
"v_mfma_f32_16x16x32_bf16 v[152:155], a[180:183], a[148:151], v[152:155]\n"
"ds_read_b64_tr_b16 a[252:253], v239 offset:16384\n"
"v_mfma_f32_16x16x32_bf16 v[24:27], a[180:183], a[152:155], v[24:27]\n"
"s_mov_b32 s52, 69248\n"
"ds_read_b64_tr_b16 a[254:255], v239 offset:17408\n"
"v_mfma_f32_16x16x32_bf16 v[156:159], a[180:183], a[156:159], v[156:159]\n"
"buffer_load_dwordx4 v245, s[44:47], s52 offen offset:3072 lds\n"
"v_mfma_f32_16x16x32_bf16 v[28:31], a[184:187], a[144:147], v[28:31]\n"
"v_mfma_f32_16x16x32_bf16 v[160:163], a[184:187], a[148:151], v[160:163]\n"
"v_mfma_f32_16x16x32_bf16 v[32:35], a[184:187], a[152:155], v[32:35]\n"
"v_mfma_f32_16x16x32_bf16 v[164:167], a[184:187], a[156:159], v[164:167]\n"
"v_mfma_f32_16x16x32_bf16 v[36:39], a[188:191], a[144:147], v[36:39]\n"
"v_mfma_f32_16x16x32_bf16 v[168:171], a[188:191], a[148:151], v[168:171]\n"
"v_mfma_f32_16x16x32_bf16 v[40:43], a[188:191], a[152:155], v[40:43]\n"
"v_mfma_f32_16x16x32_bf16 v[172:175], a[188:191], a[156:159], v[172:175]\n"
"s_waitcnt lgkmcnt(0)\n"
"v_mfma_f32_16x16x32_bf16 v[176:179], a[224:227], a[208:211], v[176:179]\n"
"s_add_u32 m0, s57, 65536\n"
"ds_read_b128 a[144:147], v243 offset:0\n"
"s_mov_b32 s52, 3145728\n"
"s_lshr_b32 s32, %2, 5\n"
"v_mfma_f32_16x16x32_bf16 v[180:183], a[224:227], a[212:215], v[180:183]\n"
"s_add_u32 s53, s54, 131072\n"
"buffer_load_dwordx4 v246, s[48:51], s52 offen offset:0 lds\n"
"s_and_b32 s59, %2, 1\n"
"v_mfma_f32_16x16x32_bf16 v[184:187], a[224:227], a[216:219], v[184:187]\n"
"s_lshl1_add_u32 s32, s32, s59\n"
"buffer_store_dwordx4 a[100:103], v254, s[84:87], s53 offen offset:1024 sc1\n"
"ds_read_b128 a[148:151], v243 offset:2048\n"
"v_mfma_f32_16x16x32_bf16 v[188:191], a[224:227], a[220:223], v[188:191]\n"
"s_and_b32 s33, %2, 6\n"
"ds_read_b128 a[152:155], v243 offset:4096\n"
"s_lshl1_add_u32 s33, s33, s76\n"
"s_lshl_b32 s33, s33, 1\n"
"v_mfma_f32_16x16x32_bf16 v[192:195], a[228:231], a[208:211], v[192:195]\n"
"s_lshr_b32 s59, %2, 2\n"
"ds_read_b128 a[156:159], v243 offset:6144\n"
"s_xor_b32 s59, s59, %2\n"
"s_mov_b32 s31, 20480\n"
"v_mfma_f32_16x16x32_bf16 v[196:199], a[228:231], a[212:215], v[196:199]\n"
"ds_read_b64_tr_b16 a[160:161], v232 offset:24576\n"
"s_bfe_u32 s59, s59, 0x10003\n"
"s_cselect_b32 s31, -20480, s31\n"
"s_add_u32 s16, s33, s59\n"
"v_mfma_f32_16x16x32_bf16 v[200:203], a[228:231], a[216:219], v[200:203]\n"
"s_mov_b32 s52, 3177472\n"
"ds_read_b64_tr_b16 a[162:163], v232 offset:25600\n"
"s_bfe_u32 s35, %2, 0x20003\n"
"s_mov_b32 s34, 757760\n"
"v_mfma_f32_16x16x32_bf16 v[204:207], a[228:231], a[220:223], v[204:207]\n"
"buffer_load_dwordx4 v246, s[48:51], s52 offen offset:1024 lds\n"
"ds_read_b64_tr_b16 a[164:165], v233 offset:24576\n"
"v_mfma_f32_16x16x32_bf16 v[208:211], a[232:235], a[208:211], v[208:211]\n"
"s_and_b32 s59, s76, 1\n"
"s_cselect_b32 s34, -757760, s34\n"
"s_lshl1_add_u32 s35, s35, s59\n"
"s_mul_i32 s35, s35, 148\n"
"v_mfma_f32_16x16x32_bf16 v[128:131], a[232:235], a[212:215], v[128:131]\n"
"s_lshl2_add_u32 s35, s16, s35\n"
"ds_read_b64_tr_b16 a[166:167], v233 offset:25600\n"
"s_mul_i32 s35, s35, 5120\n"
"s_add_u32 s35, s35, 63963136\n"
"v_mfma_f32_16x16x32_bf16 v[0:3], a[232:235], a[216:219], v[0:3]\n"
"s_movk_i32 s28, 16384\n"
"ds_read_b64_tr_b16 a[168:169], v234 offset:24576\n"
"s_movk_i32 s29, 16384\n"
"s_add_u32 s30, s35, s31\n"
"v_mfma_f32_16x16x32_bf16 v[132:135], a[232:235], a[220:223], v[132:135]\n"
"s_lshl_b32 s59, s77, 20\n"
"s_mov_b64 s[36:37], s[26:27]\n"
"s_add_u32 s36, s36, s59\n"
"s_addc_u32 s37, s37, 0\n"
"v_mfma_f32_16x16x32_bf16 v[4:7], a[236:239], a[208:211], v[4:7]\n"
"s_mov_b32 s52, 3209216\n"
"ds_read_b64_tr_b16 a[170:171], v234 offset:25600\n"
"ds_read_b64_tr_b16 a[172:173], v235 offset:24576\n"
"s_mov_b32 s38, 0\n"
"v_mfma_f32_16x16x32_bf16 v[136:139], a[236:239], a[212:215], v[136:139]\n"
"s_mov_b32 s39, 0x20000\n"
"buffer_load_dwordx4 v247, s[48:51], s52 offen offset:2048 lds\n"
"ds_read_b64_tr_b16 a[174:175], v235 offset:25600\n"
"v_mfma_f32_16x16x32_bf16 v[8:11], a[236:239], a[216:219], v[8:11]\n"
"s_sub_u32 s30, s30, s59\n"
"ds_read_b64_tr_b16 a[176:177], v236 offset:24576\n"
"s_add_u32 s38, s30, 12288\n"
"s_sub_u32 s30, s30, s57\n"
"v_mfma_f32_16x16x32_bf16 v[140:143], a[236:239], a[220:223], v[140:143]\n"
"s_mov_b64 s[40:41], s[36:37]\n"
"ds_read_b64_tr_b16 a[178:179], v236 offset:25600\n"
"s_lshl_b32 s59, s78, 16\n"
"s_sub_u32 s42, 1048576, s59\n"
"v_mfma_f32_16x16x32_bf16 v[12:15], a[240:243], a[208:211], v[12:15]\n"
"s_add_u32 s53, s54, 135168\n"
"s_mov_b32 s43, 0x20000\n"
"v_and_b32 v248, 15, v255\n"
"v_lshrrev_b32 v249, 4, v255\n"
"v_mfma_f32_16x16x32_bf16 v[144:147], a[240:243], a[212:215], v[144:147]\n"
"buffer_store_dwordx4 a[104:107], v254, s[84:87], s53 offen offset:0 sc1\n"
"ds_read_b64_tr_b16 a[180:181], v237 offset:24576\n"
"v_mfma_f32_16x16x32_bf16 v[16:19], a[240:243], a[216:219], v[16:19]\n"
"ds_read_b64_tr_b16 a[182:183], v237 offset:25600\n"
"v_lshlrev_b32 v249, 2, v249\n"
"v_sub_u32 v248, v248, v249\n"
"v_mfma_f32_16x16x32_bf16 v[148:151], a[240:243], a[220:223], v[148:151]\n"
"s_mov_b32 s52, 3240960\n"
"ds_read_b64_tr_b16 a[184:185], v238 offset:24576\n"
"v_cmp_le_i32 s[68:69], 0, v248\n"
"v_cmp_le_i32 s[70:71], 1, v248\n"
"v_mfma_f32_16x16x32_bf16 v[20:23], a[244:247], a[208:211], v[20:23]\n"
"buffer_load_dwordx4 v247, s[48:51], s52 offen offset:3072 lds\n"
"ds_read_b64_tr_b16 a[186:187], v238 offset:25600\n"
"v_mfma_f32_16x16x32_bf16 v[152:155], a[244:247], a[212:215], v[152:155]\n"
"ds_read_b64_tr_b16 a[188:189], v239 offset:24576\n"
"v_cmp_le_i32 s[72:73], 2, v248\n"
"v_cmp_le_i32 s[74:75], 3, v248\n"
"v_mfma_f32_16x16x32_bf16 v[24:27], a[244:247], a[216:219], v[24:27]\n"
"ds_read_b64_tr_b16 a[190:191], v239 offset:25600\n"
"v_accvgpr_write_b32 a192, 0\n"
"v_accvgpr_write_b32 a193, 0\n"
"v_mfma_f32_16x16x32_bf16 v[156:159], a[244:247], a[220:223], v[156:159]\n"
"v_accvgpr_write_b32 a194, 0\n"
"v_accvgpr_write_b32 a195, 0\n"
"v_mov_b32 v250, 0xff800000\n"
"v_mfma_f32_16x16x32_bf16 v[28:31], a[248:251], a[208:211], v[28:31]\n"
"v_mov_b32 v251, 0xff800000\n"
"s_mov_b32 s5, 1073741824\n"
"s_mul_i32 s8, s77, 532992\n"
"s_add_u32 s8, s8, 33554432\n"
"v_mfma_f32_16x16x32_bf16 v[160:163], a[248:251], a[212:215], v[160:163]\n"
"s_lshl_b32 s6, s79, 14\n"
"s_sub_u32 s6, s6, s57\n"
"s_add_u32 s6, s6, s8\n"
"s_lshl_b32 s7, s79, 6\n"
"v_mfma_f32_16x16x32_bf16 v[32:35], a[248:251], a[216:219], v[32:35]\n"
"s_add_u32 s7, s7, s77\n"
"s_lshl_b32 s7, s7, 2\n"
"s_add_u32 s7, s7, 81821696\n"
"s_sub_u32 s22, 31, s79\n"
"v_mfma_f32_16x16x32_bf16 v[164:167], a[248:251], a[220:223], v[164:167]\n"
"s_max_u32 s22, s22, s79\n"
"s_lshl_b32 s22, s22, 1\n"
"s_lshr_b32 s23, s76, 1\n"
"s_add_u32 s22, s22, s23\n"
"v_mfma_f32_16x16x32_bf16 v[36:39], a[252:255], a[208:211], v[36:39]\n"
"s_lshl_b32 s23, s22, 13\n"
"s_add_u32 s8, s8, s23\n"
"s_lshl_b32 s9, s22, 5\n"
"s_add_u32 s9, s9, s77\n"
"v_mfma_f32_16x16x32_bf16 v[168:171], a[252:255], a[212:215], v[168:171]\n"
"s_lshl_b32 s9, s9, 2\n"
"s_add_u32 s9, s9, 81821696\n"
"s_mul_i32 s16, s16, 532992\n"
"s_lshl_b32 s22, s32, 12\n"
"v_mfma_f32_16x16x32_bf16 v[40:43], a[252:255], a[216:219], v[40:43]\n"
"s_add_u32 s16, s16, s22\n"
"s_add_u32 s16, s16, 33554432\n"
"s_lshr_b32 s22, s32, 2\n"
"s_lshl_b32 s22, s22, 6\n"
"v_mfma_f32_16x16x32_bf16 v[172:175], a[252:255], a[220:223], v[172:175]\n"
"s_waitcnt lgkmcnt(0)\n"
"s_add_u32 s53, s54, 135168\n"
"s_add_u32 s22, s22, s33\n"
"s_lshl2_add_u32 s10, s22, 81821696\n"
"v_mfma_f32_16x16x32_bf16 v[176:179], a[160:163], a[144:147], v[176:179]\n"
"s_waitcnt vmcnt(24)\n"
"buffer_store_dwordx4 a[108:111], v254, s[84:87], s53 offen offset:1024 sc1\n"
"s_mov_b32 s33, 532992\n"
"v_mfma_f32_16x16x32_bf16 v[180:183], a[160:163], a[148:151], v[180:183]\n"
"s_barrier\n"
"s_cmp_lt_i32 s31, 0\n"
"s_cselect_b32 s33, -532992, s33\n"
"s_mov_b32 s11, 0x40567321\n"
"v_mfma_f32_16x16x32_bf16 v[184:187], a[160:163], a[152:155], v[184:187]\n"
"s_add_u32 m0, s89, 131072\n"
"ds_read_b128 a[208:211], v240 offset:0\n"
"s_mov_b32 s52, 768\n"
"s_mov_b32 s22, 0x62457310\n"
"v_mfma_f32_16x16x32_bf16 v[188:191], a[160:163], a[156:159], v[188:191]\n"
"buffer_load_dwordx4 v244, s[44:47], s52 offen offset:0 lds\n"
"v_mfma_f32_16x16x32_bf16 v[192:195], a[164:167], a[144:147], v[192:195]\n"
"s_mov_b32 s52, 9984\n"
"s_bitcmp1_b32 %2, 1\n"
"s_cselect_b32 s11, 0x51467320, s11\n"
"s_cselect_b32 s22, 0x73456210, s22\n"
"v_mfma_f32_16x16x32_bf16 v[196:199], a[164:167], a[148:151], v[196:199]\n"
"ds_read_b128 a[212:215], v240 offset:2048\n"
"ds_read_b128 a[216:219], v240 offset:4096\n"
"s_bitcmp1_b32 %2, 2\n"
"s_cselect_b32 s11, s22, s11\n"
"v_mfma_f32_16x16x32_bf16 v[200:203], a[164:167], a[152:155], v[200:203]\n"
"s_and_b32 s22, s11, 0x0f0f0f0f\n"
"buffer_load_dwordx4 v245, s[44:47], s52 offen offset:1024 lds\n"
"ds_read_b128 a[220:223], v240 offset:6144\n"
"v_mfma_f32_16x16x32_bf16 v[204:207], a[164:167], a[156:159], v[204:207]\n"
"s_mov_b32 s52, 19200\n"
"ds_read_b64_tr_b16 a[224:225], v232 offset:32768\n"
"s_lshr_b32 s23, s11, 4\n"
"s_and_b32 s23, s23, 0x0f0f0f0f\n"
"v_mfma_f32_16x16x32_bf16 v[208:211], a[168:171], a[144:147], v[208:211]\n"
"s_lshl4_add_u32 s22, s22, s23\n"
"buffer_load_dwordx4 v244, s[44:47], s52 offen offset:2048 lds\n"
"ds_read_b64_tr_b16 a[226:227], v232 offset:33792\n"
"v_mfma_f32_16x16x32_bf16 v[128:131], a[168:171], a[148:151], v[128:131]\n"
"ds_read_b64_tr_b16 a[228:229], v233 offset:32768\n"
"s_bitcmp1_b32 s76, 1\n"
"s_cselect_b32 s11, s22, s11\n"
"s_and_b32 s22, %2, 7\n"
"v_mfma_f32_16x16x32_bf16 v[0:3], a[168:171], a[152:155], v[0:3]\n"
"s_mov_b32 s52, 28416\n"
"ds_read_b64_tr_b16 a[230:231], v233 offset:33792\n"
"s_lshr_b32 s23, %2, 6\n"
"s_lshl2_add_u32 s22, s22, s23\n"
"v_mfma_f32_16x16x32_bf16 v[132:135], a[168:171], a[156:159], v[132:135]\n"
"s_lshl2_add_u32 s22, s22, s76\n"
"buffer_load_dwordx4 v245, s[44:47], s52 offen offset:3072 lds\n"
"ds_read_b64_tr_b16 a[232:233], v234 offset:32768\n"
"v_mfma_f32_16x16x32_bf16 v[4:7], a[172:175], a[144:147], v[4:7]\n"
"s_xor_b32 s23, s22, 0\n"
"ds_read_b64_tr_b16 a[234:235], v234 offset:33792\n"
"s_lshl2_add_u32 s23, s23, s76\n"
"s_mul_i32 s23, s23, 5120\n"
"v_mfma_f32_16x16x32_bf16 v[136:139], a[172:175], a[148:151], v[136:139]\n"
"s_add_u32 m0, s89, 135168\n"
"ds_read_b64_tr_b16 a[236:237], v235 offset:32768\n"
"s_mov_b32 s52, 41728\n"
"s_add_u32 s12, s23, 60817408\n"
"v_mfma_f32_16x16x32_bf16 v[8:11], a[172:175], a[152:155], v[8:11]\n"
"s_add_u32 s53, s55, 131072\n"
"buffer_load_dwordx4 v244, s[44:47], s52 offen offset:0 lds\n"
"ds_read_b64_tr_b16 a[238:239], v235 offset:33792\n"
"v_mfma_f32_16x16x32_bf16 v[140:143], a[172:175], a[156:159], v[140:143]\n"
"s_xor_b32 s23, s22, 1\n"
"buffer_store_dwordx4 a[112:115], v254, s[84:87], s53 offen offset:0 sc1\n"
"ds_read_b64_tr_b16 a[240:241], v236 offset:32768\n"
"v_mfma_f32_16x16x32_bf16 v[12:15], a[176:179], a[144:147], v[12:15]\n"
"s_mov_b32 s52, 50944\n"
"ds_read_b64_tr_b16 a[242:243], v236 offset:33792\n"
"s_lshl2_add_u32 s23, s23, s76\n"
"s_mul_i32 s23, s23, 5120\n"
"v_mfma_f32_16x16x32_bf16 v[144:147], a[176:179], a[148:151], v[144:147]\n"
"s_add_u32 s13, s23, 60817408\n"
"buffer_load_dwordx4 v245, s[44:47], s52 offen offset:1024 lds\n"
"ds_read_b64_tr_b16 a[244:245], v237 offset:32768\n"
"v_mfma_f32_16x16x32_bf16 v[16:19], a[176:179], a[152:155], v[16:19]\n"
"s_xor_b32 s23, s22, 2\n"
"ds_read_b64_tr_b16 a[246:247], v237 offset:33792\n"
"s_lshl2_add_u32 s23, s23, s76\n"
"s_mul_i32 s23, s23, 5120\n"
"v_mfma_f32_16x16x32_bf16 v[148:151], a[176:179], a[156:159], v[148:151]\n"
"s_mov_b32 s52, 60160\n"
"ds_read_b64_tr_b16 a[248:249], v238 offset:32768\n"
"s_add_u32 s14, s23, 60817408\n"
"s_xor_b32 s23, s22, 3\n"
"v_mfma_f32_16x16x32_bf16 v[20:23], a[180:183], a[144:147], v[20:23]\n"
"s_lshl2_add_u32 s23, s23, s76\n"
"buffer_load_dwordx4 v244, s[44:47], s52 offen offset:2048 lds\n"
"ds_read_b64_tr_b16 a[250:251], v238 offset:33792\n"
"v_mfma_f32_16x16x32_bf16 v[152:155], a[180:183], a[148:151], v[152:155]\n"
"s_mul_i32 s23, s23, 5120\n"
"ds_read_b64_tr_b16 a[252:253], v239 offset:32768\n"
"s_add_u32 s15, s23, 60817408\n"
"s_bfe_u32 s17, %2, 0x30003\n"
"v_mfma_f32_16x16x32_bf16 v[24:27], a[180:183], a[152:155], v[24:27]\n"
"s_mov_b32 s52, 69376\n"
"ds_read_b64_tr_b16 a[254:255], v239 offset:33792\n"
"s_mul_i32 s17, s17, 148\n"
"s_mul_i32 s22, s76, 5\n"
"v_mfma_f32_16x16x32_bf16 v[156:159], a[180:183], a[156:159], v[156:159]\n"
"s_add_u32 s17, s17, s22\n"
"buffer_load_dwordx4 v245, s[44:47], s52 offen offset:3072 lds\n"
"s_mul_i32 s17, s17, 5120\n"
"v_mfma_f32_16x16x32_bf16 v[28:31], a[184:187], a[144:147], v[28:31]\n"
"s_add_u32 s17, s17, 64618496\n"
"s_lshl_b32 s22, s76, 2\n"
"s_lshr_b32 s23, 0x4087, s22\n"
"s_lshr_b32 s22, 0x8740, s22\n"
"v_mfma_f32_16x16x32_bf16 v[160:163], a[184:187], a[148:151], v[160:163]\n"
"s_and_b32 s23, s23, 15\n"
"s_and_b32 s22, s22, 15\n"
"s_lshl_b32 s23, s23, 14\n"
"s_lshl_b32 s22, s22, 14\n"
"v_mfma_f32_16x16x32_bf16 v[32:35], a[184:187], a[152:155], v[32:35]\n"
"v_mfma_f32_16x16x32_bf16 v[164:167], a[184:187], a[156:159], v[164:167]\n"
"v_mfma_f32_16x16x32_bf16 v[36:39], a[188:191], a[144:147], v[36:39]\n"
"v_mfma_f32_16x16x32_bf16 v[168:171], a[188:191], a[148:151], v[168:171]\n"
"s_add_u32 s53, s55, 131072\n"
"v_mfma_f32_16x16x32_bf16 v[40:43], a[188:191], a[152:155], v[40:43]\n"
"buffer_store_dwordx4 a[116:119], v254, s[84:87], s53 offen offset:1024 sc1\n"
"v_mfma_f32_16x16x32_bf16 v[172:175], a[188:191], a[156:159], v[172:175]\n"
"s_waitcnt lgkmcnt(0)\n"
"v_mfma_f32_16x16x32_bf16 v[176:179], a[224:227], a[208:211], v[176:179]\n"
"s_add_u32 m0, s57, 81920\n"
"ds_read_b128 a[144:147], v241 offset:0\n"
"s_mov_b32 s52, 3670016\n"
"v_mfma_f32_16x16x32_bf16 v[180:183], a[224:227], a[212:215], v[180:183]\n"
"buffer_load_dwordx4 v246, s[48:51], s52 offen offset:0 lds\n"
"v_mfma_f32_16x16x32_bf16 v[184:187], a[224:227], a[216:219], v[184:187]\n"
".Lpad9_%=:\n"
"s_cbranch_execz .Lpad13_%=\n"
"ds_read_b128 a[148:151], v241 offset:2048\n"
"v_mfma_f32_16x16x32_bf16 v[188:191], a[224:227], a[220:223], v[188:191]\n"
"ds_read_b128 a[152:155], v241 offset:4096\n"
"v_mfma_f32_16x16x32_bf16 v[192:195], a[228:231], a[208:211], v[192:195]\n"
"ds_read_b128 a[156:159], v241 offset:6144\n"
"v_mfma_f32_16x16x32_bf16 v[196:199], a[228:231], a[212:215], v[196:199]\n"
"ds_read_b64_tr_b16 a[160:161], v232 offset:40960\n"
"v_mfma_f32_16x16x32_bf16 v[200:203], a[228:231], a[216:219], v[200:203]\n"
"s_mov_b32 s52, 3701760\n"
"ds_read_b64_tr_b16 a[162:163], v232 offset:41984\n"
"v_mfma_f32_16x16x32_bf16 v[204:207], a[228:231], a[220:223], v[204:207]\n"
"buffer_load_dwordx4 v246, s[48:51], s52 offen offset:1024 lds\n"
"ds_read_b64_tr_b16 a[164:165], v233 offset:40960\n"
"v_mfma_f32_16x16x32_bf16 v[208:211], a[232:235], a[208:211], v[208:211]\n"
"v_mfma_f32_16x16x32_bf16 v[128:131], a[232:235], a[212:215], v[128:131]\n"
"ds_read_b64_tr_b16 a[166:167], v233 offset:41984\n"
"v_mfma_f32_16x16x32_bf16 v[0:3], a[232:235], a[216:219], v[0:3]\n"
"ds_read_b64_tr_b16 a[168:169], v234 offset:40960\n"
"v_mfma_f32_16x16x32_bf16 v[132:135], a[232:235], a[220:223], v[132:135]\n"
"ds_read_b64_tr_b16 a[170:171], v234 offset:41984\n"
"v_mfma_f32_16x16x32_bf16 v[4:7], a[236:239], a[208:211], v[4:7]\n"
"s_mov_b32 s52, 3733504\n"
"ds_read_b64_tr_b16 a[172:173], v235 offset:40960\n"
"s_add_u32 s53, s55, 131072\n"
"v_mfma_f32_16x16x32_bf16 v[136:139], a[236:239], a[212:215], v[136:139]\n"
"buffer_load_dwordx4 v247, s[48:51], s52 offen offset:2048 lds\n"
"ds_read_b64_tr_b16 a[174:175], v235 offset:41984\n"
"v_mfma_f32_16x16x32_bf16 v[8:11], a[236:239], a[216:219], v[8:11]\n"
"buffer_store_dwordx4 a[120:123], v254, s[84:87], s53 offen offset:2048 sc1\n"
"ds_read_b64_tr_b16 a[176:177], v236 offset:40960\n"
"v_mfma_f32_16x16x32_bf16 v[140:143], a[236:239], a[220:223], v[140:143]\n"
"ds_read_b64_tr_b16 a[178:179], v236 offset:41984\n"
"v_mfma_f32_16x16x32_bf16 v[12:15], a[240:243], a[208:211], v[12:15]\n"
"v_mfma_f32_16x16x32_bf16 v[144:147], a[240:243], a[212:215], v[144:147]\n"
"ds_read_b64_tr_b16 a[180:181], v237 offset:40960\n"
"v_mfma_f32_16x16x32_bf16 v[16:19], a[240:243], a[216:219], v[16:19]\n"
"ds_read_b64_tr_b16 a[182:183], v237 offset:41984\n"
"v_mfma_f32_16x16x32_bf16 v[148:151], a[240:243], a[220:223], v[148:151]\n"
"s_mov_b32 s52, 3765248\n"
"ds_read_b64_tr_b16 a[184:185], v238 offset:40960\n"
"v_mfma_f32_16x16x32_bf16 v[20:23], a[244:247], a[208:211], v[20:23]\n"
"buffer_load_dwordx4 v247, s[48:51], s52 offen offset:3072 lds\n"
"ds_read_b64_tr_b16 a[186:187], v238 offset:41984\n"
"v_mfma_f32_16x16x32_bf16 v[152:155], a[244:247], a[212:215], v[152:155]\n"
"ds_read_b64_tr_b16 a[188:189], v239 offset:40960\n"
"v_mfma_f32_16x16x32_bf16 v[24:27], a[244:247], a[216:219], v[24:27]\n"
"ds_read_b64_tr_b16 a[190:191], v239 offset:41984\n"
"v_mfma_f32_16x16x32_bf16 v[156:159], a[244:247], a[220:223], v[156:159]\n"
"v_mfma_f32_16x16x32_bf16 v[28:31], a[248:251], a[208:211], v[28:31]\n"
"v_mfma_f32_16x16x32_bf16 v[160:163], a[248:251], a[212:215], v[160:163]\n"
"v_mfma_f32_16x16x32_bf16 v[32:35], a[248:251], a[216:219], v[32:35]\n"
"v_mfma_f32_16x16x32_bf16 v[164:167], a[248:251], a[220:223], v[164:167]\n"
"s_add_u32 s53, s55, 131072\n"
"v_mfma_f32_16x16x32_bf16 v[36:39], a[252:255], a[208:211], v[36:39]\n"
"buffer_store_dwordx4 a[124:127], v254, s[84:87], s53 offen offset:3072 sc1\n"
"s_mov_b64 s[66:67], s[54:55]\n"
"v_mfma_f32_16x16x32_bf16 v[168:171], a[252:255], a[212:215], v[168:171]\n"
"v_mfma_f32_16x16x32_bf16 v[40:43], a[252:255], a[216:219], v[40:43]\n"
"v_mfma_f32_16x16x32_bf16 v[172:175], a[252:255], a[220:223], v[172:175]\n"
"s_waitcnt lgkmcnt(0)\n"
"v_mfma_f32_16x16x32_bf16 v[176:179], a[160:163], a[144:147], v[176:179]\n"
"s_waitcnt vmcnt(23)\n"
"s_mul_i32 s61, s77, 24\n"
"s_add_u32 s61, s61, s76\n"
"v_mfma_f32_16x16x32_bf16 v[180:183], a[160:163], a[148:151], v[180:183]\n"
"s_barrier\n"
"s_lshl_b32 s61, s61, 11\n"
"s_add_u32 s61, s61, 71303168\n"
"v_mfma_f32_16x16x32_bf16 v[184:187], a[160:163], a[152:155], v[184:187]\n"
"s_add_u32 m0, s89, 0\n"
"s_cmp_lt_u32 s76, 3\n"
"s_cselect_b32 s61, s61, 1073741824\n"
"v_mfma_f32_16x16x32_bf16 v[188:191], a[160:163], a[156:159], v[188:191]\n"
"s_mov_b32 s52, 896\n"
"buffer_load_dwordx4 v244, s[44:47], s52 offen offset:0 lds\n"
"v_mfma_f32_16x16x32_bf16 v[192:195], a[164:167], a[144:147], v[192:195]\n"
"s_mov_b32 s52, 10112\n"
"ds_read_b128 a[208:211], v240 offset:32768\n"
"ds_read_b128 a[212:215], v240 offset:34816\n"
"v_mfma_f32_16x16x32_bf16 v[196:199], a[164:167], a[148:151], v[196:199]\n"
"s_and_b32 s60, s77, 1\n"
"buffer_load_dwordx4 v245, s[44:47], s52 offen offset:1024 lds\n"
"v_mfma_f32_16x16x32_bf16 v[200:203], a[164:167], a[152:155], v[200:203]\n"
"s_lshl_b32 s60, s60, 2\n"
"ds_read_b128 a[216:219], v240 offset:36864\n"
"ds_read_b128 a[220:223], v240 offset:38912\n"
"v_mfma_f32_16x16x32_bf16 v[204:207], a[164:167], a[156:159], v[204:207]\n"
"s_mov_b32 s52, 19328\n"
"ds_read_b64_tr_b16 a[224:225], v232 offset:49152\n"
"v_and_b32 v52, 15, v255\n"
"v_mfma_f32_16x16x32_bf16 v[208:211], a[168:171], a[144:147], v[208:211]\n"
"buffer_load_dwordx4 v244, s[44:47], s52 offen offset:2048 lds\n"
"v_mfma_f32_16x16x32_bf16 v[128:131], a[168:171], a[148:151], v[128:131]\n"
"ds_read_b64_tr_b16 a[226:227], v232 offset:50176\n"
"ds_read_b64_tr_b16 a[228:229], v233 offset:49152\n"
"v_mfma_f32_16x16x32_bf16 v[0:3], a[168:171], a[152:155], v[0:3]\n"
"s_mov_b32 s52, 28544\n"
"ds_read_b64_tr_b16 a[230:231], v233 offset:50176\n"
"v_lshrrev_b32 v53, 4, v255\n"
"v_mfma_f32_16x16x32_bf16 v[132:135], a[168:171], a[156:159], v[132:135]\n"
"buffer_load_dwordx4 v245, s[44:47], s52 offen offset:3072 lds\n"
"v_mfma_f32_16x16x32_bf16 v[4:7], a[172:175], a[144:147], v[4:7]\n"
"ds_read_b64_tr_b16 a[232:233], v234 offset:49152\n"
"ds_read_b64_tr_b16 a[234:235], v234 offset:50176\n"
"v_mfma_f32_16x16x32_bf16 v[136:139], a[172:175], a[148:151], v[136:139]\n"
"s_add_u32 m0, s89, 4096\n"
"ds_read_b64_tr_b16 a[236:237], v235 offset:49152\n"
"s_mov_b32 s52, 41856\n"
"v_mfma_f32_16x16x32_bf16 v[8:11], a[172:175], a[152:155], v[8:11]\n"
"buffer_load_dwordx4 v244, s[44:47], s52 offen offset:0 lds\n"
"v_mfma_f32_16x16x32_bf16 v[140:143], a[172:175], a[156:159], v[140:143]\n"
"ds_read_b64_tr_b16 a[238:239], v235 offset:50176\n"
"ds_read_b64_tr_b16 a[240:241], v236 offset:49152\n"
"v_mfma_f32_16x16x32_bf16 v[12:15], a[176:179], a[144:147], v[12:15]\n"
"s_mov_b32 s52, 51072\n"
"ds_read_b64_tr_b16 a[242:243], v236 offset:50176\n"
"v_subrev_u32 v48, s60, v52\n"
"v_mfma_f32_16x16x32_bf16 v[144:147], a[176:179], a[148:151], v[144:147]\n"
"buffer_load_dwordx4 v245, s[44:47], s52 offen offset:1024 lds\n"
"v_mfma_f32_16x16x32_bf16 v[16:19], a[176:179], a[152:155], v[16:19]\n"
"ds_read_b64_tr_b16 a[244:245], v237 offset:49152\n"
"ds_read_b64_tr_b16 a[246:247], v237 offset:50176\n"
"v_mfma_f32_16x16x32_bf16 v[148:151], a[176:179], a[156:159], v[148:151]\n"
"s_mov_b32 s52, 60288\n"
"ds_read_b64_tr_b16 a[248:249], v238 offset:49152\n"
"v_add_u32 v49, 8, v48\n"
"v_mfma_f32_16x16x32_bf16 v[20:23], a[180:183], a[144:147], v[20:23]\n"
"buffer_load_dwordx4 v244, s[44:47], s52 offen offset:2048 lds\n"
"v_mfma_f32_16x16x32_bf16 v[152:155], a[180:183], a[148:151], v[152:155]\n"
"ds_read_b64_tr_b16 a[250:251], v238 offset:50176\n"
"ds_read_b64_tr_b16 a[252:253], v239 offset:49152\n"
"v_mfma_f32_16x16x32_bf16 v[24:27], a[180:183], a[152:155], v[24:27]\n"
"s_mov_b32 s52, 69504\n"
"ds_read_b64_tr_b16 a[254:255], v239 offset:50176\n"
"v_add_u32 v50, -8, v48\n"
"v_mfma_f32_16x16x32_bf16 v[156:159], a[180:183], a[156:159], v[156:159]\n"
"buffer_load_dwordx4 v245, s[44:47], s52 offen offset:3072 lds\n"
"v_mfma_f32_16x16x32_bf16 v[28:31], a[184:187], a[144:147], v[28:31]\n"
"v_add_u32 v48, 16, v48\n"
"v_min_u32 v48, v48, v50\n"
"s_lshl_b32 s60, s76, 5\n"
"v_mfma_f32_16x16x32_bf16 v[160:163], a[184:187], a[148:151], v[160:163]\n"
"s_add_u32 s60, s60, 65536\n"
"v_lshl_add_u32 v50, v53, 3, s60\n"
"v_mfma_f32_16x16x32_bf16 v[32:35], a[184:187], a[152:155], v[32:35]\n"
"v_cmp_le_u32 vcc, 8, v52\n"
"s_nop 1\n"
"v_cndmask_b32 v48, 24, v48, vcc\n"
"v_mfma_f32_16x16x32_bf16 v[164:167], a[184:187], a[156:159], v[164:167]\n"
"s_movk_i32 s60, 144\n"
"v_mad_u32_u24 v48, v48, s60, v50\n"
"v_mad_u32_u24 v49, v49, s60, v50\n"
"v_mfma_f32_16x16x32_bf16 v[36:39], a[188:191], a[144:147], v[36:39]\n"
"v_lshrrev_b32 v52, 2, v52\n"
"v_and_b32 v50, 3, v255\n"
"v_mfma_f32_16x16x32_bf16 v[168:171], a[188:191], a[148:151], v[168:171]\n"
"v_mul_u32_u24 v50, 144, v50\n"
"v_add_u32 v50, 65536, v50\n"
"v_mfma_f32_16x16x32_bf16 v[40:43], a[188:191], a[152:155], v[40:43]\n"
"v_mov_b32 v51, 73728\n"
"v_mfma_f32_16x16x32_bf16 v[172:175], a[188:191], a[156:159], v[172:175]\n"
"s_waitcnt lgkmcnt(0)\n"
"v_mfma_f32_16x16x32_bf16 v[176:179], a[224:227], a[208:211], v[176:179]\n"
"s_add_u32 m0, s57, 98304\n"
"ds_read_b128 a[144:147], v241 offset:32768\n"
"s_mov_b32 s52, 4194304\n"
"v_mfma_f32_16x16x32_bf16 v[180:183], a[224:227], a[212:215], v[180:183]\n"
"buffer_load_dwordx4 v246, s[48:51], s52 offen offset:0 lds\n"
"v_mfma_f32_16x16x32_bf16 v[184:187], a[224:227], a[216:219], v[184:187]\n"
"v_cmp_eq_u32 vcc, v52, v53\n"
"s_nop 1\n"
"v_cndmask_b32 v50, v51, v50, vcc\n"
"v_mfma_f32_16x16x32_bf16 v[188:191], a[224:227], a[220:223], v[188:191]\n"
"ds_read_b128 a[148:151], v241 offset:34816\n"
"ds_read_b128 a[152:155], v241 offset:36864\n"
"v_mfma_f32_16x16x32_bf16 v[192:195], a[228:231], a[208:211], v[192:195]\n"
"ds_read_b128 a[156:159], v241 offset:38912\n"
"v_mul_u32_u24 v51, 20224, v53\n"
"v_mfma_f32_16x16x32_bf16 v[196:199], a[228:231], a[212:215], v[196:199]\n"
"ds_read_b64_tr_b16 a[160:161], v232 offset:57344\n"
"v_add_u32 v51, v51, v254\n"
"s_lshl_b32 s60, s76, 10\n"
"v_mfma_f32_16x16x32_bf16 v[200:203], a[228:231], a[216:219], v[200:203]\n"
"s_mov_b32 s52, 4226048\n"
"ds_read_b64_tr_b16 a[162:163], v232 offset:58368\n"
"s_add_u32 s60, s60, 73728\n"
"v_mfma_f32_16x16x32_bf16 v[204:207], a[228:231], a[220:223], v[204:207]\n"
"buffer_load_dwordx4 v246, s[48:51], s52 offen offset:1024 lds\n"
"v_mfma_f32_16x16x32_bf16 v[208:211], a[232:235], a[208:211], v[208:211]\n"
"ds_read_b64_tr_b16 a[164:165], v233 offset:57344\n"
"v_add_u32 v52, s60, v254\n"
"v_mfma_f32_16x16x32_bf16 v[128:131], a[232:235], a[212:215], v[128:131]\n"
"ds_read_b64_tr_b16 a[166:167], v233 offset:58368\n"
"v_mov_b32 v56, 0\n"
"v_mfma_f32_16x16x32_bf16 v[0:3], a[232:235], a[216:219], v[0:3]\n"
"ds_read_b64_tr_b16 a[168:169], v234 offset:57344\n"
"v_mov_b32 v57, 0\n"
"v_mfma_f32_16x16x32_bf16 v[132:135], a[232:235], a[220:223], v[132:135]\n"
"ds_read_b64_tr_b16 a[170:171], v234 offset:58368\n"
"v_mov_b32 v58, 0\n"
"v_mfma_f32_16x16x32_bf16 v[4:7], a[236:239], a[208:211], v[4:7]\n"
"s_mov_b32 s52, 4257792\n"
"ds_read_b64_tr_b16 a[172:173], v235 offset:57344\n"
"v_mov_b32 v59, 0\n"
"v_mfma_f32_16x16x32_bf16 v[136:139], a[236:239], a[212:215], v[136:139]\n"
"buffer_load_dwordx4 v247, s[48:51], s52 offen offset:2048 lds\n"
"v_mfma_f32_16x16x32_bf16 v[8:11], a[236:239], a[216:219], v[8:11]\n"
"ds_read_b64_tr_b16 a[174:175], v235 offset:58368\n"
"ds_read_b64_tr_b16 a[176:177], v236 offset:57344\n"
"v_mfma_f32_16x16x32_bf16 v[140:143], a[236:239], a[220:223], v[140:143]\n"
"ds_read_b64_tr_b16 a[178:179], v236 offset:58368\n"
"v_mfma_f32_16x16x32_bf16 v[12:15], a[240:243], a[208:211], v[12:15]\n"
"v_mfma_f32_16x16x32_bf16 v[144:147], a[240:243], a[212:215], v[144:147]\n"
"ds_read_b64_tr_b16 a[180:181], v237 offset:57344\n"
"v_mfma_f32_16x16x32_bf16 v[16:19], a[240:243], a[216:219], v[16:19]\n"
"ds_read_b64_tr_b16 a[182:183], v237 offset:58368\n"
"v_mfma_f32_16x16x32_bf16 v[148:151], a[240:243], a[220:223], v[148:151]\n"
"s_mov_b32 s52, 4289536\n"
"ds_read_b64_tr_b16 a[184:185], v238 offset:57344\n"
"v_mfma_f32_16x16x32_bf16 v[20:23], a[244:247], a[208:211], v[20:23]\n"
"buffer_load_dwordx4 v247, s[48:51], s52 offen offset:3072 lds\n"
"v_mfma_f32_16x16x32_bf16 v[152:155], a[244:247], a[212:215], v[152:155]\n"
"ds_read_b64_tr_b16 a[186:187], v238 offset:58368\n"
"ds_read_b64_tr_b16 a[188:189], v239 offset:57344\n"
"v_mfma_f32_16x16x32_bf16 v[24:27], a[244:247], a[216:219], v[24:27]\n"
"ds_read_b64_tr_b16 a[190:191], v239 offset:58368\n"
"v_mfma_f32_16x16x32_bf16 v[156:159], a[244:247], a[220:223], v[156:159]\n"
"v_mfma_f32_16x16x32_bf16 v[28:31], a[248:251], a[208:211], v[28:31]\n"
"v_mfma_f32_16x16x32_bf16 v[160:163], a[248:251], a[212:215], v[160:163]\n"
"v_mfma_f32_16x16x32_bf16 v[32:35], a[248:251], a[216:219], v[32:35]\n"
"v_mfma_f32_16x16x32_bf16 v[164:167], a[248:251], a[220:223], v[164:167]\n"
"v_mfma_f32_16x16x32_bf16 v[36:39], a[252:255], a[208:211], v[36:39]\n"
"v_mfma_f32_16x16x32_bf16 v[168:171], a[252:255], a[212:215], v[168:171]\n"
"s_waitcnt vmcnt(45)\n"
"v_cmp_ne_u32 vcc, s88, v45\n"
"s_cbranch_vccnz .Lslow3_%=\n"
".Lback3_%=:\n"
"v_mfma_f32_16x16x32_bf16 v[40:43], a[252:255], a[216:219], v[40:43]\n"
"s_sub_u32 s82, s82, 8192\n"
"s_lshl_b32 s53, s78, 2\n"
"s_add_u32 s53, s53, s82\n"
"v_mfma_f32_16x16x32_bf16 v[172:175], a[252:255], a[220:223], v[172:175]\n"
"s_waitcnt lgkmcnt(0)\n"
"v_mfma_f32_16x16x32_bf16 v[176:179], a[160:163], a[144:147], v[176:179]\n"
"s_waitcnt vmcnt(12)\n"
"v_mfma_f32_16x16x32_bf16 v[180:183], a[160:163], a[148:151], v[180:183]\n"
"s_barrier\n"
"v_mfma_f32_16x16x32_bf16 v[184:187], a[160:163], a[152:155], v[184:187]\n"
"s_add_u32 m0, s89, 32768\n"
"ds_read_b128 a[208:211], v242 offset:0\n"
"s_mov_b32 s52, 1024\n"
"v_mfma_f32_16x16x32_bf16 v[188:191], a[160:163], a[156:159], v[188:191]\n"
"buffer_load_dwordx4 v244, s[44:47], s52 offen offset:0 lds\n"
"v_mfma_f32_16x16x32_bf16 v[192:195], a[164:167], a[144:147], v[192:195]\n"
"s_mov_b32 s52, 10240\n"
"v_lshlrev_b32 v46, 21, v255\n"
"v_mov_b32 v47, s88\n"
"buffer_store_dword v47, v46, s[84:87], s53 offen sc1\n"
"v_mfma_f32_16x16x32_bf16 v[196:199], a[164:167], a[148:151], v[196:199]\n"
"ds_read_b128 a[212:215], v242 offset:2048\n"
"ds_read_b128 a[216:219], v242 offset:4096\n"
"v_mfma_f32_16x16x32_bf16 v[200:203], a[164:167], a[152:155], v[200:203]\n"
"buffer_load_dwordx4 v245, s[44:47], s52 offen offset:1024 lds\n"
"v_mfma_f32_16x16x32_bf16 v[204:207], a[164:167], a[156:159], v[204:207]\n"
"s_mov_b32 s52, 19456\n"
"ds_read_b128 a[220:223], v242 offset:6144\n"
"ds_read_b64_tr_b16 a[224:225], v232 offset:0\n"
"v_mfma_f32_16x16x32_bf16 v[208:211], a[168:171], a[144:147], v[208:211]\n"
"buffer_load_dwordx4 v244, s[44:47], s52 offen offset:2048 lds\n"
"v_mfma_f32_16x16x32_bf16 v[128:131], a[168:171], a[148:151], v[128:131]\n"
"ds_read_b64_tr_b16 a[226:227], v232 offset:1024\n"
"ds_read_b64_tr_b16 a[228:229], v233 offset:0\n"
"v_mfma_f32_16x16x32_bf16 v[0:3], a[168:171], a[152:155], v[0:3]\n"
"s_mov_b32 s52, 28672\n"
"ds_read_b64_tr_b16 a[230:231], v233 offset:1024\n"
"v_mfma_f32_16x16x32_bf16 v[132:135], a[168:171], a[156:159], v[132:135]\n"
"buffer_load_dwordx4 v245, s[44:47], s52 offen offset:3072 lds\n"
"v_mfma_f32_16x16x32_bf16 v[4:7], a[172:175], a[144:147], v[4:7]\n"
"ds_read_b64_tr_b16 a[232:233], v234 offset:0\n"
"ds_read_b64_tr_b16 a[234:235], v234 offset:1024\n"
"v_mfma_f32_16x16x32_bf16 v[136:139], a[172:175], a[148:151], v[136:139]\n"
"s_add_u32 m0, s89, 36864\n"
"ds_read_b64_tr_b16 a[236:237], v235 offset:0\n"
"s_mov_b32 s52, 41984\n"
"v_mfma_f32_16x16x32_bf16 v[8:11], a[172:175], a[152:155], v[8:11]\n"
"buffer_load_dwordx4 v244, s[44:47], s52 offen offset:0 lds\n"
"v_mfma_f32_16x16x32_bf16 v[140:143], a[172:175], a[156:159], v[140:143]\n"
"s_mov_b32 s53, 81813504\n"
"s_cmp_eq_u32 s76, 0\n"
"s_cselect_b32 s53, s53, 1073741824\n"
"v_mfma_f32_16x16x32_bf16 v[12:15], a[176:179], a[144:147], v[12:15]\n"
"s_mov_b32 s52, 51200\n"
"ds_read_b64_tr_b16 a[238:239], v235 offset:1024\n"
"ds_read_b64_tr_b16 a[240:241], v236 offset:0\n"
"v_mfma_f32_16x16x32_bf16 v[144:147], a[176:179], a[148:151], v[144:147]\n"
"buffer_load_dwordx4 v245, s[44:47], s52 offen offset:1024 lds\n"
"v_mfma_f32_16x16x32_bf16 v[16:19], a[176:179], a[152:155], v[16:19]\n"
"ds_read_b64_tr_b16 a[242:243], v236 offset:1024\n"
"ds_read_b64_tr_b16 a[244:245], v237 offset:0\n"
"v_mfma_f32_16x16x32_bf16 v[148:151], a[176:179], a[156:159], v[148:151]\n"
"s_mov_b32 s52, 60416\n"
"ds_read_b64_tr_b16 a[246:247], v237 offset:1024\n"
"ds_read_b64_tr_b16 a[248:249], v238 offset:0\n"
"v_mfma_f32_16x16x32_bf16 v[20:23], a[180:183], a[144:147], v[20:23]\n"
"buffer_load_dwordx4 v244, s[44:47], s52 offen offset:2048 lds\n"
"v_mfma_f32_16x16x32_bf16 v[152:155], a[180:183], a[148:151], v[152:155]\n"
"ds_read_b64_tr_b16 a[250:251], v238 offset:1024\n"
"ds_read_b64_tr_b16 a[252:253], v239 offset:0\n"
"v_mfma_f32_16x16x32_bf16 v[24:27], a[180:183], a[152:155], v[24:27]\n"
"s_mov_b32 s52, 69632\n"
"ds_read_b64_tr_b16 a[254:255], v239 offset:1024\n"
"v_and_b32 v46, 16, v254\n"
"v_mfma_f32_16x16x32_bf16 v[156:159], a[180:183], a[156:159], v[156:159]\n"
"buffer_load_dwordx4 v245, s[44:47], s52 offen offset:3072 lds\n"
"v_mfma_f32_16x16x32_bf16 v[28:31], a[184:187], a[144:147], v[28:31]\n"
"v_lshrrev_b32 v47, 1, v255\n"
"v_lshl_add_u32 v46, v47, 8, v46\n"
"v_mfma_f32_16x16x32_bf16 v[160:163], a[184:187], a[148:151], v[160:163]\n"
"v_mfma_f32_16x16x32_bf16 v[32:35], a[184:187], a[152:155], v[32:35]\n"
"v_mfma_f32_16x16x32_bf16 v[164:167], a[184:187], a[156:159], v[164:167]\n"
"v_mfma_f32_16x16x32_bf16 v[36:39], a[188:191], a[144:147], v[36:39]\n"
"v_mfma_f32_16x16x32_bf16 v[168:171], a[188:191], a[148:151], v[168:171]\n"
"v_mfma_f32_16x16x32_bf16 v[40:43], a[188:191], a[152:155], v[40:43]\n"
"v_mfma_f32_16x16x32_bf16 v[172:175], a[188:191], a[156:159], v[172:175]\n"
"s_waitcnt lgkmcnt(0)\n"
"v_mfma_f32_16x16x32_bf16 v[176:179], a[224:227], a[208:211], v[176:179]\n"
"s_add_u32 m0, s57, 114688\n"
"ds_read_b128 a[144:147], v243 offset:0\n"
"s_mov_b32 s52, 4718592\n"
"v_mfma_f32_16x16x32_bf16 v[180:183], a[224:227], a[212:215], v[180:183]\n"
"buffer_load_dwordx4 v246, s[48:51], s52 offen offset:0 lds\n"
"v_mfma_f32_16x16x32_bf16 v[184:187], a[224:227], a[216:219], v[184:187]\n"
"ds_read_b128 a[148:151], v243 offset:2048\n"
"v_mfma_f32_16x16x32_bf16 v[188:191], a[224:227], a[220:223], v[188:191]\n"
"ds_read_b128 a[152:155], v243 offset:4096\n"
"v_mfma_f32_16x16x32_bf16 v[192:195], a[228:231], a[208:211], v[192:195]\n"
"buffer_load_dwordx4 v[64:67], v254, s[84:87], s61 offen offset:0 sc1\n"
"v_mfma_f32_16x16x32_bf16 v[196:199], a[228:231], a[212:215], v[196:199]\n"
"ds_read_b128 a[156:159], v243 offset:6144\n"
"ds_read_b64_tr_b16 a[160:161], v232 offset:8192\n"
"v_mfma_f32_16x16x32_bf16 v[200:203], a[228:231], a[216:219], v[200:203]\n"
"s_mov_b32 s52, 4750336\n"
"ds_read_b64_tr_b16 a[162:163], v232 offset:9216\n"
"v_mfma_f32_16x16x32_bf16 v[204:207], a[228:231], a[220:223], v[204:207]\n"
"buffer_load_dwordx4 v246, s[48:51], s52 offen offset:1024 lds\n"
"v_mfma_f32_16x16x32_bf16 v[208:211], a[232:235], a[208:211], v[208:211]\n"
"ds_read_b64_tr_b16 a[164:165], v233 offset:8192\n"
"v_mfma_f32_16x16x32_bf16 v[128:131], a[232:235], a[212:215], v[128:131]\n"
"ds_read_b64_tr_b16 a[166:167], v233 offset:9216\n"
"v_mfma_f32_16x16x32_bf16 v[0:3], a[232:235], a[216:219], v[0:3]\n"
"buffer_load_dwordx4 v[68:71], v254, s[84:87], s61 offen offset:1024 sc1\n"
"s_add_u32 s61, s61, 6144\n"
"v_mfma_f32_16x16x32_bf16 v[132:135], a[232:235], a[220:223], v[132:135]\n"
"ds_read_b64_tr_b16 a[168:169], v234 offset:8192\n"
"ds_read_b64_tr_b16 a[170:171], v234 offset:9216\n"
"v_mfma_f32_16x16x32_bf16 v[4:7], a[236:239], a[208:211], v[4:7]\n"
"s_mov_b32 s52, 4782080\n"
"ds_read_b64_tr_b16 a[172:173], v235 offset:8192\n"
"v_mfma_f32_16x16x32_bf16 v[136:139], a[236:239], a[212:215], v[136:139]\n"
"buffer_load_dwordx4 v247, s[48:51], s52 offen offset:2048 lds\n"
"v_mfma_f32_16x16x32_bf16 v[8:11], a[236:239], a[216:219], v[8:11]\n"
"ds_read_b64_tr_b16 a[174:175], v235 offset:9216\n"
"ds_read_b64_tr_b16 a[176:177], v236 offset:8192\n"
"v_mfma_f32_16x16x32_bf16 v[140:143], a[236:239], a[220:223], v[140:143]\n"
"ds_read_b64_tr_b16 a[178:179], v236 offset:9216\n"
"v_mfma_f32_16x16x32_bf16 v[12:15], a[240:243], a[208:211], v[12:15]\n"
"buffer_load_dwordx4 v[72:75], v254, s[84:87], s61 offen offset:0 sc1\n"
"v_mfma_f32_16x16x32_bf16 v[144:147], a[240:243], a[212:215], v[144:147]\n"
"ds_read_b64_tr_b16 a[180:181], v237 offset:8192\n"
"v_mfma_f32_16x16x32_bf16 v[16:19], a[240:243], a[216:219], v[16:19]\n"
"ds_read_b64_tr_b16 a[182:183], v237 offset:9216\n"
"v_mfma_f32_16x16x32_bf16 v[148:151], a[240:243], a[220:223], v[148:151]\n"
"s_mov_b32 s52, 4813824\n"
"ds_read_b64_tr_b16 a[184:185], v238 offset:8192\n"
"v_mfma_f32_16x16x32_bf16 v[20:23], a[244:247], a[208:211], v[20:23]\n"
"buffer_load_dwordx4 v247, s[48:51], s52 offen offset:3072 lds\n"
"v_mfma_f32_16x16x32_bf16 v[152:155], a[244:247], a[212:215], v[152:155]\n"
"ds_read_b64_tr_b16 a[186:187], v238 offset:9216\n"
"ds_read_b64_tr_b16 a[188:189], v239 offset:8192\n"
"v_mfma_f32_16x16x32_bf16 v[24:27], a[244:247], a[216:219], v[24:27]\n"
"buffer_load_dwordx4 v[76:79], v254, s[84:87], s61 offen offset:1024 sc1\n"
"s_add_u32 s61, s61, 6144\n"
"v_mfma_f32_16x16x32_bf16 v[156:159], a[244:247], a[220:223], v[156:159]\n"
"ds_read_b64_tr_b16 a[190:191], v239 offset:9216\n"
"v_mfma_f32_16x16x32_bf16 v[28:31], a[248:251], a[208:211], v[28:31]\n"
"v_mfma_f32_16x16x32_bf16 v[160:163], a[248:251], a[212:215], v[160:163]\n"
"v_mfma_f32_16x16x32_bf16 v[32:35], a[248:251], a[216:219], v[32:35]\n"
"v_mfma_f32_16x16x32_bf16 v[164:167], a[248:251], a[220:223], v[164:167]\n"
"v_mfma_f32_16x16x32_bf16 v[36:39], a[252:255], a[208:211], v[36:39]\n"
"v_mfma_f32_16x16x32_bf16 v[168:171], a[252:255], a[212:215], v[168:171]\n"
"v_mfma_f32_16x16x32_bf16 v[40:43], a[252:255], a[216:219], v[40:43]\n"
"v_mfma_f32_16x16x32_bf16 v[172:175], a[252:255], a[220:223], v[172:175]\n"
"s_waitcnt lgkmcnt(0)\n"
"v_mfma_f32_16x16x32_bf16 v[176:179], a[160:163], a[144:147], v[176:179]\n"
"s_waitcnt vmcnt(21)\n"
"v_mfma_f32_16x16x32_bf16 v[180:183], a[160:163], a[148:151], v[180:183]\n"
"s_barrier\n"
"v_mfma_f32_16x16x32_bf16 v[184:187], a[160:163], a[152:155], v[184:187]\n"
"s_add_u32 m0, s89, 131072\n"
"ds_read_b128 a[208:211], v240 offset:0\n"
"s_mov_b32 s52, 1152\n"
"v_mfma_f32_16x16x32_bf16 v[188:191], a[160:163], a[156:159], v[188:191]\n"
"buffer_load_dwordx4 v244, s[44:47], s52 offen offset:0 lds\n"
"v_mfma_f32_16x16x32_bf16 v[192:195], a[164:167], a[144:147], v[192:195]\n"
"s_mov_b32 s52, 10368\n"
"ds_read_b128 a[212:215], v240 offset:2048\n"
"v_mfma_f32_16x16x32_bf16 v[196:199], a[164:167], a[148:151], v[196:199]\n"
"buffer_load_dwordx4 v245, s[44:47], s52 offen offset:1024 lds\n"
"v_mfma_f32_16x16x32_bf16 v[200:203], a[164:167], a[152:155], v[200:203]\n"
"ds_read_b128 a[216:219], v240 offset:4096\n"
"ds_read_b128 a[220:223], v240 offset:6144\n"
"v_mfma_f32_16x16x32_bf16 v[204:207], a[164:167], a[156:159], v[204:207]\n"
"s_mov_b32 s52, 19584\n"
"ds_read_b64_tr_b16 a[224:225], v232 offset:16384\n"
"v_mfma_f32_16x16x32_bf16 v[208:211], a[168:171], a[144:147], v[208:211]\n"
"buffer_load_dwordx4 v244, s[44:47], s52 offen offset:2048 lds\n"
"v_mfma_f32_16x16x32_bf16 v[128:131], a[168:171], a[148:151], v[128:131]\n"
"ds_read_b64_tr_b16 a[226:227], v232 offset:17408\n"
"ds_read_b64_tr_b16 a[228:229], v233 offset:16384\n"
"v_mfma_f32_16x16x32_bf16 v[0:3], a[168:171], a[152:155], v[0:3]\n"
"s_mov_b32 s52, 28800\n"
"ds_read_b64_tr_b16 a[230:231], v233 offset:17408\n"
"v_mfma_f32_16x16x32_bf16 v[132:135], a[168:171], a[156:159], v[132:135]\n"
"buffer_load_dwordx4 v245, s[44:47], s52 offen offset:3072 lds\n"
"v_mfma_f32_16x16x32_bf16 v[4:7], a[172:175], a[144:147], v[4:7]\n"
"ds_read_b64_tr_b16 a[232:233], v234 offset:16384\n"
"ds_read_b64_tr_b16 a[234:235], v234 offset:17408\n"
"v_mfma_f32_16x16x32_bf16 v[136:139], a[172:175], a[148:151], v[136:139]\n"
"s_add_u32 m0, s89, 135168\n"
"ds_read_b64_tr_b16 a[236:237], v235 offset:16384\n"
"s_mov_b32 s52, 42112\n"
"v_mfma_f32_16x16x32_bf16 v[8:11], a[172:175], a[152:155], v[8:11]\n"
"buffer_load_dwordx4 v244, s[44:47], s52 offen offset:0 lds\n"
"v_mfma_f32_16x16x32_bf16 v[140:143], a[172:175], a[156:159], v[140:143]\n"
"ds_read_b64_tr_b16 a[238:239], v235 offset:17408\n"
"ds_read_b64_tr_b16 a[240:241], v236 offset:16384\n"
"v_mfma_f32_16x16x32_bf16 v[12:15], a[176:179], a[144:147], v[12:15]\n"
"s_mov_b32 s52, 51328\n"
"ds_read_b64_tr_b16 a[242:243], v236 offset:17408\n"
"v_mfma_f32_16x16x32_bf16 v[144:147], a[176:179], a[148:151], v[144:147]\n"
"buffer_load_dwordx4 v245, s[44:47], s52 offen offset:1024 lds\n"
"v_mfma_f32_16x16x32_bf16 v[16:19], a[176:179], a[152:155], v[16:19]\n"
"ds_read_b64_tr_b16 a[244:245], v237 offset:16384\n"
"ds_read_b64_tr_b16 a[246:247], v237 offset:17408\n"
"v_mfma_f32_16x16x32_bf16 v[148:151], a[176:179], a[156:159], v[148:151]\n"
"s_mov_b32 s52, 60544\n"
"ds_read_b64_tr_b16 a[248:249], v238 offset:16384\n"
"v_mfma_f32_16x16x32_bf16 v[20:23], a[180:183], a[144:147], v[20:23]\n"
"buffer_load_dwordx4 v244, s[44:47], s52 offen offset:2048 lds\n"
"v_mfma_f32_16x16x32_bf16 v[152:155], a[180:183], a[148:151], v[152:155]\n"
"ds_read_b64_tr_b16 a[250:251], v238 offset:17408\n"
"ds_read_b64_tr_b16 a[252:253], v239 offset:16384\n"
"v_mfma_f32_16x16x32_bf16 v[24:27], a[180:183], a[152:155], v[24:27]\n"
"s_mov_b32 s52, 69760\n"
"ds_read_b64_tr_b16 a[254:255], v239 offset:17408\n"
"v_mfma_f32_16x16x32_bf16 v[156:159], a[180:183], a[156:159], v[156:159]\n"
"buffer_load_dwordx4 v245, s[44:47], s52 offen offset:3072 lds\n"
"v_mfma_f32_16x16x32_bf16 v[28:31], a[184:187], a[144:147], v[28:31]\n"
"v_mfma_f32_16x16x32_bf16 v[160:163], a[184:187], a[148:151], v[160:163]\n"
"v_mfma_f32_16x16x32_bf16 v[32:35], a[184:187], a[152:155], v[32:35]\n"
"buffer_load_dwordx4 v[80:83], v254, s[84:87], s61 offen offset:0 sc1\n"
"v_mfma_f32_16x16x32_bf16 v[164:167], a[184:187], a[156:159], v[164:167]\n"
"v_mfma_f32_16x16x32_bf16 v[36:39], a[188:191], a[144:147], v[36:39]\n"
"v_mfma_f32_16x16x32_bf16 v[168:171], a[188:191], a[148:151], v[168:171]\n"
"buffer_load_dwordx4 v[84:87], v254, s[84:87], s61 offen offset:1024 sc1\n"
"s_add_u32 s61, s61, 6144\n"
"v_mfma_f32_16x16x32_bf16 v[40:43], a[188:191], a[152:155], v[40:43]\n"
"v_mfma_f32_16x16x32_bf16 v[172:175], a[188:191], a[156:159], v[172:175]\n"
"s_waitcnt lgkmcnt(0)\n"
"v_mfma_f32_16x16x32_bf16 v[176:179], a[224:227], a[208:211], v[176:179]\n"
"buffer_load_dwordx4 v[88:91], v254, s[84:87], s61 offen offset:0 sc1\n"
"v_mfma_f32_16x16x32_bf16 v[180:183], a[224:227], a[212:215], v[180:183]\n"
"ds_read_b128 a[144:147], v241 offset:0\n"
"v_mfma_f32_16x16x32_bf16 v[184:187], a[224:227], a[216:219], v[184:187]\n"
"ds_read_b128 a[148:151], v241 offset:2048\n"
"v_mfma_f32_16x16x32_bf16 v[188:191], a[224:227], a[220:223], v[188:191]\n"
"ds_read_b128 a[152:155], v241 offset:4096\n"
"v_mfma_f32_16x16x32_bf16 v[192:195], a[228:231], a[208:211], v[192:195]\n"
"buffer_load_dwordx4 v[92:95], v254, s[84:87], s61 offen offset:1024 sc1\n"
"s_add_u32 s61, s61, 6144\n"
"v_mfma_f32_16x16x32_bf16 v[196:199], a[228:231], a[212:215], v[196:199]\n"
"ds_read_b128 a[156:159], v241 offset:6144\n"
"ds_read_b64_tr_b16 a[160:161], v232 offset:24576\n"
"v_mfma_f32_16x16x32_bf16 v[200:203], a[228:231], a[216:219], v[200:203]\n"
"ds_read_b64_tr_b16 a[162:163], v232 offset:25600\n"
"v_mfma_f32_16x16x32_bf16 v[204:207], a[228:231], a[220:223], v[204:207]\n"
"buffer_load_dwordx4 v[96:99], v254, s[84:87], s61 offen offset:0 sc1\n"
"v_mfma_f32_16x16x32_bf16 v[208:211], a[232:235], a[208:211], v[208:211]\n"
"ds_read_b64_tr_b16 a[164:165], v233 offset:24576\n"
"v_mfma_f32_16x16x32_bf16 v[128:131], a[232:235], a[212:215], v[128:131]\n"
"ds_read_b64_tr_b16 a[166:167], v233 offset:25600\n"
"v_mfma_f32_16x16x32_bf16 v[0:3], a[232:235], a[216:219], v[0:3]\n"
"ds_read_b64_tr_b16 a[168:169], v234 offset:24576\n"
"v_mfma_f32_16x16x32_bf16 v[132:135], a[232:235], a[220:223], v[132:135]\n"
"buffer_load_dwordx4 v[100:103], v254, s[84:87], s61 offen offset:1024 sc1\n"
"s_add_u32 s61, s61, 6144\n"
"v_mfma_f32_16x16x32_bf16 v[4:7], a[236:239], a[208:211], v[4:7]\n"
"ds_read_b64_tr_b16 a[170:171], v234 offset:25600\n"
"ds_read_b64_tr_b16 a[172:173], v235 offset:24576\n"
"v_mfma_f32_16x16x32_bf16 v[136:139], a[236:239], a[212:215], v[136:139]\n"
"ds_read_b64_tr_b16 a[174:175], v235 offset:25600\n"
"v_mfma_f32_16x16x32_bf16 v[8:11], a[236:239], a[216:219], v[8:11]\n"
"buffer_load_dwordx4 v[104:107], v254, s[84:87], s61 offen offset:0 sc1\n"
"v_mfma_f32_16x16x32_bf16 v[140:143], a[236:239], a[220:223], v[140:143]\n"
"ds_read_b64_tr_b16 a[176:177], v236 offset:24576\n"
"ds_read_b64_tr_b16 a[178:179], v236 offset:25600\n"
"v_mfma_f32_16x16x32_bf16 v[12:15], a[240:243], a[208:211], v[12:15]\n"
"v_mfma_f32_16x16x32_bf16 v[144:147], a[240:243], a[212:215], v[144:147]\n"
"buffer_load_dwordx4 v[108:111], v254, s[84:87], s61 offen offset:1024 sc1\n"
"s_add_u32 s61, s61, 6144\n"
"v_mfma_f32_16x16x32_bf16 v[16:19], a[240:243], a[216:219], v[16:19]\n"
"ds_read_b64_tr_b16 a[180:181], v237 offset:24576\n"
"ds_read_b64_tr_b16 a[182:183], v237 offset:25600\n"
"v_mfma_f32_16x16x32_bf16 v[148:151], a[240:243], a[220:223], v[148:151]\n"
"ds_read_b64_tr_b16 a[184:185], v238 offset:24576\n"
"v_mfma_f32_16x16x32_bf16 v[20:23], a[244:247], a[208:211], v[20:23]\n"
"ds_read_b64_tr_b16 a[186:187], v238 offset:25600\n"
"v_mfma_f32_16x16x32_bf16 v[152:155], a[244:247], a[212:215], v[152:155]\n"
"buffer_load_dwordx4 v[112:115], v254, s[84:87], s61 offen offset:0 sc1\n"
"v_mfma_f32_16x16x32_bf16 v[24:27], a[244:247], a[216:219], v[24:27]\n"
"ds_read_b64_tr_b16 a[188:189], v239 offset:24576\n"
"ds_read_b64_tr_b16 a[190:191], v239 offset:25600\n"
"v_mfma_f32_16x16x32_bf16 v[156:159], a[244:247], a[220:223], v[156:159]\n"
"v_mfma_f32_16x16x32_bf16 v[28:31], a[248:251], a[208:211], v[28:31]\n"
"buffer_load_dwordx4 v[116:119], v254, s[84:87], s61 offen offset:1024 sc1\n"
"s_add_u32 s61, s61, 6144\n"
"v_mfma_f32_16x16x32_bf16 v[160:163], a[248:251], a[212:215], v[160:163]\n"
"v_mfma_f32_16x16x32_bf16 v[32:35], a[248:251], a[216:219], v[32:35]\n"
"v_mfma_f32_16x16x32_bf16 v[164:167], a[248:251], a[220:223], v[164:167]\n"
"v_mfma_f32_16x16x32_bf16 v[36:39], a[252:255], a[208:211], v[36:39]\n"
"buffer_load_dwordx4 v[120:123], v254, s[84:87], s61 offen offset:0 sc1\n"
"v_mfma_f32_16x16x32_bf16 v[168:171], a[252:255], a[212:215], v[168:171]\n"
"v_mfma_f32_16x16x32_bf16 v[40:43], a[252:255], a[216:219], v[40:43]\n"
"v_mfma_f32_16x16x32_bf16 v[172:175], a[252:255], a[220:223], v[172:175]\n"
"s_waitcnt lgkmcnt(0)\n"
"buffer_load_dwordx4 v[124:127], v254, s[84:87], s61 offen offset:1024 sc1\n"
"v_mfma_f32_16x16x32_bf16 v[176:179], a[160:163], a[144:147], v[176:179]\n"
"s_waitcnt vmcnt(28)\n"
"buffer_load_dword v45, v44, s[84:87], s82 offen sc1\n"
"v_mfma_f32_16x16x32_bf16 v[180:183], a[160:163], a[148:151], v[180:183]\n"
"s_barrier\n"
"buffer_load_dwordx4 v[60:63], v46, s[84:87], s53 offen sc1\n"
".Lpad10_%=:\n"
"s_cbranch_execz .Lpad14_%=\n"
"v_mfma_f32_16x16x32_bf16 v[184:187], a[160:163], a[152:155], v[184:187]\n"
"s_lshl_b32 s47, s78, 15\n"
"ds_read_b128 a[208:211], v240 offset:32768\n"
"s_sub_u32 s47, 901120, s47\n"
"v_mfma_f32_16x16x32_bf16 v[188:191], a[160:163], a[156:159], v[188:191]\n"
"s_sub_u32 s44, s54, s47\n"
"s_sub_u32 s45, s55, s47\n"
"s_xor_b32 s44, s44, 1073741824\n"
"v_mfma_f32_16x16x32_bf16 v[192:195], a[164:167], a[144:147], v[192:195]\n"
"s_xor_b32 s45, s45, 1073741824\n"
"ds_read_b128 a[212:215], v240 offset:34816\n"
"s_add_u32 s46, s82, 32768\n"
"v_mfma_f32_16x16x32_bf16 v[196:199], a[164:167], a[148:151], v[196:199]\n"
"s_add_u32 s58, s66, 0\n"
"buffer_store_dwordx4 a[64:67], v254, s[84:87], s58 offen offset:0 sc1\n"
"v_mfma_f32_16x16x32_bf16 v[200:203], a[164:167], a[152:155], v[200:203]\n"
"s_lshl_b32 s60, s78, 2\n"
"ds_read_b128 a[216:219], v240 offset:36864\n"
"ds_read_b128 a[220:223], v240 offset:38912\n"
"v_mfma_f32_16x16x32_bf16 v[204:207], a[164:167], a[156:159], v[204:207]\n"
"s_add_u32 s60, s60, s76\n"
"ds_read_b64_tr_b16 a[224:225], v232 offset:32768\n"
"s_mul_i32 s60, s60, 81920\n"
"v_mfma_f32_16x16x32_bf16 v[208:211], a[168:171], a[144:147], v[208:211]\n"
"s_add_u32 s60, s60, 60817408\n"
"ds_read_b64_tr_b16 a[226:227], v232 offset:33792\n"
"s_lshr_b32 s56, s77, 1\n"
"v_mfma_f32_16x16x32_bf16 v[128:131], a[168:171], a[148:151], v[128:131]\n"
"s_add_u32 s58, s66, 0\n"
"ds_read_b64_tr_b16 a[228:229], v233 offset:32768\n"
"s_lshl_b32 s56, s56, 5\n"
"v_mfma_f32_16x16x32_bf16 v[0:3], a[168:171], a[152:155], v[0:3]\n"
"s_and_b32 s61, s77, 1\n"
"ds_read_b64_tr_b16 a[230:231], v233 offset:33792\n"
"s_add_u32 s56, s56, s61\n"
"v_mfma_f32_16x16x32_bf16 v[132:135], a[168:171], a[156:159], v[132:135]\n"
"s_lshl_b32 s56, s56, 3\n"
"buffer_store_dwordx4 a[68:71], v254, s[84:87], s58 offen offset:1024 sc1\n"
"v_mfma_f32_16x16x32_bf16 v[4:7], a[172:175], a[144:147], v[4:7]\n"
"s_add_u32 s56, s56, s60\n"
"ds_read_b64_tr_b16 a[232:233], v234 offset:32768\n"
"ds_read_b64_tr_b16 a[234:235], v234 offset:33792\n"
"v_mfma_f32_16x16x32_bf16 v[136:139], a[172:175], a[148:151], v[136:139]\n"
"s_add_u32 s56, s56, 16384\n"
"ds_read_b64_tr_b16 a[236:237], v235 offset:32768\n"
"s_lshl_b32 s61, s77, 9\n"
"v_mfma_f32_16x16x32_bf16 v[8:11], a[172:175], a[152:155], v[8:11]\n"
"s_add_u32 s60, s60, s61\n"
"ds_read_b64_tr_b16 a[238:239], v235 offset:33792\n"
"s_waitcnt vmcnt(24)\n"
"v_mfma_f32_16x16x32_bf16 v[140:143], a[172:175], a[156:159], v[140:143]\n"
"ds_read_b64_tr_b16 a[240:241], v236 offset:32768\n"
"v_add_f32 v64, v64, v72\n"
"v_mfma_f32_16x16x32_bf16 v[12:15], a[176:179], a[144:147], v[12:15]\n"
"ds_read_b64_tr_b16 a[242:243], v236 offset:33792\n"
"v_add_f32 v65, v65, v73\n"
"v_mfma_f32_16x16x32_bf16 v[144:147], a[176:179], a[148:151], v[144:147]\n"
"s_add_u32 s58, s66, 4096\n"
"ds_read_b64_tr_b16 a[244:245], v237 offset:32768\n"
"v_add_f32 v66, v66, v74\n"
"v_mfma_f32_16x16x32_bf16 v[16:19], a[176:179], a[152:155], v[16:19]\n"
"ds_read_b64_tr_b16 a[246:247], v237 offset:33792\n"
"v_add_f32 v67, v67, v75\n"
"v_mfma_f32_16x16x32_bf16 v[148:151], a[176:179], a[156:159], v[148:151]\n"
"buffer_store_dwordx4 a[72:75], v254, s[84:87], s58 offen offset:0 sc1\n"
"v_mfma_f32_16x16x32_bf16 v[20:23], a[180:183], a[144:147], v[20:23]\n"
"ds_read_b64_tr_b16 a[248:249], v238 offset:32768\n"
"ds_read_b64_tr_b16 a[250:251], v238 offset:33792\n"
"v_mfma_f32_16x16x32_bf16 v[152:155], a[180:183], a[148:151], v[152:155]\n"
"ds_read_b64_tr_b16 a[252:253], v239 offset:32768\n"
"v_add_f32 v68, v68, v76\n"
"v_mfma_f32_16x16x32_bf16 v[24:27], a[180:183], a[152:155], v[24:27]\n"
"ds_read_b64_tr_b16 a[254:255], v239 offset:33792\n"
"v_add_f32 v69, v69, v77\n"
"v_mfma_f32_16x16x32_bf16 v[156:159], a[180:183], a[156:159], v[156:159]\n"
"v_add_f32 v70, v70, v78\n"
"v_add_f32 v71, v71, v79\n"
"s_waitcnt vmcnt(15)\n"
"v_mfma_f32_16x16x32_bf16 v[28:31], a[184:187], a[144:147], v[28:31]\n"
"s_add_u32 s58, s66, 4096\n"
"v_add_f32 v64, v64, v80\n"
"v_add_f32 v65, v65, v81\n"
"v_mfma_f32_16x16x32_bf16 v[160:163], a[184:187], a[148:151], v[160:163]\n"
"v_add_f32 v66, v66, v82\n"
"v_add_f32 v67, v67, v83\n"
"v_mfma_f32_16x16x32_bf16 v[32:35], a[184:187], a[152:155], v[32:35]\n"
"buffer_store_dwordx4 a[76:79], v254, s[84:87], s58 offen offset:1024 sc1\n"
"v_mfma_f32_16x16x32_bf16 v[164:167], a[184:187], a[156:159], v[164:167]\n"
"v_add_f32 v68, v68, v84\n"
"v_add_f32 v69, v69, v85\n"
"v_mfma_f32_16x16x32_bf16 v[36:39], a[188:191], a[144:147], v[36:39]\n"
"v_add_f32 v70, v70, v86\n"
"v_add_f32 v71, v71, v87\n"
"s_waitcnt vmcnt(14)\n"
"v_mfma_f32_16x16x32_bf16 v[168:171], a[188:191], a[148:151], v[168:171]\n"
"v_add_f32 v64, v64, v88\n"
"v_add_f32 v65, v65, v89\n"
"v_mfma_f32_16x16x32_bf16 v[40:43], a[188:191], a[152:155], v[40:43]\n"
"v_add_f32 v66, v66, v90\n"
"v_add_f32 v67, v67, v91\n"
"v_mfma_f32_16x16x32_bf16 v[172:175], a[188:191], a[156:159], v[172:175]\n"
"s_waitcnt lgkmcnt(0)\n"
"v_add_f32 v68, v68, v92\n"
"v_add_f32 v69, v69, v93\n"
"v_mfma_f32_16x16x32_bf16 v[176:179], a[224:227], a[208:211], v[176:179]\n"
"s_add_u32 s58, s67, 0\n"
"ds_read_b128 a[144:147], v241 offset:32768\n"
"v_add_f32 v70, v70, v94\n"
"v_mfma_f32_16x16x32_bf16 v[180:183], a[224:227], a[212:215], v[180:183]\n"
"v_add_f32 v71, v71, v95\n"
"s_waitcnt vmcnt(12)\n"
"v_add_f32 v64, v64, v96\n"
"v_mfma_f32_16x16x32_bf16 v[184:187], a[224:227], a[216:219], v[184:187]\n"
"buffer_store_dwordx4 a[80:83], v254, s[84:87], s58 offen offset:0 sc1\n"
"v_mfma_f32_16x16x32_bf16 v[188:191], a[224:227], a[220:223], v[188:191]\n"
"ds_read_b128 a[148:151], v241 offset:34816\n"
"ds_read_b128 a[152:155], v241 offset:36864\n"
"v_mfma_f32_16x16x32_bf16 v[192:195], a[228:231], a[208:211], v[192:195]\n"
"ds_read_b128 a[156:159], v241 offset:38912\n"
"v_add_f32 v65, v65, v97\n"
"v_mfma_f32_16x16x32_bf16 v[196:199], a[228:231], a[212:215], v[196:199]\n"
"ds_read_b64_tr_b16 a[160:161], v232 offset:40960\n"
"v_add_f32 v66, v66, v98\n"
"v_mfma_f32_16x16x32_bf16 v[200:203], a[228:231], a[216:219], v[200:203]\n"
"ds_read_b64_tr_b16 a[162:163], v232 offset:41984\n"
"v_add_f32 v67, v67, v99\n"
"v_mfma_f32_16x16x32_bf16 v[204:207], a[228:231], a[220:223], v[204:207]\n"
"s_add_u32 s58, s67, 0\n"
"ds_read_b64_tr_b16 a[164:165], v233 offset:40960\n"
"v_add_f32 v68, v68, v100\n"
"v_mfma_f32_16x16x32_bf16 v[208:211], a[232:235], a[208:211], v[208:211]\n"
"v_add_f32 v69, v69, v101\n"
"v_add_f32 v70, v70, v102\n"
"v_mfma_f32_16x16x32_bf16 v[128:131], a[232:235], a[212:215], v[128:131]\n"
"buffer_store_dwordx4 a[84:87], v254, s[84:87], s58 offen offset:1024 sc1\n"
"v_mfma_f32_16x16x32_bf16 v[0:3], a[232:235], a[216:219], v[0:3]\n"
"ds_read_b64_tr_b16 a[166:167], v233 offset:41984\n"
"ds_read_b64_tr_b16 a[168:169], v234 offset:40960\n"
"v_mfma_f32_16x16x32_bf16 v[132:135], a[232:235], a[220:223], v[132:135]\n"
"ds_read_b64_tr_b16 a[170:171], v234 offset:41984\n"
"v_add_f32 v71, v71, v103\n"
"s_waitcnt vmcnt(12)\n"
"v_mfma_f32_16x16x32_bf16 v[4:7], a[236:239], a[208:211], v[4:7]\n"
"ds_read_b64_tr_b16 a[172:173], v235 offset:40960\n"
"v_add_f32 v64, v64, v104\n"
"v_mfma_f32_16x16x32_bf16 v[136:139], a[236:239], a[212:215], v[136:139]\n"
"ds_read_b64_tr_b16 a[174:175], v235 offset:41984\n"
"v_add_f32 v65, v65, v105\n"
"v_mfma_f32_16x16x32_bf16 v[8:11], a[236:239], a[216:219], v[8:11]\n"
"ds_read_b64_tr_b16 a[176:177], v236 offset:40960\n"
"v_add_f32 v66, v66, v106\n"
"v_mfma_f32_16x16x32_bf16 v[140:143], a[236:239], a[220:223], v[140:143]\n"
"s_add_u32 s58, s67, 0\n"
"ds_read_b64_tr_b16 a[178:179], v236 offset:41984\n"
"v_add_f32 v67, v67, v107\n"
"v_mfma_f32_16x16x32_bf16 v[12:15], a[240:243], a[208:211], v[12:15]\n"
"v_add_f32 v68, v68, v108\n"
"v_add_f32 v69, v69, v109\n"
"v_mfma_f32_16x16x32_bf16 v[144:147], a[240:243], a[212:215], v[144:147]\n"
"buffer_store_dwordx4 a[88:91], v254, s[84:87], s58 offen offset:2048 sc1\n"
"v_mfma_f32_16x16x32_bf16 v[16:19], a[240:243], a[216:219], v[16:19]\n"
"ds_read_b64_tr_b16 a[180:181], v237 offset:40960\n"
"ds_read_b64_tr_b16 a[182:183], v237 offset:41984\n"
"v_mfma_f32_16x16x32_bf16 v[148:151], a[240:243], a[220:223], v[148:151]\n"
"ds_read_b64_tr_b16 a[184:185], v238 offset:40960\n"
"v_add_f32 v70, v70, v110\n"
"v_mfma_f32_16x16x32_bf16 v[20:23], a[244:247], a[208:211], v[20:23]\n"
"ds_read_b64_tr_b16 a[186:187], v238 offset:41984\n"
"v_add_f32 v71, v71, v111\n"
"s_waitcnt vmcnt(11)\n"
"v_mfma_f32_16x16x32_bf16 v[152:155], a[244:247], a[212:215], v[152:155]\n"
"ds_read_b64_tr_b16 a[188:189], v239 offset:40960\n"
"v_add_f32 v64, v64, v112\n"
"v_mfma_f32_16x16x32_bf16 v[24:27], a[244:247], a[216:219], v[24:27]\n"
"s_add_u32 s58, s67, 0\n"
"ds_read_b64_tr_b16 a[190:191], v239 offset:41984\n"
"v_add_f32 v65, v65, v113\n"
"v_mfma_f32_16x16x32_bf16 v[156:159], a[244:247], a[220:223], v[156:159]\n"
"v_add_f32 v66, v66, v114\n"
"v_add_f32 v67, v67, v115\n"
"v_mfma_f32_16x16x32_bf16 v[28:31], a[248:251], a[208:211], v[28:31]\n"
"s_waitcnt vmcnt(7)\n"
"buffer_store_dwordx4 a[92:95], v254, s[84:87], s58 offen offset:3072 sc1\n"
"v_mfma_f32_16x16x32_bf16 v[160:163], a[248:251], a[212:215], v[160:163]\n"
"v_xor_b32 v60, s88, v60\n"
"v_xor_b32 v61, s88, v61\n"
"v_mfma_f32_16x16x32_bf16 v[32:35], a[248:251], a[216:219], v[32:35]\n"
"v_xor_b32 v62, s88, v62\n"
"v_xor_b32 v63, s88, v63\n"
"v_mfma_f32_16x16x32_bf16 v[164:167], a[248:251], a[220:223], v[164:167]\n"
"v_or_b32 v60, v60, v61\n"
"v_or_b32 v60, v60, v62\n"
"v_mfma_f32_16x16x32_bf16 v[36:39], a[252:255], a[208:211], v[36:39]\n"
"v_add_f32 v68, v68, v116\n"
"v_add_f32 v69, v69, v117\n"
"v_add_f32 v70, v70, v118\n"
"v_add_f32 v71, v71, v119\n"
"s_waitcnt vmcnt(10)\n"
"v_add_f32 v64, v64, v120\n"
"v_add_f32 v65, v65, v121\n"
"v_add_f32 v66, v66, v122\n"
"v_add_f32 v67, v67, v123\n"
"v_add_f32 v68, v68, v124\n"
"v_add_f32 v69, v69, v125\n"
"v_add_f32 v70, v70, v126\n"
"v_add_f32 v71, v71, v127\n"
"v_cvt_pk_bf16_f32 v64, v64, v65\n"
"v_cvt_pk_bf16_f32 v65, v66, v67\n"
"v_cvt_pk_bf16_f32 v66, v68, v69\n"
"v_cvt_pk_bf16_f32 v67, v70, v71\n"
"ds_write_b128 v52, v[56:59]\n"
"ds_write_b64 v48, v[64:65]\n"
"ds_write_b64 v49, v[66:67]\n"
"v_mfma_f32_16x16x32_bf16 v[168:171], a[252:255], a[212:215], v[168:171]\n"
"v_or_b32 v60, v60, v63\n"
"v_xor_b32 v45, s88, v45\n"
"v_mfma_f32_16x16x32_bf16 v[40:43], a[252:255], a[216:219], v[40:43]\n"
"v_or_b32 v45, v45, v60\n"
"v_cmp_ne_u32 vcc, 0, v45\n"
"s_cbranch_vccnz .Lslow2_%=\n"
".Lback2_%=:\n"
"v_mfma_f32_16x16x32_bf16 v[172:175], a[252:255], a[220:223], v[172:175]\n"
"s_waitcnt lgkmcnt(0)\n"
"v_mov_b32 v244, v254\n"
"v_mov_b32 v245, v254\n"
"v_add_u32 v246, s57, v254\n"
"v_and_b32 v247, 1, v255\n"
"v_mul_u32_u24 v247, 0xaaaa, v247\n"
"v_add_u32 v247, 0x4444, v247\n"
"v_mfma_f32_16x16x32_bf16 v[176:179], a[160:163], a[144:147], v[176:179]\n"
"s_waitcnt vmcnt(22)\n"
"v_mfma_f32_16x16x32_bf16 v[180:183], a[160:163], a[148:151], v[180:183]\n"
"s_barrier\n"
"v_mfma_f32_16x16x32_bf16 v[184:187], a[160:163], a[152:155], v[184:187]\n"
"s_cmp_lt_u32 s76, 2\n"
"s_cselect_b32 s52, 0, 1073741824\n"
"s_cselect_b32 s54, 16384, 0\n"
"v_mfma_f32_16x16x32_bf16 v[188:191], a[160:163], a[156:159], v[188:191]\n"
"s_sub_u32 s55, 1048576, s54\n"
"ds_read_b128 a[208:211], v242 offset:0\n"
"s_add_u32 m0, s57, 49152\n"
"v_mfma_f32_16x16x32_bf16 v[192:195], a[164:167], a[144:147], v[192:195]\n"
"buffer_load_dwordx4 v246, s[36:39], s52 offen offset:0 sc1 lds\n"
"v_mfma_f32_16x16x32_bf16 v[196:199], a[164:167], a[148:151], v[196:199]\n"
"ds_read_b128 a[212:215], v242 offset:2048\n"
"ds_read_b128 a[216:219], v242 offset:4096\n"
"v_mfma_f32_16x16x32_bf16 v[200:203], a[164:167], a[152:155], v[200:203]\n"
"buffer_load_dwordx4 v246, s[36:39], s52 offen offset:1024 sc1 lds\n"
"v_mfma_f32_16x16x32_bf16 v[204:207], a[164:167], a[156:159], v[204:207]\n"
"ds_read_b128 a[220:223], v242 offset:6144\n"
"ds_read_b64_tr_b16 a[224:225], v232 offset:49152\n"
"v_mfma_f32_16x16x32_bf16 v[208:211], a[168:171], a[144:147], v[208:211]\n"
"buffer_load_dwordx4 v246, s[36:39], s52 offen offset:2048 sc1 lds\n"
"v_mfma_f32_16x16x32_bf16 v[128:131], a[168:171], a[148:151], v[128:131]\n"
"ds_read_b64_tr_b16 a[226:227], v232 offset:50176\n"
"ds_read_b64_tr_b16 a[228:229], v233 offset:49152\n"
"v_mfma_f32_16x16x32_bf16 v[0:3], a[168:171], a[152:155], v[0:3]\n"
"ds_read_b64_tr_b16 a[230:231], v233 offset:50176\n"
"ds_read_b128 v[88:91], v50 offset:0\n"
"v_mfma_f32_16x16x32_bf16 v[132:135], a[168:171], a[156:159], v[132:135]\n"
"buffer_load_dwordx4 v246, s[36:39], s52 offen offset:3072 sc1 lds\n"
"v_mfma_f32_16x16x32_bf16 v[4:7], a[172:175], a[144:147], v[4:7]\n"
"ds_read_b64_tr_b16 a[232:233], v234 offset:49152\n"
"ds_read_b64_tr_b16 a[234:235], v234 offset:50176\n"
"v_mfma_f32_16x16x32_bf16 v[136:139], a[172:175], a[148:151], v[136:139]\n"
"s_add_u32 m0, s57, 0\n"
"ds_read_b64_tr_b16 a[236:237], v235 offset:49152\n"
"ds_read_b128 v[92:95], v50 offset:576\n"
"v_mfma_f32_16x16x32_bf16 v[8:11], a[172:175], a[152:155], v[8:11]\n"
"buffer_load_dwordx4 v246, s[36:39], s54 offen offset:0 sc1 lds\n"
"v_mfma_f32_16x16x32_bf16 v[140:143], a[172:175], a[156:159], v[140:143]\n"
"ds_read_b64_tr_b16 a[238:239], v235 offset:50176\n"
"ds_read_b64_tr_b16 a[240:241], v236 offset:49152\n"
"v_mfma_f32_16x16x32_bf16 v[12:15], a[176:179], a[144:147], v[12:15]\n"
"ds_read_b64_tr_b16 a[242:243], v236 offset:50176\n"
"ds_read_b128 v[96:99], v50 offset:1152\n"
"v_mfma_f32_16x16x32_bf16 v[144:147], a[176:179], a[148:151], v[144:147]\n"
"buffer_load_dwordx4 v246, s[36:39], s54 offen offset:1024 sc1 lds\n"
"v_mfma_f32_16x16x32_bf16 v[16:19], a[176:179], a[152:155], v[16:19]\n"
"ds_read_b64_tr_b16 a[244:245], v237 offset:49152\n"
"ds_read_b64_tr_b16 a[246:247], v237 offset:50176\n"
"v_mfma_f32_16x16x32_bf16 v[148:151], a[176:179], a[156:159], v[148:151]\n"
"ds_read_b64_tr_b16 a[248:249], v238 offset:49152\n"
"ds_read_b128 v[100:103], v50 offset:1728\n"
"v_mfma_f32_16x16x32_bf16 v[20:23], a[180:183], a[144:147], v[20:23]\n"
"buffer_load_dwordx4 v246, s[36:39], s54 offen offset:2048 sc1 lds\n"
"v_mfma_f32_16x16x32_bf16 v[152:155], a[180:183], a[148:151], v[152:155]\n"
"ds_read_b64_tr_b16 a[250:251], v238 offset:50176\n"
"ds_read_b64_tr_b16 a[252:253], v239 offset:49152\n"
"v_mfma_f32_16x16x32_bf16 v[24:27], a[180:183], a[152:155], v[24:27]\n"
"ds_read_b64_tr_b16 a[254:255], v239 offset:50176\n"
"ds_read_b128 v[104:107], v50 offset:2304\n"
"v_mfma_f32_16x16x32_bf16 v[156:159], a[180:183], a[156:159], v[156:159]\n"
"buffer_load_dwordx4 v246, s[36:39], s54 offen offset:3072 sc1 lds\n"
"v_mfma_f32_16x16x32_bf16 v[28:31], a[184:187], a[144:147], v[28:31]\n"
"ds_read_b128 v[108:111], v50 offset:2880\n"
"ds_read_b128 v[112:115], v50 offset:16\n"
"v_mfma_f32_16x16x32_bf16 v[160:163], a[184:187], a[148:151], v[160:163]\n"
"s_add_u32 s54, s54, 16384\n"
"s_add_u32 m0, s57, 16384\n"
"ds_read_b128 v[116:119], v50 offset:592\n"
"v_mfma_f32_16x16x32_bf16 v[32:35], a[184:187], a[152:155], v[32:35]\n"
"buffer_load_dwordx4 v246, s[36:39], s54 offen offset:0 sc1 lds\n"
"v_mfma_f32_16x16x32_bf16 v[164:167], a[184:187], a[156:159], v[164:167]\n"
"ds_read_b128 v[120:123], v50 offset:1168\n"
"s_waitcnt lgkmcnt(12)\n"
"v_mfma_f32_16x16x32_bf16 v[36:39], a[188:191], a[144:147], v[36:39]\n"
"v_mfma_f32_16x16x32_bf16 v[64:67], v[88:91], v[212:215], 0\n"
"ds_read_b128 v[124:127], v50 offset:1744\n"
"v_mfma_f32_16x16x32_bf16 v[168:171], a[188:191], a[148:151], v[168:171]\n"
"buffer_load_dwordx4 v246, s[36:39], s54 offen offset:1024 sc1 lds\n"
"v_mfma_f32_16x16x32_bf16 v[68:71], v[92:95], v[212:215], 0\n"
"ds_read_b128 v[88:91], v50 offset:2320\n"
"v_mfma_f32_16x16x32_bf16 v[40:43], a[188:191], a[152:155], v[40:43]\n"
"v_mfma_f32_16x16x32_bf16 v[72:75], v[96:99], v[212:215], 0\n"
"ds_read_b128 v[92:95], v50 offset:2896\n"
"s_waitcnt lgkmcnt(6)\n"
"v_mfma_f32_16x16x32_bf16 v[172:175], a[188:191], a[156:159], v[172:175]\n"
"s_waitcnt lgkmcnt(8)\n"
"v_mfma_f32_16x16x32_bf16 v[76:79], v[100:103], v[212:215], 0\n"
"ds_read_b128 v[96:99], v50 offset:32\n"
"v_mfma_f32_16x16x32_bf16 v[176:179], a[224:227], a[208:211], v[176:179]\n"
"buffer_load_dwordx4 v246, s[36:39], s54 offen offset:2048 sc1 lds\n"
"v_mfma_f32_16x16x32_bf16 v[80:83], v[104:107], v[212:215], 0\n"
"ds_read_b128 a[144:147], v243 offset:0\n"
"ds_read_b128 v[100:103], v50 offset:608\n"
"v_mfma_f32_16x16x32_bf16 v[180:183], a[224:227], a[212:215], v[180:183]\n"
"v_mfma_f32_16x16x32_bf16 v[84:87], v[108:111], v[212:215], 0\n"
"ds_read_b128 v[104:107], v50 offset:1184\n"
"s_waitcnt lgkmcnt(7)\n"
"v_mfma_f32_16x16x32_bf16 v[184:187], a[224:227], a[216:219], v[184:187]\n"
"ds_read_b128 a[148:151], v243 offset:2048\n"
"v_mfma_f32_16x16x32_bf16 v[64:67], v[112:115], v[216:219], v[64:67]\n"
"ds_read_b128 v[108:111], v50 offset:1760\n"
"v_mfma_f32_16x16x32_bf16 v[188:191], a[224:227], a[220:223], v[188:191]\n"
"buffer_load_dwordx4 v246, s[36:39], s54 offen offset:3072 sc1 lds\n"
"v_mfma_f32_16x16x32_bf16 v[68:71], v[116:119], v[216:219], v[68:71]\n"
"ds_read_b128 a[152:155], v243 offset:4096\n"
"ds_read_b128 v[112:115], v50 offset:2336\n"
"v_mfma_f32_16x16x32_bf16 v[192:195], a[228:231], a[208:211], v[192:195]\n"
"ds_read_b128 a[156:159], v243 offset:6144\n"
"v_mfma_f32_16x16x32_bf16 v[72:75], v[120:123], v[216:219], v[72:75]\n"
"ds_read_b128 v[116:119], v50 offset:2912\n"
"s_waitcnt lgkmcnt(10)\n"
"v_mfma_f32_16x16x32_bf16 v[196:199], a[228:231], a[212:215], v[196:199]\n"
"s_add_u32 s54, s54, 16384\n"
"ds_read_b64_tr_b16 a[160:161], v232 offset:57344\n"
"s_add_u32 m0, s57, 81920\n"
"v_mfma_f32_16x16x32_bf16 v[76:79], v[124:127], v[216:219], v[76:79]\n"
"ds_read_b128 v[120:123], v50 offset:48\n"
"v_mfma_f32_16x16x32_bf16 v[200:203], a[228:231], a[216:219], v[200:203]\n"
"buffer_load_dwordx4 v246, s[40:43], s55 offen offset:0 sc1 lds\n"
"v_mfma_f32_16x16x32_bf16 v[80:83], v[88:91], v[216:219], v[80:83]\n"
"ds_read_b64_tr_b16 a[162:163], v232 offset:58368\n"
"ds_read_b128 v[124:127], v50 offset:624\n"
"v_mfma_f32_16x16x32_bf16 v[204:207], a[228:231], a[220:223], v[204:207]\n"
"ds_read_b64_tr_b16 a[164:165], v233 offset:57344\n"
"v_mfma_f32_16x16x32_bf16 v[84:87], v[92:95], v[216:219], v[84:87]\n"
"ds_read_b128 v[88:91], v50 offset:1200\n"
"s_waitcnt lgkmcnt(12)\n"
"v_mfma_f32_16x16x32_bf16 v[208:211], a[232:235], a[208:211], v[208:211]\n"
"v_mfma_f32_16x16x32_bf16 v[64:67], v[96:99], v[220:223], v[64:67]\n"
"ds_read_b128 v[92:95], v50 offset:1776\n"
"v_mfma_f32_16x16x32_bf16 v[128:131], a[232:235], a[212:215], v[128:131]\n"
"buffer_load_dwordx4 v246, s[40:43], s55 offen offset:1024 sc1 lds\n"
"v_mfma_f32_16x16x32_bf16 v[68:71], v[100:103], v[220:223], v[68:71]\n"
"ds_read_b64_tr_b16 a[166:167], v233 offset:58368\n"
"ds_read_b128 v[96:99], v50 offset:2352\n"
"v_mfma_f32_16x16x32_bf16 v[0:3], a[232:235], a[216:219], v[0:3]\n"
"ds_read_b64_tr_b16 a[168:169], v234 offset:57344\n"
"v_mfma_f32_16x16x32_bf16 v[72:75], v[104:107], v[220:223], v[72:75]\n"
"ds_read_b128 v[100:103], v50 offset:2928\n"
"s_waitcnt lgkmcnt(11)\n"
"v_mfma_f32_16x16x32_bf16 v[132:135], a[232:235], a[220:223], v[132:135]\n"
"ds_read_b64_tr_b16 a[170:171], v234 offset:58368\n"
"v_mfma_f32_16x16x32_bf16 v[76:79], v[108:111], v[220:223], v[76:79]\n"
"ds_read_b128 v[104:107], v50 offset:64\n"
"v_mfma_f32_16x16x32_bf16 v[4:7], a[236:239], a[208:211], v[4:7]\n"
"buffer_load_dwordx4 v246, s[40:43], s55 offen offset:2048 sc1 lds\n"
"v_mfma_f32_16x16x32_bf16 v[80:83], v[112:115], v[220:223], v[80:83]\n"
"ds_read_b64_tr_b16 a[172:173], v235 offset:57344\n"
"ds_read_b128 v[108:111], v50 offset:640\n"
"v_mfma_f32_16x16x32_bf16 v[136:139], a[236:239], a[212:215], v[136:139]\n"
"ds_read_b64_tr_b16 a[174:175], v235 offset:58368\n"
"v_mfma_f32_16x16x32_bf16 v[84:87], v[116:119], v[220:223], v[84:87]\n"
"ds_read_b128 v[112:115], v50 offset:1216\n"
"s_waitcnt lgkmcnt(11)\n"
"v_mfma_f32_16x16x32_bf16 v[8:11], a[236:239], a[216:219], v[8:11]\n"
"ds_read_b64_tr_b16 a[176:177], v236 offset:57344\n"
"v_mfma_f32_16x16x32_bf16 v[64:67], v[120:123], v[224:227], v[64:67]\n"
"ds_read_b128 v[116:119], v50 offset:1792\n"
"v_mfma_f32_16x16x32_bf16 v[140:143], a[236:239], a[220:223], v[140:143]\n"
"buffer_load_dwordx4 v246, s[40:43], s55 offen offset:3072 sc1 lds\n"
"v_mfma_f32_16x16x32_bf16 v[68:71], v[124:127], v[224:227], v[68:71]\n"
"ds_read_b64_tr_b16 a[178:179], v236 offset:58368\n"
"ds_read_b128 v[120:123], v50 offset:2368\n"
"v_mfma_f32_16x16x32_bf16 v[12:15], a[240:243], a[208:211], v[12:15]\n"
"v_mfma_f32_16x16x32_bf16 v[72:75], v[88:91], v[224:227], v[72:75]\n"
"ds_read_b128 v[124:127], v50 offset:2944\n"
"s_waitcnt lgkmcnt(11)\n"
"v_mfma_f32_16x16x32_bf16 v[144:147], a[240:243], a[212:215], v[144:147]\n"
"s_sub_u32 s55, s55, 16384\n"
"ds_read_b64_tr_b16 a[180:181], v237 offset:57344\n"
"s_add_u32 m0, s57, 32768\n"
"v_mfma_f32_16x16x32_bf16 v[76:79], v[92:95], v[224:227], v[76:79]\n"
"v_mfma_f32_16x16x32_bf16 v[16:19], a[240:243], a[216:219], v[16:19]\n"
"buffer_load_dwordx4 v246, s[36:39], s54 offen offset:0 sc1 lds\n"
"v_mfma_f32_16x16x32_bf16 v[80:83], v[96:99], v[224:227], v[80:83]\n"
"ds_read_b64_tr_b16 a[182:183], v237 offset:58368\n"
"v_mfma_f32_16x16x32_bf16 v[148:151], a[240:243], a[220:223], v[148:151]\n"
"ds_read_b64_tr_b16 a[184:185], v238 offset:57344\n"
"v_mfma_f32_16x16x32_bf16 v[84:87], v[100:103], v[224:227], v[84:87]\n"
"s_waitcnt lgkmcnt(8)\n"
"v_mfma_f32_16x16x32_bf16 v[20:23], a[244:247], a[208:211], v[20:23]\n"
"ds_read_b64_tr_b16 a[186:187], v238 offset:58368\n"
"v_mfma_f32_16x16x32_bf16 v[64:67], v[104:107], v[228:231], v[64:67]\n"
"v_mfma_f32_16x16x32_bf16 v[152:155], a[244:247], a[212:215], v[152:155]\n"
"buffer_load_dwordx4 v246, s[36:39], s54 offen offset:1024 sc1 lds\n"
"v_mfma_f32_16x16x32_bf16 v[68:71], v[108:111], v[228:231], v[68:71]\n"
"ds_read_b64_tr_b16 a[188:189], v239 offset:57344\n"
"v_mfma_f32_16x16x32_bf16 v[24:27], a[244:247], a[216:219], v[24:27]\n"
"ds_read_b64_tr_b16 a[190:191], v239 offset:58368\n"
"v_mfma_f32_16x16x32_bf16 v[72:75], v[112:115], v[228:231], v[72:75]\n"
"s_waitcnt lgkmcnt(6)\n"
"v_mfma_f32_16x16x32_bf16 v[156:159], a[244:247], a[220:223], v[156:159]\n"
"v_mfma_f32_16x16x32_bf16 v[76:79], v[116:119], v[228:231], v[76:79]\n"
"v_mfma_f32_16x16x32_bf16 v[28:31], a[248:251], a[208:211], v[28:31]\n"
"buffer_load_dwordx4 v246, s[36:39], s54 offen offset:2048 sc1 lds\n"
"v_mfma_f32_16x16x32_bf16 v[80:83], v[120:123], v[228:231], v[80:83]\n"
"v_mfma_f32_16x16x32_bf16 v[160:163], a[248:251], a[212:215], v[160:163]\n"
"v_mfma_f32_16x16x32_bf16 v[84:87], v[124:127], v[228:231], v[84:87]\n"
"v_mfma_f32_16x16x32_bf16 v[32:35], a[248:251], a[216:219], v[32:35]\n"
"v_mfma_f32_16x16x32_bf16 v[164:167], a[248:251], a[220:223], v[164:167]\n"
"buffer_load_dwordx4 v246, s[36:39], s54 offen offset:3072 sc1 lds\n"
"v_mfma_f32_16x16x32_bf16 v[36:39], a[252:255], a[208:211], v[36:39]\n"
"s_nop 3\n"
"v_add_f32 v84, 0x358637bd, v84\n"
"v_mfma_f32_16x16x32_bf16 v[168:171], a[252:255], a[212:215], v[168:171]\n"
"s_add_u32 s54, s54, 16384\n"
"v_rcp_f32 v84, v84\n"
"v_mfma_f32_16x16x32_bf16 v[40:43], a[252:255], a[216:219], v[40:43]\n"
"v_mul_f32 v64, v64, v84\n"
"v_mul_f32 v65, v65, v84\n"
"v_mfma_f32_16x16x32_bf16 v[172:175], a[252:255], a[220:223], v[172:175]\n"
"s_waitcnt lgkmcnt(0)\n"
"v_cvt_pk_bf16_f32 v64, v64, v65\n"
"v_mul_f32 v66, v66, v84\n"
"v_mfma_f32_16x16x32_bf16 v[176:179], a[160:163], a[144:147], v[176:179]\n"
"v_mul_f32 v67, v67, v84\n"
"v_cvt_pk_bf16_f32 v65, v66, v67\n"
"v_mfma_f32_16x16x32_bf16 v[180:183], a[160:163], a[148:151], v[180:183]\n"
"s_barrier\n"
"v_mul_f32 v68, v68, v84\n"
"v_mul_f32 v69, v69, v84\n"
"v_mfma_f32_16x16x32_bf16 v[184:187], a[160:163], a[152:155], v[184:187]\n"
"v_cvt_pk_bf16_f32 v66, v68, v69\n"
"v_mul_f32 v70, v70, v84\n"
"v_mfma_f32_16x16x32_bf16 v[188:191], a[160:163], a[156:159], v[188:191]\n"
"v_mul_f32 v71, v71, v84\n"
"v_cvt_pk_bf16_f32 v67, v70, v71\n"
"v_mfma_f32_16x16x32_bf16 v[192:195], a[164:167], a[144:147], v[192:195]\n"
"v_mul_f32 v72, v72, v84\n"
"v_mul_f32 v73, v73, v84\n"
"v_mfma_f32_16x16x32_bf16 v[196:199], a[164:167], a[148:151], v[196:199]\n"
"v_cvt_pk_bf16_f32 v68, v72, v73\n"
"v_mul_f32 v74, v74, v84\n"
"v_mfma_f32_16x16x32_bf16 v[200:203], a[164:167], a[152:155], v[200:203]\n"
"v_mul_f32 v75, v75, v84\n"
"v_cvt_pk_bf16_f32 v69, v74, v75\n"
"v_mfma_f32_16x16x32_bf16 v[204:207], a[164:167], a[156:159], v[204:207]\n"
"v_mul_f32 v76, v76, v84\n"
"v_mul_f32 v77, v77, v84\n"
"v_mfma_f32_16x16x32_bf16 v[208:211], a[168:171], a[144:147], v[208:211]\n"
"v_cvt_pk_bf16_f32 v70, v76, v77\n"
"v_mul_f32 v78, v78, v84\n"
"v_mfma_f32_16x16x32_bf16 v[128:131], a[168:171], a[148:151], v[128:131]\n"
"v_mul_f32 v79, v79, v84\n"
"v_cvt_pk_bf16_f32 v71, v78, v79\n"
"v_mfma_f32_16x16x32_bf16 v[0:3], a[168:171], a[152:155], v[0:3]\n"
"v_mul_f32 v80, v80, v84\n"
"v_mul_f32 v81, v81, v84\n"
"v_mfma_f32_16x16x32_bf16 v[132:135], a[168:171], a[156:159], v[132:135]\n"
"v_cvt_pk_bf16_f32 v72, v80, v81\n"
"v_mul_f32 v82, v82, v84\n"
"v_mfma_f32_16x16x32_bf16 v[4:7], a[172:175], a[144:147], v[4:7]\n"
"v_mul_f32 v83, v83, v84\n"
"v_cvt_pk_bf16_f32 v73, v82, v83\n"
"v_mfma_f32_16x16x32_bf16 v[136:139], a[172:175], a[148:151], v[136:139]\n"
"buffer_store_dwordx4 v[64:67], v51, s[84:87], s60 offen sc1\n"
"v_mfma_f32_16x16x32_bf16 v[8:11], a[172:175], a[152:155], v[8:11]\n"
"buffer_store_dwordx4 v[68:71], v51, s[84:87], s60 offen offset:256 sc1\n"
"v_mfma_f32_16x16x32_bf16 v[140:143], a[172:175], a[156:159], v[140:143]\n"
"buffer_store_dwordx2 v[72:73], v51, s[84:87], s56 offen sc1\n"
"v_mfma_f32_16x16x32_bf16 v[12:15], a[176:179], a[144:147], v[12:15]\n"
"v_mfma_f32_16x16x32_bf16 v[144:147], a[176:179], a[148:151], v[144:147]\n"
"v_mfma_f32_16x16x32_bf16 v[16:19], a[176:179], a[152:155], v[16:19]\n"
"v_mfma_f32_16x16x32_bf16 v[148:151], a[176:179], a[156:159], v[148:151]\n"
"v_mfma_f32_16x16x32_bf16 v[20:23], a[180:183], a[144:147], v[20:23]\n"
"v_mfma_f32_16x16x32_bf16 v[152:155], a[180:183], a[148:151], v[152:155]\n"
"v_mfma_f32_16x16x32_bf16 v[24:27], a[180:183], a[152:155], v[24:27]\n"
"v_mfma_f32_16x16x32_bf16 v[156:159], a[180:183], a[156:159], v[156:159]\n"
"v_mfma_f32_16x16x32_bf16 v[28:31], a[184:187], a[144:147], v[28:31]\n"
"v_mfma_f32_16x16x32_bf16 v[160:163], a[184:187], a[148:151], v[160:163]\n"
"v_mfma_f32_16x16x32_bf16 v[32:35], a[184:187], a[152:155], v[32:35]\n"
"v_mfma_f32_16x16x32_bf16 v[164:167], a[184:187], a[156:159], v[164:167]\n"
"v_mfma_f32_16x16x32_bf16 v[36:39], a[188:191], a[144:147], v[36:39]\n"
"v_mfma_f32_16x16x32_bf16 v[168:171], a[188:191], a[148:151], v[168:171]\n"
"v_mfma_f32_16x16x32_bf16 v[40:43], a[188:191], a[152:155], v[40:43]\n"
"v_mfma_f32_16x16x32_bf16 v[172:175], a[188:191], a[156:159], v[172:175]\n"
"s_mov_b32 s80, 0x3e0293ee\n"
"s_mov_b32 s81, 0x3e0293ee\n"
"s_and_b32 s82, s76, 1\n"
"s_lshl_b32 s82, s82, 14\n"
"s_add_u32 s82, s82, 131072\n"
"v_add_u32 v249, s82, v244\n"
"v_pk_mul_f32 v[176:177], v[176:177], s[80:81]\n"
"v_pk_mul_f32 v[178:179], v[178:179], s[80:81]\n"
"v_pk_mul_f32 v[180:181], v[180:181], s[80:81]\n"
"v_pk_mul_f32 v[182:183], v[182:183], s[80:81]\n"
"v_pk_mul_f32 v[184:185], v[184:185], s[80:81]\n"
"v_pk_mul_f32 v[186:187], v[186:187], s[80:81]\n"
"v_pk_mul_f32 v[188:189], v[188:189], s[80:81]\n"
"v_pk_mul_f32 v[190:191], v[190:191], s[80:81]\n"
"v_pk_mul_f32 v[192:193], v[192:193], s[80:81]\n"
"v_pk_mul_f32 v[194:195], v[194:195], s[80:81]\n"
"v_pk_mul_f32 v[196:197], v[196:197], s[80:81]\n"
"v_pk_mul_f32 v[198:199], v[198:199], s[80:81]\n"
"v_pk_mul_f32 v[200:201], v[200:201], s[80:81]\n"
"v_pk_mul_f32 v[202:203], v[202:203], s[80:81]\n"
"v_pk_mul_f32 v[204:205], v[204:205], s[80:81]\n"
"v_pk_mul_f32 v[206:207], v[206:207], s[80:81]\n"
"v_pk_mul_f32 v[208:209], v[208:209], s[80:81]\n"
"v_pk_mul_f32 v[210:211], v[210:211], s[80:81]\n"
"v_pk_mul_f32 v[128:129], v[128:129], s[80:81]\n"
"v_pk_mul_f32 v[130:131], v[130:131], s[80:81]\n"
"v_pk_mul_f32 v[0:1], v[0:1], s[80:81]\n"
"v_pk_mul_f32 v[2:3], v[2:3], s[80:81]\n"
"v_pk_mul_f32 v[132:133], v[132:133], s[80:81]\n"
"v_pk_mul_f32 v[134:135], v[134:135], s[80:81]\n"
"v_pk_mul_f32 v[4:5], v[4:5], s[80:81]\n"
"v_pk_mul_f32 v[6:7], v[6:7], s[80:81]\n"
"v_pk_mul_f32 v[136:137], v[136:137], s[80:81]\n"
"v_pk_mul_f32 v[138:139], v[138:139], s[80:81]\n"
"v_pk_mul_f32 v[8:9], v[8:9], s[80:81]\n"
"v_pk_mul_f32 v[10:11], v[10:11], s[80:81]\n"
"v_pk_mul_f32 v[140:141], v[140:141], s[80:81]\n"
"v_pk_mul_f32 v[142:143], v[142:143], s[80:81]\n"
"v_pk_mul_f32 v[12:13], v[12:13], s[80:81]\n"
"v_pk_mul_f32 v[14:15], v[14:15], s[80:81]\n"
"v_pk_mul_f32 v[144:145], v[144:145], s[80:81]\n"
"v_pk_mul_f32 v[146:147], v[146:147], s[80:81]\n"
"v_pk_mul_f32 v[16:17], v[16:17], s[80:81]\n"
"v_pk_mul_f32 v[18:19], v[18:19], s[80:81]\n"
"v_pk_mul_f32 v[148:149], v[148:149], s[80:81]\n"
"v_pk_mul_f32 v[150:151], v[150:151], s[80:81]\n"
"v_pk_mul_f32 v[20:21], v[20:21], s[80:81]\n"
"v_pk_mul_f32 v[22:23], v[22:23], s[80:81]\n"
"v_pk_mul_f32 v[152:153], v[152:153], s[80:81]\n"
"v_pk_mul_f32 v[154:155], v[154:155], s[80:81]\n"
"v_pk_mul_f32 v[24:25], v[24:25], s[80:81]\n"
"v_pk_mul_f32 v[26:27], v[26:27], s[80:81]\n"
"v_pk_mul_f32 v[156:157], v[156:157], s[80:81]\n"
"v_pk_mul_f32 v[158:159], v[158:159], s[80:81]\n"
"v_pk_mul_f32 v[28:29], v[28:29], s[80:81]\n"
"v_pk_mul_f32 v[30:31], v[30:31], s[80:81]\n"
"v_pk_mul_f32 v[160:161], v[160:161], s[80:81]\n"
"v_pk_mul_f32 v[162:163], v[162:163], s[80:81]\n"
"v_pk_mul_f32 v[32:33], v[32:33], s[80:81]\n"
"v_pk_mul_f32 v[34:35], v[34:35], s[80:81]\n"
"v_pk_mul_f32 v[164:165], v[164:165], s[80:81]\n"
"v_pk_mul_f32 v[166:167], v[166:167], s[80:81]\n"
"v_pk_mul_f32 v[36:37], v[36:37], s[80:81]\n"
"v_pk_mul_f32 v[38:39], v[38:39], s[80:81]\n"
"v_pk_mul_f32 v[168:169], v[168:169], s[80:81]\n"
"v_pk_mul_f32 v[170:171], v[170:171], s[80:81]\n"
"v_pk_mul_f32 v[40:41], v[40:41], s[80:81]\n"
"v_pk_mul_f32 v[42:43], v[42:43], s[80:81]\n"
"v_pk_mul_f32 v[172:173], v[172:173], s[80:81]\n"
"v_pk_mul_f32 v[174:175], v[174:175], s[80:81]\n"
"v_mov_b32 v241, 0x3f803f80\n"
"s_mov_b32 vcc_lo, 0x33333333\n"
"s_mov_b32 vcc_hi, 0xcccccccc\n"
"v_cndmask_b32 v240, 0, v241, vcc\n"
"v_cndmask_b32 v241, 0, v241, vcc\n"
"v_mov_b64 v[242:243], v[240:241]\n"
"v_mfma_f32_32x32x16_bf16 v[48:63], a[192:195], a[192:195], 0\n"
"s_lshl_b32 s80, s79, 1\n"
"v_cvt_pk_bf16_f32 v176, v176, v177\n"
"v_cvt_pk_bf16_f32 v177, v178, v179\n"
"v_cvt_pk_bf16_f32 v178, v192, v193\n"
"v_cvt_pk_bf16_f32 v179, v194, v195\n"
"v_accvgpr_write_b32 a128, v176\n"
".Lpad11_%=:\n"
"s_cbranch_execz .Lpad15_%=\n"
"v_accvgpr_write_b32 a129, v177\n"
"v_accvgpr_write_b32 a130, v178\n"
"v_accvgpr_write_b32 a131, v179\n"
"v_mov_b64 v[176:177], v[250:251]\n"
"v_mov_b64 v[178:179], v[250:251]\n"
"v_mfma_f32_32x32x16_bf16 v[64:79], a[192:195], a[192:195], 0\n"
"s_add_u32 s81, s80, 2\n"
"v_cvt_pk_bf16_f32 v180, v180, v181\n"
"v_cvt_pk_bf16_f32 v181, v182, v183\n"
"v_cvt_pk_bf16_f32 v182, v196, v197\n"
"v_cvt_pk_bf16_f32 v183, v198, v199\n"
"v_accvgpr_write_b32 a144, v180\n"
"v_accvgpr_write_b32 a145, v181\n"
"v_accvgpr_write_b32 a146, v182\n"
"v_accvgpr_write_b32 a147, v183\n"
"v_mov_b64 v[180:181], v[250:251]\n"
"v_mov_b64 v[182:183], v[250:251]\n"
"v_mfma_f32_32x32x16_bf16 v[80:95], a[192:195], a[192:195], 0\n"
"s_cmp_lt_u32 s76, 2\n"
"s_cselect_b32 s53, 100, s81\n"
"s_cselect_b32 s65, s80, 100\n"
"v_cvt_pk_bf16_f32 v184, v184, v185\n"
"v_cvt_pk_bf16_f32 v185, v186, v187\n"
"v_cvt_pk_bf16_f32 v186, v200, v201\n"
"v_cvt_pk_bf16_f32 v187, v202, v203\n"
"v_accvgpr_write_b32 a160, v184\n"
"v_accvgpr_write_b32 a161, v185\n"
"v_accvgpr_write_b32 a162, v186\n"
"v_accvgpr_write_b32 a163, v187\n"
"v_mov_b64 v[184:185], v[250:251]\n"
"v_mov_b64 v[186:187], v[250:251]\n"
"v_mfma_f32_32x32x16_bf16 v[96:111], a[192:195], a[192:195], 0\n"
"s_cmp_eq_u32 s79, 0\n"
"s_cselect_b32 s60, .LevM1e_%=-.Linit_%=, 0\n"
"v_cvt_pk_bf16_f32 v188, v188, v189\n"
"v_cvt_pk_bf16_f32 v189, v190, v191\n"
"v_cvt_pk_bf16_f32 v190, v204, v205\n"
"v_cvt_pk_bf16_f32 v191, v206, v207\n"
"v_accvgpr_write_b32 a176, v188\n"
"v_accvgpr_write_b32 a177, v189\n"
"v_accvgpr_write_b32 a178, v190\n"
"v_accvgpr_write_b32 a179, v191\n"
"v_mov_b64 v[188:189], v[250:251]\n"
"v_mov_b64 v[190:191], v[250:251]\n"
"v_mfma_f32_32x32x16_bf16 v[112:127], a[192:195], a[192:195], 0\n"
"s_add_u32 s60, s60, .Linit_%=-.Lpc_%=\n"
"v_cvt_pk_bf16_f32 v208, v208, v209\n"
"v_cvt_pk_bf16_f32 v209, v210, v211\n"
"v_cvt_pk_bf16_f32 v210, v4, v5\n"
"v_cvt_pk_bf16_f32 v211, v6, v7\n"
"v_accvgpr_write_b32 a132, v208\n"
"v_accvgpr_write_b32 a133, v209\n"
"v_accvgpr_write_b32 a134, v210\n"
"v_accvgpr_write_b32 a135, v211\n"
"v_mfma_f32_32x32x16_bf16 v[224:239], a[192:195], a[192:195], 0\n"
"s_add_u32 s60, s60, s62\n"
"s_addc_u32 s61, s63, 0\n"
"v_cvt_pk_bf16_f32 v128, v128, v129\n"
"v_cvt_pk_bf16_f32 v129, v130, v131\n"
"v_cvt_pk_bf16_f32 v130, v136, v137\n"
"v_cvt_pk_bf16_f32 v131, v138, v139\n"
"v_accvgpr_write_b32 a148, v128\n"
"v_accvgpr_write_b32 a149, v129\n"
"v_accvgpr_write_b32 a150, v130\n"
"v_accvgpr_write_b32 a151, v131\n"
"v_mfma_f32_32x32x16_bf16 v[192:207], a[192:195], a[192:195], 0\n"
"s_mov_b32 s52, 0\n"
"v_cvt_pk_bf16_f32 v0, v0, v1\n"
"v_cvt_pk_bf16_f32 v1, v2, v3\n"
"v_cvt_pk_bf16_f32 v2, v8, v9\n"
"v_cvt_pk_bf16_f32 v3, v10, v11\n"
"v_accvgpr_write_b32 a164, v0\n"
"v_accvgpr_write_b32 a165, v1\n"
"v_accvgpr_write_b32 a166, v2\n"
"v_accvgpr_write_b32 a167, v3\n"
"v_mfma_f32_32x32x16_bf16 v[208:223], a[192:195], a[192:195], 0\n"
"s_mov_b32 s64, 0\n"
"v_cvt_pk_bf16_f32 v132, v132, v133\n"
"v_cvt_pk_bf16_f32 v133, v134, v135\n"
"v_cvt_pk_bf16_f32 v134, v140, v141\n"
"v_cvt_pk_bf16_f32 v135, v142, v143\n"
"v_accvgpr_write_b32 a180, v132\n"
"v_accvgpr_write_b32 a181, v133\n"
"v_accvgpr_write_b32 a182, v134\n"
"v_accvgpr_write_b32 a183, v135\n"
"s_sub_u32 s83, s53, 2\n"
"v_cvt_pk_bf16_f32 v12, v12, v13\n"
"v_cvt_pk_bf16_f32 v13, v14, v15\n"
"v_cvt_pk_bf16_f32 v14, v20, v21\n"
"v_cvt_pk_bf16_f32 v15, v22, v23\n"
"v_accvgpr_write_b32 a136, v12\n"
"v_accvgpr_write_b32 a137, v13\n"
"v_accvgpr_write_b32 a138, v14\n"
"v_accvgpr_write_b32 a139, v15\n"
"v_mfma_f32_32x32x16_bf16 v[0:15], a[192:195], a[192:195], 0\n"
"v_cvt_pk_bf16_f32 v144, v144, v145\n"
"v_cvt_pk_bf16_f32 v145, v146, v147\n"
"v_cvt_pk_bf16_f32 v146, v152, v153\n"
"v_cvt_pk_bf16_f32 v147, v154, v155\n"
"v_accvgpr_write_b32 a152, v144\n"
"v_accvgpr_write_b32 a153, v145\n"
"v_accvgpr_write_b32 a154, v146\n"
"v_accvgpr_write_b32 a155, v147\n"
"v_cvt_pk_bf16_f32 v16, v16, v17\n"
"v_cvt_pk_bf16_f32 v17, v18, v19\n"
"v_cvt_pk_bf16_f32 v18, v24, v25\n"
"v_cvt_pk_bf16_f32 v19, v26, v27\n"
"v_accvgpr_write_b32 a168, v16\n"
"v_accvgpr_write_b32 a169, v17\n"
"v_accvgpr_write_b32 a170, v18\n"
"v_accvgpr_write_b32 a171, v19\n"
"v_cvt_pk_bf16_f32 v148, v148, v149\n"
"v_cvt_pk_bf16_f32 v149, v150, v151\n"
"v_cvt_pk_bf16_f32 v150, v156, v157\n"
"v_cvt_pk_bf16_f32 v151, v158, v159\n"
"v_accvgpr_write_b32 a184, v148\n"
"v_accvgpr_write_b32 a185, v149\n"
"v_accvgpr_write_b32 a186, v150\n"
"v_accvgpr_write_b32 a187, v151\n"
"v_cvt_pk_bf16_f32 v28, v28, v29\n"
"v_cvt_pk_bf16_f32 v29, v30, v31\n"
"v_cvt_pk_bf16_f32 v30, v36, v37\n"
"v_cvt_pk_bf16_f32 v31, v38, v39\n"
"v_accvgpr_write_b32 a140, v28\n"
"v_accvgpr_write_b32 a141, v29\n"
"v_accvgpr_write_b32 a142, v30\n"
"v_accvgpr_write_b32 a143, v31\n"
"v_mfma_f32_32x32x16_bf16 v[16:31], a[192:195], a[192:195], 0\n"
"v_cvt_pk_bf16_f32 v160, v160, v161\n"
"v_cvt_pk_bf16_f32 v161, v162, v163\n"
"v_cvt_pk_bf16_f32 v162, v168, v169\n"
"v_cvt_pk_bf16_f32 v163, v170, v171\n"
"v_accvgpr_write_b32 a156, v160\n"
"v_accvgpr_write_b32 a157, v161\n"
"v_accvgpr_write_b32 a158, v162\n"
"v_accvgpr_write_b32 a159, v163\n"
"v_mov_b64 v[160:161], v[250:251]\n"
"v_mov_b64 v[162:163], v[250:251]\n"
"v_mov_b64 v[168:169], v[250:251]\n"
"v_mov_b64 v[170:171], v[250:251]\n"
"v_cvt_pk_bf16_f32 v32, v32, v33\n"
"v_cvt_pk_bf16_f32 v33, v34, v35\n"
"v_cvt_pk_bf16_f32 v34, v40, v41\n"
"v_cvt_pk_bf16_f32 v35, v42, v43\n"
"v_accvgpr_write_b32 a172, v32\n"
"v_accvgpr_write_b32 a173, v33\n"
"v_accvgpr_write_b32 a174, v34\n"
"v_accvgpr_write_b32 a175, v35\n"
"v_mfma_f32_32x32x16_bf16 v[32:47], a[192:195], a[192:195], 0\n"
"v_cvt_pk_bf16_f32 v164, v164, v165\n"
"v_cvt_pk_bf16_f32 v165, v166, v167\n"
"v_cvt_pk_bf16_f32 v166, v172, v173\n"
"v_cvt_pk_bf16_f32 v167, v174, v175\n"
"v_accvgpr_write_b32 a188, v164\n"
"v_accvgpr_write_b32 a189, v165\n"
"v_accvgpr_write_b32 a190, v166\n"
"v_accvgpr_write_b32 a191, v167\n"
"v_mov_b64 v[164:165], v[250:251]\n"
"v_mov_b64 v[166:167], v[250:251]\n"
"v_mov_b64 v[172:173], v[250:251]\n"
"v_mov_b64 v[174:175], v[250:251]\n"
"s_waitcnt vmcnt(19)\n"
"s_barrier\n"
"ds_read_b128 a[192:195], v244 offset:49152\n"
"ds_read_b128 a[196:199], v244 offset:50176\n"
"ds_read_b128 a[200:203], v244 offset:51200\n"
"ds_read_b128 a[204:207], v244 offset:52224\n"
"ds_read_b128 a[208:211], v244 offset:53248\n"
"ds_read_b128 a[212:215], v244 offset:54272\n"
"ds_read_b128 a[216:219], v244 offset:55296\n"
"ds_read_b128 a[220:223], v244 offset:56320\n"
"s_cmp_ge_u32 s76, 2\n"
"s_cbranch_scc1 .Lqp3_%=\n"
"ds_write_b128 v249, a[128:131] offset:0\n"
"ds_write_b128 v249, a[132:135] offset:1024\n"
"ds_write_b128 v249, a[136:139] offset:2048\n"
"ds_write_b128 v249, a[140:143] offset:3072\n"
"ds_write_b128 v249, a[144:147] offset:4096\n"
"ds_write_b128 v249, a[148:151] offset:5120\n"
"ds_write_b128 v249, a[152:155] offset:6144\n"
"ds_write_b128 v249, a[156:159] offset:7168\n"
"ds_write_b128 v249, a[160:163] offset:8192\n"
"ds_write_b128 v249, a[164:167] offset:9216\n"
"ds_write_b128 v249, a[168:171] offset:10240\n"
"ds_write_b128 v249, a[172:175] offset:11264\n"
"ds_write_b128 v249, a[176:179] offset:12288\n"
"ds_write_b128 v249, a[180:183] offset:13312\n"
"ds_write_b128 v249, a[184:187] offset:14336\n"
"ds_write_b128 v249, a[188:191] offset:15360\n"
"s_waitcnt lgkmcnt(15)\n"
".Lpp0_%=:\n"
"v_mfma_f32_16x16x32_bf16 v[128:131], a[192:195], a[128:131], v[192:195]\n"
"s_cmp_lt_u32 s55, 540672\n"
"v_cvt_pk_bf16_f32 v216, v236, v237\n"
"v_mfma_f32_16x16x32_bf16 v[136:139], a[192:195], a[144:147], v[196:199]\n"
"s_cselect_b32 s56, 1073741824, s55\n"
"v_exp_f32 v238, v178 clamp\n"
"v_smfmac_f32_16x16x64_bf16 v[224:227], v[240:243], v[208:215], v247\n"
"s_add_u32 m0, s57, 98304\n"
"v_mfma_f32_16x16x32_bf16 v[144:147], a[192:195], a[160:163], v[200:203]\n"
"s_cmp_eq_u32 s64, s52\n"
"v_exp_f32 v239, v179 clamp\n"
"v_mfma_f32_16x16x32_bf16 v[152:155], a[192:195], a[176:179], v[204:207]\n"
"s_cselect_b32 s58, -1.0, 0.5\n"
"ds_read_b128 a[224:227], v245 offset:57344\n"
"v_cvt_pk_bf16_f32 v217, v238, v239\n"
"v_mfma_f32_16x16x32_bf16 v[128:131], a[196:199], a[132:135], v[128:131]\n"
"v_exp_f32 v236, v180 clamp\n"
"v_mfma_f32_16x16x32_bf16 v[136:139], a[196:199], a[148:151], v[136:139]\n"
"v_exp_f32 v237, v181 clamp\n"
"v_mfma_f32_16x16x32_bf16 v[144:147], a[196:199], a[164:167], v[144:147]\n"
"ds_read_b128 a[228:231], v245 offset:58368\n"
"v_cvt_pk_bf16_f32 v218, v236, v237\n"
"v_mfma_f32_16x16x32_bf16 v[152:155], a[196:199], a[180:183], v[152:155]\n"
"v_exp_f32 v238, v182 clamp\n"
"v_mfma_f32_16x16x32_bf16 v[128:131], a[200:203], a[136:139], v[128:131]\n"
"v_exp_f32 v239, v183 clamp\n"
"v_mfma_f32_16x16x32_bf16 v[136:139], a[200:203], a[152:155], v[136:139]\n"
"ds_read_b128 a[232:235], v245 offset:59392\n"
"v_cvt_pk_bf16_f32 v219, v238, v239\n"
"v_mfma_f32_16x16x32_bf16 v[144:147], a[200:203], a[168:171], v[144:147]\n"
"v_exp_f32 v236, v184 clamp\n"
"v_mfma_f32_16x16x32_bf16 v[152:155], a[200:203], a[184:187], v[152:155]\n"
"buffer_load_dwordx4 v246, s[40:43], s56 offen offset:0 sc1 lds\n"
"v_mfma_f32_16x16x32_bf16 v[128:131], a[204:207], a[140:143], v[128:131]\n"
"v_exp_f32 v237, v185 clamp\n"
"v_mfma_f32_16x16x32_bf16 v[136:139], a[204:207], a[156:159], v[136:139]\n"
"ds_read_b128 a[236:239], v245 offset:60416\n"
"v_cvt_pk_bf16_f32 v220, v236, v237\n"
"v_mfma_f32_16x16x32_bf16 v[144:147], a[204:207], a[172:175], v[144:147]\n"
"v_exp_f32 v238, v186 clamp\n"
"v_mfma_f32_16x16x32_bf16 v[152:155], a[204:207], a[188:191], v[152:155]\n"
"s_add_u32 s47, s44, 0\n"
"v_exp_f32 v239, v187 clamp\n"
"v_mfma_f32_16x16x32_bf16 v[132:135], a[208:211], a[128:131], v[192:195]\n"
"buffer_store_dwordx4 a[0:3], v244, s[84:87], s47 offen offset:0 sc1\n"
"ds_read_b128 a[240:243], v245 offset:61440\n"
"v_cvt_pk_bf16_f32 v221, v238, v239\n"
"v_mfma_f32_16x16x32_bf16 v[140:143], a[208:211], a[144:147], v[196:199]\n"
"v_exp_f32 v236, v188 clamp\n"
"v_mfma_f32_16x16x32_bf16 v[148:151], a[208:211], a[160:163], v[200:203]\n"
"v_exp_f32 v237, v189 clamp\n"
"v_mfma_f32_16x16x32_bf16 v[156:159], a[208:211], a[176:179], v[204:207]\n"
"ds_read_b128 a[244:247], v245 offset:62464\n"
"v_cvt_pk_bf16_f32 v222, v236, v237\n"
"v_mfma_f32_16x16x32_bf16 v[132:135], a[212:215], a[132:135], v[132:135]\n"
"v_exp_f32 v238, v190 clamp\n"
"v_mfma_f32_16x16x32_bf16 v[140:143], a[212:215], a[148:151], v[140:143]\n"
"buffer_load_dwordx4 v246, s[40:43], s56 offen offset:1024 sc1 lds\n"
"v_exp_f32 v239, v191 clamp\n"
"v_mfma_f32_16x16x32_bf16 v[148:151], a[212:215], a[164:167], v[148:151]\n"
"ds_read_b128 a[248:251], v245 offset:63488\n"
"v_cvt_pk_bf16_f32 v223, v238, v239\n"
"v_mfma_f32_16x16x32_bf16 v[156:159], a[212:215], a[180:183], v[156:159]\n"
"s_nop 0\n"
"v_smfmac_f32_16x16x64_bf16 v[228:231], v[240:243], v[216:223], v247\n"
"ds_read_b128 a[252:255], v245 offset:64512\n"
"v_mfma_f32_16x16x32_bf16 v[132:135], a[216:219], a[136:139], v[132:135]\n"
"v_max3_f32 v236, v224, v225, v226\n"
"v_mfma_f32_16x16x32_bf16 v[140:143], a[216:219], a[152:155], v[140:143]\n"
"buffer_load_dwordx4 v246, s[40:43], s56 offen offset:2048 sc1 lds\n"
"v_mfma_f32_16x16x32_bf16 v[148:151], a[216:219], a[168:171], v[148:151]\n"
"v_mfma_f32_16x16x32_bf16 v[156:159], a[216:219], a[184:187], v[156:159]\n"
"s_nop 0\n"
"v_max3_f32 v236, v236, v227, v228\n"
"v_max3_f32 v236, v236, v229, v230\n"
"v_mfma_f32_16x16x32_bf16 v[132:135], a[220:223], a[140:143], v[132:135]\n"
"s_add_u32 s47, s44, 0\n"
"v_max_f32 v236, v236, v231\n"
"v_cmp_nge_f32 vcc, s58, v236\n"
"v_mfma_f32_16x16x32_bf16 v[140:143], a[220:223], a[156:159], v[140:143]\n"
"buffer_store_dwordx4 a[4:7], v244, s[84:87], s47 offen offset:1024 sc1\n"
"v_mfma_f32_16x16x32_bf16 v[148:151], a[220:223], a[172:175], v[148:151]\n"
"s_waitcnt lgkmcnt(0)\n"
"v_mfma_f32_16x16x32_bf16 v[156:159], a[220:223], a[188:191], v[156:159]\n"
"s_getpc_b64 s[66:67]\n"
"s_cbranch_vccnz .LstubG0_%=\n"
"buffer_load_dwordx4 v246, s[40:43], s56 offen offset:3072 sc1 lds\n"
".Lpp1_%=:\n"
"v_mfma_f32_16x16x32_bf16 v[0:3], a[224:227], v[208:211], v[0:3]\n"
"s_waitcnt vmcnt(21)\n"
"v_exp_f32 v236, v128 clamp\n"
"v_mfma_f32_16x16x32_bf16 v[16:19], a[228:231], v[208:211], v[16:19]\n"
"s_barrier\n"
"s_lshl_b32 s47, s78, 2\n"
"s_add_u32 s47, s47, s46\n"
"v_mfma_f32_16x16x32_bf16 v[32:35], a[232:235], v[208:211], v[32:35]\n"
"s_add_u32 s52, s52, 1\n"
"v_exp_f32 v237, v129 clamp\n"
"v_mfma_f32_16x16x32_bf16 v[48:51], a[236:239], v[208:211], v[48:51]\n"
"s_add_u32 m0, s57, 49152\n"
"v_lshlrev_b32 v253, 17, v246\n"
"v_mov_b32 v254, s88\n"
"buffer_store_dword v254, v253, s[84:87], s47 offen offset:32 sc1\n"
"ds_read_b128 a[192:195], v244 offset:0\n"
"v_mfma_f32_16x16x32_bf16 v[64:67], a[240:243], v[208:211], v[64:67]\n"
"v_exp_f32 v238, v130 clamp\n"
"v_mfma_f32_16x16x32_bf16 v[80:83], a[244:247], v[208:211], v[80:83]\n"
"s_sub_u32 s55, s55, s29\n"
"v_exp_f32 v239, v131 clamp\n"
"v_mfma_f32_16x16x32_bf16 v[96:99], a[248:251], v[208:211], v[96:99]\n"
"buffer_load_dwordx4 v246, s[36:39], s54 offen offset:0 sc1 lds\n"
"v_mfma_f32_16x16x32_bf16 v[112:115], a[252:255], v[208:211], v[112:115]\n"
"ds_read_b128 a[196:199], v244 offset:1024\n"
"v_cvt_pk_bf16_f32 v208, v236, v237\n"
"v_mfma_f32_16x16x32_bf16 v[4:7], a[224:227], v[212:215], v[4:7]\n"
"ds_read_b128 a[200:203], v244 offset:2048\n"
"v_cvt_pk_bf16_f32 v209, v238, v239\n"
"v_mfma_f32_16x16x32_bf16 v[20:23], a[228:231], v[212:215], v[20:23]\n"
"s_add_u32 s47, s44, 4096\n"
"v_exp_f32 v236, v132 clamp\n"
"v_mfma_f32_16x16x32_bf16 v[36:39], a[232:235], v[212:215], v[36:39]\n"
"buffer_store_dwordx4 a[8:11], v244, s[84:87], s47 offen offset:0 sc1\n"
"v_exp_f32 v237, v133 clamp\n"
"v_mfma_f32_16x16x32_bf16 v[52:55], a[236:239], v[212:215], v[52:55]\n"
"ds_read_b128 a[204:207], v244 offset:3072\n"
"v_cvt_pk_bf16_f32 v210, v236, v237\n"
"v_mfma_f32_16x16x32_bf16 v[68:71], a[240:243], v[212:215], v[68:71]\n"
"v_exp_f32 v238, v134 clamp\n"
"v_mfma_f32_16x16x32_bf16 v[84:87], a[244:247], v[212:215], v[84:87]\n"
"v_exp_f32 v239, v135 clamp\n"
"v_mfma_f32_16x16x32_bf16 v[100:103], a[248:251], v[212:215], v[100:103]\n"
"buffer_load_dwordx4 v246, s[36:39], s54 offen offset:1024 sc1 lds\n"
"v_mfma_f32_16x16x32_bf16 v[116:119], a[252:255], v[212:215], v[116:119]\n"
"ds_read_b128 a[208:211], v244 offset:4096\n"
"v_cvt_pk_bf16_f32 v211, v238, v239\n"
"v_mfma_f32_16x16x32_bf16 v[8:11], a[224:227], v[216:219], v[8:11]\n"
"s_add_u32 s47, s44, 4096\n"
"v_exp_f32 v236, v136 clamp\n"
"v_mfma_f32_16x16x32_bf16 v[24:27], a[228:231], v[216:219], v[24:27]\n"
"buffer_store_dwordx4 a[12:15], v244, s[84:87], s47 offen offset:1024 sc1\n"
"v_exp_f32 v237, v137 clamp\n"
"v_mfma_f32_16x16x32_bf16 v[40:43], a[232:235], v[216:219], v[40:43]\n"
"ds_read_b128 a[212:215], v244 offset:5120\n"
"v_cvt_pk_bf16_f32 v212, v236, v237\n"
"v_mfma_f32_16x16x32_bf16 v[56:59], a[236:239], v[216:219], v[56:59]\n"
"v_exp_f32 v238, v138 clamp\n"
"v_mfma_f32_16x16x32_bf16 v[72:75], a[240:243], v[216:219], v[72:75]\n"
"v_exp_f32 v239, v139 clamp\n"
"v_mfma_f32_16x16x32_bf16 v[88:91], a[244:247], v[216:219], v[88:91]\n"
"buffer_load_dwordx4 v246, s[36:39], s54 offen offset:2048 sc1 lds\n"
"v_mfma_f32_16x16x32_bf16 v[104:107], a[248:251], v[216:219], v[104:107]\n"
"ds_read_b128 a[216:219], v244 offset:6144\n"
"v_cvt_pk_bf16_f32 v213, v238, v239\n"
"v_mfma_f32_16x16x32_bf16 v[120:123], a[252:255], v[216:219], v[120:123]\n"
"v_exp_f32 v236, v140 clamp\n"
"v_mfma_f32_16x16x32_bf16 v[12:15], a[224:227], v[220:223], v[12:15]\n"
"v_exp_f32 v237, v141 clamp\n"
"v_mfma_f32_16x16x32_bf16 v[28:31], a[228:231], v[220:223], v[28:31]\n"
"ds_read_b128 a[220:223], v244 offset:7168\n"
"v_cvt_pk_bf16_f32 v214, v236, v237\n"
"v_mfma_f32_16x16x32_bf16 v[44:47], a[232:235], v[220:223], v[44:47]\n"
"v_exp_f32 v238, v142 clamp\n"
"v_mfma_f32_16x16x32_bf16 v[60:63], a[236:239], v[220:223], v[60:63]\n"
"v_exp_f32 v239, v143 clamp\n"
"v_mfma_f32_16x16x32_bf16 v[76:79], a[240:243], v[220:223], v[76:79]\n"
"buffer_load_dwordx4 v246, s[36:39], s54 offen offset:3072 sc1 lds\n"
"v_mfma_f32_16x16x32_bf16 v[92:95], a[244:247], v[220:223], v[92:95]\n"
"s_add_u32 s54, s54, s28\n"
"v_cvt_pk_bf16_f32 v215, v238, v239\n"
"v_mfma_f32_16x16x32_bf16 v[108:111], a[248:251], v[220:223], v[108:111]\n"
"v_exp_f32 v236, v144 clamp\n"
"v_mfma_f32_16x16x32_bf16 v[124:127], a[252:255], v[220:223], v[124:127]\n"
"s_waitcnt lgkmcnt(0)\n"
"v_exp_f32 v237, v145 clamp\n"
"v_mfma_f32_16x16x32_bf16 v[160:163], a[192:195], a[128:131], v[192:195]\n"
"s_cmp_lt_u32 s55, 540672\n"
"v_cvt_pk_bf16_f32 v216, v236, v237\n"
"v_mfma_f32_16x16x32_bf16 v[168:171], a[192:195], a[144:147], v[196:199]\n"
"s_cselect_b32 s56, 1073741824, s55\n"
"v_exp_f32 v238, v146 clamp\n"
"v_smfmac_f32_16x16x64_bf16 v[224:227], v[240:243], v[208:215], v247\n"
"s_add_u32 m0, s57, 114688\n"
"v_mfma_f32_16x16x32_bf16 v[176:179], a[192:195], a[160:163], v[200:203]\n"
"s_cmp_eq_u32 s64, s52\n"
"v_exp_f32 v239, v147 clamp\n"
"v_mfma_f32_16x16x32_bf16 v[184:187], a[192:195], a[176:179], v[204:207]\n"
"s_cselect_b32 s58, -1.0, 0.5\n"
"ds_read_b128 a[224:227], v245 offset:8192\n"
"v_cvt_pk_bf16_f32 v217, v238, v239\n"
"v_mfma_f32_16x16x32_bf16 v[160:163], a[196:199], a[132:135], v[160:163]\n"
"s_add_u32 s47, s45, 0\n"
"v_exp_f32 v236, v148 clamp\n"
"v_mfma_f32_16x16x32_bf16 v[168:171], a[196:199], a[148:151], v[168:171]\n"
"buffer_store_dwordx4 a[16:19], v244, s[84:87], s47 offen offset:0 sc1\n"
"v_exp_f32 v237, v149 clamp\n"
"v_mfma_f32_16x16x32_bf16 v[176:179], a[196:199], a[164:167], v[176:179]\n"
"ds_read_b128 a[228:231], v245 offset:9216\n"
"v_cvt_pk_bf16_f32 v218, v236, v237\n"
"v_mfma_f32_16x16x32_bf16 v[184:187], a[196:199], a[180:183], v[184:187]\n"
"v_exp_f32 v238, v150 clamp\n"
"v_mfma_f32_16x16x32_bf16 v[160:163], a[200:203], a[136:139], v[160:163]\n"
"v_exp_f32 v239, v151 clamp\n"
"v_mfma_f32_16x16x32_bf16 v[168:171], a[200:203], a[152:155], v[168:171]\n"
"ds_read_b128 a[232:235], v245 offset:10240\n"
"v_cvt_pk_bf16_f32 v219, v238, v239\n"
"v_mfma_f32_16x16x32_bf16 v[176:179], a[200:203], a[168:171], v[176:179]\n"
"v_exp_f32 v236, v152 clamp\n"
"v_mfma_f32_16x16x32_bf16 v[184:187], a[200:203], a[184:187], v[184:187]\n"
"buffer_load_dwordx4 v246, s[40:43], s56 offen offset:0 sc1 lds\n"
"v_mfma_f32_16x16x32_bf16 v[160:163], a[204:207], a[140:143], v[160:163]\n"
"v_exp_f32 v237, v153 clamp\n"
"v_mfma_f32_16x16x32_bf16 v[168:171], a[204:207], a[156:159], v[168:171]\n"
"ds_read_b128 a[236:239], v245 offset:11264\n"
"v_cvt_pk_bf16_f32 v220, v236, v237\n"
"v_mfma_f32_16x16x32_bf16 v[176:179], a[204:207], a[172:175], v[176:179]\n"
"v_exp_f32 v238, v154 clamp\n"
"v_mfma_f32_16x16x32_bf16 v[184:187], a[204:207], a[188:191], v[184:187]\n"
"s_add_u32 s47, s45, 0\n"
"v_exp_f32 v239, v155 clamp\n"
"v_mfma_f32_16x16x32_bf16 v[164:167], a[208:211], a[128:131], v[192:195]\n"
"buffer_store_dwordx4 a[20:23], v244, s[84:87], s47 offen offset:1024 sc1\n"
"ds_read_b128 a[240:243], v245 offset:12288\n"
"v_cvt_pk_bf16_f32 v221, v238, v239\n"
"v_mfma_f32_16x16x32_bf16 v[172:175], a[208:211], a[144:147], v[196:199]\n"
"v_exp_f32 v236, v156 clamp\n"
"v_mfma_f32_16x16x32_bf16 v[180:183], a[208:211], a[160:163], v[200:203]\n"
"v_exp_f32 v237, v157 clamp\n"
"v_mfma_f32_16x16x32_bf16 v[188:191], a[208:211], a[176:179], v[204:207]\n"
"ds_read_b128 a[244:247], v245 offset:13312\n"
"v_cvt_pk_bf16_f32 v222, v236, v237\n"
"v_mfma_f32_16x16x32_bf16 v[164:167], a[212:215], a[132:135], v[164:167]\n"
"v_exp_f32 v238, v158 clamp\n"
"v_mfma_f32_16x16x32_bf16 v[172:175], a[212:215], a[148:151], v[172:175]\n"
"buffer_load_dwordx4 v246, s[40:43], s56 offen offset:1024 sc1 lds\n"
"v_exp_f32 v239, v159 clamp\n"
"v_mfma_f32_16x16x32_bf16 v[180:183], a[212:215], a[164:167], v[180:183]\n"
"ds_read_b128 a[248:251], v245 offset:14336\n"
"v_cvt_pk_bf16_f32 v223, v238, v239\n"
"v_mfma_f32_16x16x32_bf16 v[188:191], a[212:215], a[180:183], v[188:191]\n"
"s_nop 0\n"
"v_smfmac_f32_16x16x64_bf16 v[228:231], v[240:243], v[216:223], v247\n"
"ds_read_b128 a[252:255], v245 offset:15360\n"
"v_mfma_f32_16x16x32_bf16 v[164:167], a[216:219], a[136:139], v[164:167]\n"
"v_max3_f32 v236, v224, v225, v226\n"
"v_mfma_f32_16x16x32_bf16 v[172:175], a[216:219], a[152:155], v[172:175]\n"
"buffer_load_dwordx4 v246, s[40:43], s56 offen offset:2048 sc1 lds\n"
"v_mfma_f32_16x16x32_bf16 v[180:183], a[216:219], a[168:171], v[180:183]\n"
"v_mfma_f32_16x16x32_bf16 v[188:191], a[216:219], a[184:187], v[188:191]\n"
"s_nop 0\n"
"v_max3_f32 v236, v236, v227, v228\n"
"v_max3_f32 v236, v236, v229, v230\n"
"v_mfma_f32_16x16x32_bf16 v[164:167], a[220:223], a[140:143], v[164:167]\n"
"s_add_u32 s47, s45, 0\n"
"v_max_f32 v236, v236, v231\n"
"v_cmp_nge_f32 vcc, s58, v236\n"
"v_mfma_f32_16x16x32_bf16 v[172:175], a[220:223], a[156:159], v[172:175]\n"
"buffer_store_dwordx4 a[24:27], v244, s[84:87], s47 offen offset:2048 sc1\n"
"v_mfma_f32_16x16x32_bf16 v[180:183], a[220:223], a[172:175], v[180:183]\n"
"s_waitcnt lgkmcnt(0)\n"
"v_mfma_f32_16x16x32_bf16 v[188:191], a[220:223], a[188:191], v[188:191]\n"
"s_getpc_b64 s[66:67]\n"
"s_cbranch_vccnz .LstubG1_%=\n"
"buffer_load_dwordx4 v246, s[40:43], s56 offen offset:3072 sc1 lds\n"
".Lpp2_%=:\n"
"v_mfma_f32_16x16x32_bf16 v[0:3], a[224:227], v[208:211], v[0:3]\n"
"s_waitcnt vmcnt(27)\n"
"v_exp_f32 v236, v160 clamp\n"
"v_mfma_f32_16x16x32_bf16 v[16:19], a[228:231], v[208:211], v[16:19]\n"
"s_barrier\n"
"v_mfma_f32_16x16x32_bf16 v[32:35], a[232:235], v[208:211], v[32:35]\n"
"s_add_u32 s52, s52, 1\n"
"v_exp_f32 v237, v161 clamp\n"
"v_mfma_f32_16x16x32_bf16 v[48:51], a[236:239], v[208:211], v[48:51]\n"
"s_add_u32 m0, s57, 0\n"
"ds_read_b128 a[192:195], v244 offset:16384\n"
"v_mfma_f32_16x16x32_bf16 v[64:67], a[240:243], v[208:211], v[64:67]\n"
"v_exp_f32 v238, v162 clamp\n"
"v_mfma_f32_16x16x32_bf16 v[80:83], a[244:247], v[208:211], v[80:83]\n"
"s_sub_u32 s55, s55, s29\n"
"v_exp_f32 v239, v163 clamp\n"
"v_mfma_f32_16x16x32_bf16 v[96:99], a[248:251], v[208:211], v[96:99]\n"
"buffer_load_dwordx4 v246, s[36:39], s54 offen offset:0 sc1 lds\n"
"v_mfma_f32_16x16x32_bf16 v[112:115], a[252:255], v[208:211], v[112:115]\n"
"ds_read_b128 a[196:199], v244 offset:17408\n"
"v_cvt_pk_bf16_f32 v208, v236, v237\n"
"v_mfma_f32_16x16x32_bf16 v[4:7], a[224:227], v[212:215], v[4:7]\n"
"ds_read_b128 a[200:203], v244 offset:18432\n"
"v_cvt_pk_bf16_f32 v209, v238, v239\n"
"v_mfma_f32_16x16x32_bf16 v[20:23], a[228:231], v[212:215], v[20:23]\n"
"s_add_u32 s47, s45, 0\n"
"v_exp_f32 v236, v164 clamp\n"
"v_mfma_f32_16x16x32_bf16 v[36:39], a[232:235], v[212:215], v[36:39]\n"
"buffer_store_dwordx4 a[28:31], v244, s[84:87], s47 offen offset:3072 sc1\n"
"v_exp_f32 v237, v165 clamp\n"
"v_mfma_f32_16x16x32_bf16 v[52:55], a[236:239], v[212:215], v[52:55]\n"
"ds_read_b128 a[204:207], v244 offset:19456\n"
"v_cvt_pk_bf16_f32 v210, v236, v237\n"
"v_mfma_f32_16x16x32_bf16 v[68:71], a[240:243], v[212:215], v[68:71]\n"
"v_exp_f32 v238, v166 clamp\n"
"v_mfma_f32_16x16x32_bf16 v[84:87], a[244:247], v[212:215], v[84:87]\n"
"v_exp_f32 v239, v167 clamp\n"
"v_mfma_f32_16x16x32_bf16 v[100:103], a[248:251], v[212:215], v[100:103]\n"
"buffer_load_dwordx4 v246, s[36:39], s54 offen offset:1024 sc1 lds\n"
"v_mfma_f32_16x16x32_bf16 v[116:119], a[252:255], v[212:215], v[116:119]\n"
"ds_read_b128 a[208:211], v244 offset:20480\n"
"v_cvt_pk_bf16_f32 v211, v238, v239\n"
"v_mfma_f32_16x16x32_bf16 v[8:11], a[224:227], v[216:219], v[8:11]\n"
"s_add_u32 s47, s44, 131072\n"
"v_exp_f32 v236, v168 clamp\n"
"v_mfma_f32_16x16x32_bf16 v[24:27], a[228:231], v[216:219], v[24:27]\n"
"buffer_store_dwordx4 a[32:35], v244, s[84:87], s47 offen offset:0 sc1\n"
"v_exp_f32 v237, v169 clamp\n"
"v_mfma_f32_16x16x32_bf16 v[40:43], a[232:235], v[216:219], v[40:43]\n"
"ds_read_b128 a[212:215], v244 offset:21504\n"
"v_cvt_pk_bf16_f32 v212, v236, v237\n"
"v_mfma_f32_16x16x32_bf16 v[56:59], a[236:239], v[216:219], v[56:59]\n"
"v_exp_f32 v238, v170 clamp\n"
"v_mfma_f32_16x16x32_bf16 v[72:75], a[240:243], v[216:219], v[72:75]\n"
"v_exp_f32 v239, v171 clamp\n"
"v_mfma_f32_16x16x32_bf16 v[88:91], a[244:247], v[216:219], v[88:91]\n"
"buffer_load_dwordx4 v246, s[36:39], s54 offen offset:2048 sc1 lds\n"
"v_mfma_f32_16x16x32_bf16 v[104:107], a[248:251], v[216:219], v[104:107]\n"
"ds_read_b128 a[216:219], v244 offset:22528\n"
"v_cvt_pk_bf16_f32 v213, v238, v239\n"
"v_mfma_f32_16x16x32_bf16 v[120:123], a[252:255], v[216:219], v[120:123]\n"
"v_exp_f32 v236, v172 clamp\n"
"v_mfma_f32_16x16x32_bf16 v[12:15], a[224:227], v[220:223], v[12:15]\n"
"v_exp_f32 v237, v173 clamp\n"
"v_mfma_f32_16x16x32_bf16 v[28:31], a[228:231], v[220:223], v[28:31]\n"
"ds_read_b128 a[220:223], v244 offset:23552\n"
"v_cvt_pk_bf16_f32 v214, v236, v237\n"
"v_mfma_f32_16x16x32_bf16 v[44:47], a[232:235], v[220:223], v[44:47]\n"
"v_exp_f32 v238, v174 clamp\n"
"v_mfma_f32_16x16x32_bf16 v[60:63], a[236:239], v[220:223], v[60:63]\n"
"v_exp_f32 v239, v175 clamp\n"
"v_mfma_f32_16x16x32_bf16 v[76:79], a[240:243], v[220:223], v[76:79]\n"
"buffer_load_dwordx4 v246, s[36:39], s54 offen offset:3072 sc1 lds\n"
"v_mfma_f32_16x16x32_bf16 v[92:95], a[244:247], v[220:223], v[92:95]\n"
"s_add_u32 s54, s54, s28\n"
"v_cvt_pk_bf16_f32 v215, v238, v239\n"
"v_mfma_f32_16x16x32_bf16 v[108:111], a[248:251], v[220:223], v[108:111]\n"
"v_exp_f32 v236, v176 clamp\n"
"v_mfma_f32_16x16x32_bf16 v[124:127], a[252:255], v[220:223], v[124:127]\n"
"s_waitcnt lgkmcnt(0)\n"
"v_exp_f32 v237, v177 clamp\n"
"v_mfma_f32_16x16x32_bf16 v[128:131], a[192:195], a[128:131], v[192:195]\n"
"s_cmp_lt_u32 s55, 540672\n"
"v_cvt_pk_bf16_f32 v216, v236, v237\n"
"v_mfma_f32_16x16x32_bf16 v[136:139], a[192:195], a[144:147], v[196:199]\n"
"s_cselect_b32 s56, 1073741824, s55\n"
"v_exp_f32 v238, v178 clamp\n"
"v_smfmac_f32_16x16x64_bf16 v[224:227], v[240:243], v[208:215], v247\n"
"s_add_u32 m0, s57, 65536\n"
"v_mfma_f32_16x16x32_bf16 v[144:147], a[192:195], a[160:163], v[200:203]\n"
"s_cmp_eq_u32 s64, s52\n"
".Lpad12_%=:\n"
"s_cbranch_execz .Lpad16_%=\n"
"v_exp_f32 v239, v179 clamp\n"
"v_mfma_f32_16x16x32_bf16 v[152:155], a[192:195], a[176:179], v[204:207]\n"
"s_cselect_b32 s58, -1.0, 0.5\n"
"ds_read_b128 a[224:227], v245 offset:24576\n"
"v_cvt_pk_bf16_f32 v217, v238, v239\n"
"v_mfma_f32_16x16x32_bf16 v[128:131], a[196:199], a[132:135], v[128:131]\n"
"s_add_u32 s47, s44, 131072\n"
"v_exp_f32 v236, v180 clamp\n"
"v_mfma_f32_16x16x32_bf16 v[136:139], a[196:199], a[148:151], v[136:139]\n"
"buffer_store_dwordx4 a[36:39], v244, s[84:87], s47 offen offset:1024 sc1\n"
"v_exp_f32 v237, v181 clamp\n"
"v_mfma_f32_16x16x32_bf16 v[144:147], a[196:199], a[164:167], v[144:147]\n"
"ds_read_b128 a[228:231], v245 offset:25600\n"
"v_cvt_pk_bf16_f32 v218, v236, v237\n"
"v_mfma_f32_16x16x32_bf16 v[152:155], a[196:199], a[180:183], v[152:155]\n"
"v_exp_f32 v238, v182 clamp\n"
"v_mfma_f32_16x16x32_bf16 v[128:131], a[200:203], a[136:139], v[128:131]\n"
"v_exp_f32 v239, v183 clamp\n"
"v_mfma_f32_16x16x32_bf16 v[136:139], a[200:203], a[152:155], v[136:139]\n"
"ds_read_b128 a[232:235], v245 offset:26624\n"
"v_cvt_pk_bf16_f32 v219, v238, v239\n"
"v_mfma_f32_16x16x32_bf16 v[144:147], a[200:203], a[168:171], v[144:147]\n"
"v_exp_f32 v236, v184 clamp\n"
"v_mfma_f32_16x16x32_bf16 v[152:155], a[200:203], a[184:187], v[152:155]\n"
"buffer_load_dwordx4 v246, s[40:43], s56 offen offset:0 sc1 lds\n"
"v_mfma_f32_16x16x32_bf16 v[128:131], a[204:207], a[140:143], v[128:131]\n"
"v_exp_f32 v237, v185 clamp\n"
"v_mfma_f32_16x16x32_bf16 v[136:139], a[204:207], a[156:159], v[136:139]\n"
"ds_read_b128 a[236:239], v245 offset:27648\n"
"v_cvt_pk_bf16_f32 v220, v236, v237\n"
"v_mfma_f32_16x16x32_bf16 v[144:147], a[204:207], a[172:175], v[144:147]\n"
"v_exp_f32 v238, v186 clamp\n"
"v_mfma_f32_16x16x32_bf16 v[152:155], a[204:207], a[188:191], v[152:155]\n"
"s_add_u32 s47, s44, 135168\n"
"v_exp_f32 v239, v187 clamp\n"
"v_mfma_f32_16x16x32_bf16 v[132:135], a[208:211], a[128:131], v[192:195]\n"
"buffer_store_dwordx4 a[40:43], v244, s[84:87], s47 offen offset:0 sc1\n"
"ds_read_b128 a[240:243], v245 offset:28672\n"
"v_cvt_pk_bf16_f32 v221, v238, v239\n"
"v_mfma_f32_16x16x32_bf16 v[140:143], a[208:211], a[144:147], v[196:199]\n"
"v_exp_f32 v236, v188 clamp\n"
"v_mfma_f32_16x16x32_bf16 v[148:151], a[208:211], a[160:163], v[200:203]\n"
"v_exp_f32 v237, v189 clamp\n"
"v_mfma_f32_16x16x32_bf16 v[156:159], a[208:211], a[176:179], v[204:207]\n"
"ds_read_b128 a[244:247], v245 offset:29696\n"
"v_cvt_pk_bf16_f32 v222, v236, v237\n"
"v_mfma_f32_16x16x32_bf16 v[132:135], a[212:215], a[132:135], v[132:135]\n"
"v_exp_f32 v238, v190 clamp\n"
"v_mfma_f32_16x16x32_bf16 v[140:143], a[212:215], a[148:151], v[140:143]\n"
"buffer_load_dwordx4 v246, s[40:43], s56 offen offset:1024 sc1 lds\n"
"v_exp_f32 v239, v191 clamp\n"
"v_mfma_f32_16x16x32_bf16 v[148:151], a[212:215], a[164:167], v[148:151]\n"
"ds_read_b128 a[248:251], v245 offset:30720\n"
"v_cvt_pk_bf16_f32 v223, v238, v239\n"
"v_mfma_f32_16x16x32_bf16 v[156:159], a[212:215], a[180:183], v[156:159]\n"
"s_nop 0\n"
"v_smfmac_f32_16x16x64_bf16 v[228:231], v[240:243], v[216:223], v247\n"
"ds_read_b128 a[252:255], v245 offset:31744\n"
"v_mfma_f32_16x16x32_bf16 v[132:135], a[216:219], a[136:139], v[132:135]\n"
"v_max3_f32 v236, v224, v225, v226\n"
"v_mfma_f32_16x16x32_bf16 v[140:143], a[216:219], a[152:155], v[140:143]\n"
"buffer_load_dwordx4 v246, s[40:43], s56 offen offset:2048 sc1 lds\n"
"v_mfma_f32_16x16x32_bf16 v[148:151], a[216:219], a[168:171], v[148:151]\n"
"v_mfma_f32_16x16x32_bf16 v[156:159], a[216:219], a[184:187], v[156:159]\n"
"s_nop 0\n"
"v_max3_f32 v236, v236, v227, v228\n"
"v_max3_f32 v236, v236, v229, v230\n"
"v_mfma_f32_16x16x32_bf16 v[132:135], a[220:223], a[140:143], v[132:135]\n"
"s_add_u32 s47, s44, 135168\n"
"v_max_f32 v236, v236, v231\n"
"v_cmp_nge_f32 vcc, s58, v236\n"
"v_mfma_f32_16x16x32_bf16 v[140:143], a[220:223], a[156:159], v[140:143]\n"
"buffer_store_dwordx4 a[44:47], v244, s[84:87], s47 offen offset:1024 sc1\n"
"v_mfma_f32_16x16x32_bf16 v[148:151], a[220:223], a[172:175], v[148:151]\n"
"s_waitcnt lgkmcnt(0)\n"
"v_mfma_f32_16x16x32_bf16 v[156:159], a[220:223], a[188:191], v[156:159]\n"
"s_getpc_b64 s[66:67]\n"
"s_cbranch_vccnz .LstubG0_%=\n"
"buffer_load_dwordx4 v246, s[40:43], s56 offen offset:3072 sc1 lds\n"
".Lpp3_%=:\n"
"v_mfma_f32_16x16x32_bf16 v[0:3], a[224:227], v[208:211], v[0:3]\n"
"s_waitcnt vmcnt(27)\n"
"v_exp_f32 v236, v128 clamp\n"
"v_mfma_f32_16x16x32_bf16 v[16:19], a[228:231], v[208:211], v[16:19]\n"
"s_barrier\n"
"s_lshl_b32 s47, %2, 2\n"
"s_add_u32 s47, s47, 81829888\n"
"s_cmp_eq_u32 s76, 0\n"
"s_cselect_b32 s47, s47, 1073741824\n"
"v_mfma_f32_16x16x32_bf16 v[32:35], a[232:235], v[208:211], v[32:35]\n"
"s_add_u32 s52, s52, 1\n"
"v_exp_f32 v237, v129 clamp\n"
"v_mfma_f32_16x16x32_bf16 v[48:51], a[236:239], v[208:211], v[48:51]\n"
"s_add_u32 m0, s57, 16384\n"
"v_lshlrev_b32 v253, 17, v246\n"
"v_mov_b32 v254, s88\n"
"buffer_store_dword v254, v253, s[84:87], s47 offen sc1\n"
"ds_read_b128 a[192:195], v244 offset:32768\n"
"v_mfma_f32_16x16x32_bf16 v[64:67], a[240:243], v[208:211], v[64:67]\n"
"v_exp_f32 v238, v130 clamp\n"
"v_mfma_f32_16x16x32_bf16 v[80:83], a[244:247], v[208:211], v[80:83]\n"
"s_sub_u32 s55, s55, s29\n"
"v_exp_f32 v239, v131 clamp\n"
"v_mfma_f32_16x16x32_bf16 v[96:99], a[248:251], v[208:211], v[96:99]\n"
"buffer_load_dwordx4 v246, s[36:39], s54 offen offset:0 sc1 lds\n"
"v_mfma_f32_16x16x32_bf16 v[112:115], a[252:255], v[208:211], v[112:115]\n"
"ds_read_b128 a[196:199], v244 offset:33792\n"
"v_cvt_pk_bf16_f32 v208, v236, v237\n"
"v_mfma_f32_16x16x32_bf16 v[4:7], a[224:227], v[212:215], v[4:7]\n"
"ds_read_b128 a[200:203], v244 offset:34816\n"
"v_cvt_pk_bf16_f32 v209, v238, v239\n"
"v_mfma_f32_16x16x32_bf16 v[20:23], a[228:231], v[212:215], v[20:23]\n"
"s_add_u32 s47, s45, 131072\n"
"v_exp_f32 v236, v132 clamp\n"
"v_mfma_f32_16x16x32_bf16 v[36:39], a[232:235], v[212:215], v[36:39]\n"
"buffer_store_dwordx4 a[48:51], v244, s[84:87], s47 offen offset:0 sc1\n"
"v_exp_f32 v237, v133 clamp\n"
"v_mfma_f32_16x16x32_bf16 v[52:55], a[236:239], v[212:215], v[52:55]\n"
"ds_read_b128 a[204:207], v244 offset:35840\n"
"v_cvt_pk_bf16_f32 v210, v236, v237\n"
"v_mfma_f32_16x16x32_bf16 v[68:71], a[240:243], v[212:215], v[68:71]\n"
"v_exp_f32 v238, v134 clamp\n"
"v_mfma_f32_16x16x32_bf16 v[84:87], a[244:247], v[212:215], v[84:87]\n"
"v_exp_f32 v239, v135 clamp\n"
"v_mfma_f32_16x16x32_bf16 v[100:103], a[248:251], v[212:215], v[100:103]\n"
"buffer_load_dwordx4 v246, s[36:39], s54 offen offset:1024 sc1 lds\n"
"v_mfma_f32_16x16x32_bf16 v[116:119], a[252:255], v[212:215], v[116:119]\n"
"ds_read_b128 a[208:211], v244 offset:36864\n"
"v_cvt_pk_bf16_f32 v211, v238, v239\n"
"v_mfma_f32_16x16x32_bf16 v[8:11], a[224:227], v[216:219], v[8:11]\n"
"s_add_u32 s47, s45, 131072\n"
"v_exp_f32 v236, v136 clamp\n"
"v_mfma_f32_16x16x32_bf16 v[24:27], a[228:231], v[216:219], v[24:27]\n"
"buffer_store_dwordx4 a[52:55], v244, s[84:87], s47 offen offset:1024 sc1\n"
"v_exp_f32 v237, v137 clamp\n"
"v_mfma_f32_16x16x32_bf16 v[40:43], a[232:235], v[216:219], v[40:43]\n"
"ds_read_b128 a[212:215], v244 offset:37888\n"
"v_cvt_pk_bf16_f32 v212, v236, v237\n"
"v_mfma_f32_16x16x32_bf16 v[56:59], a[236:239], v[216:219], v[56:59]\n"
"v_exp_f32 v238, v138 clamp\n"
"v_mfma_f32_16x16x32_bf16 v[72:75], a[240:243], v[216:219], v[72:75]\n"
"v_exp_f32 v239, v139 clamp\n"
"v_mfma_f32_16x16x32_bf16 v[88:91], a[244:247], v[216:219], v[88:91]\n"
"buffer_load_dwordx4 v246, s[36:39], s54 offen offset:2048 sc1 lds\n"
"v_mfma_f32_16x16x32_bf16 v[104:107], a[248:251], v[216:219], v[104:107]\n"
"ds_read_b128 a[216:219], v244 offset:38912\n"
"v_cvt_pk_bf16_f32 v213, v238, v239\n"
"v_mfma_f32_16x16x32_bf16 v[120:123], a[252:255], v[216:219], v[120:123]\n"
"v_exp_f32 v236, v140 clamp\n"
"v_mfma_f32_16x16x32_bf16 v[12:15], a[224:227], v[220:223], v[12:15]\n"
"v_exp_f32 v237, v141 clamp\n"
"v_mfma_f32_16x16x32_bf16 v[28:31], a[228:231], v[220:223], v[28:31]\n"
"ds_read_b128 a[220:223], v244 offset:39936\n"
"v_cvt_pk_bf16_f32 v214, v236, v237\n"
"v_mfma_f32_16x16x32_bf16 v[44:47], a[232:235], v[220:223], v[44:47]\n"
"v_exp_f32 v238, v142 clamp\n"
"v_mfma_f32_16x16x32_bf16 v[60:63], a[236:239], v[220:223], v[60:63]\n"
"v_exp_f32 v239, v143 clamp\n"
"v_mfma_f32_16x16x32_bf16 v[76:79], a[240:243], v[220:223], v[76:79]\n"
"buffer_load_dwordx4 v246, s[36:39], s54 offen offset:3072 sc1 lds\n"
"v_mfma_f32_16x16x32_bf16 v[92:95], a[244:247], v[220:223], v[92:95]\n"
"s_add_u32 s54, s54, s28\n"
"v_cvt_pk_bf16_f32 v215, v238, v239\n"
"v_mfma_f32_16x16x32_bf16 v[108:111], a[248:251], v[220:223], v[108:111]\n"
"v_exp_f32 v236, v144 clamp\n"
"v_mfma_f32_16x16x32_bf16 v[124:127], a[252:255], v[220:223], v[124:127]\n"
"s_waitcnt lgkmcnt(0)\n"
"v_exp_f32 v237, v145 clamp\n"
"v_mfma_f32_16x16x32_bf16 v[160:163], a[192:195], a[128:131], v[192:195]\n"
"s_cmp_lt_u32 s55, 540672\n"
"v_cvt_pk_bf16_f32 v216, v236, v237\n"
"v_mfma_f32_16x16x32_bf16 v[168:171], a[192:195], a[144:147], v[196:199]\n"
"s_cselect_b32 s56, 1073741824, s55\n"
"v_exp_f32 v238, v146 clamp\n"
"v_smfmac_f32_16x16x64_bf16 v[224:227], v[240:243], v[208:215], v247\n"
"s_add_u32 m0, s57, 81920\n"
"v_mfma_f32_16x16x32_bf16 v[176:179], a[192:195], a[160:163], v[200:203]\n"
"s_cmp_eq_u32 s64, s52\n"
"v_exp_f32 v239, v147 clamp\n"
"v_mfma_f32_16x16x32_bf16 v[184:187], a[192:195], a[176:179], v[204:207]\n"
"s_cselect_b32 s58, -1.0, 0.5\n"
"ds_read_b128 a[224:227], v245 offset:40960\n"
"v_cvt_pk_bf16_f32 v217, v238, v239\n"
"v_mfma_f32_16x16x32_bf16 v[160:163], a[196:199], a[132:135], v[160:163]\n"
"s_add_u32 s47, s45, 131072\n"
"v_exp_f32 v236, v148 clamp\n"
"v_mfma_f32_16x16x32_bf16 v[168:171], a[196:199], a[148:151], v[168:171]\n"
"buffer_store_dwordx4 a[56:59], v244, s[84:87], s47 offen offset:2048 sc1\n"
"v_exp_f32 v237, v149 clamp\n"
"v_mfma_f32_16x16x32_bf16 v[176:179], a[196:199], a[164:167], v[176:179]\n"
"ds_read_b128 a[228:231], v245 offset:41984\n"
"v_cvt_pk_bf16_f32 v218, v236, v237\n"
"v_mfma_f32_16x16x32_bf16 v[184:187], a[196:199], a[180:183], v[184:187]\n"
"v_exp_f32 v238, v150 clamp\n"
"v_mfma_f32_16x16x32_bf16 v[160:163], a[200:203], a[136:139], v[160:163]\n"
"v_exp_f32 v239, v151 clamp\n"
"v_mfma_f32_16x16x32_bf16 v[168:171], a[200:203], a[152:155], v[168:171]\n"
"ds_read_b128 a[232:235], v245 offset:43008\n"
"v_cvt_pk_bf16_f32 v219, v238, v239\n"
"v_mfma_f32_16x16x32_bf16 v[176:179], a[200:203], a[168:171], v[176:179]\n"
"v_exp_f32 v236, v152 clamp\n"
"v_mfma_f32_16x16x32_bf16 v[184:187], a[200:203], a[184:187], v[184:187]\n"
"buffer_load_dwordx4 v246, s[40:43], s56 offen offset:0 sc1 lds\n"
"v_mfma_f32_16x16x32_bf16 v[160:163], a[204:207], a[140:143], v[160:163]\n"
"v_exp_f32 v237, v153 clamp\n"
"v_mfma_f32_16x16x32_bf16 v[168:171], a[204:207], a[156:159], v[168:171]\n"
"ds_read_b128 a[236:239], v245 offset:44032\n"
"v_cvt_pk_bf16_f32 v220, v236, v237\n"
"v_mfma_f32_16x16x32_bf16 v[176:179], a[204:207], a[172:175], v[176:179]\n"
"v_exp_f32 v238, v154 clamp\n"
"v_mfma_f32_16x16x32_bf16 v[184:187], a[204:207], a[188:191], v[184:187]\n"
"s_add_u32 s47, s45, 131072\n"
"v_exp_f32 v239, v155 clamp\n"
"v_mfma_f32_16x16x32_bf16 v[164:167], a[208:211], a[128:131], v[192:195]\n"
"buffer_store_dwordx4 a[60:63], v244, s[84:87], s47 offen offset:3072 sc1\n"
"ds_read_b128 a[240:243], v245 offset:45056\n"
"v_cvt_pk_bf16_f32 v221, v238, v239\n"
"v_mfma_f32_16x16x32_bf16 v[172:175], a[208:211], a[144:147], v[196:199]\n"
"v_exp_f32 v236, v156 clamp\n"
"v_mfma_f32_16x16x32_bf16 v[180:183], a[208:211], a[160:163], v[200:203]\n"
"v_exp_f32 v237, v157 clamp\n"
"v_mfma_f32_16x16x32_bf16 v[188:191], a[208:211], a[176:179], v[204:207]\n"
"ds_read_b128 a[244:247], v245 offset:46080\n"
"v_cvt_pk_bf16_f32 v222, v236, v237\n"
"v_mfma_f32_16x16x32_bf16 v[164:167], a[212:215], a[132:135], v[164:167]\n"
"v_exp_f32 v238, v158 clamp\n"
"v_mfma_f32_16x16x32_bf16 v[172:175], a[212:215], a[148:151], v[172:175]\n"
"buffer_load_dwordx4 v246, s[40:43], s56 offen offset:1024 sc1 lds\n"
"v_exp_f32 v239, v159 clamp\n"
"v_mfma_f32_16x16x32_bf16 v[180:183], a[212:215], a[164:167], v[180:183]\n"
"ds_read_b128 a[248:251], v245 offset:47104\n"
"v_cvt_pk_bf16_f32 v223, v238, v239\n"
"v_mfma_f32_16x16x32_bf16 v[188:191], a[212:215], a[180:183], v[188:191]\n"
"s_nop 0\n"
"v_smfmac_f32_16x16x64_bf16 v[228:231], v[240:243], v[216:223], v247\n"
"ds_read_b128 a[252:255], v245 offset:48128\n"
"v_mfma_f32_16x16x32_bf16 v[164:167], a[216:219], a[136:139], v[164:167]\n"
"v_max3_f32 v236, v224, v225, v226\n"
"v_mfma_f32_16x16x32_bf16 v[172:175], a[216:219], a[152:155], v[172:175]\n"
"buffer_load_dwordx4 v246, s[40:43], s56 offen offset:2048 sc1 lds\n"
"v_mfma_f32_16x16x32_bf16 v[180:183], a[216:219], a[168:171], v[180:183]\n"
"v_mfma_f32_16x16x32_bf16 v[188:191], a[216:219], a[184:187], v[188:191]\n"
"s_lshl_b32 s47, s78, 15\n"
"s_sub_u32 s47, 376832, s47\n"
"s_add_u32 s44, s44, s47\n"
"s_add_u32 s45, s45, s47\n"
"v_max3_f32 v236, v236, v227, v228\n"
"v_max3_f32 v236, v236, v229, v230\n"
"v_mfma_f32_16x16x32_bf16 v[164:167], a[220:223], a[140:143], v[164:167]\n"
"s_add_u32 s47, s44, 0\n"
"v_max_f32 v236, v236, v231\n"
"v_cmp_nge_f32 vcc, s58, v236\n"
"v_mfma_f32_16x16x32_bf16 v[172:175], a[220:223], a[156:159], v[172:175]\n"
"buffer_store_dwordx4 a[64:67], v244, s[84:87], s47 offen offset:0 sc1\n"
"v_mfma_f32_16x16x32_bf16 v[180:183], a[220:223], a[172:175], v[180:183]\n"
"s_waitcnt lgkmcnt(0)\n"
"v_mfma_f32_16x16x32_bf16 v[188:191], a[220:223], a[188:191], v[188:191]\n"
"s_getpc_b64 s[66:67]\n"
"s_cbranch_vccnz .LstubG1_%=\n"
"buffer_load_dwordx4 v246, s[40:43], s56 offen offset:3072 sc1 lds\n"
".Lpp4_%=:\n"
"v_mfma_f32_16x16x32_bf16 v[0:3], a[224:227], v[208:211], v[0:3]\n"
"s_waitcnt vmcnt(27)\n"
"v_exp_f32 v236, v160 clamp\n"
"v_mfma_f32_16x16x32_bf16 v[16:19], a[228:231], v[208:211], v[16:19]\n"
"s_barrier\n"
"v_mfma_f32_16x16x32_bf16 v[32:35], a[232:235], v[208:211], v[32:35]\n"
"s_add_u32 s52, s52, 1\n"
"v_exp_f32 v237, v161 clamp\n"
"v_mfma_f32_16x16x32_bf16 v[48:51], a[236:239], v[208:211], v[48:51]\n"
"s_add_u32 m0, s57, 32768\n"
"ds_read_b128 a[192:195], v244 offset:49152\n"
"v_mfma_f32_16x16x32_bf16 v[64:67], a[240:243], v[208:211], v[64:67]\n"
"v_exp_f32 v238, v162 clamp\n"
"v_mfma_f32_16x16x32_bf16 v[80:83], a[244:247], v[208:211], v[80:83]\n"
"s_sub_u32 s55, s55, s29\n"
"v_exp_f32 v239, v163 clamp\n"
"v_mfma_f32_16x16x32_bf16 v[96:99], a[248:251], v[208:211], v[96:99]\n"
"s_cmp_eq_u32 s76, 0\n"
"s_cbranch_scc0 .Lsk7a_%=\n"
"v_lshrrev_b32 v253, 2, v246\n"
"v_and_b32 v253, 28, v253\n"
"buffer_load_dword v254, v253, s[84:87], s46 offen offset:32 sc1\n"
".Lsk7a_%=:\n"
"buffer_load_dwordx4 v246, s[36:39], s54 offen offset:0 sc1 lds\n"
"v_mfma_f32_16x16x32_bf16 v[112:115], a[252:255], v[208:211], v[112:115]\n"
"ds_read_b128 a[196:199], v244 offset:50176\n"
"v_cvt_pk_bf16_f32 v208, v236, v237\n"
"v_mfma_f32_16x16x32_bf16 v[4:7], a[224:227], v[212:215], v[4:7]\n"
"ds_read_b128 a[200:203], v244 offset:51200\n"
"v_cvt_pk_bf16_f32 v209, v238, v239\n"
"v_mfma_f32_16x16x32_bf16 v[20:23], a[228:231], v[212:215], v[20:23]\n"
"s_add_u32 s47, s44, 0\n"
"v_exp_f32 v236, v164 clamp\n"
"v_mfma_f32_16x16x32_bf16 v[36:39], a[232:235], v[212:215], v[36:39]\n"
"buffer_store_dwordx4 a[68:71], v244, s[84:87], s47 offen offset:1024 sc1\n"
"v_exp_f32 v237, v165 clamp\n"
"v_mfma_f32_16x16x32_bf16 v[52:55], a[236:239], v[212:215], v[52:55]\n"
"ds_read_b128 a[204:207], v244 offset:52224\n"
"v_cvt_pk_bf16_f32 v210, v236, v237\n"
"v_mfma_f32_16x16x32_bf16 v[68:71], a[240:243], v[212:215], v[68:71]\n"
"v_exp_f32 v238, v166 clamp\n"
"v_mfma_f32_16x16x32_bf16 v[84:87], a[244:247], v[212:215], v[84:87]\n"
"v_exp_f32 v239, v167 clamp\n"
"v_mfma_f32_16x16x32_bf16 v[100:103], a[248:251], v[212:215], v[100:103]\n"
"buffer_load_dwordx4 v246, s[36:39], s54 offen offset:1024 sc1 lds\n"
"v_mfma_f32_16x16x32_bf16 v[116:119], a[252:255], v[212:215], v[116:119]\n"
"ds_read_b128 a[208:211], v244 offset:53248\n"
"v_cvt_pk_bf16_f32 v211, v238, v239\n"
"v_mfma_f32_16x16x32_bf16 v[8:11], a[224:227], v[216:219], v[8:11]\n"
"s_add_u32 s47, s44, 4096\n"
"v_exp_f32 v236, v168 clamp\n"
"v_mfma_f32_16x16x32_bf16 v[24:27], a[228:231], v[216:219], v[24:27]\n"
"buffer_store_dwordx4 a[72:75], v244, s[84:87], s47 offen offset:0 sc1\n"
"v_exp_f32 v237, v169 clamp\n"
"v_mfma_f32_16x16x32_bf16 v[40:43], a[232:235], v[216:219], v[40:43]\n"
"ds_read_b128 a[212:215], v244 offset:54272\n"
"v_cvt_pk_bf16_f32 v212, v236, v237\n"
"v_mfma_f32_16x16x32_bf16 v[56:59], a[236:239], v[216:219], v[56:59]\n"
"v_exp_f32 v238, v170 clamp\n"
"v_mfma_f32_16x16x32_bf16 v[72:75], a[240:243], v[216:219], v[72:75]\n"
"v_exp_f32 v239, v171 clamp\n"
"v_mfma_f32_16x16x32_bf16 v[88:91], a[244:247], v[216:219], v[88:91]\n"
"buffer_load_dwordx4 v246, s[36:39], s54 offen offset:2048 sc1 lds\n"
"v_mfma_f32_16x16x32_bf16 v[104:107], a[248:251], v[216:219], v[104:107]\n"
"ds_read_b128 a[216:219], v244 offset:55296\n"
"v_cvt_pk_bf16_f32 v213, v238, v239\n"
"v_mfma_f32_16x16x32_bf16 v[120:123], a[252:255], v[216:219], v[120:123]\n"
"v_exp_f32 v236, v172 clamp\n"
"v_mfma_f32_16x16x32_bf16 v[12:15], a[224:227], v[220:223], v[12:15]\n"
"v_exp_f32 v237, v173 clamp\n"
"v_mfma_f32_16x16x32_bf16 v[28:31], a[228:231], v[220:223], v[28:31]\n"
"ds_read_b128 a[220:223], v244 offset:56320\n"
"v_cvt_pk_bf16_f32 v214, v236, v237\n"
"v_mfma_f32_16x16x32_bf16 v[44:47], a[232:235], v[220:223], v[44:47]\n"
"v_exp_f32 v238, v174 clamp\n"
"v_mfma_f32_16x16x32_bf16 v[60:63], a[236:239], v[220:223], v[60:63]\n"
"v_exp_f32 v239, v175 clamp\n"
"v_mfma_f32_16x16x32_bf16 v[76:79], a[240:243], v[220:223], v[76:79]\n"
"buffer_load_dwordx4 v246, s[36:39], s54 offen offset:3072 sc1 lds\n"
"v_mfma_f32_16x16x32_bf16 v[92:95], a[244:247], v[220:223], v[92:95]\n"
"s_add_u32 s54, s54, s28\n"
"v_cvt_pk_bf16_f32 v215, v238, v239\n"
"v_mfma_f32_16x16x32_bf16 v[108:111], a[248:251], v[220:223], v[108:111]\n"
"v_exp_f32 v236, v176 clamp\n"
"v_mfma_f32_16x16x32_bf16 v[124:127], a[252:255], v[220:223], v[124:127]\n"
"s_waitcnt lgkmcnt(0)\n"
"v_exp_f32 v237, v177 clamp\n"
"v_mfma_f32_16x16x32_bf16 v[128:131], a[192:195], a[128:131], v[192:195]\n"
"s_cmp_lt_u32 s55, 540672\n"
"v_cvt_pk_bf16_f32 v216, v236, v237\n"
"v_mfma_f32_16x16x32_bf16 v[136:139], a[192:195], a[144:147], v[196:199]\n"
"s_cselect_b32 s56, 1073741824, s55\n"
"v_exp_f32 v238, v178 clamp\n"
"v_smfmac_f32_16x16x64_bf16 v[224:227], v[240:243], v[208:215], v247\n"
"s_add_u32 m0, s57, 98304\n"
"v_mfma_f32_16x16x32_bf16 v[144:147], a[192:195], a[160:163], v[200:203]\n"
"s_cmp_eq_u32 s64, s52\n"
"v_exp_f32 v239, v179 clamp\n"
"v_mfma_f32_16x16x32_bf16 v[152:155], a[192:195], a[176:179], v[204:207]\n"
"s_cselect_b32 s58, -1.0, 0.5\n"
"ds_read_b128 a[224:227], v245 offset:57344\n"
"v_cvt_pk_bf16_f32 v217, v238, v239\n"
"v_mfma_f32_16x16x32_bf16 v[128:131], a[196:199], a[132:135], v[128:131]\n"
"s_add_u32 s47, s44, 4096\n"
"v_exp_f32 v236, v180 clamp\n"
"v_mfma_f32_16x16x32_bf16 v[136:139], a[196:199], a[148:151], v[136:139]\n"
"buffer_store_dwordx4 a[76:79], v244, s[84:87], s47 offen offset:1024 sc1\n"
"v_exp_f32 v237, v181 clamp\n"
"v_mfma_f32_16x16x32_bf16 v[144:147], a[196:199], a[164:167], v[144:147]\n"
"ds_read_b128 a[228:231], v245 offset:58368\n"
"v_cvt_pk_bf16_f32 v218, v236, v237\n"
"v_mfma_f32_16x16x32_bf16 v[152:155], a[196:199], a[180:183], v[152:155]\n"
"v_exp_f32 v238, v182 clamp\n"
"v_mfma_f32_16x16x32_bf16 v[128:131], a[200:203], a[136:139], v[128:131]\n"
"v_exp_f32 v239, v183 clamp\n"
"v_mfma_f32_16x16x32_bf16 v[136:139], a[200:203], a[152:155], v[136:139]\n"
"ds_read_b128 a[232:235], v245 offset:59392\n"
"v_cvt_pk_bf16_f32 v219, v238, v239\n"
"v_mfma_f32_16x16x32_bf16 v[144:147], a[200:203], a[168:171], v[144:147]\n"
"v_exp_f32 v236, v184 clamp\n"
"v_mfma_f32_16x16x32_bf16 v[152:155], a[200:203], a[184:187], v[152:155]\n"
"buffer_load_dwordx4 v246, s[40:43], s56 offen offset:0 sc1 lds\n"
"v_mfma_f32_16x16x32_bf16 v[128:131], a[204:207], a[140:143], v[128:131]\n"
"v_exp_f32 v237, v185 clamp\n"
"v_mfma_f32_16x16x32_bf16 v[136:139], a[204:207], a[156:159], v[136:139]\n"
"ds_read_b128 a[236:239], v245 offset:60416\n"
"v_cvt_pk_bf16_f32 v220, v236, v237\n"
"v_mfma_f32_16x16x32_bf16 v[144:147], a[204:207], a[172:175], v[144:147]\n"
"v_exp_f32 v238, v186 clamp\n"
"v_mfma_f32_16x16x32_bf16 v[152:155], a[204:207], a[188:191], v[152:155]\n"
"s_add_u32 s47, s45, 0\n"
"v_exp_f32 v239, v187 clamp\n"
"v_mfma_f32_16x16x32_bf16 v[132:135], a[208:211], a[128:131], v[192:195]\n"
"buffer_store_dwordx4 a[80:83], v244, s[84:87], s47 offen offset:0 sc1\n"
"ds_read_b128 a[240:243], v245 offset:61440\n"
"v_cvt_pk_bf16_f32 v221, v238, v239\n"
"v_mfma_f32_16x16x32_bf16 v[140:143], a[208:211], a[144:147], v[196:199]\n"
"v_exp_f32 v236, v188 clamp\n"
"v_mfma_f32_16x16x32_bf16 v[148:151], a[208:211], a[160:163], v[200:203]\n"
"v_exp_f32 v237, v189 clamp\n"
"v_mfma_f32_16x16x32_bf16 v[156:159], a[208:211], a[176:179], v[204:207]\n"
"ds_read_b128 a[244:247], v245 offset:62464\n"
"v_cvt_pk_bf16_f32 v222, v236, v237\n"
"v_mfma_f32_16x16x32_bf16 v[132:135], a[212:215], a[132:135], v[132:135]\n"
"v_exp_f32 v238, v190 clamp\n"
"v_mfma_f32_16x16x32_bf16 v[140:143], a[212:215], a[148:151], v[140:143]\n"
"buffer_load_dwordx4 v246, s[40:43], s56 offen offset:1024 sc1 lds\n"
"v_exp_f32 v239, v191 clamp\n"
"v_mfma_f32_16x16x32_bf16 v[148:151], a[212:215], a[164:167], v[148:151]\n"
"ds_read_b128 a[248:251], v245 offset:63488\n"
"v_cvt_pk_bf16_f32 v223, v238, v239\n"
"v_mfma_f32_16x16x32_bf16 v[156:159], a[212:215], a[180:183], v[156:159]\n"
"s_nop 0\n"
"v_smfmac_f32_16x16x64_bf16 v[228:231], v[240:243], v[216:223], v247\n"
"ds_read_b128 a[252:255], v245 offset:64512\n"
"v_mfma_f32_16x16x32_bf16 v[132:135], a[216:219], a[136:139], v[132:135]\n"
"v_max3_f32 v236, v224, v225, v226\n"
"v_mfma_f32_16x16x32_bf16 v[140:143], a[216:219], a[152:155], v[140:143]\n"
"buffer_load_dwordx4 v246, s[40:43], s56 offen offset:2048 sc1 lds\n"
"v_mfma_f32_16x16x32_bf16 v[148:151], a[216:219], a[168:171], v[148:151]\n"
"v_mfma_f32_16x16x32_bf16 v[156:159], a[216:219], a[184:187], v[156:159]\n"
"s_nop 0\n"
"v_max3_f32 v236, v236, v227, v228\n"
"v_max3_f32 v236, v236, v229, v230\n"
"v_mfma_f32_16x16x32_bf16 v[132:135], a[220:223], a[140:143], v[132:135]\n"
"s_add_u32 s47, s45, 0\n"
"v_max_f32 v236, v236, v231\n"
"v_cmp_nge_f32 vcc, s58, v236\n"
"v_mfma_f32_16x16x32_bf16 v[140:143], a[220:223], a[156:159], v[140:143]\n"
"buffer_store_dwordx4 a[84:87], v244, s[84:87], s47 offen offset:1024 sc1\n"
"v_mfma_f32_16x16x32_bf16 v[148:151], a[220:223], a[172:175], v[148:151]\n"
"s_waitcnt lgkmcnt(0)\n"
"v_mfma_f32_16x16x32_bf16 v[156:159], a[220:223], a[188:191], v[156:159]\n"
"s_getpc_b64 s[66:67]\n"
"s_cbranch_vccnz .LstubG0_%=\n"
"buffer_load_dwordx4 v246, s[40:43], s56 offen offset:3072 sc1 lds\n"
".Lpp5_%=:\n"
"v_mfma_f32_16x16x32_bf16 v[0:3], a[224:227], v[208:211], v[0:3]\n"
"s_waitcnt vmcnt(27)\n"
"v_exp_f32 v236, v128 clamp\n"
"v_mfma_f32_16x16x32_bf16 v[16:19], a[228:231], v[208:211], v[16:19]\n"
"s_barrier\n"
"v_mfma_f32_16x16x32_bf16 v[32:35], a[232:235], v[208:211], v[32:35]\n"
"s_add_u32 s52, s52, 1\n"
"v_exp_f32 v237, v129 clamp\n"
"v_mfma_f32_16x16x32_bf16 v[48:51], a[236:239], v[208:211], v[48:51]\n"
"s_add_u32 m0, s57, 49152\n"
"ds_read_b128 a[192:195], v244 offset:0\n"
"v_mfma_f32_16x16x32_bf16 v[64:67], a[240:243], v[208:211], v[64:67]\n"
"v_exp_f32 v238, v130 clamp\n"
"v_mfma_f32_16x16x32_bf16 v[80:83], a[244:247], v[208:211], v[80:83]\n"
"s_sub_u32 s55, s55, s29\n"
"v_exp_f32 v239, v131 clamp\n"
"v_mfma_f32_16x16x32_bf16 v[96:99], a[248:251], v[208:211], v[96:99]\n"
"buffer_load_dwordx4 v246, s[36:39], s54 offen offset:0 sc1 lds\n"
"v_mfma_f32_16x16x32_bf16 v[112:115], a[252:255], v[208:211], v[112:115]\n"
"ds_read_b128 a[196:199], v244 offset:1024\n"
"v_cvt_pk_bf16_f32 v208, v236, v237\n"
"v_mfma_f32_16x16x32_bf16 v[4:7], a[224:227], v[212:215], v[4:7]\n"
"ds_read_b128 a[200:203], v244 offset:2048\n"
"v_cvt_pk_bf16_f32 v209, v238, v239\n"
"v_mfma_f32_16x16x32_bf16 v[20:23], a[228:231], v[212:215], v[20:23]\n"
"s_add_u32 s47, s45, 0\n"
"v_exp_f32 v236, v132 clamp\n"
"v_mfma_f32_16x16x32_bf16 v[36:39], a[232:235], v[212:215], v[36:39]\n"
"buffer_store_dwordx4 a[88:91], v244, s[84:87], s47 offen offset:2048 sc1\n"
"v_exp_f32 v237, v133 clamp\n"
"v_mfma_f32_16x16x32_bf16 v[52:55], a[236:239], v[212:215], v[52:55]\n"
"ds_read_b128 a[204:207], v244 offset:3072\n"
"v_cvt_pk_bf16_f32 v210, v236, v237\n"
"v_mfma_f32_16x16x32_bf16 v[68:71], a[240:243], v[212:215], v[68:71]\n"
"v_exp_f32 v238, v134 clamp\n"
"v_mfma_f32_16x16x32_bf16 v[84:87], a[244:247], v[212:215], v[84:87]\n"
"v_exp_f32 v239, v135 clamp\n"
"v_mfma_f32_16x16x32_bf16 v[100:103], a[248:251], v[212:215], v[100:103]\n"
"buffer_load_dwordx4 v246, s[36:39], s54 offen offset:1024 sc1 lds\n"
"v_mfma_f32_16x16x32_bf16 v[116:119], a[252:255], v[212:215], v[116:119]\n"
"ds_read_b128 a[208:211], v244 offset:4096\n"
"v_cvt_pk_bf16_f32 v211, v238, v239\n"
"v_mfma_f32_16x16x32_bf16 v[8:11], a[224:227], v[216:219], v[8:11]\n"
"s_add_u32 s47, s45, 0\n"
"v_exp_f32 v236, v136 clamp\n"
"v_mfma_f32_16x16x32_bf16 v[24:27], a[228:231], v[216:219], v[24:27]\n"
"buffer_store_dwordx4 a[92:95], v244, s[84:87], s47 offen offset:3072 sc1\n"
"v_exp_f32 v237, v137 clamp\n"
"v_mfma_f32_16x16x32_bf16 v[40:43], a[232:235], v[216:219], v[40:43]\n"
"ds_read_b128 a[212:215], v244 offset:5120\n"
"v_cvt_pk_bf16_f32 v212, v236, v237\n"
"v_mfma_f32_16x16x32_bf16 v[56:59], a[236:239], v[216:219], v[56:59]\n"
"v_exp_f32 v238, v138 clamp\n"
"v_mfma_f32_16x16x32_bf16 v[72:75], a[240:243], v[216:219], v[72:75]\n"
"v_exp_f32 v239, v139 clamp\n"
"v_mfma_f32_16x16x32_bf16 v[88:91], a[244:247], v[216:219], v[88:91]\n"
"buffer_load_dwordx4 v246, s[36:39], s54 offen offset:2048 sc1 lds\n"
"v_mfma_f32_16x16x32_bf16 v[104:107], a[248:251], v[216:219], v[104:107]\n"
"ds_read_b128 a[216:219], v244 offset:6144\n"
"v_cvt_pk_bf16_f32 v213, v238, v239\n"
"v_mfma_f32_16x16x32_bf16 v[120:123], a[252:255], v[216:219], v[120:123]\n"
"v_exp_f32 v236, v140 clamp\n"
"v_mfma_f32_16x16x32_bf16 v[12:15], a[224:227], v[220:223], v[12:15]\n"
"v_exp_f32 v237, v141 clamp\n"
"v_mfma_f32_16x16x32_bf16 v[28:31], a[228:231], v[220:223], v[28:31]\n"
"ds_read_b128 a[220:223], v244 offset:7168\n"
"v_cvt_pk_bf16_f32 v214, v236, v237\n"
"v_mfma_f32_16x16x32_bf16 v[44:47], a[232:235], v[220:223], v[44:47]\n"
"v_exp_f32 v238, v142 clamp\n"
"v_mfma_f32_16x16x32_bf16 v[60:63], a[236:239], v[220:223], v[60:63]\n"
"v_exp_f32 v239, v143 clamp\n"
"v_mfma_f32_16x16x32_bf16 v[76:79], a[240:243], v[220:223], v[76:79]\n"
"buffer_load_dwordx4 v246, s[36:39], s54 offen offset:3072 sc1 lds\n"
"v_mfma_f32_16x16x32_bf16 v[92:95], a[244:247], v[220:223], v[92:95]\n"
"s_add_u32 s54, s54, s28\n"
"v_cvt_pk_bf16_f32 v215, v238, v239\n"
"v_mfma_f32_16x16x32_bf16 v[108:111], a[248:251], v[220:223], v[108:111]\n"
"v_exp_f32 v236, v144 clamp\n"
"v_mfma_f32_16x16x32_bf16 v[124:127], a[252:255], v[220:223], v[124:127]\n"
"s_waitcnt lgkmcnt(0)\n"
"v_exp_f32 v237, v145 clamp\n"
"v_mfma_f32_16x16x32_bf16 v[160:163], a[192:195], a[128:131], v[192:195]\n"
"s_cmp_lt_u32 s55, 540672\n"
"v_cvt_pk_bf16_f32 v216, v236, v237\n"
"v_mfma_f32_16x16x32_bf16 v[168:171], a[192:195], a[144:147], v[196:199]\n"
"s_cselect_b32 s56, 1073741824, s55\n"
"v_exp_f32 v238, v146 clamp\n"
"v_smfmac_f32_16x16x64_bf16 v[224:227], v[240:243], v[208:215], v247\n"
"s_add_u32 m0, s57, 114688\n"
"v_mfma_f32_16x16x32_bf16 v[176:179], a[192:195], a[160:163], v[200:203]\n"
"s_cmp_eq_u32 s64, s52\n"
"v_exp_f32 v239, v147 clamp\n"
"v_mfma_f32_16x16x32_bf16 v[184:187], a[192:195], a[176:179], v[204:207]\n"
"s_cselect_b32 s58, -1.0, 0.5\n"
"ds_read_b128 a[224:227], v245 offset:8192\n"
"v_cvt_pk_bf16_f32 v217, v238, v239\n"
"v_mfma_f32_16x16x32_bf16 v[160:163], a[196:199], a[132:135], v[160:163]\n"
"s_add_u32 s47, s44, 131072\n"
"v_exp_f32 v236, v148 clamp\n"
"v_mfma_f32_16x16x32_bf16 v[168:171], a[196:199], a[148:151], v[168:171]\n"
"buffer_store_dwordx4 a[96:99], v244, s[84:87], s47 offen offset:0 sc1\n"
"v_exp_f32 v237, v149 clamp\n"
"v_mfma_f32_16x16x32_bf16 v[176:179], a[196:199], a[164:167], v[176:179]\n"
"ds_read_b128 a[228:231], v245 offset:9216\n"
"v_cvt_pk_bf16_f32 v218, v236, v237\n"
"v_mfma_f32_16x16x32_bf16 v[184:187], a[196:199], a[180:183], v[184:187]\n"
"v_exp_f32 v238, v150 clamp\n"
"v_mfma_f32_16x16x32_bf16 v[160:163], a[200:203], a[136:139], v[160:163]\n"
"v_exp_f32 v239, v151 clamp\n"
"v_mfma_f32_16x16x32_bf16 v[168:171], a[200:203], a[152:155], v[168:171]\n"
"ds_read_b128 a[232:235], v245 offset:10240\n"
".Lpad13_%=:\n"
"s_cbranch_execz .Lpadqp3_%=\n"
"v_cvt_pk_bf16_f32 v219, v238, v239\n"
"v_mfma_f32_16x16x32_bf16 v[176:179], a[200:203], a[168:171], v[176:179]\n"
"v_exp_f32 v236, v152 clamp\n"
"v_mfma_f32_16x16x32_bf16 v[184:187], a[200:203], a[184:187], v[184:187]\n"
"buffer_load_dwordx4 v246, s[40:43], s56 offen offset:0 sc1 lds\n"
"v_mfma_f32_16x16x32_bf16 v[160:163], a[204:207], a[140:143], v[160:163]\n"
"v_exp_f32 v237, v153 clamp\n"
"v_mfma_f32_16x16x32_bf16 v[168:171], a[204:207], a[156:159], v[168:171]\n"
"ds_read_b128 a[236:239], v245 offset:11264\n"
"v_cvt_pk_bf16_f32 v220, v236, v237\n"
"v_mfma_f32_16x16x32_bf16 v[176:179], a[204:207], a[172:175], v[176:179]\n"
"v_exp_f32 v238, v154 clamp\n"
"v_mfma_f32_16x16x32_bf16 v[184:187], a[204:207], a[188:191], v[184:187]\n"
"s_add_u32 s47, s44, 131072\n"
"v_exp_f32 v239, v155 clamp\n"
"v_mfma_f32_16x16x32_bf16 v[164:167], a[208:211], a[128:131], v[192:195]\n"
"buffer_store_dwordx4 a[100:103], v244, s[84:87], s47 offen offset:1024 sc1\n"
"ds_read_b128 a[240:243], v245 offset:12288\n"
"v_cvt_pk_bf16_f32 v221, v238, v239\n"
"v_mfma_f32_16x16x32_bf16 v[172:175], a[208:211], a[144:147], v[196:199]\n"
"v_exp_f32 v236, v156 clamp\n"
"v_mfma_f32_16x16x32_bf16 v[180:183], a[208:211], a[160:163], v[200:203]\n"
"v_exp_f32 v237, v157 clamp\n"
"v_mfma_f32_16x16x32_bf16 v[188:191], a[208:211], a[176:179], v[204:207]\n"
"ds_read_b128 a[244:247], v245 offset:13312\n"
"v_cvt_pk_bf16_f32 v222, v236, v237\n"
"v_mfma_f32_16x16x32_bf16 v[164:167], a[212:215], a[132:135], v[164:167]\n"
"v_exp_f32 v238, v158 clamp\n"
"v_mfma_f32_16x16x32_bf16 v[172:175], a[212:215], a[148:151], v[172:175]\n"
"buffer_load_dwordx4 v246, s[40:43], s56 offen offset:1024 sc1 lds\n"
"v_exp_f32 v239, v159 clamp\n"
"v_mfma_f32_16x16x32_bf16 v[180:183], a[212:215], a[164:167], v[180:183]\n"
"ds_read_b128 a[248:251], v245 offset:14336\n"
"v_cvt_pk_bf16_f32 v223, v238, v239\n"
"v_mfma_f32_16x16x32_bf16 v[188:191], a[212:215], a[180:183], v[188:191]\n"
"s_nop 0\n"
"v_smfmac_f32_16x16x64_bf16 v[228:231], v[240:243], v[216:223], v247\n"
"ds_read_b128 a[252:255], v245 offset:15360\n"
"v_mfma_f32_16x16x32_bf16 v[164:167], a[216:219], a[136:139], v[164:167]\n"
"v_max3_f32 v236, v224, v225, v226\n"
"v_mfma_f32_16x16x32_bf16 v[172:175], a[216:219], a[152:155], v[172:175]\n"
"buffer_load_dwordx4 v246, s[40:43], s56 offen offset:2048 sc1 lds\n"
"v_mfma_f32_16x16x32_bf16 v[180:183], a[216:219], a[168:171], v[180:183]\n"
"v_mfma_f32_16x16x32_bf16 v[188:191], a[216:219], a[184:187], v[188:191]\n"
"s_nop 0\n"
"v_max3_f32 v236, v236, v227, v228\n"
"v_max3_f32 v236, v236, v229, v230\n"
"v_mfma_f32_16x16x32_bf16 v[164:167], a[220:223], a[140:143], v[164:167]\n"
"s_add_u32 s47, s44, 135168\n"
"v_max_f32 v236, v236, v231\n"
"v_cmp_nge_f32 vcc, s58, v236\n"
"v_mfma_f32_16x16x32_bf16 v[172:175], a[220:223], a[156:159], v[172:175]\n"
"buffer_store_dwordx4 a[104:107], v244, s[84:87], s47 offen offset:0 sc1\n"
"v_mfma_f32_16x16x32_bf16 v[180:183], a[220:223], a[172:175], v[180:183]\n"
"s_waitcnt lgkmcnt(0)\n"
"v_mfma_f32_16x16x32_bf16 v[188:191], a[220:223], a[188:191], v[188:191]\n"
"s_getpc_b64 s[66:67]\n"
"s_cbranch_vccnz .LstubG1_%=\n"
"buffer_load_dwordx4 v246, s[40:43], s56 offen offset:3072 sc1 lds\n"
".Lpp6_%=:\n"
"v_mfma_f32_16x16x32_bf16 v[0:3], a[224:227], v[208:211], v[0:3]\n"
"s_waitcnt vmcnt(26)\n"
"v_exp_f32 v236, v160 clamp\n"
"v_mfma_f32_16x16x32_bf16 v[16:19], a[228:231], v[208:211], v[16:19]\n"
"s_barrier\n"
"v_mfma_f32_16x16x32_bf16 v[32:35], a[232:235], v[208:211], v[32:35]\n"
"s_add_u32 s52, s52, 1\n"
"v_exp_f32 v237, v161 clamp\n"
"v_mfma_f32_16x16x32_bf16 v[48:51], a[236:239], v[208:211], v[48:51]\n"
"s_add_u32 m0, s57, 0\n"
"ds_read_b128 a[192:195], v244 offset:16384\n"
"v_mfma_f32_16x16x32_bf16 v[64:67], a[240:243], v[208:211], v[64:67]\n"
"v_exp_f32 v238, v162 clamp\n"
"v_mfma_f32_16x16x32_bf16 v[80:83], a[244:247], v[208:211], v[80:83]\n"
"s_sub_u32 s55, s55, s29\n"
"v_exp_f32 v239, v163 clamp\n"
"v_mfma_f32_16x16x32_bf16 v[96:99], a[248:251], v[208:211], v[96:99]\n"
"buffer_load_dwordx4 v246, s[36:39], s54 offen offset:0 sc1 lds\n"
"v_mfma_f32_16x16x32_bf16 v[112:115], a[252:255], v[208:211], v[112:115]\n"
"ds_read_b128 a[196:199], v244 offset:17408\n"
"v_cvt_pk_bf16_f32 v208, v236, v237\n"
"v_mfma_f32_16x16x32_bf16 v[4:7], a[224:227], v[212:215], v[4:7]\n"
"ds_read_b128 a[200:203], v244 offset:18432\n"
"v_cvt_pk_bf16_f32 v209, v238, v239\n"
"v_mfma_f32_16x16x32_bf16 v[20:23], a[228:231], v[212:215], v[20:23]\n"
"s_add_u32 s47, s44, 135168\n"
"v_exp_f32 v236, v164 clamp\n"
"v_mfma_f32_16x16x32_bf16 v[36:39], a[232:235], v[212:215], v[36:39]\n"
"buffer_store_dwordx4 a[108:111], v244, s[84:87], s47 offen offset:1024 sc1\n"
"v_exp_f32 v237, v165 clamp\n"
"v_mfma_f32_16x16x32_bf16 v[52:55], a[236:239], v[212:215], v[52:55]\n"
"ds_read_b128 a[204:207], v244 offset:19456\n"
"v_cvt_pk_bf16_f32 v210, v236, v237\n"
"v_mfma_f32_16x16x32_bf16 v[68:71], a[240:243], v[212:215], v[68:71]\n"
"v_exp_f32 v238, v166 clamp\n"
"v_mfma_f32_16x16x32_bf16 v[84:87], a[244:247], v[212:215], v[84:87]\n"
"v_exp_f32 v239, v167 clamp\n"
"v_mfma_f32_16x16x32_bf16 v[100:103], a[248:251], v[212:215], v[100:103]\n"
"buffer_load_dwordx4 v246, s[36:39], s54 offen offset:1024 sc1 lds\n"
"v_mfma_f32_16x16x32_bf16 v[116:119], a[252:255], v[212:215], v[116:119]\n"
"ds_read_b128 a[208:211], v244 offset:20480\n"
"v_cvt_pk_bf16_f32 v211, v238, v239\n"
"v_mfma_f32_16x16x32_bf16 v[8:11], a[224:227], v[216:219], v[8:11]\n"
"s_add_u32 s47, s45, 131072\n"
"v_exp_f32 v236, v168 clamp\n"
"v_mfma_f32_16x16x32_bf16 v[24:27], a[228:231], v[216:219], v[24:27]\n"
"buffer_store_dwordx4 a[112:115], v244, s[84:87], s47 offen offset:0 sc1\n"
"v_exp_f32 v237, v169 clamp\n"
"v_mfma_f32_16x16x32_bf16 v[40:43], a[232:235], v[216:219], v[40:43]\n"
"ds_read_b128 a[212:215], v244 offset:21504\n"
"v_cvt_pk_bf16_f32 v212, v236, v237\n"
"v_mfma_f32_16x16x32_bf16 v[56:59], a[236:239], v[216:219], v[56:59]\n"
"v_exp_f32 v238, v170 clamp\n"
"v_mfma_f32_16x16x32_bf16 v[72:75], a[240:243], v[216:219], v[72:75]\n"
"v_exp_f32 v239, v171 clamp\n"
"v_mfma_f32_16x16x32_bf16 v[88:91], a[244:247], v[216:219], v[88:91]\n"
"buffer_load_dwordx4 v246, s[36:39], s54 offen offset:2048 sc1 lds\n"
"v_mfma_f32_16x16x32_bf16 v[104:107], a[248:251], v[216:219], v[104:107]\n"
"ds_read_b128 a[216:219], v244 offset:22528\n"
"v_cvt_pk_bf16_f32 v213, v238, v239\n"
"v_mfma_f32_16x16x32_bf16 v[120:123], a[252:255], v[216:219], v[120:123]\n"
"v_exp_f32 v236, v172 clamp\n"
"v_mfma_f32_16x16x32_bf16 v[12:15], a[224:227], v[220:223], v[12:15]\n"
"v_exp_f32 v237, v173 clamp\n"
"v_mfma_f32_16x16x32_bf16 v[28:31], a[228:231], v[220:223], v[28:31]\n"
"ds_read_b128 a[220:223], v244 offset:23552\n"
"v_cvt_pk_bf16_f32 v214, v236, v237\n"
"v_mfma_f32_16x16x32_bf16 v[44:47], a[232:235], v[220:223], v[44:47]\n"
"v_exp_f32 v238, v174 clamp\n"
"v_mfma_f32_16x16x32_bf16 v[60:63], a[236:239], v[220:223], v[60:63]\n"
"v_exp_f32 v239, v175 clamp\n"
"v_mfma_f32_16x16x32_bf16 v[76:79], a[240:243], v[220:223], v[76:79]\n"
"buffer_load_dwordx4 v246, s[36:39], s54 offen offset:3072 sc1 lds\n"
"v_mfma_f32_16x16x32_bf16 v[92:95], a[244:247], v[220:223], v[92:95]\n"
"s_add_u32 s54, s54, s28\n"
"v_cvt_pk_bf16_f32 v215, v238, v239\n"
"v_mfma_f32_16x16x32_bf16 v[108:111], a[248:251], v[220:223], v[108:111]\n"
"v_exp_f32 v236, v176 clamp\n"
"v_mfma_f32_16x16x32_bf16 v[124:127], a[252:255], v[220:223], v[124:127]\n"
"s_waitcnt lgkmcnt(0)\n"
"v_exp_f32 v237, v177 clamp\n"
"v_mfma_f32_16x16x32_bf16 v[128:131], a[192:195], a[128:131], v[192:195]\n"
"s_cmp_lt_u32 s55, 540672\n"
"v_cvt_pk_bf16_f32 v216, v236, v237\n"
"v_mfma_f32_16x16x32_bf16 v[136:139], a[192:195], a[144:147], v[196:199]\n"
"s_cselect_b32 s56, 1073741824, s55\n"
"v_exp_f32 v238, v178 clamp\n"
"v_smfmac_f32_16x16x64_bf16 v[224:227], v[240:243], v[208:215], v247\n"
"s_add_u32 m0, s57, 65536\n"
"v_mfma_f32_16x16x32_bf16 v[144:147], a[192:195], a[160:163], v[200:203]\n"
"s_cmp_eq_u32 s64, s52\n"
"v_exp_f32 v239, v179 clamp\n"
"v_mfma_f32_16x16x32_bf16 v[152:155], a[192:195], a[176:179], v[204:207]\n"
"s_cselect_b32 s58, -1.0, 0.5\n"
"ds_read_b128 a[224:227], v245 offset:24576\n"
"v_cvt_pk_bf16_f32 v217, v238, v239\n"
"v_mfma_f32_16x16x32_bf16 v[128:131], a[196:199], a[132:135], v[128:131]\n"
"s_add_u32 s47, s45, 131072\n"
"v_exp_f32 v236, v180 clamp\n"
"v_mfma_f32_16x16x32_bf16 v[136:139], a[196:199], a[148:151], v[136:139]\n"
"buffer_store_dwordx4 a[116:119], v244, s[84:87], s47 offen offset:1024 sc1\n"
"v_exp_f32 v237, v181 clamp\n"
"v_mfma_f32_16x16x32_bf16 v[144:147], a[196:199], a[164:167], v[144:147]\n"
"ds_read_b128 a[228:231], v245 offset:25600\n"
"v_cvt_pk_bf16_f32 v218, v236, v237\n"
"v_mfma_f32_16x16x32_bf16 v[152:155], a[196:199], a[180:183], v[152:155]\n"
"v_exp_f32 v238, v182 clamp\n"
"v_mfma_f32_16x16x32_bf16 v[128:131], a[200:203], a[136:139], v[128:131]\n"
"v_exp_f32 v239, v183 clamp\n"
"v_mfma_f32_16x16x32_bf16 v[136:139], a[200:203], a[152:155], v[136:139]\n"
"ds_read_b128 a[232:235], v245 offset:26624\n"
"v_cvt_pk_bf16_f32 v219, v238, v239\n"
"v_mfma_f32_16x16x32_bf16 v[144:147], a[200:203], a[168:171], v[144:147]\n"
"v_exp_f32 v236, v184 clamp\n"
"v_mfma_f32_16x16x32_bf16 v[152:155], a[200:203], a[184:187], v[152:155]\n"
"buffer_load_dwordx4 v246, s[40:43], s56 offen offset:0 sc1 lds\n"
"v_mfma_f32_16x16x32_bf16 v[128:131], a[204:207], a[140:143], v[128:131]\n"
"v_exp_f32 v237, v185 clamp\n"
"v_mfma_f32_16x16x32_bf16 v[136:139], a[204:207], a[156:159], v[136:139]\n"
"ds_read_b128 a[236:239], v245 offset:27648\n"
"v_cvt_pk_bf16_f32 v220, v236, v237\n"
"v_mfma_f32_16x16x32_bf16 v[144:147], a[204:207], a[172:175], v[144:147]\n"
"v_exp_f32 v238, v186 clamp\n"
"v_mfma_f32_16x16x32_bf16 v[152:155], a[204:207], a[188:191], v[152:155]\n"
"s_add_u32 s47, s45, 131072\n"
"v_exp_f32 v239, v187 clamp\n"
"v_mfma_f32_16x16x32_bf16 v[132:135], a[208:211], a[128:131], v[192:195]\n"
"buffer_store_dwordx4 a[120:123], v244, s[84:87], s47 offen offset:2048 sc1\n"
"ds_read_b128 a[240:243], v245 offset:28672\n"
"v_cvt_pk_bf16_f32 v221, v238, v239\n"
"v_mfma_f32_16x16x32_bf16 v[140:143], a[208:211], a[144:147], v[196:199]\n"
"v_exp_f32 v236, v188 clamp\n"
"v_mfma_f32_16x16x32_bf16 v[148:151], a[208:211], a[160:163], v[200:203]\n"
"v_exp_f32 v237, v189 clamp\n"
"v_mfma_f32_16x16x32_bf16 v[156:159], a[208:211], a[176:179], v[204:207]\n"
"ds_read_b128 a[244:247], v245 offset:29696\n"
"v_cvt_pk_bf16_f32 v222, v236, v237\n"
"v_mfma_f32_16x16x32_bf16 v[132:135], a[212:215], a[132:135], v[132:135]\n"
"v_exp_f32 v238, v190 clamp\n"
"v_mfma_f32_16x16x32_bf16 v[140:143], a[212:215], a[148:151], v[140:143]\n"
"buffer_load_dwordx4 v246, s[40:43], s56 offen offset:1024 sc1 lds\n"
"v_exp_f32 v239, v191 clamp\n"
"v_mfma_f32_16x16x32_bf16 v[148:151], a[212:215], a[164:167], v[148:151]\n"
"ds_read_b128 a[248:251], v245 offset:30720\n"
"v_cvt_pk_bf16_f32 v223, v238, v239\n"
"v_mfma_f32_16x16x32_bf16 v[156:159], a[212:215], a[180:183], v[156:159]\n"
"s_nop 0\n"
"v_smfmac_f32_16x16x64_bf16 v[228:231], v[240:243], v[216:223], v247\n"
"ds_read_b128 a[252:255], v245 offset:31744\n"
"v_mfma_f32_16x16x32_bf16 v[132:135], a[216:219], a[136:139], v[132:135]\n"
"v_max3_f32 v236, v224, v225, v226\n"
"v_mfma_f32_16x16x32_bf16 v[140:143], a[216:219], a[152:155], v[140:143]\n"
"buffer_load_dwordx4 v246, s[40:43], s56 offen offset:2048 sc1 lds\n"
"v_mfma_f32_16x16x32_bf16 v[148:151], a[216:219], a[168:171], v[148:151]\n"
"v_mfma_f32_16x16x32_bf16 v[156:159], a[216:219], a[184:187], v[156:159]\n"
"s_nop 0\n"
"v_max3_f32 v236, v236, v227, v228\n"
"v_max3_f32 v236, v236, v229, v230\n"
"v_mfma_f32_16x16x32_bf16 v[132:135], a[220:223], a[140:143], v[132:135]\n"
"s_add_u32 s47, s45, 131072\n"
"v_max_f32 v236, v236, v231\n"
"v_cmp_nge_f32 vcc, s58, v236\n"
"v_mfma_f32_16x16x32_bf16 v[140:143], a[220:223], a[156:159], v[140:143]\n"
"buffer_store_dwordx4 a[124:127], v244, s[84:87], s47 offen offset:3072 sc1\n"
"v_mfma_f32_16x16x32_bf16 v[148:151], a[220:223], a[172:175], v[148:151]\n"
"s_waitcnt lgkmcnt(0)\n"
"v_mfma_f32_16x16x32_bf16 v[156:159], a[220:223], a[188:191], v[156:159]\n"
"s_getpc_b64 s[66:67]\n"
"s_cbranch_vccnz .LstubG0_%=\n"
"buffer_load_dwordx4 v246, s[40:43], s56 offen offset:3072 sc1 lds\n"
"s_cmp_eq_u32 s76, 0\n"
"s_cbranch_scc0 .Lback7_%=\n"
"s_waitcnt vmcnt(39)\n"
"v_cmp_ne_u32 vcc, s88, v254\n"
"s_cbranch_vccz .Lback7_%=\n"
"s_mov_b32 s47, 0x8000\n"
".Lslowl7_%=:\n"
"buffer_load_dword v254, v253, s[84:87], s46 offen offset:32 sc1\n"
"s_waitcnt vmcnt(0)\n"
"v_cmp_ne_u32 vcc, s88, v254\n"
"s_cbranch_vccz .Lback7_%=\n"
"s_sleep 2\n"
"s_sub_u32 s47, s47, 1\n"
"s_cbranch_scc0 .Lslowl7_%=\n"
".Lback7_%=:\n"
".Lpp7_%=:\n"
"v_mfma_f32_16x16x32_bf16 v[0:3], a[224:227], v[208:211], v[0:3]\n"
"s_waitcnt vmcnt(26)\n"
"s_sub_u32 s49, s35, s57\n"
"s_add_u32 s48, s49, 0\n"
"v_exp_f32 v236, v128 clamp\n"
"v_mfma_f32_16x16x32_bf16 v[16:19], a[228:231], v[208:211], v[16:19]\n"
"s_barrier\n"
"buffer_load_dwordx4 a[0:3], v246, s[84:87], s48 offen offset:0 sc1\n"
"v_mfma_f32_16x16x32_bf16 v[32:35], a[232:235], v[208:211], v[32:35]\n"
"s_add_u32 s52, s52, 1\n"
"v_exp_f32 v237, v129 clamp\n"
"v_mfma_f32_16x16x32_bf16 v[48:51], a[236:239], v[208:211], v[48:51]\n"
"s_add_u32 m0, s57, 16384\n"
"ds_read_b128 a[192:195], v244 offset:32768\n"
"v_mfma_f32_16x16x32_bf16 v[64:67], a[240:243], v[208:211], v[64:67]\n"
"v_exp_f32 v238, v130 clamp\n"
"v_mfma_f32_16x16x32_bf16 v[80:83], a[244:247], v[208:211], v[80:83]\n"
"s_sub_u32 s55, s55, s29\n"
"v_exp_f32 v239, v131 clamp\n"
"v_mfma_f32_16x16x32_bf16 v[96:99], a[248:251], v[208:211], v[96:99]\n"
"buffer_load_dwordx4 v246, s[36:39], s54 offen offset:0 sc1 lds\n"
"v_mfma_f32_16x16x32_bf16 v[112:115], a[252:255], v[208:211], v[112:115]\n"
"ds_read_b128 a[196:199], v244 offset:33792\n"
"v_cvt_pk_bf16_f32 v208, v236, v237\n"
"v_mfma_f32_16x16x32_bf16 v[4:7], a[224:227], v[212:215], v[4:7]\n"
"ds_read_b128 a[200:203], v244 offset:34816\n"
"v_cvt_pk_bf16_f32 v209, v238, v239\n"
"v_mfma_f32_16x16x32_bf16 v[20:23], a[228:231], v[212:215], v[20:23]\n"
"v_exp_f32 v236, v132 clamp\n"
"v_mfma_f32_16x16x32_bf16 v[36:39], a[232:235], v[212:215], v[36:39]\n"
"buffer_load_dwordx4 a[4:7], v246, s[84:87], s48 offen offset:1024 sc1\n"
"v_exp_f32 v237, v133 clamp\n"
"v_mfma_f32_16x16x32_bf16 v[52:55], a[236:239], v[212:215], v[52:55]\n"
"ds_read_b128 a[204:207], v244 offset:35840\n"
"v_cvt_pk_bf16_f32 v210, v236, v237\n"
"v_mfma_f32_16x16x32_bf16 v[68:71], a[240:243], v[212:215], v[68:71]\n"
"v_exp_f32 v238, v134 clamp\n"
"v_mfma_f32_16x16x32_bf16 v[84:87], a[244:247], v[212:215], v[84:87]\n"
"v_exp_f32 v239, v135 clamp\n"
"v_mfma_f32_16x16x32_bf16 v[100:103], a[248:251], v[212:215], v[100:103]\n"
"buffer_load_dwordx4 v246, s[36:39], s54 offen offset:1024 sc1 lds\n"
"v_mfma_f32_16x16x32_bf16 v[116:119], a[252:255], v[212:215], v[116:119]\n"
"ds_read_b128 a[208:211], v244 offset:36864\n"
"v_cvt_pk_bf16_f32 v211, v238, v239\n"
"v_mfma_f32_16x16x32_bf16 v[8:11], a[224:227], v[216:219], v[8:11]\n"
"v_exp_f32 v236, v136 clamp\n"
"v_mfma_f32_16x16x32_bf16 v[24:27], a[228:231], v[216:219], v[24:27]\n"
"buffer_load_dwordx4 a[8:11], v246, s[84:87], s48 offen offset:2048 sc1\n"
"v_exp_f32 v237, v137 clamp\n"
"v_mfma_f32_16x16x32_bf16 v[40:43], a[232:235], v[216:219], v[40:43]\n"
"ds_read_b128 a[212:215], v244 offset:37888\n"
"v_cvt_pk_bf16_f32 v212, v236, v237\n"
"v_mfma_f32_16x16x32_bf16 v[56:59], a[236:239], v[216:219], v[56:59]\n"
"v_exp_f32 v238, v138 clamp\n"
"v_mfma_f32_16x16x32_bf16 v[72:75], a[240:243], v[216:219], v[72:75]\n"
"v_exp_f32 v239, v139 clamp\n"
"v_mfma_f32_16x16x32_bf16 v[88:91], a[244:247], v[216:219], v[88:91]\n"
"buffer_load_dwordx4 v246, s[36:39], s54 offen offset:2048 sc1 lds\n"
"v_mfma_f32_16x16x32_bf16 v[104:107], a[248:251], v[216:219], v[104:107]\n"
"ds_read_b128 a[216:219], v244 offset:38912\n"
"v_cvt_pk_bf16_f32 v213, v238, v239\n"
"v_mfma_f32_16x16x32_bf16 v[120:123], a[252:255], v[216:219], v[120:123]\n"
"v_exp_f32 v236, v140 clamp\n"
"v_mfma_f32_16x16x32_bf16 v[12:15], a[224:227], v[220:223], v[12:15]\n"
"v_exp_f32 v237, v141 clamp\n"
"v_mfma_f32_16x16x32_bf16 v[28:31], a[228:231], v[220:223], v[28:31]\n"
"ds_read_b128 a[220:223], v244 offset:39936\n"
"v_cvt_pk_bf16_f32 v214, v236, v237\n"
"v_mfma_f32_16x16x32_bf16 v[44:47], a[232:235], v[220:223], v[44:47]\n"
"v_exp_f32 v238, v142 clamp\n"
"v_mfma_f32_16x16x32_bf16 v[60:63], a[236:239], v[220:223], v[60:63]\n"
"v_exp_f32 v239, v143 clamp\n"
"v_mfma_f32_16x16x32_bf16 v[76:79], a[240:243], v[220:223], v[76:79]\n"
"buffer_load_dwordx4 v246, s[36:39], s54 offen offset:3072 sc1 lds\n"
"v_mfma_f32_16x16x32_bf16 v[92:95], a[244:247], v[220:223], v[92:95]\n"
"s_add_u32 s54, s54, s28\n"
"v_cvt_pk_bf16_f32 v215, v238, v239\n"
"v_mfma_f32_16x16x32_bf16 v[108:111], a[248:251], v[220:223], v[108:111]\n"
"v_exp_f32 v236, v144 clamp\n"
"v_mfma_f32_16x16x32_bf16 v[124:127], a[252:255], v[220:223], v[124:127]\n"
"s_waitcnt lgkmcnt(0)\n"
"v_exp_f32 v237, v145 clamp\n"
"v_mfma_f32_16x16x32_bf16 v[160:163], a[192:195], a[128:131], v[192:195]\n"
"s_cmp_lt_u32 s55, 540672\n"
"v_cvt_pk_bf16_f32 v216, v236, v237\n"
"v_mfma_f32_16x16x32_bf16 v[168:171], a[192:195], a[144:147], v[196:199]\n"
"s_cselect_b32 s56, 1073741824, s55\n"
"v_exp_f32 v238, v146 clamp\n"
"v_smfmac_f32_16x16x64_bf16 v[224:227], v[240:243], v[208:215], v247\n"
"s_add_u32 m0, s57, 81920\n"
"v_mfma_f32_16x16x32_bf16 v[176:179], a[192:195], a[160:163], v[200:203]\n"
"s_cmp_eq_u32 s64, s52\n"
"v_exp_f32 v239, v147 clamp\n"
"v_mfma_f32_16x16x32_bf16 v[184:187], a[192:195], a[176:179], v[204:207]\n"
"s_cselect_b32 s58, -1.0, 0.5\n"
"ds_read_b128 a[224:227], v245 offset:40960\n"
"v_cvt_pk_bf16_f32 v217, v238, v239\n"
"v_mfma_f32_16x16x32_bf16 v[160:163], a[196:199], a[132:135], v[160:163]\n"
"v_exp_f32 v236, v148 clamp\n"
"v_mfma_f32_16x16x32_bf16 v[168:171], a[196:199], a[148:151], v[168:171]\n"
"buffer_load_dwordx4 a[12:15], v246, s[84:87], s48 offen offset:3072 sc1\n"
"v_exp_f32 v237, v149 clamp\n"
"v_mfma_f32_16x16x32_bf16 v[176:179], a[196:199], a[164:167], v[176:179]\n"
"ds_read_b128 a[228:231], v245 offset:41984\n"
"v_cvt_pk_bf16_f32 v218, v236, v237\n"
"v_mfma_f32_16x16x32_bf16 v[184:187], a[196:199], a[180:183], v[184:187]\n"
"v_exp_f32 v238, v150 clamp\n"
"v_mfma_f32_16x16x32_bf16 v[160:163], a[200:203], a[136:139], v[160:163]\n"
"v_exp_f32 v239, v151 clamp\n"
"v_mfma_f32_16x16x32_bf16 v[168:171], a[200:203], a[152:155], v[168:171]\n"
"ds_read_b128 a[232:235], v245 offset:43008\n"
"v_cvt_pk_bf16_f32 v219, v238, v239\n"
"v_mfma_f32_16x16x32_bf16 v[176:179], a[200:203], a[168:171], v[176:179]\n"
"v_exp_f32 v236, v152 clamp\n"
"v_mfma_f32_16x16x32_bf16 v[184:187], a[200:203], a[184:187], v[184:187]\n"
"buffer_load_dwordx4 v246, s[40:43], s56 offen offset:0 sc1 lds\n"
"v_mfma_f32_16x16x32_bf16 v[160:163], a[204:207], a[140:143], v[160:163]\n"
"v_exp_f32 v237, v153 clamp\n"
"v_mfma_f32_16x16x32_bf16 v[168:171], a[204:207], a[156:159], v[168:171]\n"
"ds_read_b128 a[236:239], v245 offset:44032\n"
"v_cvt_pk_bf16_f32 v220, v236, v237\n"
"v_mfma_f32_16x16x32_bf16 v[176:179], a[204:207], a[172:175], v[176:179]\n"
"v_exp_f32 v238, v154 clamp\n"
"v_mfma_f32_16x16x32_bf16 v[184:187], a[204:207], a[188:191], v[184:187]\n"
"s_add_u32 s48, s48, 4096\n"
"v_exp_f32 v239, v155 clamp\n"
"v_mfma_f32_16x16x32_bf16 v[164:167], a[208:211], a[128:131], v[192:195]\n"
"buffer_load_dwordx4 a[16:19], v246, s[84:87], s48 offen offset:0 sc1\n"
"ds_read_b128 a[240:243], v245 offset:45056\n"
"v_cvt_pk_bf16_f32 v221, v238, v239\n"
"v_mfma_f32_16x16x32_bf16 v[172:175], a[208:211], a[144:147], v[196:199]\n"
"v_exp_f32 v236, v156 clamp\n"
"v_mfma_f32_16x16x32_bf16 v[180:183], a[208:211], a[160:163], v[200:203]\n"
"v_exp_f32 v237, v157 clamp\n"
"v_mfma_f32_16x16x32_bf16 v[188:191], a[208:211], a[176:179], v[204:207]\n"
"ds_read_b128 a[244:247], v245 offset:46080\n"
"v_cvt_pk_bf16_f32 v222, v236, v237\n"
"v_mfma_f32_16x16x32_bf16 v[164:167], a[212:215], a[132:135], v[164:167]\n"
"v_exp_f32 v238, v158 clamp\n"
"v_mfma_f32_16x16x32_bf16 v[172:175], a[212:215], a[148:151], v[172:175]\n"
"buffer_load_dwordx4 v246, s[40:43], s56 offen offset:1024 sc1 lds\n"
"v_exp_f32 v239, v159 clamp\n"
"v_mfma_f32_16x16x32_bf16 v[180:183], a[212:215], a[164:167], v[180:183]\n"
"ds_read_b128 a[248:251], v245 offset:47104\n"
"v_cvt_pk_bf16_f32 v223, v238, v239\n"
"v_mfma_f32_16x16x32_bf16 v[188:191], a[212:215], a[180:183], v[188:191]\n"
"s_nop 0\n"
"v_smfmac_f32_16x16x64_bf16 v[228:231], v[240:243], v[216:223], v247\n"
"s_add_u32 s48, s49, 5120\n"
"ds_read_b128 a[252:255], v245 offset:48128\n"
"v_mfma_f32_16x16x32_bf16 v[164:167], a[216:219], a[136:139], v[164:167]\n"
"buffer_load_dwordx4 a[20:23], v246, s[84:87], s48 offen offset:0 sc1\n"
"v_max3_f32 v236, v224, v225, v226\n"
"v_mfma_f32_16x16x32_bf16 v[172:175], a[216:219], a[152:155], v[172:175]\n"
"buffer_load_dwordx4 v246, s[40:43], s56 offen offset:2048 sc1 lds\n"
"v_mfma_f32_16x16x32_bf16 v[180:183], a[216:219], a[168:171], v[180:183]\n"
"v_mfma_f32_16x16x32_bf16 v[188:191], a[216:219], a[184:187], v[188:191]\n"
"v_max3_f32 v236, v236, v227, v228\n"
"v_max3_f32 v236, v236, v229, v230\n"
"v_mfma_f32_16x16x32_bf16 v[164:167], a[220:223], a[140:143], v[164:167]\n"
"v_max_f32 v236, v236, v231\n"
"v_cmp_nge_f32 vcc, s58, v236\n"
"v_mfma_f32_16x16x32_bf16 v[172:175], a[220:223], a[156:159], v[172:175]\n"
"v_mfma_f32_16x16x32_bf16 v[180:183], a[220:223], a[172:175], v[180:183]\n"
"s_waitcnt lgkmcnt(0)\n"
"v_mfma_f32_16x16x32_bf16 v[188:191], a[220:223], a[188:191], v[188:191]\n"
"s_getpc_b64 s[66:67]\n"
"s_cbranch_vccnz .LstubG1_%=\n"
"buffer_load_dwordx4 v246, s[40:43], s56 offen offset:3072 sc1 lds\n"
".Lpp8_%=:\n"
"v_mfma_f32_16x16x32_bf16 v[0:3], a[224:227], v[208:211], v[0:3]\n"
"s_waitcnt vmcnt(15)\n"
"v_exp_f32 v236, v160 clamp\n"
"v_mfma_f32_16x16x32_bf16 v[16:19], a[228:231], v[208:211], v[16:19]\n"
"s_barrier\n"
"s_lshl_b32 s47, s78, 2\n"
"s_add_u32 s47, s47, s46\n"
"buffer_load_dwordx4 a[24:27], v246, s[84:87], s48 offen offset:1024 sc1\n"
"v_mfma_f32_16x16x32_bf16 v[32:35], a[232:235], v[208:211], v[32:35]\n"
"s_add_u32 s52, s52, 1\n"
"v_exp_f32 v237, v161 clamp\n"
"v_mfma_f32_16x16x32_bf16 v[48:51], a[236:239], v[208:211], v[48:51]\n"
"s_add_u32 m0, s57, 32768\n"
"v_lshlrev_b32 v253, 17, v246\n"
"v_mov_b32 v254, s88\n"
"buffer_store_dword v254, v253, s[84:87], s47 offen sc1\n"
"ds_read_b128 a[192:195], v244 offset:49152\n"
"v_mfma_f32_16x16x32_bf16 v[64:67], a[240:243], v[208:211], v[64:67]\n"
"v_exp_f32 v238, v162 clamp\n"
"v_mfma_f32_16x16x32_bf16 v[80:83], a[244:247], v[208:211], v[80:83]\n"
"s_sub_u32 s55, s55, s29\n"
"v_exp_f32 v239, v163 clamp\n"
"v_mfma_f32_16x16x32_bf16 v[96:99], a[248:251], v[208:211], v[96:99]\n"
"buffer_load_dwordx4 v246, s[36:39], s54 offen offset:0 sc1 lds\n"
"v_mfma_f32_16x16x32_bf16 v[112:115], a[252:255], v[208:211], v[112:115]\n"
"s_mov_b32 s44, 81829888\n"
"s_cmp_eq_u32 s76, 0\n"
"s_cselect_b32 s44, s44, 1073741824\n"
"ds_read_b128 a[196:199], v244 offset:50176\n"
"v_cvt_pk_bf16_f32 v208, v236, v237\n"
"v_mfma_f32_16x16x32_bf16 v[4:7], a[224:227], v[212:215], v[4:7]\n"
"ds_read_b128 a[200:203], v244 offset:51200\n"
"v_cvt_pk_bf16_f32 v209, v238, v239\n"
"v_mfma_f32_16x16x32_bf16 v[20:23], a[228:231], v[212:215], v[20:23]\n"
"v_exp_f32 v236, v164 clamp\n"
"v_mfma_f32_16x16x32_bf16 v[36:39], a[232:235], v[212:215], v[36:39]\n"
"buffer_load_dwordx4 a[28:31], v246, s[84:87], s48 offen offset:2048 sc1\n"
"v_exp_f32 v237, v165 clamp\n"
"v_mfma_f32_16x16x32_bf16 v[52:55], a[236:239], v[212:215], v[52:55]\n"
"ds_read_b128 a[204:207], v244 offset:52224\n"
"v_cvt_pk_bf16_f32 v210, v236, v237\n"
"v_mfma_f32_16x16x32_bf16 v[68:71], a[240:243], v[212:215], v[68:71]\n"
"v_exp_f32 v238, v166 clamp\n"
"v_mfma_f32_16x16x32_bf16 v[84:87], a[244:247], v[212:215], v[84:87]\n"
"v_exp_f32 v239, v167 clamp\n"
"v_mfma_f32_16x16x32_bf16 v[100:103], a[248:251], v[212:215], v[100:103]\n"
"buffer_load_dwordx4 v246, s[36:39], s54 offen offset:1024 sc1 lds\n"
"v_mfma_f32_16x16x32_bf16 v[116:119], a[252:255], v[212:215], v[116:119]\n"
"ds_read_b128 a[208:211], v244 offset:53248\n"
"v_cvt_pk_bf16_f32 v211, v238, v239\n"
"v_mfma_f32_16x16x32_bf16 v[8:11], a[224:227], v[216:219], v[8:11]\n"
"v_exp_f32 v236, v168 clamp\n"
"v_mfma_f32_16x16x32_bf16 v[24:27], a[228:231], v[216:219], v[24:27]\n"
"buffer_load_dwordx4 a[32:35], v246, s[84:87], s48 offen offset:3072 sc1\n"
"v_exp_f32 v237, v169 clamp\n"
"v_mfma_f32_16x16x32_bf16 v[40:43], a[232:235], v[216:219], v[40:43]\n"
"ds_read_b128 a[212:215], v244 offset:54272\n"
"v_cvt_pk_bf16_f32 v212, v236, v237\n"
"v_mfma_f32_16x16x32_bf16 v[56:59], a[236:239], v[216:219], v[56:59]\n"
"v_exp_f32 v238, v170 clamp\n"
"v_mfma_f32_16x16x32_bf16 v[72:75], a[240:243], v[216:219], v[72:75]\n"
"v_exp_f32 v239, v171 clamp\n"
"v_mfma_f32_16x16x32_bf16 v[88:91], a[244:247], v[216:219], v[88:91]\n"
"buffer_load_dwordx4 v246, s[36:39], s54 offen offset:2048 sc1 lds\n"
"v_mfma_f32_16x16x32_bf16 v[104:107], a[248:251], v[216:219], v[104:107]\n"
"ds_read_b128 a[216:219], v244 offset:55296\n"
"v_cvt_pk_bf16_f32 v213, v238, v239\n"
"v_mfma_f32_16x16x32_bf16 v[120:123], a[252:255], v[216:219], v[120:123]\n"
"v_exp_f32 v236, v172 clamp\n"
"v_mfma_f32_16x16x32_bf16 v[12:15], a[224:227], v[220:223], v[12:15]\n"
"v_exp_f32 v237, v173 clamp\n"
"v_mfma_f32_16x16x32_bf16 v[28:31], a[228:231], v[220:223], v[28:31]\n"
"ds_read_b128 a[220:223], v244 offset:56320\n"
"v_cvt_pk_bf16_f32 v214, v236, v237\n"
"v_mfma_f32_16x16x32_bf16 v[44:47], a[232:235], v[220:223], v[44:47]\n"
"v_exp_f32 v238, v174 clamp\n"
"v_mfma_f32_16x16x32_bf16 v[60:63], a[236:239], v[220:223], v[60:63]\n"
"v_exp_f32 v239, v175 clamp\n"
"v_mfma_f32_16x16x32_bf16 v[76:79], a[240:243], v[220:223], v[76:79]\n"
"buffer_load_dwordx4 v246, s[36:39], s54 offen offset:3072 sc1 lds\n"
"v_mfma_f32_16x16x32_bf16 v[92:95], a[244:247], v[220:223], v[92:95]\n"
"s_add_u32 s54, s54, s28\n"
"v_cvt_pk_bf16_f32 v215, v238, v239\n"
"v_mfma_f32_16x16x32_bf16 v[108:111], a[248:251], v[220:223], v[108:111]\n"
"v_exp_f32 v236, v176 clamp\n"
"v_mfma_f32_16x16x32_bf16 v[124:127], a[252:255], v[220:223], v[124:127]\n"
"s_waitcnt lgkmcnt(0)\n"
"v_exp_f32 v237, v177 clamp\n"
"v_mfma_f32_16x16x32_bf16 v[128:131], a[192:195], a[128:131], v[192:195]\n"
"s_cmp_lt_u32 s55, 540672\n"
"v_cvt_pk_bf16_f32 v216, v236, v237\n"
"v_mfma_f32_16x16x32_bf16 v[136:139], a[192:195], a[144:147], v[196:199]\n"
"s_cselect_b32 s56, 1073741824, s55\n"
"v_exp_f32 v238, v178 clamp\n"
"v_smfmac_f32_16x16x64_bf16 v[224:227], v[240:243], v[208:215], v247\n"
"s_add_u32 m0, s57, 98304\n"
"v_mfma_f32_16x16x32_bf16 v[144:147], a[192:195], a[160:163], v[200:203]\n"
"s_cmp_eq_u32 s64, s52\n"
"v_exp_f32 v239, v179 clamp\n"
"v_mfma_f32_16x16x32_bf16 v[152:155], a[192:195], a[176:179], v[204:207]\n"
"s_cselect_b32 s58, -1.0, 0.5\n"
"ds_read_b128 a[224:227], v245 offset:57344\n"
"v_cvt_pk_bf16_f32 v217, v238, v239\n"
"v_mfma_f32_16x16x32_bf16 v[128:131], a[196:199], a[132:135], v[128:131]\n"
"s_add_u32 s48, s48, 4096\n"
"v_exp_f32 v236, v180 clamp\n"
"v_mfma_f32_16x16x32_bf16 v[136:139], a[196:199], a[148:151], v[136:139]\n"
"buffer_load_dwordx4 a[36:39], v246, s[84:87], s48 offen offset:0 sc1\n"
"v_exp_f32 v237, v181 clamp\n"
"v_mfma_f32_16x16x32_bf16 v[144:147], a[196:199], a[164:167], v[144:147]\n"
"ds_read_b128 a[228:231], v245 offset:58368\n"
"v_cvt_pk_bf16_f32 v218, v236, v237\n"
"v_mfma_f32_16x16x32_bf16 v[152:155], a[196:199], a[180:183], v[152:155]\n"
"v_exp_f32 v238, v182 clamp\n"
"v_mfma_f32_16x16x32_bf16 v[128:131], a[200:203], a[136:139], v[128:131]\n"
"v_exp_f32 v239, v183 clamp\n"
"v_mfma_f32_16x16x32_bf16 v[136:139], a[200:203], a[152:155], v[136:139]\n"
"ds_read_b128 a[232:235], v245 offset:59392\n"
"v_cvt_pk_bf16_f32 v219, v238, v239\n"
"v_mfma_f32_16x16x32_bf16 v[144:147], a[200:203], a[168:171], v[144:147]\n"
"v_exp_f32 v236, v184 clamp\n"
"v_mfma_f32_16x16x32_bf16 v[152:155], a[200:203], a[184:187], v[152:155]\n"
"buffer_load_dwordx4 v246, s[40:43], s56 offen offset:0 sc1 lds\n"
"v_mfma_f32_16x16x32_bf16 v[128:131], a[204:207], a[140:143], v[128:131]\n"
"v_exp_f32 v237, v185 clamp\n"
"v_mfma_f32_16x16x32_bf16 v[136:139], a[204:207], a[156:159], v[136:139]\n"
"ds_read_b128 a[236:239], v245 offset:60416\n"
"v_cvt_pk_bf16_f32 v220, v236, v237\n"
"v_mfma_f32_16x16x32_bf16 v[144:147], a[204:207], a[172:175], v[144:147]\n"
"v_exp_f32 v238, v186 clamp\n"
"v_mfma_f32_16x16x32_bf16 v[152:155], a[204:207], a[188:191], v[152:155]\n"
"s_add_u32 s48, s49, 10240\n"
"v_exp_f32 v239, v187 clamp\n"
"v_mfma_f32_16x16x32_bf16 v[132:135], a[208:211], a[128:131], v[192:195]\n"
".Lpad14_%=:\n"
"s_cbranch_execz .Lpadmid3_%=\n"
"buffer_load_dwordx4 a[40:43], v246, s[84:87], s48 offen offset:0 sc1\n"
"ds_read_b128 a[240:243], v245 offset:61440\n"
"v_cvt_pk_bf16_f32 v221, v238, v239\n"
"v_mfma_f32_16x16x32_bf16 v[140:143], a[208:211], a[144:147], v[196:199]\n"
"v_exp_f32 v236, v188 clamp\n"
"v_mfma_f32_16x16x32_bf16 v[148:151], a[208:211], a[160:163], v[200:203]\n"
"v_exp_f32 v237, v189 clamp\n"
"v_mfma_f32_16x16x32_bf16 v[156:159], a[208:211], a[176:179], v[204:207]\n"
"ds_read_b128 a[244:247], v245 offset:62464\n"
"v_cvt_pk_bf16_f32 v222, v236, v237\n"
"v_mfma_f32_16x16x32_bf16 v[132:135], a[212:215], a[132:135], v[132:135]\n"
"v_exp_f32 v238, v190 clamp\n"
"v_mfma_f32_16x16x32_bf16 v[140:143], a[212:215], a[148:151], v[140:143]\n"
"buffer_load_dwordx4 v246, s[40:43], s56 offen offset:1024 sc1 lds\n"
"v_exp_f32 v239, v191 clamp\n"
"v_mfma_f32_16x16x32_bf16 v[148:151], a[212:215], a[164:167], v[148:151]\n"
"ds_read_b128 a[248:251], v245 offset:63488\n"
"v_cvt_pk_bf16_f32 v223, v238, v239\n"
"v_mfma_f32_16x16x32_bf16 v[156:159], a[212:215], a[180:183], v[156:159]\n"
"s_nop 0\n"
"v_smfmac_f32_16x16x64_bf16 v[228:231], v[240:243], v[216:223], v247\n"
"ds_read_b128 a[252:255], v245 offset:64512\n"
"v_mfma_f32_16x16x32_bf16 v[132:135], a[216:219], a[136:139], v[132:135]\n"
"buffer_load_dwordx4 a[44:47], v246, s[84:87], s48 offen offset:1024 sc1\n"
"v_max3_f32 v236, v224, v225, v226\n"
"v_mfma_f32_16x16x32_bf16 v[140:143], a[216:219], a[152:155], v[140:143]\n"
"buffer_load_dwordx4 v246, s[40:43], s56 offen offset:2048 sc1 lds\n"
"v_mfma_f32_16x16x32_bf16 v[148:151], a[216:219], a[168:171], v[148:151]\n"
"v_mfma_f32_16x16x32_bf16 v[156:159], a[216:219], a[184:187], v[156:159]\n"
"v_max3_f32 v236, v236, v227, v228\n"
"v_max3_f32 v236, v236, v229, v230\n"
"v_mfma_f32_16x16x32_bf16 v[132:135], a[220:223], a[140:143], v[132:135]\n"
"v_max_f32 v236, v236, v231\n"
"v_cmp_nge_f32 vcc, s58, v236\n"
"v_mfma_f32_16x16x32_bf16 v[140:143], a[220:223], a[156:159], v[140:143]\n"
"v_mfma_f32_16x16x32_bf16 v[148:151], a[220:223], a[172:175], v[148:151]\n"
"s_waitcnt lgkmcnt(0)\n"
"v_mfma_f32_16x16x32_bf16 v[156:159], a[220:223], a[188:191], v[156:159]\n"
"s_getpc_b64 s[66:67]\n"
"s_cbranch_vccnz .LstubG0_%=\n"
"buffer_load_dwordx4 v246, s[40:43], s56 offen offset:3072 sc1 lds\n"
".Lpp9_%=:\n"
"v_mfma_f32_16x16x32_bf16 v[0:3], a[224:227], v[208:211], v[0:3]\n"
"s_waitcnt vmcnt(29)\n"
"v_exp_f32 v236, v128 clamp\n"
"v_mfma_f32_16x16x32_bf16 v[16:19], a[228:231], v[208:211], v[16:19]\n"
"s_barrier\n"
"buffer_load_dwordx4 a[48:51], v246, s[84:87], s48 offen offset:2048 sc1\n"
"v_mfma_f32_16x16x32_bf16 v[32:35], a[232:235], v[208:211], v[32:35]\n"
"s_add_u32 s52, s52, 1\n"
"v_exp_f32 v237, v129 clamp\n"
"v_mfma_f32_16x16x32_bf16 v[48:51], a[236:239], v[208:211], v[48:51]\n"
"s_add_u32 m0, s57, 49152\n"
"ds_read_b128 a[192:195], v244 offset:0\n"
"v_mfma_f32_16x16x32_bf16 v[64:67], a[240:243], v[208:211], v[64:67]\n"
"v_exp_f32 v238, v130 clamp\n"
"v_mfma_f32_16x16x32_bf16 v[80:83], a[244:247], v[208:211], v[80:83]\n"
"s_sub_u32 s55, s55, s29\n"
"v_exp_f32 v239, v131 clamp\n"
"v_mfma_f32_16x16x32_bf16 v[96:99], a[248:251], v[208:211], v[96:99]\n"
"buffer_load_dwordx4 v246, s[36:39], s54 offen offset:0 sc1 lds\n"
"v_mfma_f32_16x16x32_bf16 v[112:115], a[252:255], v[208:211], v[112:115]\n"
"ds_read_b128 a[196:199], v244 offset:1024\n"
"v_cvt_pk_bf16_f32 v208, v236, v237\n"
"v_mfma_f32_16x16x32_bf16 v[4:7], a[224:227], v[212:215], v[4:7]\n"
"ds_read_b128 a[200:203], v244 offset:2048\n"
"v_cvt_pk_bf16_f32 v209, v238, v239\n"
"v_mfma_f32_16x16x32_bf16 v[20:23], a[228:231], v[212:215], v[20:23]\n"
"v_exp_f32 v236, v132 clamp\n"
"v_mfma_f32_16x16x32_bf16 v[36:39], a[232:235], v[212:215], v[36:39]\n"
"buffer_load_dwordx4 a[52:55], v246, s[84:87], s48 offen offset:3072 sc1\n"
"v_exp_f32 v237, v133 clamp\n"
"v_mfma_f32_16x16x32_bf16 v[52:55], a[236:239], v[212:215], v[52:55]\n"
"ds_read_b128 a[204:207], v244 offset:3072\n"
"v_cvt_pk_bf16_f32 v210, v236, v237\n"
"v_mfma_f32_16x16x32_bf16 v[68:71], a[240:243], v[212:215], v[68:71]\n"
"v_exp_f32 v238, v134 clamp\n"
"v_mfma_f32_16x16x32_bf16 v[84:87], a[244:247], v[212:215], v[84:87]\n"
"v_exp_f32 v239, v135 clamp\n"
"v_mfma_f32_16x16x32_bf16 v[100:103], a[248:251], v[212:215], v[100:103]\n"
"buffer_load_dwordx4 v246, s[36:39], s54 offen offset:1024 sc1 lds\n"
"v_mfma_f32_16x16x32_bf16 v[116:119], a[252:255], v[212:215], v[116:119]\n"
"ds_read_b128 a[208:211], v244 offset:4096\n"
"v_cvt_pk_bf16_f32 v211, v238, v239\n"
"v_mfma_f32_16x16x32_bf16 v[8:11], a[224:227], v[216:219], v[8:11]\n"
"s_add_u32 s48, s48, 4096\n"
"v_exp_f32 v236, v136 clamp\n"
"v_mfma_f32_16x16x32_bf16 v[24:27], a[228:231], v[216:219], v[24:27]\n"
"buffer_load_dwordx4 a[56:59], v246, s[84:87], s48 offen offset:0 sc1\n"
"v_exp_f32 v237, v137 clamp\n"
"v_mfma_f32_16x16x32_bf16 v[40:43], a[232:235], v[216:219], v[40:43]\n"
"ds_read_b128 a[212:215], v244 offset:5120\n"
"v_cvt_pk_bf16_f32 v212, v236, v237\n"
"v_mfma_f32_16x16x32_bf16 v[56:59], a[236:239], v[216:219], v[56:59]\n"
"v_exp_f32 v238, v138 clamp\n"
"v_mfma_f32_16x16x32_bf16 v[72:75], a[240:243], v[216:219], v[72:75]\n"
"v_exp_f32 v239, v139 clamp\n"
"v_mfma_f32_16x16x32_bf16 v[88:91], a[244:247], v[216:219], v[88:91]\n"
"buffer_load_dwordx4 v246, s[36:39], s54 offen offset:2048 sc1 lds\n"
"v_mfma_f32_16x16x32_bf16 v[104:107], a[248:251], v[216:219], v[104:107]\n"
"ds_read_b128 a[216:219], v244 offset:6144\n"
"v_cvt_pk_bf16_f32 v213, v238, v239\n"
"v_mfma_f32_16x16x32_bf16 v[120:123], a[252:255], v[216:219], v[120:123]\n"
"v_exp_f32 v236, v140 clamp\n"
"v_mfma_f32_16x16x32_bf16 v[12:15], a[224:227], v[220:223], v[12:15]\n"
"v_exp_f32 v237, v141 clamp\n"
"v_mfma_f32_16x16x32_bf16 v[28:31], a[228:231], v[220:223], v[28:31]\n"
"ds_read_b128 a[220:223], v244 offset:7168\n"
"v_cvt_pk_bf16_f32 v214, v236, v237\n"
"v_mfma_f32_16x16x32_bf16 v[44:47], a[232:235], v[220:223], v[44:47]\n"
"v_exp_f32 v238, v142 clamp\n"
"v_mfma_f32_16x16x32_bf16 v[60:63], a[236:239], v[220:223], v[60:63]\n"
"v_exp_f32 v239, v143 clamp\n"
"v_mfma_f32_16x16x32_bf16 v[76:79], a[240:243], v[220:223], v[76:79]\n"
"buffer_load_dwordx4 v246, s[36:39], s54 offen offset:3072 sc1 lds\n"
"v_mfma_f32_16x16x32_bf16 v[92:95], a[244:247], v[220:223], v[92:95]\n"
"s_add_u32 s54, s54, s28\n"
"v_cvt_pk_bf16_f32 v215, v238, v239\n"
"v_mfma_f32_16x16x32_bf16 v[108:111], a[248:251], v[220:223], v[108:111]\n"
"v_exp_f32 v236, v144 clamp\n"
"v_mfma_f32_16x16x32_bf16 v[124:127], a[252:255], v[220:223], v[124:127]\n"
"s_waitcnt lgkmcnt(0)\n"
"v_exp_f32 v237, v145 clamp\n"
"v_mfma_f32_16x16x32_bf16 v[160:163], a[192:195], a[128:131], v[192:195]\n"
"s_cmp_lt_u32 s55, 540672\n"
"v_cvt_pk_bf16_f32 v216, v236, v237\n"
"v_mfma_f32_16x16x32_bf16 v[168:171], a[192:195], a[144:147], v[196:199]\n"
"s_cselect_b32 s56, 1073741824, s55\n"
"v_exp_f32 v238, v146 clamp\n"
"v_smfmac_f32_16x16x64_bf16 v[224:227], v[240:243], v[208:215], v247\n"
"s_add_u32 m0, s57, 114688\n"
"v_mfma_f32_16x16x32_bf16 v[176:179], a[192:195], a[160:163], v[200:203]\n"
"s_cmp_eq_u32 s64, s52\n"
"v_exp_f32 v239, v147 clamp\n"
"v_mfma_f32_16x16x32_bf16 v[184:187], a[192:195], a[176:179], v[204:207]\n"
"s_cselect_b32 s58, -1.0, 0.5\n"
"ds_read_b128 a[224:227], v245 offset:8192\n"
"v_cvt_pk_bf16_f32 v217, v238, v239\n"
"v_mfma_f32_16x16x32_bf16 v[160:163], a[196:199], a[132:135], v[160:163]\n"
"s_add_u32 s48, s49, 15360\n"
"v_exp_f32 v236, v148 clamp\n"
"v_mfma_f32_16x16x32_bf16 v[168:171], a[196:199], a[148:151], v[168:171]\n"
"buffer_load_dwordx4 a[60:63], v246, s[84:87], s48 offen offset:0 sc1\n"
"v_exp_f32 v237, v149 clamp\n"
"v_mfma_f32_16x16x32_bf16 v[176:179], a[196:199], a[164:167], v[176:179]\n"
"ds_read_b128 a[228:231], v245 offset:9216\n"
"v_cvt_pk_bf16_f32 v218, v236, v237\n"
"v_mfma_f32_16x16x32_bf16 v[184:187], a[196:199], a[180:183], v[184:187]\n"
"v_exp_f32 v238, v150 clamp\n"
"v_mfma_f32_16x16x32_bf16 v[160:163], a[200:203], a[136:139], v[160:163]\n"
"v_exp_f32 v239, v151 clamp\n"
"v_mfma_f32_16x16x32_bf16 v[168:171], a[200:203], a[152:155], v[168:171]\n"
"ds_read_b128 a[232:235], v245 offset:10240\n"
"v_cvt_pk_bf16_f32 v219, v238, v239\n"
"v_mfma_f32_16x16x32_bf16 v[176:179], a[200:203], a[168:171], v[176:179]\n"
"v_exp_f32 v236, v152 clamp\n"
"v_mfma_f32_16x16x32_bf16 v[184:187], a[200:203], a[184:187], v[184:187]\n"
"buffer_load_dwordx4 v246, s[40:43], s56 offen offset:0 sc1 lds\n"
"v_mfma_f32_16x16x32_bf16 v[160:163], a[204:207], a[140:143], v[160:163]\n"
"v_exp_f32 v237, v153 clamp\n"
"v_mfma_f32_16x16x32_bf16 v[168:171], a[204:207], a[156:159], v[168:171]\n"
"ds_read_b128 a[236:239], v245 offset:11264\n"
"v_cvt_pk_bf16_f32 v220, v236, v237\n"
"v_mfma_f32_16x16x32_bf16 v[176:179], a[204:207], a[172:175], v[176:179]\n"
"v_exp_f32 v238, v154 clamp\n"
"v_mfma_f32_16x16x32_bf16 v[184:187], a[204:207], a[188:191], v[184:187]\n"
"v_exp_f32 v239, v155 clamp\n"
"v_mfma_f32_16x16x32_bf16 v[164:167], a[208:211], a[128:131], v[192:195]\n"
"buffer_load_dwordx4 a[64:67], v246, s[84:87], s48 offen offset:1024 sc1\n"
"ds_read_b128 a[240:243], v245 offset:12288\n"
"v_cvt_pk_bf16_f32 v221, v238, v239\n"
"v_mfma_f32_16x16x32_bf16 v[172:175], a[208:211], a[144:147], v[196:199]\n"
"v_exp_f32 v236, v156 clamp\n"
"v_mfma_f32_16x16x32_bf16 v[180:183], a[208:211], a[160:163], v[200:203]\n"
"v_exp_f32 v237, v157 clamp\n"
"v_mfma_f32_16x16x32_bf16 v[188:191], a[208:211], a[176:179], v[204:207]\n"
"ds_read_b128 a[244:247], v245 offset:13312\n"
"v_cvt_pk_bf16_f32 v222, v236, v237\n"
"v_mfma_f32_16x16x32_bf16 v[164:167], a[212:215], a[132:135], v[164:167]\n"
"v_exp_f32 v238, v158 clamp\n"
"v_mfma_f32_16x16x32_bf16 v[172:175], a[212:215], a[148:151], v[172:175]\n"
"buffer_load_dwordx4 v246, s[40:43], s56 offen offset:1024 sc1 lds\n"
"v_exp_f32 v239, v159 clamp\n"
"v_mfma_f32_16x16x32_bf16 v[180:183], a[212:215], a[164:167], v[180:183]\n"
"ds_read_b128 a[248:251], v245 offset:14336\n"
"v_cvt_pk_bf16_f32 v223, v238, v239\n"
"v_mfma_f32_16x16x32_bf16 v[188:191], a[212:215], a[180:183], v[188:191]\n"
"s_nop 0\n"
"v_smfmac_f32_16x16x64_bf16 v[228:231], v[240:243], v[216:223], v247\n"
"ds_read_b128 a[252:255], v245 offset:15360\n"
"v_mfma_f32_16x16x32_bf16 v[164:167], a[216:219], a[136:139], v[164:167]\n"
"buffer_load_dwordx4 a[68:71], v246, s[84:87], s48 offen offset:2048 sc1\n"
"v_max3_f32 v236, v224, v225, v226\n"
"v_mfma_f32_16x16x32_bf16 v[172:175], a[216:219], a[152:155], v[172:175]\n"
"buffer_load_dwordx4 v246, s[40:43], s56 offen offset:2048 sc1 lds\n"
"v_mfma_f32_16x16x32_bf16 v[180:183], a[216:219], a[168:171], v[180:183]\n"
"v_mfma_f32_16x16x32_bf16 v[188:191], a[216:219], a[184:187], v[188:191]\n"
"v_max3_f32 v236, v236, v227, v228\n"
"v_max3_f32 v236, v236, v229, v230\n"
"v_mfma_f32_16x16x32_bf16 v[164:167], a[220:223], a[140:143], v[164:167]\n"
"v_max_f32 v236, v236, v231\n"
"v_cmp_nge_f32 vcc, s58, v236\n"
"v_mfma_f32_16x16x32_bf16 v[172:175], a[220:223], a[156:159], v[172:175]\n"
"v_mfma_f32_16x16x32_bf16 v[180:183], a[220:223], a[172:175], v[180:183]\n"
"s_waitcnt lgkmcnt(0)\n"
"v_mfma_f32_16x16x32_bf16 v[188:191], a[220:223], a[188:191], v[188:191]\n"
"s_getpc_b64 s[66:67]\n"
"s_cbranch_vccnz .LstubG1_%=\n"
"buffer_load_dwordx4 v246, s[40:43], s56 offen offset:3072 sc1 lds\n"
".Lpp10_%=:\n"
"v_mfma_f32_16x16x32_bf16 v[0:3], a[224:227], v[208:211], v[0:3]\n"
"s_waitcnt vmcnt(29)\n"
"v_exp_f32 v236, v160 clamp\n"
"v_mfma_f32_16x16x32_bf16 v[16:19], a[228:231], v[208:211], v[16:19]\n"
"s_barrier\n"
"buffer_load_dwordx4 a[72:75], v246, s[84:87], s48 offen offset:3072 sc1\n"
"v_mfma_f32_16x16x32_bf16 v[32:35], a[232:235], v[208:211], v[32:35]\n"
"s_add_u32 s52, s52, 1\n"
"v_exp_f32 v237, v161 clamp\n"
"v_mfma_f32_16x16x32_bf16 v[48:51], a[236:239], v[208:211], v[48:51]\n"
"s_add_u32 m0, s57, 0\n"
"ds_read_b128 a[192:195], v244 offset:16384\n"
"v_mfma_f32_16x16x32_bf16 v[64:67], a[240:243], v[208:211], v[64:67]\n"
"v_exp_f32 v238, v162 clamp\n"
"v_mfma_f32_16x16x32_bf16 v[80:83], a[244:247], v[208:211], v[80:83]\n"
"s_sub_u32 s55, s55, s29\n"
"v_exp_f32 v239, v163 clamp\n"
"v_mfma_f32_16x16x32_bf16 v[96:99], a[248:251], v[208:211], v[96:99]\n"
"buffer_load_dwordx4 v246, s[36:39], s54 offen offset:0 sc1 lds\n"
"v_mfma_f32_16x16x32_bf16 v[112:115], a[252:255], v[208:211], v[112:115]\n"
"ds_read_b128 a[196:199], v244 offset:17408\n"
"v_cvt_pk_bf16_f32 v208, v236, v237\n"
"v_mfma_f32_16x16x32_bf16 v[4:7], a[224:227], v[212:215], v[4:7]\n"
"ds_read_b128 a[200:203], v244 offset:18432\n"
"v_cvt_pk_bf16_f32 v209, v238, v239\n"
"v_mfma_f32_16x16x32_bf16 v[20:23], a[228:231], v[212:215], v[20:23]\n"
"s_add_u32 s48, s48, 4096\n"
"v_exp_f32 v236, v164 clamp\n"
"v_mfma_f32_16x16x32_bf16 v[36:39], a[232:235], v[212:215], v[36:39]\n"
"buffer_load_dwordx4 a[76:79], v246, s[84:87], s48 offen offset:0 sc1\n"
"v_exp_f32 v237, v165 clamp\n"
"v_mfma_f32_16x16x32_bf16 v[52:55], a[236:239], v[212:215], v[52:55]\n"
"ds_read_b128 a[204:207], v244 offset:19456\n"
"v_cvt_pk_bf16_f32 v210, v236, v237\n"
"v_mfma_f32_16x16x32_bf16 v[68:71], a[240:243], v[212:215], v[68:71]\n"
"v_exp_f32 v238, v166 clamp\n"
"v_mfma_f32_16x16x32_bf16 v[84:87], a[244:247], v[212:215], v[84:87]\n"
"v_exp_f32 v239, v167 clamp\n"
"v_mfma_f32_16x16x32_bf16 v[100:103], a[248:251], v[212:215], v[100:103]\n"
"buffer_load_dwordx4 v246, s[36:39], s54 offen offset:1024 sc1 lds\n"
"v_mfma_f32_16x16x32_bf16 v[116:119], a[252:255], v[212:215], v[116:119]\n"
"ds_read_b128 a[208:211], v244 offset:20480\n"
"v_cvt_pk_bf16_f32 v211, v238, v239\n"
"v_mfma_f32_16x16x32_bf16 v[8:11], a[224:227], v[216:219], v[8:11]\n"
"s_add_u32 s48, s49, s34\n"
"v_exp_f32 v236, v168 clamp\n"
"v_mfma_f32_16x16x32_bf16 v[24:27], a[228:231], v[216:219], v[24:27]\n"
"buffer_load_dwordx4 a[80:83], v246, s[84:87], s48 offen offset:0 sc1\n"
"v_exp_f32 v237, v169 clamp\n"
"v_mfma_f32_16x16x32_bf16 v[40:43], a[232:235], v[216:219], v[40:43]\n"
"ds_read_b128 a[212:215], v244 offset:21504\n"
"v_cvt_pk_bf16_f32 v212, v236, v237\n"
"v_mfma_f32_16x16x32_bf16 v[56:59], a[236:239], v[216:219], v[56:59]\n"
"v_exp_f32 v238, v170 clamp\n"
"v_mfma_f32_16x16x32_bf16 v[72:75], a[240:243], v[216:219], v[72:75]\n"
"v_exp_f32 v239, v171 clamp\n"
"v_mfma_f32_16x16x32_bf16 v[88:91], a[244:247], v[216:219], v[88:91]\n"
"buffer_load_dwordx4 v246, s[36:39], s54 offen offset:2048 sc1 lds\n"
"v_mfma_f32_16x16x32_bf16 v[104:107], a[248:251], v[216:219], v[104:107]\n"
"ds_read_b128 a[216:219], v244 offset:22528\n"
"v_cvt_pk_bf16_f32 v213, v238, v239\n"
"v_mfma_f32_16x16x32_bf16 v[120:123], a[252:255], v[216:219], v[120:123]\n"
"v_exp_f32 v236, v172 clamp\n"
"v_mfma_f32_16x16x32_bf16 v[12:15], a[224:227], v[220:223], v[12:15]\n"
"v_exp_f32 v237, v173 clamp\n"
"v_mfma_f32_16x16x32_bf16 v[28:31], a[228:231], v[220:223], v[28:31]\n"
"ds_read_b128 a[220:223], v244 offset:23552\n"
"v_cvt_pk_bf16_f32 v214, v236, v237\n"
"v_mfma_f32_16x16x32_bf16 v[44:47], a[232:235], v[220:223], v[44:47]\n"
"v_exp_f32 v238, v174 clamp\n"
"v_mfma_f32_16x16x32_bf16 v[60:63], a[236:239], v[220:223], v[60:63]\n"
"v_exp_f32 v239, v175 clamp\n"
"v_mfma_f32_16x16x32_bf16 v[76:79], a[240:243], v[220:223], v[76:79]\n"
"buffer_load_dwordx4 v246, s[36:39], s54 offen offset:3072 sc1 lds\n"
"v_mfma_f32_16x16x32_bf16 v[92:95], a[244:247], v[220:223], v[92:95]\n"
"s_add_u32 s54, s54, s28\n"
"v_cvt_pk_bf16_f32 v215, v238, v239\n"
"v_mfma_f32_16x16x32_bf16 v[108:111], a[248:251], v[220:223], v[108:111]\n"
"v_exp_f32 v236, v176 clamp\n"
"v_mfma_f32_16x16x32_bf16 v[124:127], a[252:255], v[220:223], v[124:127]\n"
"s_waitcnt lgkmcnt(0)\n"
"v_exp_f32 v237, v177 clamp\n"
"v_mfma_f32_16x16x32_bf16 v[128:131], a[192:195], a[128:131], v[192:195]\n"
"s_cmp_lt_u32 s55, 540672\n"
"v_cvt_pk_bf16_f32 v216, v236, v237\n"
"v_mfma_f32_16x16x32_bf16 v[136:139], a[192:195], a[144:147], v[196:199]\n"
"s_cselect_b32 s56, 1073741824, s55\n"
"v_exp_f32 v238, v178 clamp\n"
"v_smfmac_f32_16x16x64_bf16 v[224:227], v[240:243], v[208:215], v247\n"
"s_add_u32 m0, s57, 65536\n"
"v_mfma_f32_16x16x32_bf16 v[144:147], a[192:195], a[160:163], v[200:203]\n"
"s_cmp_eq_u32 s64, s52\n"
"v_exp_f32 v239, v179 clamp\n"
"v_mfma_f32_16x16x32_bf16 v[152:155], a[192:195], a[176:179], v[204:207]\n"
"s_cselect_b32 s58, -1.0, 0.5\n"
"ds_read_b128 a[224:227], v245 offset:24576\n"
"v_cvt_pk_bf16_f32 v217, v238, v239\n"
"v_mfma_f32_16x16x32_bf16 v[128:131], a[196:199], a[132:135], v[128:131]\n"
"v_exp_f32 v236, v180 clamp\n"
"v_mfma_f32_16x16x32_bf16 v[136:139], a[196:199], a[148:151], v[136:139]\n"
"buffer_load_dwordx4 a[84:87], v246, s[84:87], s48 offen offset:1024 sc1\n"
"v_exp_f32 v237, v181 clamp\n"
"v_mfma_f32_16x16x32_bf16 v[144:147], a[196:199], a[164:167], v[144:147]\n"
"ds_read_b128 a[228:231], v245 offset:25600\n"
"v_cvt_pk_bf16_f32 v218, v236, v237\n"
"v_mfma_f32_16x16x32_bf16 v[152:155], a[196:199], a[180:183], v[152:155]\n"
"v_exp_f32 v238, v182 clamp\n"
"v_mfma_f32_16x16x32_bf16 v[128:131], a[200:203], a[136:139], v[128:131]\n"
"v_exp_f32 v239, v183 clamp\n"
"v_mfma_f32_16x16x32_bf16 v[136:139], a[200:203], a[152:155], v[136:139]\n"
"ds_read_b128 a[232:235], v245 offset:26624\n"
"v_cvt_pk_bf16_f32 v219, v238, v239\n"
"v_mfma_f32_16x16x32_bf16 v[144:147], a[200:203], a[168:171], v[144:147]\n"
"v_exp_f32 v236, v184 clamp\n"
"v_mfma_f32_16x16x32_bf16 v[152:155], a[200:203], a[184:187], v[152:155]\n"
"buffer_load_dwordx4 v246, s[40:43], s56 offen offset:0 sc1 lds\n"
"v_mfma_f32_16x16x32_bf16 v[128:131], a[204:207], a[140:143], v[128:131]\n"
"v_exp_f32 v237, v185 clamp\n"
"v_mfma_f32_16x16x32_bf16 v[136:139], a[204:207], a[156:159], v[136:139]\n"
"ds_read_b128 a[236:239], v245 offset:27648\n"
"v_cvt_pk_bf16_f32 v220, v236, v237\n"
"v_mfma_f32_16x16x32_bf16 v[144:147], a[204:207], a[172:175], v[144:147]\n"
"v_exp_f32 v238, v186 clamp\n"
"v_mfma_f32_16x16x32_bf16 v[152:155], a[204:207], a[188:191], v[152:155]\n"
"v_exp_f32 v239, v187 clamp\n"
"v_mfma_f32_16x16x32_bf16 v[132:135], a[208:211], a[128:131], v[192:195]\n"
"buffer_load_dwordx4 a[88:91], v246, s[84:87], s48 offen offset:2048 sc1\n"
"ds_read_b128 a[240:243], v245 offset:28672\n"
"v_cvt_pk_bf16_f32 v221, v238, v239\n"
"v_mfma_f32_16x16x32_bf16 v[140:143], a[208:211], a[144:147], v[196:199]\n"
"v_exp_f32 v236, v188 clamp\n"
"v_mfma_f32_16x16x32_bf16 v[148:151], a[208:211], a[160:163], v[200:203]\n"
"v_exp_f32 v237, v189 clamp\n"
"v_mfma_f32_16x16x32_bf16 v[156:159], a[208:211], a[176:179], v[204:207]\n"
"ds_read_b128 a[244:247], v245 offset:29696\n"
"v_cvt_pk_bf16_f32 v222, v236, v237\n"
"v_mfma_f32_16x16x32_bf16 v[132:135], a[212:215], a[132:135], v[132:135]\n"
"v_exp_f32 v238, v190 clamp\n"
"v_mfma_f32_16x16x32_bf16 v[140:143], a[212:215], a[148:151], v[140:143]\n"
"buffer_load_dwordx4 v246, s[40:43], s56 offen offset:1024 sc1 lds\n"
"v_exp_f32 v239, v191 clamp\n"
"v_mfma_f32_16x16x32_bf16 v[148:151], a[212:215], a[164:167], v[148:151]\n"
"ds_read_b128 a[248:251], v245 offset:30720\n"
"v_cvt_pk_bf16_f32 v223, v238, v239\n"
"v_mfma_f32_16x16x32_bf16 v[156:159], a[212:215], a[180:183], v[156:159]\n"
"s_nop 0\n"
"v_smfmac_f32_16x16x64_bf16 v[228:231], v[240:243], v[216:223], v247\n"
"ds_read_b128 a[252:255], v245 offset:31744\n"
"v_mfma_f32_16x16x32_bf16 v[132:135], a[216:219], a[136:139], v[132:135]\n"
"buffer_load_dwordx4 a[92:95], v246, s[84:87], s48 offen offset:3072 sc1\n"
"v_max3_f32 v236, v224, v225, v226\n"
"v_mfma_f32_16x16x32_bf16 v[140:143], a[216:219], a[152:155], v[140:143]\n"
"buffer_load_dwordx4 v246, s[40:43], s56 offen offset:2048 sc1 lds\n"
"v_mfma_f32_16x16x32_bf16 v[148:151], a[216:219], a[168:171], v[148:151]\n"
"v_mfma_f32_16x16x32_bf16 v[156:159], a[216:219], a[184:187], v[156:159]\n"
"v_max3_f32 v236, v236, v227, v228\n"
"v_max3_f32 v236, v236, v229, v230\n"
"v_mfma_f32_16x16x32_bf16 v[132:135], a[220:223], a[140:143], v[132:135]\n"
"v_max_f32 v236, v236, v231\n"
"v_cmp_nge_f32 vcc, s58, v236\n"
"v_mfma_f32_16x16x32_bf16 v[140:143], a[220:223], a[156:159], v[140:143]\n"
"v_mfma_f32_16x16x32_bf16 v[148:151], a[220:223], a[172:175], v[148:151]\n"
"s_waitcnt lgkmcnt(0)\n"
"v_mfma_f32_16x16x32_bf16 v[156:159], a[220:223], a[188:191], v[156:159]\n"
"s_getpc_b64 s[66:67]\n"
"s_cbranch_vccnz .LstubG0_%=\n"
"buffer_load_dwordx4 v246, s[40:43], s56 offen offset:3072 sc1 lds\n"
".Lpp11_%=:\n"
"v_mfma_f32_16x16x32_bf16 v[0:3], a[224:227], v[208:211], v[0:3]\n"
"s_waitcnt vmcnt(28)\n"
"s_add_u32 s48, s48, 4096\n"
"v_exp_f32 v236, v128 clamp\n"
"v_mfma_f32_16x16x32_bf16 v[16:19], a[228:231], v[208:211], v[16:19]\n"
"s_barrier\n"
"buffer_load_dwordx4 a[96:99], v246, s[84:87], s48 offen offset:0 sc1\n"
"v_mfma_f32_16x16x32_bf16 v[32:35], a[232:235], v[208:211], v[32:35]\n"
"s_add_u32 s52, s52, 1\n"
"v_exp_f32 v237, v129 clamp\n"
"v_mfma_f32_16x16x32_bf16 v[48:51], a[236:239], v[208:211], v[48:51]\n"
"s_add_u32 m0, s57, 16384\n"
"ds_read_b128 a[192:195], v244 offset:32768\n"
"v_mfma_f32_16x16x32_bf16 v[64:67], a[240:243], v[208:211], v[64:67]\n"
"v_exp_f32 v238, v130 clamp\n"
"v_mfma_f32_16x16x32_bf16 v[80:83], a[244:247], v[208:211], v[80:83]\n"
"s_sub_u32 s55, s55, s29\n"
"v_exp_f32 v239, v131 clamp\n"
"v_mfma_f32_16x16x32_bf16 v[96:99], a[248:251], v[208:211], v[96:99]\n"
"buffer_load_dwordx4 v246, s[36:39], s54 offen offset:0 sc1 lds\n"
"v_mfma_f32_16x16x32_bf16 v[112:115], a[252:255], v[208:211], v[112:115]\n"
"ds_read_b128 a[196:199], v244 offset:33792\n"
"v_cvt_pk_bf16_f32 v208, v236, v237\n"
"v_mfma_f32_16x16x32_bf16 v[4:7], a[224:227], v[212:215], v[4:7]\n"
"s_add_u32 s48, s49, s34\n"
"ds_read_b128 a[200:203], v244 offset:34816\n"
"v_cvt_pk_bf16_f32 v209, v238, v239\n"
"v_mfma_f32_16x16x32_bf16 v[20:23], a[228:231], v[212:215], v[20:23]\n"
"s_add_u32 s48, s48, 5120\n"
"v_exp_f32 v236, v132 clamp\n"
"v_mfma_f32_16x16x32_bf16 v[36:39], a[232:235], v[212:215], v[36:39]\n"
"buffer_load_dwordx4 a[100:103], v246, s[84:87], s48 offen offset:0 sc1\n"
"v_exp_f32 v237, v133 clamp\n"
"v_mfma_f32_16x16x32_bf16 v[52:55], a[236:239], v[212:215], v[52:55]\n"
"ds_read_b128 a[204:207], v244 offset:35840\n"
"v_cvt_pk_bf16_f32 v210, v236, v237\n"
"v_mfma_f32_16x16x32_bf16 v[68:71], a[240:243], v[212:215], v[68:71]\n"
"v_exp_f32 v238, v134 clamp\n"
"v_mfma_f32_16x16x32_bf16 v[84:87], a[244:247], v[212:215], v[84:87]\n"
"v_exp_f32 v239, v135 clamp\n"
"v_mfma_f32_16x16x32_bf16 v[100:103], a[248:251], v[212:215], v[100:103]\n"
"buffer_load_dwordx4 v246, s[36:39], s54 offen offset:1024 sc1 lds\n"
"v_mfma_f32_16x16x32_bf16 v[116:119], a[252:255], v[212:215], v[116:119]\n"
"ds_read_b128 a[208:211], v244 offset:36864\n"
"v_cvt_pk_bf16_f32 v211, v238, v239\n"
"v_mfma_f32_16x16x32_bf16 v[8:11], a[224:227], v[216:219], v[8:11]\n"
"v_exp_f32 v236, v136 clamp\n"
"v_mfma_f32_16x16x32_bf16 v[24:27], a[228:231], v[216:219], v[24:27]\n"
"buffer_load_dwordx4 a[104:107], v246, s[84:87], s48 offen offset:1024 sc1\n"
"v_exp_f32 v237, v137 clamp\n"
"v_mfma_f32_16x16x32_bf16 v[40:43], a[232:235], v[216:219], v[40:43]\n"
"ds_read_b128 a[212:215], v244 offset:37888\n"
"v_cvt_pk_bf16_f32 v212, v236, v237\n"
"v_mfma_f32_16x16x32_bf16 v[56:59], a[236:239], v[216:219], v[56:59]\n"
"v_exp_f32 v238, v138 clamp\n"
"v_mfma_f32_16x16x32_bf16 v[72:75], a[240:243], v[216:219], v[72:75]\n"
"v_exp_f32 v239, v139 clamp\n"
"v_mfma_f32_16x16x32_bf16 v[88:91], a[244:247], v[216:219], v[88:91]\n"
"buffer_load_dwordx4 v246, s[36:39], s54 offen offset:2048 sc1 lds\n"
"v_mfma_f32_16x16x32_bf16 v[104:107], a[248:251], v[216:219], v[104:107]\n"
"ds_read_b128 a[216:219], v244 offset:38912\n"
"v_cvt_pk_bf16_f32 v213, v238, v239\n"
"v_mfma_f32_16x16x32_bf16 v[120:123], a[252:255], v[216:219], v[120:123]\n"
"v_exp_f32 v236, v140 clamp\n"
"v_mfma_f32_16x16x32_bf16 v[12:15], a[224:227], v[220:223], v[12:15]\n"
"v_exp_f32 v237, v141 clamp\n"
"v_mfma_f32_16x16x32_bf16 v[28:31], a[228:231], v[220:223], v[28:31]\n"
"ds_read_b128 a[220:223], v244 offset:39936\n"
"v_cvt_pk_bf16_f32 v214, v236, v237\n"
"v_mfma_f32_16x16x32_bf16 v[44:47], a[232:235], v[220:223], v[44:47]\n"
"v_exp_f32 v238, v142 clamp\n"
"v_mfma_f32_16x16x32_bf16 v[60:63], a[236:239], v[220:223], v[60:63]\n"
"v_exp_f32 v239, v143 clamp\n"
"v_mfma_f32_16x16x32_bf16 v[76:79], a[240:243], v[220:223], v[76:79]\n"
"buffer_load_dwordx4 v246, s[36:39], s54 offen offset:3072 sc1 lds\n"
"v_mfma_f32_16x16x32_bf16 v[92:95], a[244:247], v[220:223], v[92:95]\n"
"s_add_u32 s54, s54, s28\n"
"v_cvt_pk_bf16_f32 v215, v238, v239\n"
"v_mfma_f32_16x16x32_bf16 v[108:111], a[248:251], v[220:223], v[108:111]\n"
"v_exp_f32 v236, v144 clamp\n"
"v_mfma_f32_16x16x32_bf16 v[124:127], a[252:255], v[220:223], v[124:127]\n"
"s_waitcnt lgkmcnt(0)\n"
"v_exp_f32 v237, v145 clamp\n"
"v_mfma_f32_16x16x32_bf16 v[160:163], a[192:195], a[128:131], v[192:195]\n"
"s_cmp_lt_u32 s55, 540672\n"
"v_cvt_pk_bf16_f32 v216, v236, v237\n"
"v_mfma_f32_16x16x32_bf16 v[168:171], a[192:195], a[144:147], v[196:199]\n"
"s_cselect_b32 s56, 1073741824, s55\n"
"v_exp_f32 v238, v146 clamp\n"
"v_smfmac_f32_16x16x64_bf16 v[224:227], v[240:243], v[208:215], v247\n"
"s_add_u32 m0, s57, 81920\n"
"v_mfma_f32_16x16x32_bf16 v[176:179], a[192:195], a[160:163], v[200:203]\n"
"s_cmp_eq_u32 s64, s52\n"
"v_exp_f32 v239, v147 clamp\n"
"v_mfma_f32_16x16x32_bf16 v[184:187], a[192:195], a[176:179], v[204:207]\n"
"s_cselect_b32 s58, -1.0, 0.5\n"
"ds_read_b128 a[224:227], v245 offset:40960\n"
"v_cvt_pk_bf16_f32 v217, v238, v239\n"
"v_mfma_f32_16x16x32_bf16 v[160:163], a[196:199], a[132:135], v[160:163]\n"
"v_exp_f32 v236, v148 clamp\n"
"v_mfma_f32_16x16x32_bf16 v[168:171], a[196:199], a[148:151], v[168:171]\n"
"buffer_load_dwordx4 a[108:111], v246, s[84:87], s48 offen offset:2048 sc1\n"
"v_exp_f32 v237, v149 clamp\n"
"v_mfma_f32_16x16x32_bf16 v[176:179], a[196:199], a[164:167], v[176:179]\n"
"ds_read_b128 a[228:231], v245 offset:41984\n"
"v_cvt_pk_bf16_f32 v218, v236, v237\n"
"v_mfma_f32_16x16x32_bf16 v[184:187], a[196:199], a[180:183], v[184:187]\n"
"v_exp_f32 v238, v150 clamp\n"
"v_mfma_f32_16x16x32_bf16 v[160:163], a[200:203], a[136:139], v[160:163]\n"
"v_exp_f32 v239, v151 clamp\n"
"v_mfma_f32_16x16x32_bf16 v[168:171], a[200:203], a[152:155], v[168:171]\n"
"ds_read_b128 a[232:235], v245 offset:43008\n"
"v_cvt_pk_bf16_f32 v219, v238, v239\n"
"v_mfma_f32_16x16x32_bf16 v[176:179], a[200:203], a[168:171], v[176:179]\n"
"v_exp_f32 v236, v152 clamp\n"
"v_mfma_f32_16x16x32_bf16 v[184:187], a[200:203], a[184:187], v[184:187]\n"
"buffer_load_dwordx4 v246, s[40:43], s56 offen offset:0 sc1 lds\n"
"v_mfma_f32_16x16x32_bf16 v[160:163], a[204:207], a[140:143], v[160:163]\n"
"v_exp_f32 v237, v153 clamp\n"
"v_mfma_f32_16x16x32_bf16 v[168:171], a[204:207], a[156:159], v[168:171]\n"
"ds_read_b128 a[236:239], v245 offset:44032\n"
"v_cvt_pk_bf16_f32 v220, v236, v237\n"
"v_mfma_f32_16x16x32_bf16 v[176:179], a[204:207], a[172:175], v[176:179]\n"
"v_exp_f32 v238, v154 clamp\n"
"v_mfma_f32_16x16x32_bf16 v[184:187], a[204:207], a[188:191], v[184:187]\n"
"v_exp_f32 v239, v155 clamp\n"
"v_mfma_f32_16x16x32_bf16 v[164:167], a[208:211], a[128:131], v[192:195]\n"
"buffer_load_dwordx4 a[112:115], v246, s[84:87], s48 offen offset:3072 sc1\n"
"ds_read_b128 a[240:243], v245 offset:45056\n"
"v_cvt_pk_bf16_f32 v221, v238, v239\n"
"v_mfma_f32_16x16x32_bf16 v[172:175], a[208:211], a[144:147], v[196:199]\n"
"v_exp_f32 v236, v156 clamp\n"
"v_mfma_f32_16x16x32_bf16 v[180:183], a[208:211], a[160:163], v[200:203]\n"
"v_exp_f32 v237, v157 clamp\n"
"v_mfma_f32_16x16x32_bf16 v[188:191], a[208:211], a[176:179], v[204:207]\n"
"ds_read_b128 a[244:247], v245 offset:46080\n"
"v_cvt_pk_bf16_f32 v222, v236, v237\n"
"v_mfma_f32_16x16x32_bf16 v[164:167], a[212:215], a[132:135], v[164:167]\n"
"v_exp_f32 v238, v158 clamp\n"
"v_mfma_f32_16x16x32_bf16 v[172:175], a[212:215], a[148:151], v[172:175]\n"
"buffer_load_dwordx4 v246, s[40:43], s56 offen offset:1024 sc1 lds\n"
"v_exp_f32 v239, v159 clamp\n"
"v_mfma_f32_16x16x32_bf16 v[180:183], a[212:215], a[164:167], v[180:183]\n"
"ds_read_b128 a[248:251], v245 offset:47104\n"
"v_cvt_pk_bf16_f32 v223, v238, v239\n"
"v_mfma_f32_16x16x32_bf16 v[188:191], a[212:215], a[180:183], v[188:191]\n"
"v_lshrrev_b32 v253, 2, v246\n"
"v_and_b32 v253, 28, v253\n"
"v_smfmac_f32_16x16x64_bf16 v[228:231], v[240:243], v[216:223], v247\n"
"s_add_u32 s48, s48, 4096\n"
"ds_read_b128 a[252:255], v245 offset:48128\n"
"v_mfma_f32_16x16x32_bf16 v[164:167], a[216:219], a[136:139], v[164:167]\n"
"buffer_load_dwordx4 a[116:119], v246, s[84:87], s48 offen offset:0 sc1\n"
"v_max3_f32 v236, v224, v225, v226\n"
"v_mfma_f32_16x16x32_bf16 v[172:175], a[216:219], a[152:155], v[172:175]\n"
"buffer_load_dwordx4 v246, s[40:43], s56 offen offset:2048 sc1 lds\n"
"v_mfma_f32_16x16x32_bf16 v[180:183], a[216:219], a[168:171], v[180:183]\n"
"buffer_load_dword a124, v253, s[84:87], s46 offen sc1\n"
"v_mfma_f32_16x16x32_bf16 v[188:191], a[216:219], a[184:187], v[188:191]\n"
"v_max3_f32 v236, v236, v227, v228\n"
"v_max3_f32 v236, v236, v229, v230\n"
".Lpad15_%=:\n"
"s_cbranch_execz .LpadloopV_%=\n"
"v_mfma_f32_16x16x32_bf16 v[164:167], a[220:223], a[140:143], v[164:167]\n"
"buffer_load_dwordx4 a[120:123], v246, s[84:87], s44 offen sc1\n"
"v_max_f32 v236, v236, v231\n"
"v_cmp_nge_f32 vcc, s58, v236\n"
"v_mfma_f32_16x16x32_bf16 v[172:175], a[220:223], a[156:159], v[172:175]\n"
"v_mfma_f32_16x16x32_bf16 v[180:183], a[220:223], a[172:175], v[180:183]\n"
"s_waitcnt lgkmcnt(0)\n"
"v_mfma_f32_16x16x32_bf16 v[188:191], a[220:223], a[188:191], v[188:191]\n"
"s_getpc_b64 s[66:67]\n"
"s_cbranch_vccnz .LstubG1_%=\n"
"buffer_load_dwordx4 v246, s[40:43], s56 offen offset:3072 sc1 lds\n"
".Lpp12_%=:\n"
"v_mfma_f32_16x16x32_bf16 v[0:3], a[224:227], v[208:211], v[0:3]\n"
"s_waitcnt vmcnt(30)\n"
"v_exp_f32 v236, v160 clamp\n"
"v_mfma_f32_16x16x32_bf16 v[16:19], a[228:231], v[208:211], v[16:19]\n"
"s_barrier\n"
"v_mfma_f32_16x16x32_bf16 v[32:35], a[232:235], v[208:211], v[32:35]\n"
"s_add_u32 s52, s52, 1\n"
"v_exp_f32 v237, v161 clamp\n"
"v_mfma_f32_16x16x32_bf16 v[48:51], a[236:239], v[208:211], v[48:51]\n"
"s_add_u32 m0, s57, 32768\n"
"ds_read_b128 a[192:195], v244 offset:49152\n"
"v_mfma_f32_16x16x32_bf16 v[64:67], a[240:243], v[208:211], v[64:67]\n"
"v_exp_f32 v238, v162 clamp\n"
"v_mfma_f32_16x16x32_bf16 v[80:83], a[244:247], v[208:211], v[80:83]\n"
"s_sub_u32 s55, s55, s29\n"
"v_exp_f32 v239, v163 clamp\n"
"v_mfma_f32_16x16x32_bf16 v[96:99], a[248:251], v[208:211], v[96:99]\n"
"buffer_load_dwordx4 v246, s[36:39], s54 offen offset:0 sc1 lds\n"
"v_mfma_f32_16x16x32_bf16 v[112:115], a[252:255], v[208:211], v[112:115]\n"
"ds_read_b128 a[196:199], v244 offset:50176\n"
"v_cvt_pk_bf16_f32 v208, v236, v237\n"
"v_mfma_f32_16x16x32_bf16 v[4:7], a[224:227], v[212:215], v[4:7]\n"
"ds_read_b128 a[200:203], v244 offset:51200\n"
"v_cvt_pk_bf16_f32 v209, v238, v239\n"
"v_mfma_f32_16x16x32_bf16 v[20:23], a[228:231], v[212:215], v[20:23]\n"
"v_exp_f32 v236, v164 clamp\n"
"v_mfma_f32_16x16x32_bf16 v[36:39], a[232:235], v[212:215], v[36:39]\n"
"v_exp_f32 v237, v165 clamp\n"
"v_mfma_f32_16x16x32_bf16 v[52:55], a[236:239], v[212:215], v[52:55]\n"
"ds_read_b128 a[204:207], v244 offset:52224\n"
"v_cvt_pk_bf16_f32 v210, v236, v237\n"
"v_mfma_f32_16x16x32_bf16 v[68:71], a[240:243], v[212:215], v[68:71]\n"
"v_exp_f32 v238, v166 clamp\n"
"v_mfma_f32_16x16x32_bf16 v[84:87], a[244:247], v[212:215], v[84:87]\n"
"v_exp_f32 v239, v167 clamp\n"
"v_mfma_f32_16x16x32_bf16 v[100:103], a[248:251], v[212:215], v[100:103]\n"
"buffer_load_dwordx4 v246, s[36:39], s54 offen offset:1024 sc1 lds\n"
"v_mfma_f32_16x16x32_bf16 v[116:119], a[252:255], v[212:215], v[116:119]\n"
"ds_read_b128 a[208:211], v244 offset:53248\n"
"v_cvt_pk_bf16_f32 v211, v238, v239\n"
"v_mfma_f32_16x16x32_bf16 v[8:11], a[224:227], v[216:219], v[8:11]\n"
"v_exp_f32 v236, v168 clamp\n"
"v_mfma_f32_16x16x32_bf16 v[24:27], a[228:231], v[216:219], v[24:27]\n"
"v_exp_f32 v237, v169 clamp\n"
"v_mfma_f32_16x16x32_bf16 v[40:43], a[232:235], v[216:219], v[40:43]\n"
"ds_read_b128 a[212:215], v244 offset:54272\n"
"v_cvt_pk_bf16_f32 v212, v236, v237\n"
"v_mfma_f32_16x16x32_bf16 v[56:59], a[236:239], v[216:219], v[56:59]\n"
"v_exp_f32 v238, v170 clamp\n"
"v_mfma_f32_16x16x32_bf16 v[72:75], a[240:243], v[216:219], v[72:75]\n"
"v_exp_f32 v239, v171 clamp\n"
"v_mfma_f32_16x16x32_bf16 v[88:91], a[244:247], v[216:219], v[88:91]\n"
"buffer_load_dwordx4 v246, s[36:39], s54 offen offset:2048 sc1 lds\n"
"v_mfma_f32_16x16x32_bf16 v[104:107], a[248:251], v[216:219], v[104:107]\n"
"ds_read_b128 a[216:219], v244 offset:55296\n"
"v_cvt_pk_bf16_f32 v213, v238, v239\n"
"v_mfma_f32_16x16x32_bf16 v[120:123], a[252:255], v[216:219], v[120:123]\n"
"v_exp_f32 v236, v172 clamp\n"
"v_mfma_f32_16x16x32_bf16 v[12:15], a[224:227], v[220:223], v[12:15]\n"
"v_exp_f32 v237, v173 clamp\n"
"v_mfma_f32_16x16x32_bf16 v[28:31], a[228:231], v[220:223], v[28:31]\n"
"ds_read_b128 a[220:223], v244 offset:56320\n"
"v_cvt_pk_bf16_f32 v214, v236, v237\n"
"v_mfma_f32_16x16x32_bf16 v[44:47], a[232:235], v[220:223], v[44:47]\n"
"v_exp_f32 v238, v174 clamp\n"
"v_mfma_f32_16x16x32_bf16 v[60:63], a[236:239], v[220:223], v[60:63]\n"
"v_exp_f32 v239, v175 clamp\n"
"v_mfma_f32_16x16x32_bf16 v[76:79], a[240:243], v[220:223], v[76:79]\n"
"buffer_load_dwordx4 v246, s[36:39], s54 offen offset:3072 sc1 lds\n"
"v_mfma_f32_16x16x32_bf16 v[92:95], a[244:247], v[220:223], v[92:95]\n"
"s_add_u32 s54, s54, s28\n"
"v_cvt_pk_bf16_f32 v215, v238, v239\n"
"v_mfma_f32_16x16x32_bf16 v[108:111], a[248:251], v[220:223], v[108:111]\n"
"v_exp_f32 v236, v176 clamp\n"
"v_mfma_f32_16x16x32_bf16 v[124:127], a[252:255], v[220:223], v[124:127]\n"
"s_waitcnt lgkmcnt(0)\n"
"v_exp_f32 v237, v177 clamp\n"
"v_mfma_f32_16x16x32_bf16 v[128:131], a[192:195], a[128:131], v[192:195]\n"
"s_cmp_lt_u32 s55, 540672\n"
"v_cvt_pk_bf16_f32 v216, v236, v237\n"
"v_mfma_f32_16x16x32_bf16 v[136:139], a[192:195], a[144:147], v[196:199]\n"
"s_cselect_b32 s56, 1073741824, s55\n"
"v_exp_f32 v238, v178 clamp\n"
"v_smfmac_f32_16x16x64_bf16 v[224:227], v[240:243], v[208:215], v247\n"
"s_add_u32 m0, s57, 98304\n"
"v_mfma_f32_16x16x32_bf16 v[144:147], a[192:195], a[160:163], v[200:203]\n"
"s_cmp_eq_u32 s64, s52\n"
"v_exp_f32 v239, v179 clamp\n"
"v_mfma_f32_16x16x32_bf16 v[152:155], a[192:195], a[176:179], v[204:207]\n"
"s_cselect_b32 s58, -1.0, 0.5\n"
"ds_read_b128 a[224:227], v245 offset:57344\n"
"v_cvt_pk_bf16_f32 v217, v238, v239\n"
"v_mfma_f32_16x16x32_bf16 v[128:131], a[196:199], a[132:135], v[128:131]\n"
"v_exp_f32 v236, v180 clamp\n"
"v_mfma_f32_16x16x32_bf16 v[136:139], a[196:199], a[148:151], v[136:139]\n"
"v_exp_f32 v237, v181 clamp\n"
"v_mfma_f32_16x16x32_bf16 v[144:147], a[196:199], a[164:167], v[144:147]\n"
"ds_read_b128 a[228:231], v245 offset:58368\n"
"v_cvt_pk_bf16_f32 v218, v236, v237\n"
"v_mfma_f32_16x16x32_bf16 v[152:155], a[196:199], a[180:183], v[152:155]\n"
"v_exp_f32 v238, v182 clamp\n"
"v_mfma_f32_16x16x32_bf16 v[128:131], a[200:203], a[136:139], v[128:131]\n"
"v_exp_f32 v239, v183 clamp\n"
"v_mfma_f32_16x16x32_bf16 v[136:139], a[200:203], a[152:155], v[136:139]\n"
"ds_read_b128 a[232:235], v245 offset:59392\n"
"v_cvt_pk_bf16_f32 v219, v238, v239\n"
"v_mfma_f32_16x16x32_bf16 v[144:147], a[200:203], a[168:171], v[144:147]\n"
"v_exp_f32 v236, v184 clamp\n"
"v_mfma_f32_16x16x32_bf16 v[152:155], a[200:203], a[184:187], v[152:155]\n"
"buffer_load_dwordx4 v246, s[40:43], s56 offen offset:0 sc1 lds\n"
"v_mfma_f32_16x16x32_bf16 v[128:131], a[204:207], a[140:143], v[128:131]\n"
"v_exp_f32 v237, v185 clamp\n"
"v_mfma_f32_16x16x32_bf16 v[136:139], a[204:207], a[156:159], v[136:139]\n"
"ds_read_b128 a[236:239], v245 offset:60416\n"
"v_cvt_pk_bf16_f32 v220, v236, v237\n"
"v_mfma_f32_16x16x32_bf16 v[144:147], a[204:207], a[172:175], v[144:147]\n"
"v_exp_f32 v238, v186 clamp\n"
"v_mfma_f32_16x16x32_bf16 v[152:155], a[204:207], a[188:191], v[152:155]\n"
"v_exp_f32 v239, v187 clamp\n"
"v_mfma_f32_16x16x32_bf16 v[132:135], a[208:211], a[128:131], v[192:195]\n"
"ds_read_b128 a[240:243], v245 offset:61440\n"
"v_cvt_pk_bf16_f32 v221, v238, v239\n"
"v_mfma_f32_16x16x32_bf16 v[140:143], a[208:211], a[144:147], v[196:199]\n"
"v_exp_f32 v236, v188 clamp\n"
"v_mfma_f32_16x16x32_bf16 v[148:151], a[208:211], a[160:163], v[200:203]\n"
"v_exp_f32 v237, v189 clamp\n"
"v_mfma_f32_16x16x32_bf16 v[156:159], a[208:211], a[176:179], v[204:207]\n"
"ds_read_b128 a[244:247], v245 offset:62464\n"
"v_cvt_pk_bf16_f32 v222, v236, v237\n"
"v_mfma_f32_16x16x32_bf16 v[132:135], a[212:215], a[132:135], v[132:135]\n"
"v_exp_f32 v238, v190 clamp\n"
"v_mfma_f32_16x16x32_bf16 v[140:143], a[212:215], a[148:151], v[140:143]\n"
"buffer_load_dwordx4 v246, s[40:43], s56 offen offset:1024 sc1 lds\n"
"v_exp_f32 v239, v191 clamp\n"
"v_mfma_f32_16x16x32_bf16 v[148:151], a[212:215], a[164:167], v[148:151]\n"
"ds_read_b128 a[248:251], v245 offset:63488\n"
"v_cvt_pk_bf16_f32 v223, v238, v239\n"
"v_mfma_f32_16x16x32_bf16 v[156:159], a[212:215], a[180:183], v[156:159]\n"
"s_nop 0\n"
"v_smfmac_f32_16x16x64_bf16 v[228:231], v[240:243], v[216:223], v247\n"
"ds_read_b128 a[252:255], v245 offset:64512\n"
"v_mfma_f32_16x16x32_bf16 v[132:135], a[216:219], a[136:139], v[132:135]\n"
"v_max3_f32 v236, v224, v225, v226\n"
"v_mfma_f32_16x16x32_bf16 v[140:143], a[216:219], a[152:155], v[140:143]\n"
"buffer_load_dwordx4 v246, s[40:43], s56 offen offset:2048 sc1 lds\n"
"v_mfma_f32_16x16x32_bf16 v[148:151], a[216:219], a[168:171], v[148:151]\n"
"v_mfma_f32_16x16x32_bf16 v[156:159], a[216:219], a[184:187], v[156:159]\n"
"s_nop 0\n"
"v_max3_f32 v236, v236, v227, v228\n"
"v_max3_f32 v236, v236, v229, v230\n"
"v_mfma_f32_16x16x32_bf16 v[132:135], a[220:223], a[140:143], v[132:135]\n"
"v_max_f32 v236, v236, v231\n"
"v_cmp_nge_f32 vcc, s58, v236\n"
"v_mfma_f32_16x16x32_bf16 v[140:143], a[220:223], a[156:159], v[140:143]\n"
"v_mfma_f32_16x16x32_bf16 v[148:151], a[220:223], a[172:175], v[148:151]\n"
"s_waitcnt lgkmcnt(0)\n"
"v_mfma_f32_16x16x32_bf16 v[156:159], a[220:223], a[188:191], v[156:159]\n"
"s_getpc_b64 s[66:67]\n"
"s_cbranch_vccnz .LstubG0_%=\n"
"buffer_load_dwordx4 v246, s[40:43], s56 offen offset:3072 sc1 lds\n"
"s_waitcnt vmcnt(9)\n"
"v_accvgpr_read_b32 v236, a120\n"
"v_accvgpr_read_b32 v237, a121\n"
"v_accvgpr_read_b32 v238, a122\n"
"v_accvgpr_read_b32 v239, a123\n"
"v_accvgpr_read_b32 v254, a124\n"
"v_xor_b32 v236, s88, v236\n"
"v_xor_b32 v237, s88, v237\n"
"v_xor_b32_e64 v238, s88, v238\n"
"v_xor_b32_e64 v239, s88, v239\n"
"v_or_b32_e64 v236, v236, v237\n"
"v_or_b32_e64 v236, v236, v238\n"
"v_or_b32_e64 v236, v236, v239\n"
"v_xor_b32_e64 v254, s88, v254\n"
"v_or_b32_e64 v254, v254, v236\n"
"v_cmp_ne_u32_e64 vcc, 0, v254\n"
"s_cbranch_vccnz .Lslow5_%=\n"
".Lback5_%=:\n"
".LloopV_%=:\n"
".LpadloopV_%=:\n"
"s_cbranch_execz .Lhopi2_%=\n"
".Lper0_%=:\n"
"v_mfma_f32_16x16x32_bf16 v[0:3], a[224:227], v[208:211], v[0:3]\n"
"s_waitcnt vmcnt(16)\n"
"s_cmp_eq_u32 s52, s83\n"
"v_exp_f32 v236, v128 clamp\n"
"v_mfma_f32_16x16x32_bf16 v[16:19], a[228:231], v[208:211], v[16:19]\n"
"s_barrier\n"
"s_cbranch_scc1 .Lsw_%=\n"
".Lret0_%=:\n"
"v_mfma_f32_16x16x32_bf16 v[32:35], a[232:235], v[208:211], v[32:35]\n"
"s_add_u32 s52, s52, 1\n"
"v_exp_f32 v237, v129 clamp\n"
"v_mfma_f32_16x16x32_bf16 v[48:51], a[236:239], v[208:211], v[48:51]\n"
"s_add_u32 m0, s57, 49152\n"
"ds_read_b128 a[192:195], v244 offset:0\n"
"v_mfma_f32_16x16x32_bf16 v[64:67], a[240:243], v[208:211], v[64:67]\n"
"v_exp_f32 v238, v130 clamp\n"
"v_mfma_f32_16x16x32_bf16 v[80:83], a[244:247], v[208:211], v[80:83]\n"
"s_sub_u32 s55, s55, s29\n"
"v_exp_f32 v239, v131 clamp\n"
"v_mfma_f32_16x16x32_bf16 v[96:99], a[248:251], v[208:211], v[96:99]\n"
"buffer_load_dwordx4 v246, s[36:39], s54 offen offset:0 sc1 lds\n"
"v_mfma_f32_16x16x32_bf16 v[112:115], a[252:255], v[208:211], v[112:115]\n"
"ds_read_b128 a[196:199], v244 offset:1024\n"
"v_cvt_pk_bf16_f32 v208, v236, v237\n"
"v_mfma_f32_16x16x32_bf16 v[4:7], a[224:227], v[212:215], v[4:7]\n"
"ds_read_b128 a[200:203], v244 offset:2048\n"
"v_cvt_pk_bf16_f32 v209, v238, v239\n"
"v_mfma_f32_16x16x32_bf16 v[20:23], a[228:231], v[212:215], v[20:23]\n"
"v_exp_f32 v236, v132 clamp\n"
"v_mfma_f32_16x16x32_bf16 v[36:39], a[232:235], v[212:215], v[36:39]\n"
"v_exp_f32 v237, v133 clamp\n"
"v_mfma_f32_16x16x32_bf16 v[52:55], a[236:239], v[212:215], v[52:55]\n"
"ds_read_b128 a[204:207], v244 offset:3072\n"
"v_cvt_pk_bf16_f32 v210, v236, v237\n"
"v_mfma_f32_16x16x32_bf16 v[68:71], a[240:243], v[212:215], v[68:71]\n"
"v_exp_f32 v238, v134 clamp\n"
"v_mfma_f32_16x16x32_bf16 v[84:87], a[244:247], v[212:215], v[84:87]\n"
"v_exp_f32 v239, v135 clamp\n"
"v_mfma_f32_16x16x32_bf16 v[100:103], a[248:251], v[212:215], v[100:103]\n"
"buffer_load_dwordx4 v246, s[36:39], s54 offen offset:1024 sc1 lds\n"
"v_mfma_f32_16x16x32_bf16 v[116:119], a[252:255], v[212:215], v[116:119]\n"
"ds_read_b128 a[208:211], v244 offset:4096\n"
"v_cvt_pk_bf16_f32 v211, v238, v239\n"
"v_mfma_f32_16x16x32_bf16 v[8:11], a[224:227], v[216:219], v[8:11]\n"
"v_exp_f32 v236, v136 clamp\n"
"v_mfma_f32_16x16x32_bf16 v[24:27], a[228:231], v[216:219], v[24:27]\n"
"v_exp_f32 v237, v137 clamp\n"
"v_mfma_f32_16x16x32_bf16 v[40:43], a[232:235], v[216:219], v[40:43]\n"
"ds_read_b128 a[212:215], v244 offset:5120\n"
"v_cvt_pk_bf16_f32 v212, v236, v237\n"
"v_mfma_f32_16x16x32_bf16 v[56:59], a[236:239], v[216:219], v[56:59]\n"
"v_exp_f32 v238, v138 clamp\n"
"v_mfma_f32_16x16x32_bf16 v[72:75], a[240:243], v[216:219], v[72:75]\n"
"v_exp_f32 v239, v139 clamp\n"
"v_mfma_f32_16x16x32_bf16 v[88:91], a[244:247], v[216:219], v[88:91]\n"
"buffer_load_dwordx4 v246, s[36:39], s54 offen offset:2048 sc1 lds\n"
"v_mfma_f32_16x16x32_bf16 v[104:107], a[248:251], v[216:219], v[104:107]\n"
"ds_read_b128 a[216:219], v244 offset:6144\n"
"v_cvt_pk_bf16_f32 v213, v238, v239\n"
"v_mfma_f32_16x16x32_bf16 v[120:123], a[252:255], v[216:219], v[120:123]\n"
"v_exp_f32 v236, v140 clamp\n"
"v_mfma_f32_16x16x32_bf16 v[12:15], a[224:227], v[220:223], v[12:15]\n"
"v_exp_f32 v237, v141 clamp\n"
"v_mfma_f32_16x16x32_bf16 v[28:31], a[228:231], v[220:223], v[28:31]\n"
"ds_read_b128 a[220:223], v244 offset:7168\n"
"v_cvt_pk_bf16_f32 v214, v236, v237\n"
"v_mfma_f32_16x16x32_bf16 v[44:47], a[232:235], v[220:223], v[44:47]\n"
"v_exp_f32 v238, v142 clamp\n"
"v_mfma_f32_16x16x32_bf16 v[60:63], a[236:239], v[220:223], v[60:63]\n"
"v_exp_f32 v239, v143 clamp\n"
"v_mfma_f32_16x16x32_bf16 v[76:79], a[240:243], v[220:223], v[76:79]\n"
"buffer_load_dwordx4 v246, s[36:39], s54 offen offset:3072 sc1 lds\n"
"v_mfma_f32_16x16x32_bf16 v[92:95], a[244:247], v[220:223], v[92:95]\n"
"s_add_u32 s54, s54, s28\n"
"v_cvt_pk_bf16_f32 v215, v238, v239\n"
"v_mfma_f32_16x16x32_bf16 v[108:111], a[248:251], v[220:223], v[108:111]\n"
"v_exp_f32 v236, v144 clamp\n"
"v_mfma_f32_16x16x32_bf16 v[124:127], a[252:255], v[220:223], v[124:127]\n"
"s_waitcnt lgkmcnt(0)\n"
"v_exp_f32 v237, v145 clamp\n"
"v_mfma_f32_16x16x32_bf16 v[160:163], a[192:195], a[128:131], v[192:195]\n"
"s_cmp_lt_u32 s55, 540672\n"
"v_cvt_pk_bf16_f32 v216, v236, v237\n"
"v_mfma_f32_16x16x32_bf16 v[168:171], a[192:195], a[144:147], v[196:199]\n"
"s_cselect_b32 s56, 1073741824, s55\n"
"v_exp_f32 v238, v146 clamp\n"
"v_smfmac_f32_16x16x64_bf16 v[224:227], v[240:243], v[208:215], v247\n"
"s_add_u32 m0, s57, 114688\n"
"v_mfma_f32_16x16x32_bf16 v[176:179], a[192:195], a[160:163], v[200:203]\n"
"s_cmp_eq_u32 s64, s52\n"
"v_exp_f32 v239, v147 clamp\n"
"v_mfma_f32_16x16x32_bf16 v[184:187], a[192:195], a[176:179], v[204:207]\n"
"s_cselect_b32 s58, -1.0, 0.5\n"
"ds_read_b128 a[224:227], v245 offset:8192\n"
"v_cvt_pk_bf16_f32 v217, v238, v239\n"
"v_mfma_f32_16x16x32_bf16 v[160:163], a[196:199], a[132:135], v[160:163]\n"
"v_exp_f32 v236, v148 clamp\n"
"v_mfma_f32_16x16x32_bf16 v[168:171], a[196:199], a[148:151], v[168:171]\n"
"v_exp_f32 v237, v149 clamp\n"
"v_mfma_f32_16x16x32_bf16 v[176:179], a[196:199], a[164:167], v[176:179]\n"
"ds_read_b128 a[228:231], v245 offset:9216\n"
"v_cvt_pk_bf16_f32 v218, v236, v237\n"
"v_mfma_f32_16x16x32_bf16 v[184:187], a[196:199], a[180:183], v[184:187]\n"
"v_exp_f32 v238, v150 clamp\n"
"v_mfma_f32_16x16x32_bf16 v[160:163], a[200:203], a[136:139], v[160:163]\n"
"v_exp_f32 v239, v151 clamp\n"
"v_mfma_f32_16x16x32_bf16 v[168:171], a[200:203], a[152:155], v[168:171]\n"
"ds_read_b128 a[232:235], v245 offset:10240\n"
"v_cvt_pk_bf16_f32 v219, v238, v239\n"
"v_mfma_f32_16x16x32_bf16 v[176:179], a[200:203], a[168:171], v[176:179]\n"
"v_exp_f32 v236, v152 clamp\n"
"v_mfma_f32_16x16x32_bf16 v[184:187], a[200:203], a[184:187], v[184:187]\n"
"buffer_load_dwordx4 v246, s[40:43], s56 offen offset:0 sc1 lds\n"
"v_mfma_f32_16x16x32_bf16 v[160:163], a[204:207], a[140:143], v[160:163]\n"
"v_exp_f32 v237, v153 clamp\n"
"v_mfma_f32_16x16x32_bf16 v[168:171], a[204:207], a[156:159], v[168:171]\n"
"ds_read_b128 a[236:239], v245 offset:11264\n"
"v_cvt_pk_bf16_f32 v220, v236, v237\n"
"v_mfma_f32_16x16x32_bf16 v[176:179], a[204:207], a[172:175], v[176:179]\n"
"v_exp_f32 v238, v154 clamp\n"
"v_mfma_f32_16x16x32_bf16 v[184:187], a[204:207], a[188:191], v[184:187]\n"
"v_exp_f32 v239, v155 clamp\n"
"v_mfma_f32_16x16x32_bf16 v[164:167], a[208:211], a[128:131], v[192:195]\n"
"ds_read_b128 a[240:243], v245 offset:12288\n"
"v_cvt_pk_bf16_f32 v221, v238, v239\n"
"v_mfma_f32_16x16x32_bf16 v[172:175], a[208:211], a[144:147], v[196:199]\n"
"v_exp_f32 v236, v156 clamp\n"
"v_mfma_f32_16x16x32_bf16 v[180:183], a[208:211], a[160:163], v[200:203]\n"
"v_exp_f32 v237, v157 clamp\n"
"v_mfma_f32_16x16x32_bf16 v[188:191], a[208:211], a[176:179], v[204:207]\n"
"ds_read_b128 a[244:247], v245 offset:13312\n"
"v_cvt_pk_bf16_f32 v222, v236, v237\n"
"v_mfma_f32_16x16x32_bf16 v[164:167], a[212:215], a[132:135], v[164:167]\n"
"v_exp_f32 v238, v158 clamp\n"
"v_mfma_f32_16x16x32_bf16 v[172:175], a[212:215], a[148:151], v[172:175]\n"
"buffer_load_dwordx4 v246, s[40:43], s56 offen offset:1024 sc1 lds\n"
"v_exp_f32 v239, v159 clamp\n"
"v_mfma_f32_16x16x32_bf16 v[180:183], a[212:215], a[164:167], v[180:183]\n"
"ds_read_b128 a[248:251], v245 offset:14336\n"
"v_cvt_pk_bf16_f32 v223, v238, v239\n"
"v_mfma_f32_16x16x32_bf16 v[188:191], a[212:215], a[180:183], v[188:191]\n"
"s_nop 0\n"
"v_smfmac_f32_16x16x64_bf16 v[228:231], v[240:243], v[216:223], v247\n"
"ds_read_b128 a[252:255], v245 offset:15360\n"
"v_mfma_f32_16x16x32_bf16 v[164:167], a[216:219], a[136:139], v[164:167]\n"
"v_max3_f32 v236, v224, v225, v226\n"
"v_mfma_f32_16x16x32_bf16 v[172:175], a[216:219], a[152:155], v[172:175]\n"
"buffer_load_dwordx4 v246, s[40:43], s56 offen offset:2048 sc1 lds\n"
"v_mfma_f32_16x16x32_bf16 v[180:183], a[216:219], a[168:171], v[180:183]\n"
"v_mfma_f32_16x16x32_bf16 v[188:191], a[216:219], a[184:187], v[188:191]\n"
"s_nop 0\n"
"v_max3_f32 v236, v236, v227, v228\n"
"v_max3_f32 v236, v236, v229, v230\n"
"v_mfma_f32_16x16x32_bf16 v[164:167], a[220:223], a[140:143], v[164:167]\n"
"v_max_f32 v236, v236, v231\n"
"v_cmp_nge_f32 vcc, s58, v236\n"
"v_mfma_f32_16x16x32_bf16 v[172:175], a[220:223], a[156:159], v[172:175]\n"
"v_mfma_f32_16x16x32_bf16 v[180:183], a[220:223], a[172:175], v[180:183]\n"
"s_waitcnt lgkmcnt(0)\n"
"v_mfma_f32_16x16x32_bf16 v[188:191], a[220:223], a[188:191], v[188:191]\n"
"s_cbranch_vccnz .LstubV0_%=\n"
".LbackV0_%=:\n"
"buffer_load_dwordx4 v246, s[40:43], s56 offen offset:3072 sc1 lds\n"
".Lper1_%=:\n"
"v_mfma_f32_16x16x32_bf16 v[0:3], a[224:227], v[208:211], v[0:3]\n"
"s_waitcnt vmcnt(16)\n"
"v_exp_f32 v236, v160 clamp\n"
"v_mfma_f32_16x16x32_bf16 v[16:19], a[228:231], v[208:211], v[16:19]\n"
"s_barrier\n"
"v_mfma_f32_16x16x32_bf16 v[32:35], a[232:235], v[208:211], v[32:35]\n"
"s_add_u32 s52, s52, 1\n"
"v_exp_f32 v237, v161 clamp\n"
"v_mfma_f32_16x16x32_bf16 v[48:51], a[236:239], v[208:211], v[48:51]\n"
"s_add_u32 m0, s57, 0\n"
"ds_read_b128 a[192:195], v244 offset:16384\n"
"v_mfma_f32_16x16x32_bf16 v[64:67], a[240:243], v[208:211], v[64:67]\n"
"v_exp_f32 v238, v162 clamp\n"
"v_mfma_f32_16x16x32_bf16 v[80:83], a[244:247], v[208:211], v[80:83]\n"
"s_sub_u32 s55, s55, s29\n"
"v_exp_f32 v239, v163 clamp\n"
"v_mfma_f32_16x16x32_bf16 v[96:99], a[248:251], v[208:211], v[96:99]\n"
"buffer_load_dwordx4 v246, s[36:39], s54 offen offset:0 sc1 lds\n"
"v_mfma_f32_16x16x32_bf16 v[112:115], a[252:255], v[208:211], v[112:115]\n"
"ds_read_b128 a[196:199], v244 offset:17408\n"
"v_cvt_pk_bf16_f32 v208, v236, v237\n"
"v_mfma_f32_16x16x32_bf16 v[4:7], a[224:227], v[212:215], v[4:7]\n"
"ds_read_b128 a[200:203], v244 offset:18432\n"
"v_cvt_pk_bf16_f32 v209, v238, v239\n"
"v_mfma_f32_16x16x32_bf16 v[20:23], a[228:231], v[212:215], v[20:23]\n"
"v_exp_f32 v236, v164 clamp\n"
"v_mfma_f32_16x16x32_bf16 v[36:39], a[232:235], v[212:215], v[36:39]\n"
"v_exp_f32 v237, v165 clamp\n"
"v_mfma_f32_16x16x32_bf16 v[52:55], a[236:239], v[212:215], v[52:55]\n"
"ds_read_b128 a[204:207], v244 offset:19456\n"
"v_cvt_pk_bf16_f32 v210, v236, v237\n"
"v_mfma_f32_16x16x32_bf16 v[68:71], a[240:243], v[212:215], v[68:71]\n"
"v_exp_f32 v238, v166 clamp\n"
"v_mfma_f32_16x16x32_bf16 v[84:87], a[244:247], v[212:215], v[84:87]\n"
"v_exp_f32 v239, v167 clamp\n"
"v_mfma_f32_16x16x32_bf16 v[100:103], a[248:251], v[212:215], v[100:103]\n"
"buffer_load_dwordx4 v246, s[36:39], s54 offen offset:1024 sc1 lds\n"
"v_mfma_f32_16x16x32_bf16 v[116:119], a[252:255], v[212:215], v[116:119]\n"
"ds_read_b128 a[208:211], v244 offset:20480\n"
"v_cvt_pk_bf16_f32 v211, v238, v239\n"
"v_mfma_f32_16x16x32_bf16 v[8:11], a[224:227], v[216:219], v[8:11]\n"
"v_exp_f32 v236, v168 clamp\n"
"v_mfma_f32_16x16x32_bf16 v[24:27], a[228:231], v[216:219], v[24:27]\n"
"v_exp_f32 v237, v169 clamp\n"
"v_mfma_f32_16x16x32_bf16 v[40:43], a[232:235], v[216:219], v[40:43]\n"
"ds_read_b128 a[212:215], v244 offset:21504\n"
"v_cvt_pk_bf16_f32 v212, v236, v237\n"
"v_mfma_f32_16x16x32_bf16 v[56:59], a[236:239], v[216:219], v[56:59]\n"
"v_exp_f32 v238, v170 clamp\n"
"v_mfma_f32_16x16x32_bf16 v[72:75], a[240:243], v[216:219], v[72:75]\n"
"v_exp_f32 v239, v171 clamp\n"
"v_mfma_f32_16x16x32_bf16 v[88:91], a[244:247], v[216:219], v[88:91]\n"
"buffer_load_dwordx4 v246, s[36:39], s54 offen offset:2048 sc1 lds\n"
"v_mfma_f32_16x16x32_bf16 v[104:107], a[248:251], v[216:219], v[104:107]\n"
"ds_read_b128 a[216:219], v244 offset:22528\n"
"v_cvt_pk_bf16_f32 v213, v238, v239\n"
"v_mfma_f32_16x16x32_bf16 v[120:123], a[252:255], v[216:219], v[120:123]\n"
"v_exp_f32 v236, v172 clamp\n"
"v_mfma_f32_16x16x32_bf16 v[12:15], a[224:227], v[220:223], v[12:15]\n"
"v_exp_f32 v237, v173 clamp\n"
"v_mfma_f32_16x16x32_bf16 v[28:31], a[228:231], v[220:223], v[28:31]\n"
"ds_read_b128 a[220:223], v244 offset:23552\n"
"v_cvt_pk_bf16_f32 v214, v236, v237\n"
"v_mfma_f32_16x16x32_bf16 v[44:47], a[232:235], v[220:223], v[44:47]\n"
"v_exp_f32 v238, v174 clamp\n"
"v_mfma_f32_16x16x32_bf16 v[60:63], a[236:239], v[220:223], v[60:63]\n"
"v_exp_f32 v239, v175 clamp\n"
"v_mfma_f32_16x16x32_bf16 v[76:79], a[240:243], v[220:223], v[76:79]\n"
"buffer_load_dwordx4 v246, s[36:39], s54 offen offset:3072 sc1 lds\n"
"v_mfma_f32_16x16x32_bf16 v[92:95], a[244:247], v[220:223], v[92:95]\n"
"s_add_u32 s54, s54, s28\n"
"v_cvt_pk_bf16_f32 v215, v238, v239\n"
"v_mfma_f32_16x16x32_bf16 v[108:111], a[248:251], v[220:223], v[108:111]\n"
"v_exp_f32 v236, v176 clamp\n"
"v_mfma_f32_16x16x32_bf16 v[124:127], a[252:255], v[220:223], v[124:127]\n"
"s_waitcnt lgkmcnt(0)\n"
"v_exp_f32 v237, v177 clamp\n"
".Lmid1_%=:\n"
"v_mfma_f32_16x16x32_bf16 v[128:131], a[192:195], a[128:131], v[192:195]\n"
"s_cmp_lt_u32 s55, 540672\n"
"v_cvt_pk_bf16_f32 v216, v236, v237\n"
"v_mfma_f32_16x16x32_bf16 v[136:139], a[192:195], a[144:147], v[196:199]\n"
"s_cselect_b32 s56, 1073741824, s55\n"
"v_exp_f32 v238, v178 clamp\n"
"v_smfmac_f32_16x16x64_bf16 v[224:227], v[240:243], v[208:215], v247\n"
"s_add_u32 m0, s57, 65536\n"
"v_mfma_f32_16x16x32_bf16 v[144:147], a[192:195], a[160:163], v[200:203]\n"
"s_cmp_eq_u32 s64, s52\n"
"v_exp_f32 v239, v179 clamp\n"
"v_mfma_f32_16x16x32_bf16 v[152:155], a[192:195], a[176:179], v[204:207]\n"
"s_cselect_b32 s58, -1.0, 0.5\n"
"ds_read_b128 a[224:227], v245 offset:24576\n"
"v_cvt_pk_bf16_f32 v217, v238, v239\n"
"v_mfma_f32_16x16x32_bf16 v[128:131], a[196:199], a[132:135], v[128:131]\n"
"s_cmp_eq_u32 s52, 30\n"
"v_exp_f32 v236, v180 clamp\n"
"v_mfma_f32_16x16x32_bf16 v[136:139], a[196:199], a[148:151], v[136:139]\n"
"s_cselect_b32 s54, s30, s54\n"
"v_exp_f32 v237, v181 clamp\n"
"v_mfma_f32_16x16x32_bf16 v[144:147], a[196:199], a[164:167], v[144:147]\n"
"s_cselect_b32 s28, 0x1000, s28\n"
"ds_read_b128 a[228:231], v245 offset:25600\n"
"v_cvt_pk_bf16_f32 v218, v236, v237\n"
"v_mfma_f32_16x16x32_bf16 v[152:155], a[196:199], a[180:183], v[152:155]\n"
"v_exp_f32 v238, v182 clamp\n"
"v_mfma_f32_16x16x32_bf16 v[128:131], a[200:203], a[136:139], v[128:131]\n"
"v_exp_f32 v239, v183 clamp\n"
"v_mfma_f32_16x16x32_bf16 v[136:139], a[200:203], a[152:155], v[136:139]\n"
"ds_read_b128 a[232:235], v245 offset:26624\n"
"v_cvt_pk_bf16_f32 v219, v238, v239\n"
"v_mfma_f32_16x16x32_bf16 v[144:147], a[200:203], a[168:171], v[144:147]\n"
"v_exp_f32 v236, v184 clamp\n"
"v_mfma_f32_16x16x32_bf16 v[152:155], a[200:203], a[184:187], v[152:155]\n"
"buffer_load_dwordx4 v246, s[40:43], s56 offen offset:0 sc1 lds\n"
"v_mfma_f32_16x16x32_bf16 v[128:131], a[204:207], a[140:143], v[128:131]\n"
"v_exp_f32 v237, v185 clamp\n"
"v_mfma_f32_16x16x32_bf16 v[136:139], a[204:207], a[156:159], v[136:139]\n"
"ds_read_b128 a[236:239], v245 offset:27648\n"
"v_cvt_pk_bf16_f32 v220, v236, v237\n"
"v_mfma_f32_16x16x32_bf16 v[144:147], a[204:207], a[172:175], v[144:147]\n"
"v_exp_f32 v238, v186 clamp\n"
"v_mfma_f32_16x16x32_bf16 v[152:155], a[204:207], a[188:191], v[152:155]\n"
"v_exp_f32 v239, v187 clamp\n"
"v_mfma_f32_16x16x32_bf16 v[132:135], a[208:211], a[128:131], v[192:195]\n"
"ds_read_b128 a[240:243], v245 offset:28672\n"
"v_cvt_pk_bf16_f32 v221, v238, v239\n"
"v_mfma_f32_16x16x32_bf16 v[140:143], a[208:211], a[144:147], v[196:199]\n"
"v_exp_f32 v236, v188 clamp\n"
"v_mfma_f32_16x16x32_bf16 v[148:151], a[208:211], a[160:163], v[200:203]\n"
"v_exp_f32 v237, v189 clamp\n"
"v_mfma_f32_16x16x32_bf16 v[156:159], a[208:211], a[176:179], v[204:207]\n"
"ds_read_b128 a[244:247], v245 offset:29696\n"
"v_cvt_pk_bf16_f32 v222, v236, v237\n"
"v_mfma_f32_16x16x32_bf16 v[132:135], a[212:215], a[132:135], v[132:135]\n"
"v_exp_f32 v238, v190 clamp\n"
"v_mfma_f32_16x16x32_bf16 v[140:143], a[212:215], a[148:151], v[140:143]\n"
"buffer_load_dwordx4 v246, s[40:43], s56 offen offset:1024 sc1 lds\n"
"v_exp_f32 v239, v191 clamp\n"
"v_mfma_f32_16x16x32_bf16 v[148:151], a[212:215], a[164:167], v[148:151]\n"
"ds_read_b128 a[248:251], v245 offset:30720\n"
"v_cvt_pk_bf16_f32 v223, v238, v239\n"
"v_mfma_f32_16x16x32_bf16 v[156:159], a[212:215], a[180:183], v[156:159]\n"
"s_nop 0\n"
"v_smfmac_f32_16x16x64_bf16 v[228:231], v[240:243], v[216:223], v247\n"
"ds_read_b128 a[252:255], v245 offset:31744\n"
"v_mfma_f32_16x16x32_bf16 v[132:135], a[216:219], a[136:139], v[132:135]\n"
"v_max3_f32 v236, v224, v225, v226\n"
"v_mfma_f32_16x16x32_bf16 v[140:143], a[216:219], a[152:155], v[140:143]\n"
"buffer_load_dwordx4 v246, s[40:43], s56 offen offset:2048 sc1 lds\n"
"v_mfma_f32_16x16x32_bf16 v[148:151], a[216:219], a[168:171], v[148:151]\n"
"v_mfma_f32_16x16x32_bf16 v[156:159], a[216:219], a[184:187], v[156:159]\n"
"s_nop 0\n"
"v_max3_f32 v236, v236, v227, v228\n"
"v_max3_f32 v236, v236, v229, v230\n"
"v_mfma_f32_16x16x32_bf16 v[132:135], a[220:223], a[140:143], v[132:135]\n"
"v_max_f32 v236, v236, v231\n"
"v_cmp_nge_f32 vcc, s58, v236\n"
"v_mfma_f32_16x16x32_bf16 v[140:143], a[220:223], a[156:159], v[140:143]\n"
"v_mfma_f32_16x16x32_bf16 v[148:151], a[220:223], a[172:175], v[148:151]\n"
"s_waitcnt lgkmcnt(0)\n"
"v_mfma_f32_16x16x32_bf16 v[156:159], a[220:223], a[188:191], v[156:159]\n"
"s_cbranch_vccnz .LstubV1_%=\n"
".LbackV1_%=:\n"
"buffer_load_dwordx4 v246, s[40:43], s56 offen offset:3072 sc1 lds\n"
".Lper2_%=:\n"
"v_mfma_f32_16x16x32_bf16 v[0:3], a[224:227], v[208:211], v[0:3]\n"
"s_waitcnt vmcnt(16)\n"
"s_cmp_eq_u32 s52, s83\n"
"v_exp_f32 v236, v128 clamp\n"
"v_mfma_f32_16x16x32_bf16 v[16:19], a[228:231], v[208:211], v[16:19]\n"
"s_barrier\n"
"s_cbranch_scc1 .Lsw_%=\n"
".Lret2_%=:\n"
"v_mfma_f32_16x16x32_bf16 v[32:35], a[232:235], v[208:211], v[32:35]\n"
"s_add_u32 s52, s52, 1\n"
"v_exp_f32 v237, v129 clamp\n"
"v_mfma_f32_16x16x32_bf16 v[48:51], a[236:239], v[208:211], v[48:51]\n"
"s_add_u32 m0, s57, 16384\n"
"ds_read_b128 a[192:195], v244 offset:32768\n"
"v_mfma_f32_16x16x32_bf16 v[64:67], a[240:243], v[208:211], v[64:67]\n"
"v_exp_f32 v238, v130 clamp\n"
"v_mfma_f32_16x16x32_bf16 v[80:83], a[244:247], v[208:211], v[80:83]\n"
"s_sub_u32 s55, s55, s29\n"
"v_exp_f32 v239, v131 clamp\n"
"v_mfma_f32_16x16x32_bf16 v[96:99], a[248:251], v[208:211], v[96:99]\n"
"s_add_u32 s59, s30, 12288\n"
"buffer_load_dwordx4 v246, s[36:39], s54 offen offset:0 sc1 lds\n"
"v_mfma_f32_16x16x32_bf16 v[112:115], a[252:255], v[208:211], v[112:115]\n"
"s_add_u32 s56, s38, 8192\n"
"ds_read_b128 a[196:199], v244 offset:33792\n"
"v_cvt_pk_bf16_f32 v208, v236, v237\n"
"v_mfma_f32_16x16x32_bf16 v[4:7], a[224:227], v[212:215], v[4:7]\n"
"s_cmp_eq_u32 s52, 31\n"
"ds_read_b128 a[200:203], v244 offset:34816\n"
"v_cvt_pk_bf16_f32 v209, v238, v239\n"
"v_mfma_f32_16x16x32_bf16 v[20:23], a[228:231], v[212:215], v[20:23]\n"
"s_cselect_b32 s55, s59, s55\n"
"v_exp_f32 v236, v132 clamp\n"
"v_mfma_f32_16x16x32_bf16 v[36:39], a[232:235], v[212:215], v[36:39]\n"
"s_cselect_b32 s42, s56, s42\n"
"v_exp_f32 v237, v133 clamp\n"
"v_mfma_f32_16x16x32_bf16 v[52:55], a[236:239], v[212:215], v[52:55]\n"
"s_cselect_b32 s29, 0xfffff000, s29\n"
"ds_read_b128 a[204:207], v244 offset:35840\n"
"v_cvt_pk_bf16_f32 v210, v236, v237\n"
"v_mfma_f32_16x16x32_bf16 v[68:71], a[240:243], v[212:215], v[68:71]\n"
".Lpad16_%=:\n"
"s_cbranch_execz .Lpadinit_%=\n"
"v_exp_f32 v238, v134 clamp\n"
"v_mfma_f32_16x16x32_bf16 v[84:87], a[244:247], v[212:215], v[84:87]\n"
"v_exp_f32 v239, v135 clamp\n"
"v_mfma_f32_16x16x32_bf16 v[100:103], a[248:251], v[212:215], v[100:103]\n"
"buffer_load_dwordx4 v246, s[36:39], s54 offen offset:1024 sc1 lds\n"
"v_mfma_f32_16x16x32_bf16 v[116:119], a[252:255], v[212:215], v[116:119]\n"
"ds_read_b128 a[208:211], v244 offset:36864\n"
"v_cvt_pk_bf16_f32 v211, v238, v239\n"
"v_mfma_f32_16x16x32_bf16 v[8:11], a[224:227], v[216:219], v[8:11]\n"
"v_exp_f32 v236, v136 clamp\n"
"v_mfma_f32_16x16x32_bf16 v[24:27], a[228:231], v[216:219], v[24:27]\n"
"v_exp_f32 v237, v137 clamp\n"
"v_mfma_f32_16x16x32_bf16 v[40:43], a[232:235], v[216:219], v[40:43]\n"
"ds_read_b128 a[212:215], v244 offset:37888\n"
"v_cvt_pk_bf16_f32 v212, v236, v237\n"
"v_mfma_f32_16x16x32_bf16 v[56:59], a[236:239], v[216:219], v[56:59]\n"
"v_exp_f32 v238, v138 clamp\n"
"v_mfma_f32_16x16x32_bf16 v[72:75], a[240:243], v[216:219], v[72:75]\n"
"v_exp_f32 v239, v139 clamp\n"
"v_mfma_f32_16x16x32_bf16 v[88:91], a[244:247], v[216:219], v[88:91]\n"
"buffer_load_dwordx4 v246, s[36:39], s54 offen offset:2048 sc1 lds\n"
"v_mfma_f32_16x16x32_bf16 v[104:107], a[248:251], v[216:219], v[104:107]\n"
"ds_read_b128 a[216:219], v244 offset:38912\n"
"v_cvt_pk_bf16_f32 v213, v238, v239\n"
"v_mfma_f32_16x16x32_bf16 v[120:123], a[252:255], v[216:219], v[120:123]\n"
"v_exp_f32 v236, v140 clamp\n"
"v_mfma_f32_16x16x32_bf16 v[12:15], a[224:227], v[220:223], v[12:15]\n"
"v_exp_f32 v237, v141 clamp\n"
"v_mfma_f32_16x16x32_bf16 v[28:31], a[228:231], v[220:223], v[28:31]\n"
"ds_read_b128 a[220:223], v244 offset:39936\n"
"v_cvt_pk_bf16_f32 v214, v236, v237\n"
"v_mfma_f32_16x16x32_bf16 v[44:47], a[232:235], v[220:223], v[44:47]\n"
"v_exp_f32 v238, v142 clamp\n"
"v_mfma_f32_16x16x32_bf16 v[60:63], a[236:239], v[220:223], v[60:63]\n"
"v_exp_f32 v239, v143 clamp\n"
"v_mfma_f32_16x16x32_bf16 v[76:79], a[240:243], v[220:223], v[76:79]\n"
"buffer_load_dwordx4 v246, s[36:39], s54 offen offset:3072 sc1 lds\n"
"v_mfma_f32_16x16x32_bf16 v[92:95], a[244:247], v[220:223], v[92:95]\n"
"s_add_u32 s54, s54, s28\n"
"v_cvt_pk_bf16_f32 v215, v238, v239\n"
"v_mfma_f32_16x16x32_bf16 v[108:111], a[248:251], v[220:223], v[108:111]\n"
"v_exp_f32 v236, v144 clamp\n"
"v_mfma_f32_16x16x32_bf16 v[124:127], a[252:255], v[220:223], v[124:127]\n"
"s_waitcnt lgkmcnt(0)\n"
"v_exp_f32 v237, v145 clamp\n"
"v_mfma_f32_16x16x32_bf16 v[160:163], a[192:195], a[128:131], v[192:195]\n"
"s_cmp_lt_u32 s55, 540672\n"
"v_cvt_pk_bf16_f32 v216, v236, v237\n"
"v_mfma_f32_16x16x32_bf16 v[168:171], a[192:195], a[144:147], v[196:199]\n"
"s_cselect_b32 s56, 1073741824, s55\n"
"v_exp_f32 v238, v146 clamp\n"
"v_smfmac_f32_16x16x64_bf16 v[224:227], v[240:243], v[208:215], v247\n"
"s_add_u32 m0, s57, 81920\n"
"v_mfma_f32_16x16x32_bf16 v[176:179], a[192:195], a[160:163], v[200:203]\n"
"s_cmp_eq_u32 s64, s52\n"
"v_exp_f32 v239, v147 clamp\n"
"v_mfma_f32_16x16x32_bf16 v[184:187], a[192:195], a[176:179], v[204:207]\n"
"s_cselect_b32 s58, -1.0, 0.5\n"
"ds_read_b128 a[224:227], v245 offset:40960\n"
"v_cvt_pk_bf16_f32 v217, v238, v239\n"
"v_mfma_f32_16x16x32_bf16 v[160:163], a[196:199], a[132:135], v[160:163]\n"
"v_exp_f32 v236, v148 clamp\n"
"v_mfma_f32_16x16x32_bf16 v[168:171], a[196:199], a[148:151], v[168:171]\n"
"v_exp_f32 v237, v149 clamp\n"
"v_mfma_f32_16x16x32_bf16 v[176:179], a[196:199], a[164:167], v[176:179]\n"
"ds_read_b128 a[228:231], v245 offset:41984\n"
"v_cvt_pk_bf16_f32 v218, v236, v237\n"
"v_mfma_f32_16x16x32_bf16 v[184:187], a[196:199], a[180:183], v[184:187]\n"
"v_exp_f32 v238, v150 clamp\n"
"v_mfma_f32_16x16x32_bf16 v[160:163], a[200:203], a[136:139], v[160:163]\n"
"v_exp_f32 v239, v151 clamp\n"
"v_mfma_f32_16x16x32_bf16 v[168:171], a[200:203], a[152:155], v[168:171]\n"
"ds_read_b128 a[232:235], v245 offset:43008\n"
"v_cvt_pk_bf16_f32 v219, v238, v239\n"
"v_mfma_f32_16x16x32_bf16 v[176:179], a[200:203], a[168:171], v[176:179]\n"
"v_exp_f32 v236, v152 clamp\n"
"v_mfma_f32_16x16x32_bf16 v[184:187], a[200:203], a[184:187], v[184:187]\n"
"buffer_load_dwordx4 v246, s[40:43], s56 offen offset:0 sc1 lds\n"
"v_mfma_f32_16x16x32_bf16 v[160:163], a[204:207], a[140:143], v[160:163]\n"
"v_exp_f32 v237, v153 clamp\n"
"v_mfma_f32_16x16x32_bf16 v[168:171], a[204:207], a[156:159], v[168:171]\n"
"ds_read_b128 a[236:239], v245 offset:44032\n"
"v_cvt_pk_bf16_f32 v220, v236, v237\n"
"v_mfma_f32_16x16x32_bf16 v[176:179], a[204:207], a[172:175], v[176:179]\n"
"v_exp_f32 v238, v154 clamp\n"
"v_mfma_f32_16x16x32_bf16 v[184:187], a[204:207], a[188:191], v[184:187]\n"
"v_exp_f32 v239, v155 clamp\n"
"v_mfma_f32_16x16x32_bf16 v[164:167], a[208:211], a[128:131], v[192:195]\n"
"ds_read_b128 a[240:243], v245 offset:45056\n"
"v_cvt_pk_bf16_f32 v221, v238, v239\n"
"v_mfma_f32_16x16x32_bf16 v[172:175], a[208:211], a[144:147], v[196:199]\n"
"v_exp_f32 v236, v156 clamp\n"
"v_mfma_f32_16x16x32_bf16 v[180:183], a[208:211], a[160:163], v[200:203]\n"
"v_exp_f32 v237, v157 clamp\n"
"v_mfma_f32_16x16x32_bf16 v[188:191], a[208:211], a[176:179], v[204:207]\n"
"ds_read_b128 a[244:247], v245 offset:46080\n"
"v_cvt_pk_bf16_f32 v222, v236, v237\n"
"v_mfma_f32_16x16x32_bf16 v[164:167], a[212:215], a[132:135], v[164:167]\n"
"v_exp_f32 v238, v158 clamp\n"
"v_mfma_f32_16x16x32_bf16 v[172:175], a[212:215], a[148:151], v[172:175]\n"
"buffer_load_dwordx4 v246, s[40:43], s56 offen offset:1024 sc1 lds\n"
"v_exp_f32 v239, v159 clamp\n"
"v_mfma_f32_16x16x32_bf16 v[180:183], a[212:215], a[164:167], v[180:183]\n"
"ds_read_b128 a[248:251], v245 offset:47104\n"
"v_cvt_pk_bf16_f32 v223, v238, v239\n"
"v_mfma_f32_16x16x32_bf16 v[188:191], a[212:215], a[180:183], v[188:191]\n"
"s_nop 0\n"
"v_smfmac_f32_16x16x64_bf16 v[228:231], v[240:243], v[216:223], v247\n"
"ds_read_b128 a[252:255], v245 offset:48128\n"
"v_mfma_f32_16x16x32_bf16 v[164:167], a[216:219], a[136:139], v[164:167]\n"
"v_max3_f32 v236, v224, v225, v226\n"
"v_mfma_f32_16x16x32_bf16 v[172:175], a[216:219], a[152:155], v[172:175]\n"
"buffer_load_dwordx4 v246, s[40:43], s56 offen offset:2048 sc1 lds\n"
"v_mfma_f32_16x16x32_bf16 v[180:183], a[216:219], a[168:171], v[180:183]\n"
"v_mfma_f32_16x16x32_bf16 v[188:191], a[216:219], a[184:187], v[188:191]\n"
"s_nop 0\n"
"v_max3_f32 v236, v236, v227, v228\n"
"v_max3_f32 v236, v236, v229, v230\n"
"v_mfma_f32_16x16x32_bf16 v[164:167], a[220:223], a[140:143], v[164:167]\n"
"v_max_f32 v236, v236, v231\n"
"v_cmp_nge_f32 vcc, s58, v236\n"
"v_mfma_f32_16x16x32_bf16 v[172:175], a[220:223], a[156:159], v[172:175]\n"
"v_mfma_f32_16x16x32_bf16 v[180:183], a[220:223], a[172:175], v[180:183]\n"
"s_waitcnt lgkmcnt(0)\n"
"v_mfma_f32_16x16x32_bf16 v[188:191], a[220:223], a[188:191], v[188:191]\n"
"s_cbranch_vccnz .LstubV2_%=\n"
".LbackV2_%=:\n"
"buffer_load_dwordx4 v246, s[40:43], s56 offen offset:3072 sc1 lds\n"
".Lper3_%=:\n"
"v_mfma_f32_16x16x32_bf16 v[0:3], a[224:227], v[208:211], v[0:3]\n"
"s_waitcnt vmcnt(16)\n"
"v_exp_f32 v236, v160 clamp\n"
"v_mfma_f32_16x16x32_bf16 v[16:19], a[228:231], v[208:211], v[16:19]\n"
"s_barrier\n"
"v_mfma_f32_16x16x32_bf16 v[32:35], a[232:235], v[208:211], v[32:35]\n"
"s_add_u32 s52, s52, 1\n"
"v_exp_f32 v237, v161 clamp\n"
"v_mfma_f32_16x16x32_bf16 v[48:51], a[236:239], v[208:211], v[48:51]\n"
"s_add_u32 m0, s57, 32768\n"
"ds_read_b128 a[192:195], v244 offset:49152\n"
"v_mfma_f32_16x16x32_bf16 v[64:67], a[240:243], v[208:211], v[64:67]\n"
"v_exp_f32 v238, v162 clamp\n"
"v_mfma_f32_16x16x32_bf16 v[80:83], a[244:247], v[208:211], v[80:83]\n"
"s_sub_u32 s55, s55, s29\n"
"v_exp_f32 v239, v163 clamp\n"
"v_mfma_f32_16x16x32_bf16 v[96:99], a[248:251], v[208:211], v[96:99]\n"
"buffer_load_dwordx4 v246, s[36:39], s54 offen offset:0 sc1 lds\n"
"v_mfma_f32_16x16x32_bf16 v[112:115], a[252:255], v[208:211], v[112:115]\n"
"ds_read_b128 a[196:199], v244 offset:50176\n"
"v_cvt_pk_bf16_f32 v208, v236, v237\n"
"v_mfma_f32_16x16x32_bf16 v[4:7], a[224:227], v[212:215], v[4:7]\n"
"ds_read_b128 a[200:203], v244 offset:51200\n"
"v_cvt_pk_bf16_f32 v209, v238, v239\n"
"v_mfma_f32_16x16x32_bf16 v[20:23], a[228:231], v[212:215], v[20:23]\n"
"v_exp_f32 v236, v164 clamp\n"
"v_mfma_f32_16x16x32_bf16 v[36:39], a[232:235], v[212:215], v[36:39]\n"
"v_exp_f32 v237, v165 clamp\n"
"v_mfma_f32_16x16x32_bf16 v[52:55], a[236:239], v[212:215], v[52:55]\n"
"ds_read_b128 a[204:207], v244 offset:52224\n"
"v_cvt_pk_bf16_f32 v210, v236, v237\n"
"v_mfma_f32_16x16x32_bf16 v[68:71], a[240:243], v[212:215], v[68:71]\n"
"v_exp_f32 v238, v166 clamp\n"
"v_mfma_f32_16x16x32_bf16 v[84:87], a[244:247], v[212:215], v[84:87]\n"
"v_exp_f32 v239, v167 clamp\n"
"v_mfma_f32_16x16x32_bf16 v[100:103], a[248:251], v[212:215], v[100:103]\n"
"buffer_load_dwordx4 v246, s[36:39], s54 offen offset:1024 sc1 lds\n"
"v_mfma_f32_16x16x32_bf16 v[116:119], a[252:255], v[212:215], v[116:119]\n"
"ds_read_b128 a[208:211], v244 offset:53248\n"
"v_cvt_pk_bf16_f32 v211, v238, v239\n"
"v_mfma_f32_16x16x32_bf16 v[8:11], a[224:227], v[216:219], v[8:11]\n"
"v_exp_f32 v236, v168 clamp\n"
"v_mfma_f32_16x16x32_bf16 v[24:27], a[228:231], v[216:219], v[24:27]\n"
"v_exp_f32 v237, v169 clamp\n"
"v_mfma_f32_16x16x32_bf16 v[40:43], a[232:235], v[216:219], v[40:43]\n"
"ds_read_b128 a[212:215], v244 offset:54272\n"
"v_cvt_pk_bf16_f32 v212, v236, v237\n"
"v_mfma_f32_16x16x32_bf16 v[56:59], a[236:239], v[216:219], v[56:59]\n"
"v_exp_f32 v238, v170 clamp\n"
"v_mfma_f32_16x16x32_bf16 v[72:75], a[240:243], v[216:219], v[72:75]\n"
"v_exp_f32 v239, v171 clamp\n"
"v_mfma_f32_16x16x32_bf16 v[88:91], a[244:247], v[216:219], v[88:91]\n"
"buffer_load_dwordx4 v246, s[36:39], s54 offen offset:2048 sc1 lds\n"
"v_mfma_f32_16x16x32_bf16 v[104:107], a[248:251], v[216:219], v[104:107]\n"
"ds_read_b128 a[216:219], v244 offset:55296\n"
"v_cvt_pk_bf16_f32 v213, v238, v239\n"
"v_mfma_f32_16x16x32_bf16 v[120:123], a[252:255], v[216:219], v[120:123]\n"
"v_exp_f32 v236, v172 clamp\n"
"v_mfma_f32_16x16x32_bf16 v[12:15], a[224:227], v[220:223], v[12:15]\n"
"v_exp_f32 v237, v173 clamp\n"
"v_mfma_f32_16x16x32_bf16 v[28:31], a[228:231], v[220:223], v[28:31]\n"
"ds_read_b128 a[220:223], v244 offset:56320\n"
"v_cvt_pk_bf16_f32 v214, v236, v237\n"
"v_mfma_f32_16x16x32_bf16 v[44:47], a[232:235], v[220:223], v[44:47]\n"
"v_exp_f32 v238, v174 clamp\n"
"v_mfma_f32_16x16x32_bf16 v[60:63], a[236:239], v[220:223], v[60:63]\n"
"v_exp_f32 v239, v175 clamp\n"
"v_mfma_f32_16x16x32_bf16 v[76:79], a[240:243], v[220:223], v[76:79]\n"
"buffer_load_dwordx4 v246, s[36:39], s54 offen offset:3072 sc1 lds\n"
"v_mfma_f32_16x16x32_bf16 v[92:95], a[244:247], v[220:223], v[92:95]\n"
"s_add_u32 s54, s54, s28\n"
"v_cvt_pk_bf16_f32 v215, v238, v239\n"
"v_mfma_f32_16x16x32_bf16 v[108:111], a[248:251], v[220:223], v[108:111]\n"
"v_exp_f32 v236, v176 clamp\n"
"v_mfma_f32_16x16x32_bf16 v[124:127], a[252:255], v[220:223], v[124:127]\n"
"s_waitcnt lgkmcnt(0)\n"
"v_exp_f32 v237, v177 clamp\n"
".Lmid3_%=:\n"
".Lpadmid3_%=:\n"
"s_cbranch_execz .Lhopi2_%=\n"
"v_mfma_f32_16x16x32_bf16 v[128:131], a[192:195], a[128:131], v[192:195]\n"
"s_cmp_lt_u32 s55, 540672\n"
"v_cvt_pk_bf16_f32 v216, v236, v237\n"
"v_mfma_f32_16x16x32_bf16 v[136:139], a[192:195], a[144:147], v[196:199]\n"
"s_cselect_b32 s56, 1073741824, s55\n"
"v_exp_f32 v238, v178 clamp\n"
"v_smfmac_f32_16x16x64_bf16 v[224:227], v[240:243], v[208:215], v247\n"
"s_add_u32 m0, s57, 98304\n"
"v_mfma_f32_16x16x32_bf16 v[144:147], a[192:195], a[160:163], v[200:203]\n"
"s_cmp_eq_u32 s64, s52\n"
"v_exp_f32 v239, v179 clamp\n"
"v_mfma_f32_16x16x32_bf16 v[152:155], a[192:195], a[176:179], v[204:207]\n"
"s_cselect_b32 s58, -1.0, 0.5\n"
"ds_read_b128 a[224:227], v245 offset:57344\n"
"v_cvt_pk_bf16_f32 v217, v238, v239\n"
"v_mfma_f32_16x16x32_bf16 v[128:131], a[196:199], a[132:135], v[128:131]\n"
"v_exp_f32 v236, v180 clamp\n"
"v_mfma_f32_16x16x32_bf16 v[136:139], a[196:199], a[148:151], v[136:139]\n"
"v_exp_f32 v237, v181 clamp\n"
"v_mfma_f32_16x16x32_bf16 v[144:147], a[196:199], a[164:167], v[144:147]\n"
"ds_read_b128 a[228:231], v245 offset:58368\n"
"v_cvt_pk_bf16_f32 v218, v236, v237\n"
"v_mfma_f32_16x16x32_bf16 v[152:155], a[196:199], a[180:183], v[152:155]\n"
"v_exp_f32 v238, v182 clamp\n"
"v_mfma_f32_16x16x32_bf16 v[128:131], a[200:203], a[136:139], v[128:131]\n"
"v_exp_f32 v239, v183 clamp\n"
"v_mfma_f32_16x16x32_bf16 v[136:139], a[200:203], a[152:155], v[136:139]\n"
"ds_read_b128 a[232:235], v245 offset:59392\n"
"v_cvt_pk_bf16_f32 v219, v238, v239\n"
"v_mfma_f32_16x16x32_bf16 v[144:147], a[200:203], a[168:171], v[144:147]\n"
"v_exp_f32 v236, v184 clamp\n"
"v_mfma_f32_16x16x32_bf16 v[152:155], a[200:203], a[184:187], v[152:155]\n"
"buffer_load_dwordx4 v246, s[40:43], s56 offen offset:0 sc1 lds\n"
"v_mfma_f32_16x16x32_bf16 v[128:131], a[204:207], a[140:143], v[128:131]\n"
"v_exp_f32 v237, v185 clamp\n"
"v_mfma_f32_16x16x32_bf16 v[136:139], a[204:207], a[156:159], v[136:139]\n"
"ds_read_b128 a[236:239], v245 offset:60416\n"
"v_cvt_pk_bf16_f32 v220, v236, v237\n"
"v_mfma_f32_16x16x32_bf16 v[144:147], a[204:207], a[172:175], v[144:147]\n"
"v_exp_f32 v238, v186 clamp\n"
"v_mfma_f32_16x16x32_bf16 v[152:155], a[204:207], a[188:191], v[152:155]\n"
"v_exp_f32 v239, v187 clamp\n"
"v_mfma_f32_16x16x32_bf16 v[132:135], a[208:211], a[128:131], v[192:195]\n"
"ds_read_b128 a[240:243], v245 offset:61440\n"
"v_cvt_pk_bf16_f32 v221, v238, v239\n"
"v_mfma_f32_16x16x32_bf16 v[140:143], a[208:211], a[144:147], v[196:199]\n"
"v_exp_f32 v236, v188 clamp\n"
"v_mfma_f32_16x16x32_bf16 v[148:151], a[208:211], a[160:163], v[200:203]\n"
"v_exp_f32 v237, v189 clamp\n"
"v_mfma_f32_16x16x32_bf16 v[156:159], a[208:211], a[176:179], v[204:207]\n"
"ds_read_b128 a[244:247], v245 offset:62464\n"
"v_cvt_pk_bf16_f32 v222, v236, v237\n"
"v_mfma_f32_16x16x32_bf16 v[132:135], a[212:215], a[132:135], v[132:135]\n"
"v_exp_f32 v238, v190 clamp\n"
"v_mfma_f32_16x16x32_bf16 v[140:143], a[212:215], a[148:151], v[140:143]\n"
"buffer_load_dwordx4 v246, s[40:43], s56 offen offset:1024 sc1 lds\n"
"v_exp_f32 v239, v191 clamp\n"
"v_mfma_f32_16x16x32_bf16 v[148:151], a[212:215], a[164:167], v[148:151]\n"
"ds_read_b128 a[248:251], v245 offset:63488\n"
"v_cvt_pk_bf16_f32 v223, v238, v239\n"
"v_mfma_f32_16x16x32_bf16 v[156:159], a[212:215], a[180:183], v[156:159]\n"
"s_nop 0\n"
"v_smfmac_f32_16x16x64_bf16 v[228:231], v[240:243], v[216:223], v247\n"
"ds_read_b128 a[252:255], v245 offset:64512\n"
"v_mfma_f32_16x16x32_bf16 v[132:135], a[216:219], a[136:139], v[132:135]\n"
"v_max3_f32 v236, v224, v225, v226\n"
"v_mfma_f32_16x16x32_bf16 v[140:143], a[216:219], a[152:155], v[140:143]\n"
"buffer_load_dwordx4 v246, s[40:43], s56 offen offset:2048 sc1 lds\n"
"v_mfma_f32_16x16x32_bf16 v[148:151], a[216:219], a[168:171], v[148:151]\n"
"v_mfma_f32_16x16x32_bf16 v[156:159], a[216:219], a[184:187], v[156:159]\n"
"s_nop 0\n"
"v_max3_f32 v236, v236, v227, v228\n"
"v_max3_f32 v236, v236, v229, v230\n"
"v_mfma_f32_16x16x32_bf16 v[132:135], a[220:223], a[140:143], v[132:135]\n"
"v_max_f32 v236, v236, v231\n"
"v_cmp_nge_f32 vcc, s58, v236\n"
"v_mfma_f32_16x16x32_bf16 v[140:143], a[220:223], a[156:159], v[140:143]\n"
"v_mfma_f32_16x16x32_bf16 v[148:151], a[220:223], a[172:175], v[148:151]\n"
"s_waitcnt lgkmcnt(0)\n"
"v_mfma_f32_16x16x32_bf16 v[156:159], a[220:223], a[188:191], v[156:159]\n"
"s_cbranch_vccnz .LstubV3_%=\n"
".LbackV3_%=:\n"
"buffer_load_dwordx4 v246, s[40:43], s56 offen offset:3072 sc1 lds\n"
"s_cmp_eq_u32 s52, 32\n"
"s_cbranch_scc0 .LloopV_%=\n"
"v_mfma_f32_16x16x32_bf16 v[0:3], a[224:227], v[208:211], v[0:3]\n"
"s_waitcnt vmcnt(16)\n"
"v_subrev_u32 v248, s57, v246\n"
"v_lshlrev_b32 v238, 5, v246\n"
"v_and_b32 v238, 0x1c00, v238\n"
"v_exp_f32 v232, v128 clamp\n"
"v_mfma_f32_16x16x32_bf16 v[16:19], a[228:231], v[208:211], v[16:19]\n"
"s_barrier\n"
"v_bfe_u32 v236, v246, 4, 1\n"
"v_lshl_add_u32 v238, v236, 2, v238\n"
"v_min_u32 v236, 2052, v238\n"
"v_exp_f32 v233, v129 clamp\n"
"v_mfma_f32_16x16x32_bf16 v[32:35], a[232:235], v[208:211], v[32:35]\n"
"ds_read_b128 v[160:163], v245 offset:8192\n"
"v_exp_f32 v234, v130 clamp\n"
"v_mfma_f32_16x16x32_bf16 v[48:51], a[236:239], v[208:211], v[48:51]\n"
"buffer_load_dword a220, v236, s[84:87], s10 offen sc1\n"
"s_add_u32 s58, s35, s34\n"
"s_add_u32 s58, s58, s31\n"
"v_exp_f32 v235, v131 clamp\n"
"v_mfma_f32_16x16x32_bf16 v[64:67], a[240:243], v[208:211], v[64:67]\n"
"v_exp_f32 v244, v132 clamp\n"
"v_mfma_f32_16x16x32_bf16 v[80:83], a[244:247], v[208:211], v[80:83]\n"
"buffer_load_dwordx4 a[160:163], v248, s[84:87], s58 offen offset:0 sc1\n"
"ds_read_b128 v[164:167], v245 offset:9216\n"
"v_exp_f32 v249, v133 clamp\n"
"v_mfma_f32_16x16x32_bf16 v[96:99], a[248:251], v[208:211], v[96:99]\n"
"v_exp_f32 v250, v134 clamp\n"
"v_mfma_f32_16x16x32_bf16 v[112:115], a[252:255], v[208:211], v[112:115]\n"
"buffer_load_dwordx4 a[164:167], v248, s[84:87], s58 offen offset:1024 sc1\n"
"v_cvt_pk_bf16_f32 v208, v232, v233\n"
"v_cvt_pk_bf16_f32 v209, v234, v235\n"
"v_cvt_pk_bf16_f32 v210, v244, v249\n"
"v_exp_f32 v251, v135 clamp\n"
"v_mfma_f32_16x16x32_bf16 v[4:7], a[224:227], v[212:215], v[4:7]\n"
"ds_read_b128 v[168:171], v245 offset:10240\n"
"v_cvt_pk_bf16_f32 v211, v250, v251\n"
"v_exp_f32 v252, v136 clamp\n"
"v_mfma_f32_16x16x32_bf16 v[20:23], a[228:231], v[212:215], v[20:23]\n"
"v_exp_f32 v253, v137 clamp\n"
"v_mfma_f32_16x16x32_bf16 v[36:39], a[232:235], v[212:215], v[36:39]\n"
"buffer_load_dwordx4 a[168:171], v248, s[84:87], s58 offen offset:2048 sc1\n"
"buffer_load_dwordx4 a[172:175], v248, s[84:87], s58 offen offset:3072 sc1\n"
"s_add_u32 s58, s58, 4096\n"
"v_exp_f32 v254, v138 clamp\n"
"v_mfma_f32_16x16x32_bf16 v[52:55], a[236:239], v[212:215], v[52:55]\n"
"ds_read_b128 v[172:175], v245 offset:11264\n"
"buffer_load_dwordx4 a[176:179], v248, s[84:87], s58 offen offset:0 sc1\n"
"s_add_u32 s58, s35, s34\n"
"v_exp_f32 v255, v139 clamp\n"
"v_mfma_f32_16x16x32_bf16 v[68:71], a[240:243], v[212:215], v[68:71]\n"
"s_add_u32 m0, s57, 49152\n"
"s_add_u32 s52, s52, 1\n"
"v_exp_f32 v232, v140 clamp\n"
"v_mfma_f32_16x16x32_bf16 v[84:87], a[244:247], v[212:215], v[84:87]\n"
"buffer_load_dwordx4 v246, s[36:39], s54 offen offset:0 sc1 lds\n"
"v_exp_f32 v233, v141 clamp\n"
"v_mfma_f32_16x16x32_bf16 v[100:103], a[248:251], v[212:215], v[100:103]\n"
"ds_read_b128 v[176:179], v245 offset:12288\n"
"v_exp_f32 v234, v142 clamp\n"
"v_mfma_f32_16x16x32_bf16 v[116:119], a[252:255], v[212:215], v[116:119]\n"
"buffer_load_dwordx4 v246, s[36:39], s54 offen offset:1024 sc1 lds\n"
"v_cvt_pk_bf16_f32 v212, v252, v253\n"
"v_cvt_pk_bf16_f32 v213, v254, v255\n"
"v_cvt_pk_bf16_f32 v214, v232, v233\n"
"v_exp_f32 v235, v143 clamp\n"
"v_mfma_f32_16x16x32_bf16 v[8:11], a[224:227], v[216:219], v[8:11]\n"
"v_cvt_pk_bf16_f32 v215, v234, v235\n"
"v_exp_f32 v244, v144 clamp\n"
"v_mfma_f32_16x16x32_bf16 v[24:27], a[228:231], v[216:219], v[24:27]\n"
"buffer_load_dwordx4 v246, s[36:39], s54 offen offset:2048 sc1 lds\n"
"ds_read_b128 v[180:183], v245 offset:13312\n"
"v_exp_f32 v249, v145 clamp\n"
"v_mfma_f32_16x16x32_bf16 v[40:43], a[232:235], v[216:219], v[40:43]\n"
"buffer_load_dwordx4 v246, s[36:39], s54 offen offset:3072 sc1 lds\n"
"s_add_u32 s58, s58, 10240\n"
"v_exp_f32 v250, v146 clamp\n"
"v_mfma_f32_16x16x32_bf16 v[56:59], a[236:239], v[216:219], v[56:59]\n"
"v_exp_f32 v251, v147 clamp\n"
"v_smfmac_f32_16x16x64_bf16 v[224:227], v[240:243], v[208:215], v247\n"
"buffer_load_dwordx4 a[120:123], v248, s[84:87], s58 offen offset:0 sc1\n"
"ds_read_b128 v[184:187], v245 offset:14336\n"
"v_exp_f32 v252, v148 clamp\n"
"v_mfma_f32_16x16x32_bf16 v[72:75], a[240:243], v[216:219], v[72:75]\n"
"v_exp_f32 v253, v149 clamp\n"
"v_mfma_f32_16x16x32_bf16 v[88:91], a[244:247], v[216:219], v[88:91]\n"
"buffer_load_dwordx4 a[124:127], v248, s[84:87], s58 offen offset:1024 sc1\n"
"v_exp_f32 v254, v150 clamp\n"
"v_mfma_f32_16x16x32_bf16 v[104:107], a[248:251], v[216:219], v[104:107]\n"
"buffer_load_dwordx4 a[128:131], v248, s[84:87], s58 offen offset:2048 sc1\n"
"ds_read_b128 v[188:191], v245 offset:15360\n"
"v_exp_f32 v255, v151 clamp\n"
"v_mfma_f32_16x16x32_bf16 v[120:123], a[252:255], v[216:219], v[120:123]\n"
"v_cvt_pk_bf16_f32 v216, v244, v249\n"
"v_cvt_pk_bf16_f32 v217, v250, v251\n"
"v_cvt_pk_bf16_f32 v218, v252, v253\n"
"v_exp_f32 v232, v152 clamp\n"
"v_mfma_f32_16x16x32_bf16 v[12:15], a[224:227], v[220:223], v[12:15]\n"
"v_cvt_pk_bf16_f32 v219, v254, v255\n"
"buffer_load_dwordx4 a[132:135], v248, s[84:87], s58 offen offset:3072 sc1\n"
"s_add_u32 s58, s58, 4096\n"
"v_exp_f32 v233, v153 clamp\n"
"v_mfma_f32_16x16x32_bf16 v[28:31], a[228:231], v[220:223], v[28:31]\n"
"buffer_load_dwordx4 a[136:139], v248, s[84:87], s58 offen offset:0 sc1\n"
"s_add_u32 s58, s35, s34\n"
"s_add_u32 s58, s58, s31\n"
"v_exp_f32 v234, v154 clamp\n"
"v_mfma_f32_16x16x32_bf16 v[44:47], a[232:235], v[220:223], v[44:47]\n"
"s_add_u32 s58, s58, 5120\n"
"v_exp_f32 v235, v155 clamp\n"
"v_mfma_f32_16x16x32_bf16 v[60:63], a[236:239], v[220:223], v[60:63]\n"
"buffer_load_dwordx4 a[180:183], v248, s[84:87], s58 offen offset:0 sc1\n"
"v_exp_f32 v244, v156 clamp\n"
"v_mfma_f32_16x16x32_bf16 v[76:79], a[240:243], v[220:223], v[76:79]\n"
"v_exp_f32 v249, v157 clamp\n"
"v_mfma_f32_16x16x32_bf16 v[92:95], a[244:247], v[220:223], v[92:95]\n"
"buffer_load_dwordx4 a[184:187], v248, s[84:87], s58 offen offset:1024 sc1\n"
"v_exp_f32 v250, v158 clamp\n"
"v_mfma_f32_16x16x32_bf16 v[108:111], a[248:251], v[220:223], v[108:111]\n"
"v_exp_f32 v251, v159 clamp\n"
"v_mfma_f32_16x16x32_bf16 v[124:127], a[252:255], v[220:223], v[124:127]\n"
"buffer_load_dwordx4 a[188:191], v248, s[84:87], s58 offen offset:2048 sc1\n"
"v_cvt_pk_bf16_f32 v220, v232, v233\n"
"v_cvt_pk_bf16_f32 v221, v234, v235\n"
"v_cvt_pk_bf16_f32 v222, v244, v249\n"
"v_cvt_pk_bf16_f32 v223, v250, v251\n"
"s_nop 1\n"
"v_smfmac_f32_16x16x64_bf16 v[228:231], v[240:243], v[216:223], v247\n"
"buffer_load_dwordx4 a[192:195], v248, s[84:87], s58 offen offset:3072 sc1\n"
"s_add_u32 s58, s58, 4096\n"
"buffer_load_dwordx4 a[196:199], v248, s[84:87], s58 offen offset:0 sc1\n"
"v_max3_f32 v249, v224, v225, v226\n"
"s_nop 3\n"
"v_max3_f32 v249, v249, v227, v228\n"
"v_max3_f32 v249, v249, v229, v230\n"
"v_max_f32 v249, v249, v231\n"
"v_cmp_nge_f32 vcc, 0.5, v249\n"
"s_waitcnt lgkmcnt(0)\n"
"s_cbranch_vccnz .LstubSF_%=\n"
".LbackSF_%=:\n"
".Lend_%=:\n"
"v_mfma_f32_16x16x32_bf16 v[8:11], v[160:163], v[216:219], v[8:11]\n"
"s_lshl_b32 s56, s76, 10\n"
"s_add_u32 s56, s56, 147456\n"
"v_add_f32 v228, v228, v229\n"
"v_add_f32 v229, v230, v231\n"
"v_mfma_f32_16x16x32_bf16 v[24:27], v[164:167], v[216:219], v[24:27]\n"
"v_mov_b32 v230, v200\n"
"v_mov_b32 v231, v204\n"
"v_add_u32 v242, s56, v248\n"
"v_mfma_f32_16x16x32_bf16 v[40:43], v[168:171], v[216:219], v[40:43]\n"
"s_waitcnt vmcnt(19)\n"
"ds_write_b128 v242, v[228:231]\n"
"v_xor_b32 v242, 2048, v242\n"
"v_mfma_f32_16x16x32_bf16 v[56:59], v[172:175], v[216:219], v[56:59]\n"
"v_add_u32 v240, s22, v248\n"
"v_add_u32 v241, s23, v248\n"
"v_accvgpr_read_b32 v237, a220\n"
"v_mfma_f32_16x16x32_bf16 v[72:75], v[176:179], v[216:219], v[72:75]\n"
"v_cmp_ne_u32 vcc, s4, v237\n"
"s_cbranch_vccnz .Lslow4_%=\n"
".Lback4_%=:\n"
"v_add_f32 v224, v224, v225\n"
"v_add_f32 v225, v226, v227\n"
"v_mfma_f32_16x16x32_bf16 v[88:91], v[180:183], v[216:219], v[88:91]\n"
"s_waitcnt lgkmcnt(0)\n"
"s_barrier\n"
"ds_read_b128 v[228:231], v242\n"
"v_lshlrev_b32 v246, 17, v248\n"
"v_mfma_f32_16x16x32_bf16 v[104:107], v[184:187], v[216:219], v[104:107]\n"
"ds_write_b128 v240, v[8:11] offset:0\n"
"v_min_u32 v236, 3076, v238\n"
"s_bfe_u32 s74, s11, 0x30000\n"
"v_mfma_f32_16x16x32_bf16 v[120:123], v[188:191], v[216:219], v[120:123]\n"
"s_lshl_b32 s74, s74, 16\n"
"ds_write_b128 v240, v[24:27] offset:1024\n"
"s_add_u32 s18, s74, s16\n"
"v_mfma_f32_16x16x32_bf16 v[12:15], v[160:163], v[220:223], v[12:15]\n"
"s_add_u32 s19, s18, s33\n"
"ds_write_b128 v240, v[40:43] offset:2048\n"
"v_mov_b32 v247, s4\n"
"v_mfma_f32_16x16x32_bf16 v[28:31], v[164:167], v[220:223], v[28:31]\n"
"buffer_load_dwordx4 v[128:131], v248, s[84:87], s18 offen offset:0 sc1\n"
"s_bfe_u32 s74, s11, 0x30004\n"
"s_lshl_b32 s74, s74, 16\n"
"v_mfma_f32_16x16x32_bf16 v[44:47], v[168:171], v[220:223], v[44:47]\n"
"s_add_u32 s20, s74, s16\n"
"ds_write_b128 v240, v[56:59] offset:3072\n"
"s_add_u32 s21, s20, s33\n"
"v_mfma_f32_16x16x32_bf16 v[60:63], v[172:175], v[220:223], v[60:63]\n"
"s_waitcnt lgkmcnt(4)\n"
"ds_write_b128 v240, v[72:75] offset:4096\n"
"v_min_f32 v244, v192, v230\n"
"v_mfma_f32_16x16x32_bf16 v[76:79], v[176:179], v[220:223], v[76:79]\n"
"buffer_load_dwordx4 v[132:135], v248, s[84:87], s20 offen offset:0 sc1\n"
"v_min_f32 v245, v196, v231\n"
"v_mfma_f32_16x16x32_bf16 v[92:95], v[180:183], v[220:223], v[92:95]\n"
"ds_write_b128 v240, v[88:91] offset:5120\n"
"v_sub_f32 v232, v244, v192\n"
"v_mfma_f32_16x16x32_bf16 v[108:111], v[184:187], v[220:223], v[108:111]\n"
"ds_write_b128 v240, v[104:107] offset:6144\n"
"v_sub_f32 v234, v244, v230\n"
"v_mfma_f32_16x16x32_bf16 v[124:127], v[188:191], v[220:223], v[124:127]\n"
"buffer_load_dwordx4 v[136:139], v248, s[84:87], s18 offen offset:1024 sc1\n"
"v_sub_f32 v233, v245, v196\n"
"v_mfma_f32_16x16x32_bf16 v[0:3], v[160:163], v[208:211], v[0:3]\n"
"ds_write_b128 v240, v[120:123] offset:7168\n"
"v_sub_f32 v235, v245, v231\n"
"v_mfma_f32_16x16x32_bf16 v[16:19], v[164:167], v[208:211], v[16:19]\n"
"ds_write_b128 v240, v[12:15] offset:8192\n"
"v_cvt_i32_f32 v232, v232\n"
"v_mfma_f32_16x16x32_bf16 v[32:35], v[168:171], v[208:211], v[32:35]\n"
"ds_write_b128 v240, v[28:31] offset:9216\n"
"v_cvt_i32_f32 v233, v233\n"
"v_mfma_f32_16x16x32_bf16 v[48:51], v[172:175], v[208:211], v[48:51]\n"
"buffer_load_dwordx4 v[140:143], v248, s[84:87], s20 offen offset:1024 sc1\n"
"v_cvt_i32_f32 v234, v234\n"
"v_mfma_f32_16x16x32_bf16 v[64:67], v[176:179], v[208:211], v[64:67]\n"
"ds_write_b128 v240, v[44:47] offset:10240\n"
"v_cvt_i32_f32 v235, v235\n"
"v_mfma_f32_16x16x32_bf16 v[80:83], v[180:183], v[208:211], v[80:83]\n"
"ds_write_b128 v240, v[60:63] offset:11264\n"
"v_ldexp_f32 v224, v224, v232\n"
"v_mfma_f32_16x16x32_bf16 v[96:99], v[184:187], v[208:211], v[96:99]\n"
"buffer_load_dwordx4 v[144:147], v248, s[84:87], s19 offen offset:0 sc1\n"
"v_ldexp_f32 v228, v228, v234\n"
"v_mfma_f32_16x16x32_bf16 v[112:115], v[188:191], v[208:211], v[112:115]\n"
"ds_write_b128 v240, v[76:79] offset:12288\n"
"v_ldexp_f32 v225, v225, v233\n"
"v_mfma_f32_16x16x32_bf16 v[4:7], v[160:163], v[212:215], v[4:7]\n"
"ds_write_b128 v240, v[92:95] offset:13312\n"
"v_ldexp_f32 v229, v229, v235\n"
"v_mfma_f32_16x16x32_bf16 v[20:23], v[164:167], v[212:215], v[20:23]\n"
"buffer_load_dwordx4 v[148:151], v248, s[84:87], s21 offen offset:0 sc1\n"
"v_add_f32 v224, v224, v228\n"
"v_mfma_f32_16x16x32_bf16 v[36:39], v[168:171], v[212:215], v[36:39]\n"
"ds_write_b128 v240, v[108:111] offset:14336\n"
"v_add_f32 v225, v225, v229\n"
"v_mfma_f32_16x16x32_bf16 v[52:55], v[172:175], v[212:215], v[52:55]\n"
"ds_write_b128 v240, v[124:127] offset:15360\n"
"v_mfma_f32_16x16x32_bf16 v[68:71], v[176:179], v[212:215], v[68:71]\n"
"buffer_load_dwordx4 v[152:155], v248, s[84:87], s18 offen offset:2048 sc1\n"
"v_mfma_f32_16x16x32_bf16 v[84:87], v[180:183], v[212:215], v[84:87]\n"
"buffer_load_dwordx4 v[156:159], v248, s[84:87], s20 offen offset:2048 sc1\n"
"v_mfma_f32_16x16x32_bf16 v[100:103], v[184:187], v[212:215], v[100:103]\n"
"v_rcp_f32 v224, v224\n"
"v_mfma_f32_16x16x32_bf16 v[116:119], v[188:191], v[212:215], v[116:119]\n"
"v_ldexp_f32 v232, v224, v232\n"
"v_rcp_f32 v225, v225\n"
"s_nop 0\n"
"v_ldexp_f32 v233, v225, v233\n"
"v_ldexp_f32 v234, v224, v234\n"
"v_ldexp_f32 v235, v225, v235\n"
"v_pk_mul_f32 v[0:1], v[0:1], v[232:233] op_sel:[0,0] op_sel_hi:[1,0]\n"
"v_pk_mul_f32 v[2:3], v[2:3], v[232:233] op_sel:[0,0] op_sel_hi:[1,0]\n"
"v_pk_mul_f32 v[16:17], v[16:17], v[232:233] op_sel:[0,0] op_sel_hi:[1,0]\n"
"v_pk_mul_f32 v[18:19], v[18:19], v[232:233] op_sel:[0,0] op_sel_hi:[1,0]\n"
"v_pk_mul_f32 v[32:33], v[32:33], v[232:233] op_sel:[0,0] op_sel_hi:[1,0]\n"
"v_pk_mul_f32 v[34:35], v[34:35], v[232:233] op_sel:[0,0] op_sel_hi:[1,0]\n"
"v_pk_mul_f32 v[48:49], v[48:49], v[232:233] op_sel:[0,0] op_sel_hi:[1,0]\n"
"v_pk_mul_f32 v[50:51], v[50:51], v[232:233] op_sel:[0,0] op_sel_hi:[1,0]\n"
"v_pk_mul_f32 v[64:65], v[64:65], v[232:233] op_sel:[0,0] op_sel_hi:[1,0]\n"
"v_pk_mul_f32 v[66:67], v[66:67], v[232:233] op_sel:[0,0] op_sel_hi:[1,0]\n"
"v_pk_mul_f32 v[80:81], v[80:81], v[232:233] op_sel:[0,0] op_sel_hi:[1,0]\n"
"v_pk_mul_f32 v[82:83], v[82:83], v[232:233] op_sel:[0,0] op_sel_hi:[1,0]\n"
"v_pk_mul_f32 v[96:97], v[96:97], v[232:233] op_sel:[0,0] op_sel_hi:[1,0]\n"
"v_pk_mul_f32 v[98:99], v[98:99], v[232:233] op_sel:[0,0] op_sel_hi:[1,0]\n"
"v_pk_mul_f32 v[112:113], v[112:113], v[232:233] op_sel:[0,0] op_sel_hi:[1,0]\n"
"v_pk_mul_f32 v[114:115], v[114:115], v[232:233] op_sel:[0,0] op_sel_hi:[1,0]\n"
"s_waitcnt lgkmcnt(0)\n"
"s_barrier\n"
"ds_read_b128 v[8:11], v241 offset:0\n"
"ds_read_b128 v[24:27], v241 offset:1024\n"
"ds_read_b128 v[40:43], v241 offset:2048\n"
"ds_read_b128 v[56:59], v241 offset:3072\n"
"ds_read_b128 v[72:75], v241 offset:4096\n"
"ds_read_b128 v[88:91], v241 offset:5120\n"
"ds_read_b128 v[104:107], v241 offset:6144\n"
"ds_read_b128 v[120:123], v241 offset:7168\n"
"ds_read_b128 v[12:15], v241 offset:8192\n"
"ds_read_b128 v[28:31], v241 offset:9216\n"
"ds_read_b128 v[44:47], v241 offset:10240\n"
"ds_read_b128 v[60:63], v241 offset:11264\n"
"ds_read_b128 v[76:79], v241 offset:12288\n"
"ds_read_b128 v[92:95], v241 offset:13312\n"
"ds_read_b128 v[108:111], v241 offset:14336\n"
"ds_read_b128 v[124:127], v241 offset:15360\n"
"s_cmp_eq_u32 s5, 1073741824\n"
"s_cbranch_scc1 .Lnx_%=\n"
"s_waitcnt vmcnt(8)\n"
"buffer_store_dword v247, v246, s[84:87], s5 offen sc1\n"
".Lnx_%=:\n"
"buffer_load_dwordx4 a[240:243], v248, s[84:87], s19 offen offset:1024 sc1\n"
"buffer_load_dwordx4 a[244:247], v248, s[84:87], s21 offen offset:1024 sc1\n"
"buffer_load_dwordx4 a[248:251], v248, s[84:87], s18 offen offset:3072 sc1\n"
"buffer_load_dwordx4 a[252:255], v248, s[84:87], s20 offen offset:3072 sc1\n"
"v_pk_mul_f32 v[4:5], v[4:5], v[232:233] op_sel:[0,1] op_sel_hi:[1,1]\n"
"v_pk_mul_f32 v[6:7], v[6:7], v[232:233] op_sel:[0,1] op_sel_hi:[1,1]\n"
"v_pk_mul_f32 v[20:21], v[20:21], v[232:233] op_sel:[0,1] op_sel_hi:[1,1]\n"
"v_pk_mul_f32 v[22:23], v[22:23], v[232:233] op_sel:[0,1] op_sel_hi:[1,1]\n"
"v_pk_mul_f32 v[36:37], v[36:37], v[232:233] op_sel:[0,1] op_sel_hi:[1,1]\n"
"v_pk_mul_f32 v[38:39], v[38:39], v[232:233] op_sel:[0,1] op_sel_hi:[1,1]\n"
"v_pk_mul_f32 v[52:53], v[52:53], v[232:233] op_sel:[0,1] op_sel_hi:[1,1]\n"
"v_pk_mul_f32 v[54:55], v[54:55], v[232:233] op_sel:[0,1] op_sel_hi:[1,1]\n"
"v_pk_mul_f32 v[68:69], v[68:69], v[232:233] op_sel:[0,1] op_sel_hi:[1,1]\n"
"v_pk_mul_f32 v[70:71], v[70:71], v[232:233] op_sel:[0,1] op_sel_hi:[1,1]\n"
"v_pk_mul_f32 v[84:85], v[84:85], v[232:233] op_sel:[0,1] op_sel_hi:[1,1]\n"
"v_pk_mul_f32 v[86:87], v[86:87], v[232:233] op_sel:[0,1] op_sel_hi:[1,1]\n"
"v_pk_mul_f32 v[100:101], v[100:101], v[232:233] op_sel:[0,1] op_sel_hi:[1,1]\n"
"v_pk_mul_f32 v[102:103], v[102:103], v[232:233] op_sel:[0,1] op_sel_hi:[1,1]\n"
"v_pk_mul_f32 v[116:117], v[116:117], v[232:233] op_sel:[0,1] op_sel_hi:[1,1]\n"
"v_pk_mul_f32 v[118:119], v[118:119], v[232:233] op_sel:[0,1] op_sel_hi:[1,1]\n"
"s_waitcnt lgkmcnt(14)\n"
"v_pk_fma_f32 v[0:1], v[8:9], v[234:235], v[0:1] op_sel:[0,0,0] op_sel_hi:[1,0,1]\n"
"v_pk_fma_f32 v[2:3], v[10:11], v[234:235], v[2:3] op_sel:[0,0,0] op_sel_hi:[1,0,1]\n"
"v_pk_fma_f32 v[16:17], v[24:25], v[234:235], v[16:17] op_sel:[0,0,0] op_sel_hi:[1,0,1]\n"
"v_pk_fma_f32 v[18:19], v[26:27], v[234:235], v[18:19] op_sel:[0,0,0] op_sel_hi:[1,0,1]\n"
"v_cvt_pk_bf16_f32 v0, v0, v1\n"
"v_cvt_pk_bf16_f32 v1, v2, v3\n"
"v_cvt_pk_bf16_f32 v2, v16, v17\n"
"v_cvt_pk_bf16_f32 v3, v18, v19\n"
"buffer_store_dwordx4 v[0:3], v248, s[84:87], s8 offen offset:0 sc1\n"
"s_waitcnt lgkmcnt(12)\n"
"v_pk_fma_f32 v[32:33], v[40:41], v[234:235], v[32:33] op_sel:[0,0,0] op_sel_hi:[1,0,1]\n"
"v_pk_fma_f32 v[34:35], v[42:43], v[234:235], v[34:35] op_sel:[0,0,0] op_sel_hi:[1,0,1]\n"
"v_pk_fma_f32 v[48:49], v[56:57], v[234:235], v[48:49] op_sel:[0,0,0] op_sel_hi:[1,0,1]\n"
"v_pk_fma_f32 v[50:51], v[58:59], v[234:235], v[50:51] op_sel:[0,0,0] op_sel_hi:[1,0,1]\n"
"v_cvt_pk_bf16_f32 v32, v32, v33\n"
"v_cvt_pk_bf16_f32 v33, v34, v35\n"
"v_cvt_pk_bf16_f32 v34, v48, v49\n"
"v_cvt_pk_bf16_f32 v35, v50, v51\n"
"buffer_store_dwordx4 v[32:35], v248, s[84:87], s8 offen offset:1024 sc1\n"
"s_waitcnt lgkmcnt(10)\n"
"v_pk_fma_f32 v[64:65], v[72:73], v[234:235], v[64:65] op_sel:[0,0,0] op_sel_hi:[1,0,1]\n"
"v_pk_fma_f32 v[66:67], v[74:75], v[234:235], v[66:67] op_sel:[0,0,0] op_sel_hi:[1,0,1]\n"
"v_pk_fma_f32 v[80:81], v[88:89], v[234:235], v[80:81] op_sel:[0,0,0] op_sel_hi:[1,0,1]\n"
"v_pk_fma_f32 v[82:83], v[90:91], v[234:235], v[82:83] op_sel:[0,0,0] op_sel_hi:[1,0,1]\n"
"v_cvt_pk_bf16_f32 v64, v64, v65\n"
"v_cvt_pk_bf16_f32 v65, v66, v67\n"
"v_cvt_pk_bf16_f32 v66, v80, v81\n"
"v_cvt_pk_bf16_f32 v67, v82, v83\n"
"buffer_store_dwordx4 v[64:67], v248, s[84:87], s8 offen offset:2048 sc1\n"
"s_waitcnt lgkmcnt(8)\n"
"v_pk_fma_f32 v[96:97], v[104:105], v[234:235], v[96:97] op_sel:[0,0,0] op_sel_hi:[1,0,1]\n"
"v_pk_fma_f32 v[98:99], v[106:107], v[234:235], v[98:99] op_sel:[0,0,0] op_sel_hi:[1,0,1]\n"
"v_pk_fma_f32 v[112:113], v[120:121], v[234:235], v[112:113] op_sel:[0,0,0] op_sel_hi:[1,0,1]\n"
"v_pk_fma_f32 v[114:115], v[122:123], v[234:235], v[114:115] op_sel:[0,0,0] op_sel_hi:[1,0,1]\n"
"v_cvt_pk_bf16_f32 v96, v96, v97\n"
"v_cvt_pk_bf16_f32 v97, v98, v99\n"
"v_cvt_pk_bf16_f32 v98, v112, v113\n"
"v_cvt_pk_bf16_f32 v99, v114, v115\n"
"buffer_store_dwordx4 v[96:99], v248, s[84:87], s8 offen offset:3072 sc1\n"
"s_waitcnt lgkmcnt(6)\n"
"v_pk_fma_f32 v[4:5], v[12:13], v[234:235], v[4:5] op_sel:[0,1,0] op_sel_hi:[1,1,1]\n"
"v_pk_fma_f32 v[6:7], v[14:15], v[234:235], v[6:7] op_sel:[0,1,0] op_sel_hi:[1,1,1]\n"
"v_pk_fma_f32 v[20:21], v[28:29], v[234:235], v[20:21] op_sel:[0,1,0] op_sel_hi:[1,1,1]\n"
"v_pk_fma_f32 v[22:23], v[30:31], v[234:235], v[22:23] op_sel:[0,1,0] op_sel_hi:[1,1,1]\n"
"s_add_u32 s8, s8, 4096\n"
"v_cvt_pk_bf16_f32 v4, v4, v5\n"
"v_cvt_pk_bf16_f32 v5, v6, v7\n"
"v_cvt_pk_bf16_f32 v6, v20, v21\n"
"v_cvt_pk_bf16_f32 v7, v22, v23\n"
"buffer_store_dwordx4 v[4:7], v248, s[84:87], s8 offen offset:0 sc1\n"
"s_waitcnt lgkmcnt(4)\n"
"v_pk_fma_f32 v[36:37], v[44:45], v[234:235], v[36:37] op_sel:[0,1,0] op_sel_hi:[1,1,1]\n"
"v_pk_fma_f32 v[38:39], v[46:47], v[234:235], v[38:39] op_sel:[0,1,0] op_sel_hi:[1,1,1]\n"
"v_pk_fma_f32 v[52:53], v[60:61], v[234:235], v[52:53] op_sel:[0,1,0] op_sel_hi:[1,1,1]\n"
"v_pk_fma_f32 v[54:55], v[62:63], v[234:235], v[54:55] op_sel:[0,1,0] op_sel_hi:[1,1,1]\n"
"v_cvt_pk_bf16_f32 v36, v36, v37\n"
"v_cvt_pk_bf16_f32 v37, v38, v39\n"
"v_cvt_pk_bf16_f32 v38, v52, v53\n"
"v_cvt_pk_bf16_f32 v39, v54, v55\n"
"buffer_store_dwordx4 v[36:39], v248, s[84:87], s8 offen offset:1024 sc1\n"
"s_waitcnt lgkmcnt(2)\n"
"v_pk_fma_f32 v[68:69], v[76:77], v[234:235], v[68:69] op_sel:[0,1,0] op_sel_hi:[1,1,1]\n"
"v_pk_fma_f32 v[70:71], v[78:79], v[234:235], v[70:71] op_sel:[0,1,0] op_sel_hi:[1,1,1]\n"
"v_pk_fma_f32 v[84:85], v[92:93], v[234:235], v[84:85] op_sel:[0,1,0] op_sel_hi:[1,1,1]\n"
"v_pk_fma_f32 v[86:87], v[94:95], v[234:235], v[86:87] op_sel:[0,1,0] op_sel_hi:[1,1,1]\n"
"v_cvt_pk_bf16_f32 v68, v68, v69\n"
"v_cvt_pk_bf16_f32 v69, v70, v71\n"
"v_cvt_pk_bf16_f32 v70, v84, v85\n"
"v_cvt_pk_bf16_f32 v71, v86, v87\n"
"buffer_store_dwordx4 v[68:71], v248, s[84:87], s8 offen offset:2048 sc1\n"
"s_waitcnt lgkmcnt(0)\n"
"v_pk_fma_f32 v[100:101], v[108:109], v[234:235], v[100:101] op_sel:[0,1,0] op_sel_hi:[1,1,1]\n"
"v_pk_fma_f32 v[102:103], v[110:111], v[234:235], v[102:103] op_sel:[0,1,0] op_sel_hi:[1,1,1]\n"
"v_pk_fma_f32 v[116:117], v[124:125], v[234:235], v[116:117] op_sel:[0,1,0] op_sel_hi:[1,1,1]\n"
"v_pk_fma_f32 v[118:119], v[126:127], v[234:235], v[118:119] op_sel:[0,1,0] op_sel_hi:[1,1,1]\n"
"v_cvt_pk_bf16_f32 v100, v100, v101\n"
"v_cvt_pk_bf16_f32 v101, v102, v103\n"
"v_cvt_pk_bf16_f32 v102, v116, v117\n"
"v_cvt_pk_bf16_f32 v103, v118, v119\n"
"buffer_store_dwordx4 v[100:103], v248, s[84:87], s8 offen offset:3072 sc1\n"
"s_mov_b64 exec, 0\n"
"s_cmp_eq_u32 s76, 0\n"
"s_cbranch_scc1 .Lpad18_%=\n"
"s_cmp_eq_u32 s76, 1\n"
"s_cbranch_scc1 .Lpad19_%=\n"
"s_cmp_eq_u32 s76, 2\n"
"s_cbranch_scc1 .Lpad20_%=\n"
".Lhopfin_%=:\n"
"s_mov_b64 exec, -1\n"
"s_waitcnt vmcnt(18)\n"
"v_mfma_f32_16x16x32_bf16 v[0:3], a[0:3], v[128:131], 0\n"
"v_bfe_u32 v250, v248, 4, 4\n"
"v_lshrrev_b32 v251, 8, v248\n"
"v_mfma_f32_16x16x32_bf16 v[40:43], a[0:3], v[132:135], 0\n"
"v_mul_u32_u24 v250, 2560, v250\n"
"v_lshl_add_u32 v253, v251, 4, v250\n"
"v_mfma_f32_16x16x32_bf16 v[4:7], a[4:7], v[128:131], 0\n"
"v_lshl_add_u32 v254, v251, 3, v250\n"
"v_add_u32 v249, s57, v248\n"
"v_mfma_f32_16x16x32_bf16 v[44:47], a[4:7], v[132:135], 0\n"
"v_add_u32 v250, 65536, v249\n"
"v_lshrrev_b32 v126, 0, v248\n"
"s_xor_b32 s59, s76, 0\n"
"v_mfma_f32_16x16x32_bf16 v[8:11], a[8:11], v[128:131], 0\n"
"ds_read_b128 v[200:203], v249 offset:16384\n"
"s_lshl_b32 s56, s59, 14\n"
"s_cselect_b32 s59, 98304, 0\n"
"v_mfma_f32_16x16x32_bf16 v[48:51], a[8:11], v[132:135], 0\n"
"s_add_u32 s56, s56, s59\n"
"ds_read_b128 v[204:207], v249 offset:17408\n"
"v_add_u32 v123, s56, v126\n"
"v_mfma_f32_16x16x32_bf16 v[12:15], a[12:15], v[128:131], 0\n"
"s_xor_b32 s59, s76, 1\n"
"s_lshl_b32 s56, s59, 14\n"
"s_cselect_b32 s59, 98304, 0\n"
"v_mfma_f32_16x16x32_bf16 v[52:55], a[12:15], v[132:135], 0\n"
"s_add_u32 s56, s56, s59\n"
"v_add_u32 v120, s56, v126\n"
"s_xor_b32 s59, s76, 2\n"
"v_mfma_f32_16x16x32_bf16 v[16:19], a[16:19], v[128:131], 0\n"
"s_lshl_b32 s56, s59, 14\n"
"s_cselect_b32 s59, 98304, 0\n"
"s_add_u32 s56, s56, s59\n"
"v_mfma_f32_16x16x32_bf16 v[56:59], a[16:19], v[132:135], 0\n"
"s_add_u32 s56, s56, 5120\n"
"ds_read_b128 v[208:211], v249 offset:18432\n"
"v_add_u32 v121, s56, v126\n"
"v_mfma_f32_16x16x32_bf16 v[20:23], a[80:83], v[128:131], 0\n"
"s_xor_b32 s59, s76, 3\n"
"s_lshl_b32 s56, s59, 14\n"
"s_cselect_b32 s59, 98304, 0\n"
"v_mfma_f32_16x16x32_bf16 v[60:63], a[80:83], v[132:135], 0\n"
"s_add_u32 s56, s56, s59\n"
"s_add_u32 s56, s56, 10240\n"
"v_add_u32 v122, s56, v126\n"
"v_mfma_f32_16x16x32_bf16 v[24:27], a[84:87], v[128:131], 0\n"
"s_bitcmp1_b32 s76, 1\n"
"s_cselect_b64 s[68:69], -1, 0\n"
"v_add_u32 v126, 65536, v126\n"
"v_mfma_f32_16x16x32_bf16 v[64:67], a[84:87], v[132:135], 0\n"
"ds_read_b128 v[212:215], v249 offset:19456\n"
"v_cndmask_b32 v124, v126, v121, s[68:69]\n"
"v_mfma_f32_16x16x32_bf16 v[28:31], a[88:91], v[128:131], 0\n"
"v_cndmask_b32 v126, v121, v126, s[68:69]\n"
"v_cndmask_b32 v125, v120, v122, s[68:69]\n"
"v_mfma_f32_16x16x32_bf16 v[68:71], a[88:91], v[132:135], 0\n"
"v_cndmask_b32 v127, v122, v120, s[68:69]\n"
"v_lshrrev_b32 v252, 1, v248\n"
"s_mov_b64 s[36:37], s[24:25]\n"
"v_mfma_f32_16x16x32_bf16 v[32:35], a[92:95], v[128:131], 0\n"
"s_mov_b32 s38, 5242880\n"
"s_mov_b32 s39, 0x20000\n"
"s_and_b32 s56, s76, 1\n"
"v_mfma_f32_16x16x32_bf16 v[72:75], a[92:95], v[132:135], 0\n"
"s_andn2_b32 s66, %2, 6\n"
"ds_read_b128 v[216:219], v249 offset:32768\n"
"s_lshl2_add_u32 s66, s66, s56\n"
"v_mfma_f32_16x16x32_bf16 v[36:39], a[96:99], v[128:131], 0\n"
"s_bfe_u32 s59, %2, 0x20003\n"
"s_lshl1_add_u32 s56, s59, s56\n"
"s_bfe_u32 s59, s11, 0x30018\n"
"v_mfma_f32_16x16x32_bf16 v[76:79], a[96:99], v[132:135], 0\n"
"s_waitcnt vmcnt(16)\n"
"s_lshl4_add_u32 s59, s59, s32\n"
"s_lshl_b32 s59, s59, 8\n"
"v_mfma_f32_16x16x32_bf16 v[0:3], a[20:23], v[136:139], v[0:3]\n"
"s_add_u32 s59, s59, s56\n"
"s_mul_i32 s71, s59, 160\n"
"s_and_b32 s59, %2, 7\n"
"v_mfma_f32_16x16x32_bf16 v[40:43], a[20:23], v[140:143], v[40:43]\n"
"s_lshr_b32 s56, %2, 6\n"
"s_lshl2_add_u32 s59, s59, s56\n"
"s_lshl2_add_u32 s59, s59, s76\n"
"v_mfma_f32_16x16x32_bf16 v[4:7], a[24:27], v[136:139], v[4:7]\n"
"s_lshl_b32 s59, s59, 8\n"
"s_bfe_u32 s56, %2, 0x30003\n"
"s_add_u32 s59, s59, s56\n"
"v_mfma_f32_16x16x32_bf16 v[44:47], a[24:27], v[140:143], v[44:47]\n"
"s_mul_i32 s70, s59, 160\n"
"s_lshl2_add_u32 s59, %2, s76\n"
"s_lshl4_add_u32 s73, s59, 81846272\n"
"v_mfma_f32_16x16x32_bf16 v[8:11], a[28:31], v[136:139], v[8:11]\n"
"s_mul_i32 s72, s59, 7680\n"
"s_add_u32 s72, s72, 73400320\n"
"v_mfma_f32_16x16x32_bf16 v[48:51], a[28:31], v[140:143], v[48:51]\n"
"v_mfma_f32_16x16x32_bf16 v[12:15], a[32:35], v[136:139], v[12:15]\n"
"v_mfma_f32_16x16x32_bf16 v[52:55], a[32:35], v[140:143], v[52:55]\n"
"v_mfma_f32_16x16x32_bf16 v[16:19], a[36:39], v[136:139], v[16:19]\n"
"v_mfma_f32_16x16x32_bf16 v[56:59], a[36:39], v[140:143], v[56:59]\n"
"v_mfma_f32_16x16x32_bf16 v[20:23], a[100:103], v[136:139], v[20:23]\n"
"v_mfma_f32_16x16x32_bf16 v[60:63], a[100:103], v[140:143], v[60:63]\n"
"v_mfma_f32_16x16x32_bf16 v[24:27], a[104:107], v[136:139], v[24:27]\n"
"v_mfma_f32_16x16x32_bf16 v[64:67], a[104:107], v[140:143], v[64:67]\n"
"v_mfma_f32_16x16x32_bf16 v[28:31], a[108:111], v[136:139], v[28:31]\n"
"v_mfma_f32_16x16x32_bf16 v[68:71], a[108:111], v[140:143], v[68:71]\n"
"v_mfma_f32_16x16x32_bf16 v[32:35], a[112:115], v[136:139], v[32:35]\n"
"v_mfma_f32_16x16x32_bf16 v[72:75], a[112:115], v[140:143], v[72:75]\n"
"v_mfma_f32_16x16x32_bf16 v[36:39], a[116:119], v[136:139], v[36:39]\n"
"v_mfma_f32_16x16x32_bf16 v[76:79], a[116:119], v[140:143], v[76:79]\n"
"s_waitcnt vmcnt(14)\n"
"s_waitcnt lgkmcnt(0)\n"
"v_mfma_f32_16x16x32_bf16 v[0:3], v[200:203], v[144:147], v[0:3]\n"
"v_mfma_f32_16x16x32_bf16 v[40:43], v[200:203], v[148:151], v[40:43]\n"
"s_add_u32 s58, s35, s34\n"
"ds_read_b128 v[200:203], v249 offset:33792\n"
"s_add_u32 s58, s58, 15360\n"
"v_mfma_f32_16x16x32_bf16 v[4:7], v[204:207], v[144:147], v[4:7]\n"
"v_mfma_f32_16x16x32_bf16 v[44:47], v[204:207], v[148:151], v[44:47]\n"
"ds_read_b128 v[204:207], v249 offset:34816\n"
"v_mfma_f32_16x16x32_bf16 v[8:11], v[208:211], v[144:147], v[8:11]\n"
"s_waitcnt vmcnt(0)\n"
"buffer_store_dword v247, v246, s[84:87], s9 offen sc1\n"
"v_mfma_f32_16x16x32_bf16 v[48:51], v[208:211], v[148:151], v[48:51]\n"
"buffer_load_dwordx4 a[140:143], v248, s[84:87], s58 offen offset:0 sc1\n"
"v_mfma_f32_16x16x32_bf16 v[12:15], v[212:215], v[144:147], v[12:15]\n"
"ds_read_b128 v[208:211], v249 offset:35840\n"
"v_mfma_f32_16x16x32_bf16 v[52:55], v[212:215], v[148:151], v[52:55]\n"
"v_mfma_f32_16x16x32_bf16 v[16:19], v[216:219], v[144:147], v[16:19]\n"
"buffer_load_dwordx4 a[144:147], v248, s[84:87], s58 offen offset:1024 sc1\n"
"v_mfma_f32_16x16x32_bf16 v[56:59], v[216:219], v[148:151], v[56:59]\n"
"s_waitcnt vmcnt(37)\n"
"ds_read_b128 v[212:215], v249 offset:49152\n"
"v_mfma_f32_16x16x32_bf16 v[20:23], a[160:163], v[144:147], v[20:23]\n"
"v_mfma_f32_16x16x32_bf16 v[60:63], a[160:163], v[148:151], v[60:63]\n"
"buffer_load_dwordx4 a[148:151], v248, s[84:87], s58 offen offset:2048 sc1\n"
"v_mfma_f32_16x16x32_bf16 v[24:27], a[164:167], v[144:147], v[24:27]\n"
"ds_read_b128 v[216:219], v249 offset:50176\n"
"v_mfma_f32_16x16x32_bf16 v[64:67], a[164:167], v[148:151], v[64:67]\n"
"v_mfma_f32_16x16x32_bf16 v[28:31], a[168:171], v[144:147], v[28:31]\n"
"buffer_load_dwordx4 a[152:155], v248, s[84:87], s58 offen offset:3072 sc1\n"
"s_add_u32 s58, s58, 4096\n"
"v_mfma_f32_16x16x32_bf16 v[68:71], a[168:171], v[148:151], v[68:71]\n"
"v_mfma_f32_16x16x32_bf16 v[32:35], a[172:175], v[144:147], v[32:35]\n"
"v_mfma_f32_16x16x32_bf16 v[72:75], a[172:175], v[148:151], v[72:75]\n"
"buffer_load_dwordx4 a[156:159], v248, s[84:87], s58 offen offset:0 sc1\n"
"v_mfma_f32_16x16x32_bf16 v[36:39], a[176:179], v[144:147], v[36:39]\n"
"v_mfma_f32_16x16x32_bf16 v[76:79], a[176:179], v[148:151], v[76:79]\n"
"s_waitcnt vmcnt(18)\n"
"v_mfma_f32_16x16x32_bf16 v[0:3], a[40:43], v[152:155], v[0:3]\n"
"buffer_load_dword v237, v236, s[84:87], s10 offen sc1\n"
"v_mfma_f32_16x16x32_bf16 v[40:43], a[40:43], v[156:159], v[40:43]\n"
"buffer_load_dwordx4 v[160:163], v248, s[84:87], s19 offen offset:2048 sc1\n"
"v_mfma_f32_16x16x32_bf16 v[4:7], a[44:47], v[152:155], v[4:7]\n"
"v_mfma_f32_16x16x32_bf16 v[44:47], a[44:47], v[156:159], v[44:47]\n"
"v_mfma_f32_16x16x32_bf16 v[8:11], a[48:51], v[152:155], v[8:11]\n"
"buffer_load_dwordx4 v[164:167], v248, s[84:87], s21 offen offset:2048 sc1\n"
"s_add_u32 s58, s35, s34\n"
"v_mfma_f32_16x16x32_bf16 v[48:51], a[48:51], v[156:159], v[48:51]\n"
"s_add_u32 s58, s58, s31\n"
"s_add_u32 s58, s58, 10240\n"
"v_mfma_f32_16x16x32_bf16 v[12:15], a[52:55], v[152:155], v[12:15]\n"
"v_mfma_f32_16x16x32_bf16 v[52:55], a[52:55], v[156:159], v[52:55]\n"
"buffer_load_dwordx4 a[200:203], v248, s[84:87], s58 offen offset:0 sc1\n"
"v_mfma_f32_16x16x32_bf16 v[16:19], a[56:59], v[152:155], v[16:19]\n"
"v_mfma_f32_16x16x32_bf16 v[56:59], a[56:59], v[156:159], v[56:59]\n"
"s_waitcnt vmcnt(35)\n"
"v_mfma_f32_16x16x32_bf16 v[20:23], a[120:123], v[152:155], v[20:23]\n"
"buffer_load_dwordx4 a[204:207], v248, s[84:87], s58 offen offset:1024 sc1\n"
"v_mfma_f32_16x16x32_bf16 v[60:63], a[120:123], v[156:159], v[60:63]\n"
"v_mfma_f32_16x16x32_bf16 v[24:27], a[124:127], v[152:155], v[24:27]\n"
"v_mfma_f32_16x16x32_bf16 v[64:67], a[124:127], v[156:159], v[64:67]\n"
"buffer_load_dwordx4 a[208:211], v248, s[84:87], s58 offen offset:2048 sc1\n"
"v_mfma_f32_16x16x32_bf16 v[28:31], a[128:131], v[152:155], v[28:31]\n"
"v_mfma_f32_16x16x32_bf16 v[68:71], a[128:131], v[156:159], v[68:71]\n"
"v_mfma_f32_16x16x32_bf16 v[32:35], a[132:135], v[152:155], v[32:35]\n"
"buffer_load_dwordx4 a[212:215], v248, s[84:87], s58 offen offset:3072 sc1\n"
"s_add_u32 s58, s58, 4096\n"
"v_mfma_f32_16x16x32_bf16 v[72:75], a[132:135], v[156:159], v[72:75]\n"
"v_mfma_f32_16x16x32_bf16 v[36:39], a[136:139], v[152:155], v[36:39]\n"
"v_mfma_f32_16x16x32_bf16 v[76:79], a[136:139], v[156:159], v[76:79]\n"
"s_waitcnt vmcnt(23)\n"
"s_waitcnt lgkmcnt(0)\n"
"v_mfma_f32_16x16x32_bf16 v[0:3], v[200:203], a[240:243], v[0:3]\n"
"buffer_load_dwordx4 a[216:219], v248, s[84:87], s58 offen offset:0 sc1\n"
"v_mfma_f32_16x16x32_bf16 v[40:43], v[200:203], a[244:247], v[40:43]\n"
"ds_read_b128 v[200:203], v249 offset:51200\n"
"v_mfma_f32_16x16x32_bf16 v[4:7], v[204:207], a[240:243], v[4:7]\n"
"v_mfma_f32_16x16x32_bf16 v[44:47], v[204:207], a[244:247], v[44:47]\n"
"buffer_load_dwordx4 v[168:171], v248, s[84:87], s19 offen offset:3072 sc1\n"
"v_mfma_f32_16x16x32_bf16 v[8:11], v[208:211], a[240:243], v[8:11]\n"
"ds_read_b128 v[204:207], v249 offset:52224\n"
"v_mfma_f32_16x16x32_bf16 v[48:51], v[208:211], a[244:247], v[48:51]\n"
"v_mfma_f32_16x16x32_bf16 v[12:15], v[212:215], a[240:243], v[12:15]\n"
"buffer_load_dwordx4 v[172:175], v248, s[84:87], s21 offen offset:3072 sc1\n"
"s_add_u32 s58, s35, s34\n"
"v_mfma_f32_16x16x32_bf16 v[52:55], v[212:215], a[244:247], v[52:55]\n"
"s_add_u32 s58, s58, s31\n"
"ds_read_b128 v[208:211], v250 offset:16384\n"
"s_add_u32 s58, s58, 15360\n"
"v_mfma_f32_16x16x32_bf16 v[16:19], v[216:219], a[240:243], v[16:19]\n"
"v_mfma_f32_16x16x32_bf16 v[56:59], v[216:219], a[244:247], v[56:59]\n"
"s_waitcnt vmcnt(36)\n"
"buffer_load_dwordx4 a[220:223], v248, s[84:87], s58 offen offset:0 sc1\n"
"v_mfma_f32_16x16x32_bf16 v[20:23], a[180:183], a[240:243], v[20:23]\n"
"ds_read_b128 v[212:215], v250 offset:17408\n"
"v_mfma_f32_16x16x32_bf16 v[60:63], a[180:183], a[244:247], v[60:63]\n"
"v_mfma_f32_16x16x32_bf16 v[24:27], a[184:187], a[240:243], v[24:27]\n"
"buffer_load_dwordx4 a[224:227], v248, s[84:87], s58 offen offset:1024 sc1\n"
"v_mfma_f32_16x16x32_bf16 v[64:67], a[184:187], a[244:247], v[64:67]\n"
"ds_read_b128 v[216:219], v250 offset:18432\n"
"v_mfma_f32_16x16x32_bf16 v[28:31], a[188:191], a[240:243], v[28:31]\n"
"v_mfma_f32_16x16x32_bf16 v[68:71], a[188:191], a[244:247], v[68:71]\n"
"buffer_load_dwordx4 a[228:231], v248, s[84:87], s58 offen offset:2048 sc1\n"
"v_mfma_f32_16x16x32_bf16 v[32:35], a[192:195], a[240:243], v[32:35]\n"
"v_mfma_f32_16x16x32_bf16 v[72:75], a[192:195], a[244:247], v[72:75]\n"
"v_mfma_f32_16x16x32_bf16 v[36:39], a[196:199], a[240:243], v[36:39]\n"
"buffer_load_dwordx4 a[232:235], v248, s[84:87], s58 offen offset:3072 sc1\n"
"s_add_u32 s58, s58, 4096\n"
"v_mfma_f32_16x16x32_bf16 v[76:79], a[196:199], a[244:247], v[76:79]\n"
"s_waitcnt vmcnt(28)\n"
"v_mfma_f32_16x16x32_bf16 v[0:3], a[60:63], a[248:251], v[0:3]\n"
"v_mfma_f32_16x16x32_bf16 v[40:43], a[60:63], a[252:255], v[40:43]\n"
"buffer_load_dwordx4 a[236:239], v248, s[84:87], s58 offen offset:0 sc1\n"
"v_mfma_f32_16x16x32_bf16 v[4:7], a[64:67], a[248:251], v[4:7]\n"
"v_mfma_f32_16x16x32_bf16 v[44:47], a[64:67], a[252:255], v[44:47]\n"
"v_mfma_f32_16x16x32_bf16 v[8:11], a[68:71], a[248:251], v[8:11]\n"
"v_mfma_f32_16x16x32_bf16 v[48:51], a[68:71], a[252:255], v[48:51]\n"
"v_mfma_f32_16x16x32_bf16 v[12:15], a[72:75], a[248:251], v[12:15]\n"
"v_mfma_f32_16x16x32_bf16 v[52:55], a[72:75], a[252:255], v[52:55]\n"
"s_bfe_u32 s74, s11, 0x30008\n"
"s_bfe_u32 s75, s11, 0x3000c\n"
"s_min_u32 s74, s74, s75\n"
"v_mfma_f32_16x16x32_bf16 v[16:19], a[76:79], a[248:251], v[16:19]\n"
"s_lshl_b32 s74, s74, 16\n"
"s_add_u32 s18, s74, s16\n"
"s_add_u32 s19, s18, s33\n"
"v_mfma_f32_16x16x32_bf16 v[56:59], a[76:79], a[252:255], v[56:59]\n"
"s_waitcnt vmcnt(15)\n"
"v_mfma_f32_16x16x32_bf16 v[20:23], a[140:143], a[248:251], v[20:23]\n"
"s_waitcnt vmcnt(14)\n"
"v_mfma_f32_16x16x32_bf16 v[60:63], a[140:143], a[252:255], v[60:63]\n"
"v_cmp_ne_u32 vcc, s4, v237\n"
"s_cbranch_vccz .Lbackx_%=\n"
"s_waitcnt vmcnt(0)\n"
"buffer_store_dword v247, v246, s[84:87], s9 offen sc1\n"
"s_mov_b32 s61, 0x8000\n"
".Lslowlx_%=:\n"
"buffer_load_dword v237, v236, s[84:87], s10 offen sc1\n"
"s_waitcnt vmcnt(0)\n"
"v_cmp_ne_u32 vcc, s4, v237\n"
"s_cbranch_vccz .Lbackx_%=\n"
"s_sleep 1\n"
"s_sub_u32 s61, s61, 1\n"
"s_cbranch_scc0 .Lslowlx_%=\n"
".Lbackx_%=:\n"
"v_mfma_f32_16x16x32_bf16 v[24:27], a[144:147], a[248:251], v[24:27]\n"
"s_lshl_b32 s61, s32, 6\n"
"s_and_b32 s61, s61, 128\n"
"s_add_u32 s10, s10, s61\n"
"v_mfma_f32_16x16x32_bf16 v[64:67], a[144:147], a[252:255], v[64:67]\n"
"buffer_load_dwordx4 v[128:131], v248, s[84:87], s18 offen offset:0 sc1\n"
"v_mfma_f32_16x16x32_bf16 v[28:31], a[148:151], a[248:251], v[28:31]\n"
"v_or_b32 v236, 0x1800, v238\n"
"v_mfma_f32_16x16x32_bf16 v[68:71], a[148:151], a[252:255], v[68:71]\n"
"v_mfma_f32_16x16x32_bf16 v[32:35], a[152:155], a[248:251], v[32:35]\n"
"buffer_load_dwordx4 v[132:135], v248, s[84:87], s19 offen offset:0 sc1\n"
"v_mfma_f32_16x16x32_bf16 v[72:75], a[152:155], a[252:255], v[72:75]\n"
"v_mfma_f32_16x16x32_bf16 v[36:39], a[156:159], a[248:251], v[36:39]\n"
"v_mfma_f32_16x16x32_bf16 v[76:79], a[156:159], a[252:255], v[76:79]\n"
"s_waitcnt vmcnt(14)\n"
"s_waitcnt lgkmcnt(0)\n"
"v_mfma_f32_16x16x32_bf16 v[0:3], v[200:203], v[160:163], v[0:3]\n"
"buffer_load_dwordx4 v[136:139], v248, s[84:87], s18 offen offset:1024 sc1\n"
"v_mfma_f32_16x16x32_bf16 v[40:43], v[200:203], v[164:167], v[40:43]\n"
"ds_read_b128 v[200:203], v250 offset:19456\n"
"v_mfma_f32_16x16x32_bf16 v[4:7], v[204:207], v[160:163], v[4:7]\n"
"v_mfma_f32_16x16x32_bf16 v[44:47], v[204:207], v[164:167], v[44:47]\n"
"buffer_load_dwordx4 v[140:143], v248, s[84:87], s19 offen offset:1024 sc1\n"
"v_mfma_f32_16x16x32_bf16 v[8:11], v[208:211], v[160:163], v[8:11]\n"
"ds_read_b128 v[204:207], v250 offset:32768\n"
"v_mfma_f32_16x16x32_bf16 v[48:51], v[208:211], v[164:167], v[48:51]\n"
"v_mfma_f32_16x16x32_bf16 v[12:15], v[212:215], v[160:163], v[12:15]\n"
"buffer_load_dwordx4 v[144:147], v248, s[84:87], s18 offen offset:2048 sc1\n"
"v_mfma_f32_16x16x32_bf16 v[52:55], v[212:215], v[164:167], v[52:55]\n"
"ds_read_b128 v[208:211], v250 offset:33792\n"
"v_mfma_f32_16x16x32_bf16 v[16:19], v[216:219], v[160:163], v[16:19]\n"
"v_mfma_f32_16x16x32_bf16 v[56:59], v[216:219], v[164:167], v[56:59]\n"
"s_waitcnt vmcnt(12)\n"
"buffer_load_dwordx4 v[148:151], v248, s[84:87], s19 offen offset:2048 sc1\n"
"v_mfma_f32_16x16x32_bf16 v[20:23], a[200:203], v[160:163], v[20:23]\n"
"s_barrier\n"
"ds_read_b128 v[212:215], v250 offset:34816\n"
"v_mfma_f32_16x16x32_bf16 v[60:63], a[200:203], v[164:167], v[60:63]\n"
"v_mfma_f32_16x16x32_bf16 v[24:27], a[204:207], v[160:163], v[24:27]\n"
"buffer_load_dwordx4 v[152:155], v248, s[84:87], s18 offen offset:3072 sc1\n"
"v_mfma_f32_16x16x32_bf16 v[64:67], a[204:207], v[164:167], v[64:67]\n"
"ds_read_b128 v[216:219], v250 offset:35840\n"
"v_mfma_f32_16x16x32_bf16 v[28:31], a[208:211], v[160:163], v[28:31]\n"
"v_mfma_f32_16x16x32_bf16 v[68:71], a[208:211], v[164:167], v[68:71]\n"
"buffer_load_dword v237, v236, s[84:87], s10 offen sc1\n"
"v_mfma_f32_16x16x32_bf16 v[32:35], a[212:215], v[160:163], v[32:35]\n"
"buffer_load_dwordx4 v[156:159], v248, s[84:87], s19 offen offset:3072 sc1\n"
"v_mfma_f32_16x16x32_bf16 v[72:75], a[212:215], v[164:167], v[72:75]\n"
"v_mfma_f32_16x16x32_bf16 v[36:39], a[216:219], v[160:163], v[36:39]\n"
"v_mfma_f32_16x16x32_bf16 v[76:79], a[216:219], v[164:167], v[76:79]\n"
"s_waitcnt vmcnt(9)\n"
"v_mfma_f32_16x16x32_bf16 v[20:23], a[220:223], v[168:171], v[20:23]\n"
"v_mfma_f32_16x16x32_bf16 v[60:63], a[220:223], v[172:175], v[60:63]\n"
"v_mfma_f32_16x16x32_bf16 v[24:27], a[224:227], v[168:171], v[24:27]\n"
"v_mfma_f32_16x16x32_bf16 v[64:67], a[224:227], v[172:175], v[64:67]\n"
"v_mfma_f32_16x16x32_bf16 v[28:31], a[228:231], v[168:171], v[28:31]\n"
"v_mfma_f32_16x16x32_bf16 v[68:71], a[228:231], v[172:175], v[68:71]\n"
"s_nop 2\n"
"ds_write_b128 v120, v[20:23] offset:0\n"
"v_mfma_f32_16x16x32_bf16 v[32:35], a[232:235], v[168:171], v[32:35]\n"
"v_mfma_f32_16x16x32_bf16 v[72:75], a[232:235], v[172:175], v[72:75]\n"
"ds_write_b128 v122, v[60:63] offset:0\n"
"v_mfma_f32_16x16x32_bf16 v[36:39], a[236:239], v[168:171], v[36:39]\n"
"v_mfma_f32_16x16x32_bf16 v[76:79], a[236:239], v[172:175], v[76:79]\n"
"s_waitcnt lgkmcnt(2)\n"
"v_mfma_f32_16x16x32_bf16 v[0:3], v[200:203], v[168:171], v[0:3]\n"
"ds_write_b128 v120, v[24:27] offset:1024\n"
"v_mfma_f32_16x16x32_bf16 v[40:43], v[200:203], v[172:175], v[40:43]\n"
"ds_read_b128 v[200:203], v249 offset:16384\n"
"v_mfma_f32_16x16x32_bf16 v[4:7], v[204:207], v[168:171], v[4:7]\n"
"v_mfma_f32_16x16x32_bf16 v[44:47], v[204:207], v[172:175], v[44:47]\n"
"ds_write_b128 v122, v[64:67] offset:1024\n"
"v_mfma_f32_16x16x32_bf16 v[8:11], v[208:211], v[168:171], v[8:11]\n"
"ds_read_b128 v[204:207], v249 offset:17408\n"
"v_mfma_f32_16x16x32_bf16 v[48:51], v[208:211], v[172:175], v[48:51]\n"
"ds_write_b128 v120, v[28:31] offset:2048\n"
"v_mfma_f32_16x16x32_bf16 v[12:15], v[212:215], v[168:171], v[12:15]\n"
"ds_read_b128 v[208:211], v249 offset:18432\n"
"v_mfma_f32_16x16x32_bf16 v[52:55], v[212:215], v[172:175], v[52:55]\n"
"v_mfma_f32_16x16x32_bf16 v[16:19], v[216:219], v[168:171], v[16:19]\n"
"ds_write_b128 v122, v[68:71] offset:2048\n"
"v_mfma_f32_16x16x32_bf16 v[56:59], v[216:219], v[172:175], v[56:59]\n"
"s_waitcnt vmcnt(8)\n"
"ds_read_b128 v[212:215], v249 offset:19456\n"
"v_mfma_f32_16x16x32_bf16 v[80:83], a[0:3], v[128:131], 0\n"
"s_bfe_u32 s54, s11, 0x20000\n"
"s_lshl3_add_u32 s67, s54, s66\n"
"s_bfe_u32 s55, s11, 0x10002\n"
"v_mfma_f32_16x16x32_bf16 v[84:87], a[4:7], v[128:131], 0\n"
"s_lshl1_add_u32 s67, s55, s67\n"
"ds_write_b128 v120, v[32:35] offset:3072\n"
"v_mfma_f32_16x16x32_bf16 v[88:91], a[8:11], v[128:131], 0\n"
"s_bfe_u32 s55, %2, 0x20001\n"
"ds_read_b128 v[216:219], v249 offset:32768\n"
"s_xor_b32 s54, s54, s55\n"
"v_mfma_f32_16x16x32_bf16 v[92:95], a[12:15], v[128:131], 0\n"
"s_lshl2_add_u32 s54, s67, s54\n"
"s_lshl2_add_u32 s60, s54, 81846272\n"
"s_sub_u32 s67, s54, s67\n"
"v_mfma_f32_16x16x32_bf16 v[96:99], a[16:19], v[128:131], 0\n"
"s_mul_i32 s67, s67, 2560\n"
"ds_write_b128 v122, v[72:75] offset:3072\n"
"v_mfma_f32_16x16x32_bf16 v[100:103], a[80:83], v[128:131], 0\n"
"s_add_u32 s67, s67, 73397760\n"
"v_mfma_f32_16x16x32_bf16 v[104:107], a[84:87], v[128:131], 0\n"
"ds_write_b128 v120, v[36:39] offset:4096\n"
"v_mfma_f32_16x16x32_bf16 v[108:111], a[88:91], v[128:131], 0\n"
"s_bfe_u32 s74, s11, 0x30008\n"
"s_bfe_u32 s75, s11, 0x3000c\n"
"s_max_u32 s74, s74, s75\n"
"v_mfma_f32_16x16x32_bf16 v[112:115], a[92:95], v[128:131], 0\n"
"s_lshl_b32 s74, s74, 16\n"
"s_add_u32 s20, s74, s16\n"
"s_add_u32 s21, s20, s33\n"
"v_mfma_f32_16x16x32_bf16 v[116:119], a[96:99], v[128:131], 0\n"
"s_waitcnt vmcnt(7)\n"
"ds_write_b128 v122, v[76:79] offset:4096\n"
"v_mfma_f32_16x16x32_bf16 v[100:103], a[160:163], v[132:135], v[100:103]\n"
"s_waitcnt vmcnt(1)\n"
"v_mfma_f32_16x16x32_bf16 v[104:107], a[164:167], v[132:135], v[104:107]\n"
"v_cmp_ne_u32 vcc, s4, v237\n"
"s_cbranch_vccz .Lbacky_%=\n"
"s_mov_b32 s61, 0x8000\n"
".Lslowly_%=:\n"
"buffer_load_dword v237, v236, s[84:87], s10 offen sc1\n"
"s_waitcnt vmcnt(0)\n"
"v_cmp_ne_u32 vcc, s4, v237\n"
"s_cbranch_vccz .Lbacky_%=\n"
"s_sleep 1\n"
"s_sub_u32 s61, s61, 1\n"
"s_cbranch_scc0 .Lslowly_%=\n"
".Lbacky_%=:\n"
"v_mfma_f32_16x16x32_bf16 v[108:111], a[168:171], v[132:135], v[108:111]\n"
"buffer_load_dwordx4 a[240:243], v248, s[84:87], s20 offen offset:0 sc1\n"
"v_mfma_f32_16x16x32_bf16 v[112:115], a[172:175], v[132:135], v[112:115]\n"
"ds_write_b128 v121, v[40:43] offset:0\n"
"v_mfma_f32_16x16x32_bf16 v[116:119], a[176:179], v[132:135], v[116:119]\n"
"s_waitcnt lgkmcnt(4)\n"
"v_xor_b32 v236, 0x800, v236\n"
"v_mfma_f32_16x16x32_bf16 v[80:83], v[200:203], v[132:135], v[80:83]\n"
"buffer_load_dwordx4 a[244:247], v248, s[84:87], s21 offen offset:0 sc1\n"
"v_mfma_f32_16x16x32_bf16 v[84:87], v[204:207], v[132:135], v[84:87]\n"
"ds_write_b128 v121, v[44:47] offset:1024\n"
"v_mfma_f32_16x16x32_bf16 v[88:91], v[208:211], v[132:135], v[88:91]\n"
"ds_read_b128 v[200:203], v249 offset:33792\n"
"ds_read_b128 v[204:207], v249 offset:34816\n"
"v_mfma_f32_16x16x32_bf16 v[92:95], v[212:215], v[132:135], v[92:95]\n"
"buffer_load_dwordx4 a[248:251], v248, s[84:87], s20 offen offset:1024 sc1\n"
"v_mfma_f32_16x16x32_bf16 v[96:99], v[216:219], v[132:135], v[96:99]\n"
"s_waitcnt vmcnt(9)\n"
"ds_write_b128 v121, v[48:51] offset:2048\n"
"v_mfma_f32_16x16x32_bf16 v[80:83], a[20:23], v[136:139], v[80:83]\n"
"ds_write_b128 v121, v[52:55] offset:3072\n"
"v_mfma_f32_16x16x32_bf16 v[84:87], a[24:27], v[136:139], v[84:87]\n"
"ds_read_b128 v[208:211], v249 offset:35840\n"
"ds_read_b128 v[212:215], v249 offset:49152\n"
"v_mfma_f32_16x16x32_bf16 v[88:91], a[28:31], v[136:139], v[88:91]\n"
"buffer_load_dwordx4 a[252:255], v248, s[84:87], s21 offen offset:1024 sc1\n"
"v_mfma_f32_16x16x32_bf16 v[92:95], a[32:35], v[136:139], v[92:95]\n"
"ds_write_b128 v121, v[56:59] offset:4096\n"
"v_mfma_f32_16x16x32_bf16 v[96:99], a[36:39], v[136:139], v[96:99]\n"
"ds_read_b128 v[216:219], v249 offset:50176\n"
"v_mfma_f32_16x16x32_bf16 v[100:103], a[100:103], v[136:139], v[100:103]\n"
"buffer_load_dwordx4 v[128:131], v248, s[84:87], s20 offen offset:2048 sc1\n"
"v_mfma_f32_16x16x32_bf16 v[104:107], a[104:107], v[136:139], v[104:107]\n"
"v_mfma_f32_16x16x32_bf16 v[108:111], a[108:111], v[136:139], v[108:111]\n"
"v_mfma_f32_16x16x32_bf16 v[112:115], a[112:115], v[136:139], v[112:115]\n"
"buffer_load_dwordx4 v[132:135], v248, s[84:87], s21 offen offset:2048 sc1\n"
"v_mfma_f32_16x16x32_bf16 v[116:119], a[116:119], v[136:139], v[116:119]\n"
"s_waitcnt lgkmcnt(1)\n"
"s_waitcnt vmcnt(11)\n"
"v_mfma_f32_16x16x32_bf16 v[100:103], a[180:183], v[140:143], v[100:103]\n"
"s_barrier\n"
"v_mfma_f32_16x16x32_bf16 v[104:107], a[184:187], v[140:143], v[104:107]\n"
"buffer_load_dwordx4 v[136:139], v248, s[84:87], s20 offen offset:3072 sc1\n"
"v_mfma_f32_16x16x32_bf16 v[108:111], a[188:191], v[140:143], v[108:111]\n"
"ds_read_b128 v[20:23], v123 offset:0\n"
"v_mfma_f32_16x16x32_bf16 v[112:115], a[192:195], v[140:143], v[112:115]\n"
"ds_read_b128 v[40:43], v123 offset:5120\n"
"v_mfma_f32_16x16x32_bf16 v[116:119], a[196:199], v[140:143], v[116:119]\n"
"s_waitcnt lgkmcnt(2)\n"
"ds_read_b128 v[60:63], v123 offset:10240\n"
"v_mfma_f32_16x16x32_bf16 v[80:83], v[200:203], v[140:143], v[80:83]\n"
"ds_read_b128 v[200:203], v249 offset:51200\n"
"ds_read_b128 v[24:27], v123 offset:1024\n"
".Lpad18_%=:\n"
"s_cbranch_execz .Lhopfin_%=\n"
"v_mfma_f32_16x16x32_bf16 v[84:87], v[204:207], v[140:143], v[84:87]\n"
"ds_read_b128 v[44:47], v123 offset:6144\n"
"v_mfma_f32_16x16x32_bf16 v[88:91], v[208:211], v[140:143], v[88:91]\n"
"ds_read_b128 v[204:207], v249 offset:52224\n"
"ds_read_b128 v[64:67], v123 offset:11264\n"
"v_mfma_f32_16x16x32_bf16 v[92:95], v[212:215], v[140:143], v[92:95]\n"
"ds_read_b128 v[28:31], v123 offset:2048\n"
"v_mfma_f32_16x16x32_bf16 v[96:99], v[216:219], v[140:143], v[96:99]\n"
"s_waitcnt vmcnt(11)\n"
"buffer_load_dwordx4 v[140:143], v248, s[84:87], s21 offen offset:3072 sc1\n"
"v_mfma_f32_16x16x32_bf16 v[80:83], a[40:43], v[144:147], v[80:83]\n"
"ds_read_b128 v[208:211], v250 offset:16384\n"
"ds_read_b128 v[48:51], v123 offset:7168\n"
"v_mfma_f32_16x16x32_bf16 v[84:87], a[44:47], v[144:147], v[84:87]\n"
"ds_read_b128 v[212:215], v250 offset:17408\n"
"ds_read_b128 v[68:71], v123 offset:12288\n"
"v_mfma_f32_16x16x32_bf16 v[88:91], a[48:51], v[144:147], v[88:91]\n"
"ds_read_b128 v[32:35], v123 offset:3072\n"
"v_mfma_f32_16x16x32_bf16 v[92:95], a[52:55], v[144:147], v[92:95]\n"
"ds_read_b128 v[216:219], v250 offset:18432\n"
"ds_read_b128 v[52:55], v123 offset:8192\n"
"v_mfma_f32_16x16x32_bf16 v[96:99], a[56:59], v[144:147], v[96:99]\n"
"ds_read_b128 v[72:75], v123 offset:13312\n"
"v_mfma_f32_16x16x32_bf16 v[100:103], a[120:123], v[144:147], v[100:103]\n"
"ds_read_b128 v[36:39], v123 offset:4096\n"
"v_mfma_f32_16x16x32_bf16 v[104:107], a[124:127], v[144:147], v[104:107]\n"
"ds_read_b128 v[56:59], v123 offset:9216\n"
"v_mfma_f32_16x16x32_bf16 v[108:111], a[128:131], v[144:147], v[108:111]\n"
"ds_read_b128 v[76:79], v123 offset:14336\n"
"v_mfma_f32_16x16x32_bf16 v[112:115], a[132:135], v[144:147], v[112:115]\n"
"v_mfma_f32_16x16x32_bf16 v[116:119], a[136:139], v[144:147], v[116:119]\n"
"s_waitcnt vmcnt(11)\n"
"v_mfma_f32_16x16x32_bf16 v[100:103], a[200:203], v[148:151], v[100:103]\n"
"s_waitcnt lgkmcnt(15)\n"
"v_add_f32 v20, v20, v40\n"
"v_add_f32 v0, v0, v60\n"
"v_mfma_f32_16x16x32_bf16 v[104:107], a[204:207], v[148:151], v[104:107]\n"
"v_add_f32 v21, v21, v41\n"
"v_add_f32 v1, v1, v61\n"
"v_mfma_f32_16x16x32_bf16 v[108:111], a[208:211], v[148:151], v[108:111]\n"
"v_add_f32 v22, v22, v42\n"
"v_add_f32 v2, v2, v62\n"
"v_mfma_f32_16x16x32_bf16 v[112:115], a[212:215], v[148:151], v[112:115]\n"
"v_add_f32 v23, v23, v43\n"
"v_add_f32 v3, v3, v63\n"
"v_mfma_f32_16x16x32_bf16 v[116:119], a[216:219], v[148:151], v[116:119]\n"
"s_waitcnt lgkmcnt(5)\n"
"v_add_f32 v0, v20, v0\n"
"v_add_f32 v1, v21, v1\n"
"v_mfma_f32_16x16x32_bf16 v[80:83], v[200:203], v[148:151], v[80:83]\n"
"buffer_load_dword v237, v236, s[84:87], s10 offen sc1\n"
"v_mfma_f32_16x16x32_bf16 v[84:87], v[204:207], v[148:151], v[84:87]\n"
"ds_read_b128 v[200:203], v250 offset:19456\n"
"v_add_f32 v2, v22, v2\n"
"v_mfma_f32_16x16x32_bf16 v[88:91], v[208:211], v[148:151], v[88:91]\n"
"ds_read_b128 v[204:207], v250 offset:32768\n"
"v_add_f32 v3, v23, v3\n"
"s_waitcnt lgkmcnt(14)\n"
"v_mfma_f32_16x16x32_bf16 v[92:95], v[212:215], v[148:151], v[92:95]\n"
"v_add_f32 v24, v24, v44\n"
"v_add_f32 v4, v4, v64\n"
"v_mfma_f32_16x16x32_bf16 v[96:99], v[216:219], v[148:151], v[96:99]\n"
"s_waitcnt vmcnt(11)\n"
"ds_read_b128 v[208:211], v250 offset:33792\n"
"v_add_f32 v25, v25, v45\n"
"v_mfma_f32_16x16x32_bf16 v[80:83], a[60:63], v[152:155], v[80:83]\n"
"v_add_f32 v5, v5, v65\n"
"v_add_f32 v26, v26, v46\n"
"v_mfma_f32_16x16x32_bf16 v[84:87], a[64:67], v[152:155], v[84:87]\n"
"ds_read_b128 v[212:215], v250 offset:34816\n"
"v_add_f32 v6, v6, v66\n"
"v_mfma_f32_16x16x32_bf16 v[88:91], a[68:71], v[152:155], v[88:91]\n"
"v_add_f32 v27, v27, v47\n"
"v_add_f32 v7, v7, v67\n"
"v_mfma_f32_16x16x32_bf16 v[92:95], a[72:75], v[152:155], v[92:95]\n"
"ds_read_b128 v[216:219], v250 offset:35840\n"
"v_add_f32 v4, v24, v4\n"
"v_mfma_f32_16x16x32_bf16 v[96:99], a[76:79], v[152:155], v[96:99]\n"
"v_add_f32 v5, v25, v5\n"
"v_add_f32 v6, v26, v6\n"
"v_mfma_f32_16x16x32_bf16 v[100:103], a[140:143], v[152:155], v[100:103]\n"
"v_add_f32 v7, v27, v7\n"
"v_cvt_pk_bf16_f32 v0, v0, v1\n"
"v_mfma_f32_16x16x32_bf16 v[104:107], a[144:147], v[152:155], v[104:107]\n"
"v_cvt_pk_bf16_f32 v1, v2, v3\n"
"v_cvt_pk_bf16_f32 v2, v4, v5\n"
"v_mfma_f32_16x16x32_bf16 v[108:111], a[148:151], v[152:155], v[108:111]\n"
"v_cvt_pk_bf16_f32 v3, v6, v7\n"
"s_waitcnt lgkmcnt(12)\n"
"v_add_f32 v28, v28, v48\n"
"v_mfma_f32_16x16x32_bf16 v[112:115], a[152:155], v[152:155], v[112:115]\n"
"v_add_f32 v8, v8, v68\n"
"v_add_f32 v29, v29, v49\n"
"v_mfma_f32_16x16x32_bf16 v[116:119], a[156:159], v[152:155], v[116:119]\n"
"s_waitcnt vmcnt(9)\n"
"v_add_f32 v9, v9, v69\n"
"v_add_f32 v30, v30, v50\n"
"v_mfma_f32_16x16x32_bf16 v[100:103], a[220:223], v[156:159], v[100:103]\n"
"v_add_f32 v10, v10, v70\n"
"v_add_f32 v31, v31, v51\n"
"v_mfma_f32_16x16x32_bf16 v[104:107], a[224:227], v[156:159], v[104:107]\n"
"v_add_f32 v11, v11, v71\n"
"v_add_f32 v8, v28, v8\n"
"v_mfma_f32_16x16x32_bf16 v[108:111], a[228:231], v[156:159], v[108:111]\n"
"buffer_store_dwordx4 v[0:3], v248, s[84:87], s67 offen sc1\n"
"v_mfma_f32_16x16x32_bf16 v[112:115], a[232:235], v[156:159], v[112:115]\n"
"v_add_f32 v9, v29, v9\n"
"v_add_f32 v10, v30, v10\n"
"v_mfma_f32_16x16x32_bf16 v[116:119], a[236:239], v[156:159], v[116:119]\n"
"s_waitcnt lgkmcnt(0)\n"
"v_add_f32 v11, v31, v11\n"
"s_waitcnt lgkmcnt(8)\n"
"v_mfma_f32_16x16x32_bf16 v[80:83], v[200:203], v[156:159], v[80:83]\n"
"ds_read_b128 v[200:203], v249 offset:16384\n"
"v_add_f32 v32, v32, v52\n"
"v_mfma_f32_16x16x32_bf16 v[84:87], v[204:207], v[156:159], v[84:87]\n"
"v_add_f32 v12, v12, v72\n"
"v_add_f32 v33, v33, v53\n"
"v_mfma_f32_16x16x32_bf16 v[88:91], v[208:211], v[156:159], v[88:91]\n"
"ds_read_b128 v[204:207], v249 offset:17408\n"
"v_add_f32 v13, v13, v73\n"
"v_mfma_f32_16x16x32_bf16 v[92:95], v[212:215], v[156:159], v[92:95]\n"
"v_add_f32 v34, v34, v54\n"
"v_add_f32 v14, v14, v74\n"
"v_mfma_f32_16x16x32_bf16 v[96:99], v[216:219], v[156:159], v[96:99]\n"
"s_waitcnt vmcnt(9)\n"
"ds_read_b128 v[208:211], v249 offset:18432\n"
"v_add_f32 v35, v35, v55\n"
"v_mfma_f32_16x16x32_bf16 v[160:163], a[0:3], a[240:243], 0\n"
"v_add_f32 v15, v15, v75\n"
"v_add_f32 v12, v32, v12\n"
"v_mfma_f32_16x16x32_bf16 v[164:167], a[4:7], a[240:243], 0\n"
"ds_read_b128 v[212:215], v249 offset:19456\n"
"v_add_f32 v13, v33, v13\n"
"v_mfma_f32_16x16x32_bf16 v[168:171], a[8:11], a[240:243], 0\n"
"v_add_f32 v14, v34, v14\n"
"v_add_f32 v15, v35, v15\n"
"v_mfma_f32_16x16x32_bf16 v[172:175], a[12:15], a[240:243], 0\n"
"ds_read_b128 v[216:219], v249 offset:32768\n"
"v_cvt_pk_bf16_f32 v4, v8, v9\n"
"v_mfma_f32_16x16x32_bf16 v[176:179], a[16:19], a[240:243], 0\n"
"v_cvt_pk_bf16_f32 v5, v10, v11\n"
"v_cvt_pk_bf16_f32 v6, v12, v13\n"
"v_mfma_f32_16x16x32_bf16 v[180:183], a[80:83], a[240:243], 0\n"
"v_cvt_pk_bf16_f32 v7, v14, v15\n"
"s_waitcnt lgkmcnt(10)\n"
"v_add_f32 v36, v36, v56\n"
"v_mfma_f32_16x16x32_bf16 v[184:187], a[84:87], a[240:243], 0\n"
"v_add_f32 v16, v16, v76\n"
"v_add_f32 v37, v37, v57\n"
"v_mfma_f32_16x16x32_bf16 v[188:191], a[88:91], a[240:243], 0\n"
"s_bfe_u32 s74, s11, 0x30010\n"
"s_lshl_b32 s74, s74, 16\n"
"s_add_u32 s18, s74, s16\n"
"v_mfma_f32_16x16x32_bf16 v[192:195], a[92:95], a[240:243], 0\n"
"s_add_u32 s19, s18, s33\n"
"v_add_f32 v17, v17, v77\n"
"v_add_f32 v38, v38, v58\n"
"v_mfma_f32_16x16x32_bf16 v[196:199], a[96:99], a[240:243], 0\n"
"s_waitcnt vmcnt(8)\n"
"v_add_f32 v18, v18, v78\n"
"v_add_f32 v39, v39, v59\n"
"v_mfma_f32_16x16x32_bf16 v[180:183], a[160:163], a[244:247], v[180:183]\n"
"s_waitcnt vmcnt(1)\n"
"v_add_f32 v19, v19, v79\n"
"v_add_f32 v16, v36, v16\n"
"v_mfma_f32_16x16x32_bf16 v[184:187], a[164:167], a[244:247], v[184:187]\n"
"v_cmp_ne_u32 vcc, s4, v237\n"
"s_cbranch_vccz .Lbackz_%=\n"
"s_mov_b32 s61, 0x8000\n"
".Lslowlz_%=:\n"
"buffer_load_dword v237, v236, s[84:87], s10 offen sc1\n"
"s_waitcnt vmcnt(0)\n"
"v_cmp_ne_u32 vcc, s4, v237\n"
"s_cbranch_vccz .Lbackz_%=\n"
"s_sleep 1\n"
"s_sub_u32 s61, s61, 1\n"
"s_cbranch_scc0 .Lslowlz_%=\n"
".Lbackz_%=:\n"
"v_mfma_f32_16x16x32_bf16 v[188:191], a[168:171], a[244:247], v[188:191]\n"
"v_bfe_u32 v236, v248, 4, 2\n"
"v_max_u32 v236, 1, v236\n"
"v_mfma_f32_16x16x32_bf16 v[192:195], a[172:175], a[244:247], v[192:195]\n"
"v_lshlrev_b32 v236, 2, v236\n"
"v_add_f32 v17, v37, v17\n"
"v_mfma_f32_16x16x32_bf16 v[196:199], a[176:179], a[244:247], v[196:199]\n"
"s_waitcnt lgkmcnt(0)\n"
"buffer_load_dwordx4 v[144:147], v248, s[84:87], s18 offen offset:0 sc1\n"
"v_mfma_f32_16x16x32_bf16 v[160:163], v[200:203], a[244:247], v[160:163]\n"
"ds_read_b128 v[200:203], v249 offset:33792\n"
"v_add_f32 v18, v38, v18\n"
"v_mfma_f32_16x16x32_bf16 v[164:167], v[204:207], a[244:247], v[164:167]\n"
"v_add_f32 v19, v39, v19\n"
"v_cvt_pk_bf16_f32 v8, v16, v17\n"
"v_mfma_f32_16x16x32_bf16 v[168:171], v[208:211], a[244:247], v[168:171]\n"
"ds_read_b128 v[204:207], v249 offset:34816\n"
"v_cvt_pk_bf16_f32 v9, v18, v19\n"
"v_mfma_f32_16x16x32_bf16 v[172:175], v[212:215], a[244:247], v[172:175]\n"
"buffer_store_dwordx4 v[4:7], v248, s[84:87], s67 offen offset:1024 sc1\n"
"v_mfma_f32_16x16x32_bf16 v[176:179], v[216:219], a[244:247], v[176:179]\n"
"s_waitcnt vmcnt(9)\n"
"ds_read_b128 v[208:211], v249 offset:35840\n"
"v_mfma_f32_16x16x32_bf16 v[160:163], a[20:23], a[248:251], v[160:163]\n"
"v_mfma_f32_16x16x32_bf16 v[164:167], a[24:27], a[248:251], v[164:167]\n"
"ds_read_b128 v[212:215], v249 offset:49152\n"
"v_mfma_f32_16x16x32_bf16 v[168:171], a[28:31], a[248:251], v[168:171]\n"
"v_mfma_f32_16x16x32_bf16 v[172:175], a[32:35], a[248:251], v[172:175]\n"
"ds_read_b128 v[216:219], v249 offset:50176\n"
"v_mfma_f32_16x16x32_bf16 v[176:179], a[36:39], a[248:251], v[176:179]\n"
"v_mfma_f32_16x16x32_bf16 v[180:183], a[100:103], a[248:251], v[180:183]\n"
"v_mfma_f32_16x16x32_bf16 v[184:187], a[104:107], a[248:251], v[184:187]\n"
"v_mfma_f32_16x16x32_bf16 v[188:191], a[108:111], a[248:251], v[188:191]\n"
"v_mfma_f32_16x16x32_bf16 v[192:195], a[112:115], a[248:251], v[192:195]\n"
"buffer_store_dwordx2 v[8:9], v252, s[84:87], s67 offen offset:2048 sc1\n"
"s_bfe_u32 s74, s11, 0x30014\n"
"v_mfma_f32_16x16x32_bf16 v[196:199], a[116:119], a[248:251], v[196:199]\n"
"s_waitcnt vmcnt(9)\n"
"s_lshl_b32 s74, s74, 16\n"
"s_add_u32 s20, s74, s16\n"
"v_mfma_f32_16x16x32_bf16 v[180:183], a[180:183], a[252:255], v[180:183]\n"
"s_add_u32 s21, s20, s33\n"
"v_mfma_f32_16x16x32_bf16 v[184:187], a[184:187], a[252:255], v[184:187]\n"
"v_mfma_f32_16x16x32_bf16 v[188:191], a[188:191], a[252:255], v[188:191]\n"
"v_mfma_f32_16x16x32_bf16 v[192:195], a[192:195], a[252:255], v[192:195]\n"
"buffer_load_dwordx4 v[148:151], v248, s[84:87], s20 offen offset:0 sc1\n"
"v_mfma_f32_16x16x32_bf16 v[196:199], a[196:199], a[252:255], v[196:199]\n"
"s_waitcnt lgkmcnt(0)\n"
"v_mfma_f32_16x16x32_bf16 v[160:163], v[200:203], a[252:255], v[160:163]\n"
"ds_read_b128 v[200:203], v249 offset:51200\n"
"v_mfma_f32_16x16x32_bf16 v[164:167], v[204:207], a[252:255], v[164:167]\n"
"v_mfma_f32_16x16x32_bf16 v[168:171], v[208:211], a[252:255], v[168:171]\n"
"buffer_load_dwordx4 v[152:155], v248, s[84:87], s19 offen offset:0 sc1\n"
"v_mfma_f32_16x16x32_bf16 v[172:175], v[212:215], a[252:255], v[172:175]\n"
"ds_read_b128 v[204:207], v249 offset:52224\n"
"v_mfma_f32_16x16x32_bf16 v[176:179], v[216:219], a[252:255], v[176:179]\n"
"s_waitcnt vmcnt(10)\n"
"ds_read_b128 v[208:211], v250 offset:16384\n"
"v_mfma_f32_16x16x32_bf16 v[160:163], a[40:43], v[128:131], v[160:163]\n"
"v_mfma_f32_16x16x32_bf16 v[164:167], a[44:47], v[128:131], v[164:167]\n"
"buffer_load_dwordx4 v[156:159], v248, s[84:87], s21 offen offset:0 sc1\n"
"v_mfma_f32_16x16x32_bf16 v[168:171], a[48:51], v[128:131], v[168:171]\n"
"ds_read_b128 v[212:215], v250 offset:17408\n"
"v_mfma_f32_16x16x32_bf16 v[172:175], a[52:55], v[128:131], v[172:175]\n"
"ds_read_b128 v[216:219], v250 offset:18432\n"
"v_mfma_f32_16x16x32_bf16 v[176:179], a[56:59], v[128:131], v[176:179]\n"
"v_mfma_f32_16x16x32_bf16 v[180:183], a[120:123], v[128:131], v[180:183]\n"
"buffer_load_dwordx4 a[240:243], v248, s[84:87], s18 offen offset:1024 sc1\n"
"v_mfma_f32_16x16x32_bf16 v[184:187], a[124:127], v[128:131], v[184:187]\n"
"v_mfma_f32_16x16x32_bf16 v[188:191], a[128:131], v[128:131], v[188:191]\n"
"v_mfma_f32_16x16x32_bf16 v[192:195], a[132:135], v[128:131], v[192:195]\n"
"v_mfma_f32_16x16x32_bf16 v[196:199], a[136:139], v[128:131], v[196:199]\n"
"s_waitcnt vmcnt(11)\n"
"buffer_load_dwordx4 a[244:247], v248, s[84:87], s20 offen offset:1024 sc1\n"
"v_mfma_f32_16x16x32_bf16 v[180:183], a[200:203], v[132:135], v[180:183]\n"
"s_barrier\n"
"v_mfma_f32_16x16x32_bf16 v[184:187], a[204:207], v[132:135], v[184:187]\n"
"ds_write_b128 v124, v[80:83] offset:0\n"
"v_mfma_f32_16x16x32_bf16 v[188:191], a[208:211], v[132:135], v[188:191]\n"
"v_mfma_f32_16x16x32_bf16 v[192:195], a[212:215], v[132:135], v[192:195]\n"
"buffer_load_dwordx4 a[248:251], v248, s[84:87], s19 offen offset:1024 sc1\n"
"v_mfma_f32_16x16x32_bf16 v[196:199], a[216:219], v[132:135], v[196:199]\n"
"s_waitcnt lgkmcnt(1)\n"
"ds_write_b128 v124, v[84:87] offset:1024\n"
"v_mfma_f32_16x16x32_bf16 v[160:163], v[200:203], v[132:135], v[160:163]\n"
"ds_read_b128 v[200:203], v250 offset:19456\n"
"v_mfma_f32_16x16x32_bf16 v[164:167], v[204:207], v[132:135], v[164:167]\n"
"v_mfma_f32_16x16x32_bf16 v[168:171], v[208:211], v[132:135], v[168:171]\n"
"buffer_load_dwordx4 a[252:255], v248, s[84:87], s21 offen offset:1024 sc1\n"
"v_mfma_f32_16x16x32_bf16 v[172:175], v[212:215], v[132:135], v[172:175]\n"
"ds_write_b128 v124, v[88:91] offset:2048\n"
"v_mfma_f32_16x16x32_bf16 v[176:179], v[216:219], v[132:135], v[176:179]\n"
"s_waitcnt vmcnt(13)\n"
"ds_read_b128 v[204:207], v250 offset:32768\n"
"ds_read_b128 v[208:211], v250 offset:33792\n"
"v_mfma_f32_16x16x32_bf16 v[160:163], a[60:63], v[136:139], v[160:163]\n"
"ds_write_b128 v124, v[92:95] offset:3072\n"
"v_mfma_f32_16x16x32_bf16 v[164:167], a[64:67], v[136:139], v[164:167]\n"
"buffer_load_dwordx4 v[128:131], v248, s[84:87], s18 offen offset:2048 sc1\n"
"v_mfma_f32_16x16x32_bf16 v[168:171], a[68:71], v[136:139], v[168:171]\n"
"ds_read_b128 v[212:215], v250 offset:34816\n"
"v_mfma_f32_16x16x32_bf16 v[172:175], a[72:75], v[136:139], v[172:175]\n"
"ds_write_b128 v124, v[96:99] offset:4096\n"
"v_mfma_f32_16x16x32_bf16 v[176:179], a[76:79], v[136:139], v[176:179]\n"
"ds_read_b128 v[216:219], v250 offset:35840\n"
"v_mfma_f32_16x16x32_bf16 v[180:183], a[140:143], v[136:139], v[180:183]\n"
"buffer_load_dwordx4 v[132:135], v248, s[84:87], s20 offen offset:2048 sc1\n"
"v_mfma_f32_16x16x32_bf16 v[184:187], a[144:147], v[136:139], v[184:187]\n"
"ds_write_b128 v125, v[100:103] offset:0\n"
"v_mfma_f32_16x16x32_bf16 v[188:191], a[148:151], v[136:139], v[188:191]\n"
"v_mfma_f32_16x16x32_bf16 v[192:195], a[152:155], v[136:139], v[192:195]\n"
"v_mfma_f32_16x16x32_bf16 v[196:199], a[156:159], v[136:139], v[196:199]\n"
"s_waitcnt vmcnt(14)\n"
"buffer_load_dwordx4 v[136:139], v248, s[84:87], s19 offen offset:2048 sc1\n"
"v_mfma_f32_16x16x32_bf16 v[180:183], a[220:223], v[140:143], v[180:183]\n"
"s_waitcnt vmcnt(10)\n"
"buffer_store_dword v247, v246, s[84:87], s60 offen sc1\n"
"v_mfma_f32_16x16x32_bf16 v[184:187], a[224:227], v[140:143], v[184:187]\n"
"ds_write_b128 v125, v[104:107] offset:1024\n"
"v_mfma_f32_16x16x32_bf16 v[188:191], a[228:231], v[140:143], v[188:191]\n"
"ds_write_b128 v125, v[108:111] offset:2048\n"
"v_mfma_f32_16x16x32_bf16 v[192:195], a[232:235], v[140:143], v[192:195]\n"
"v_mfma_f32_16x16x32_bf16 v[196:199], a[236:239], v[140:143], v[196:199]\n"
"s_waitcnt lgkmcnt(3)\n"
"v_mfma_f32_16x16x32_bf16 v[160:163], v[200:203], v[140:143], v[160:163]\n"
"ds_write_b128 v125, v[112:115] offset:3072\n"
"v_mfma_f32_16x16x32_bf16 v[164:167], v[204:207], v[140:143], v[164:167]\n"
"ds_read_b128 v[200:203], v249 offset:16384\n"
"v_mfma_f32_16x16x32_bf16 v[168:171], v[208:211], v[140:143], v[168:171]\n"
"ds_read_b128 v[204:207], v249 offset:17408\n"
"v_mfma_f32_16x16x32_bf16 v[172:175], v[212:215], v[140:143], v[172:175]\n"
"ds_write_b128 v125, v[116:119] offset:4096\n"
"v_mfma_f32_16x16x32_bf16 v[176:179], v[216:219], v[140:143], v[176:179]\n"
"s_waitcnt vmcnt(10)\n"
"buffer_load_dwordx4 v[140:143], v248, s[84:87], s21 offen offset:2048 sc1\n"
"v_mfma_f32_16x16x32_bf16 v[0:3], a[0:3], v[144:147], 0\n"
"s_bfe_u32 s54, s11, 0x20008\n"
"ds_read_b128 v[208:211], v249 offset:18432\n"
"s_lshl3_add_u32 s67, s54, s66\n"
"v_mfma_f32_16x16x32_bf16 v[40:43], a[0:3], v[148:151], 0\n"
"s_bfe_u32 s55, s11, 0x1000a\n"
"ds_write_b128 v127, v[180:183] offset:0\n"
"v_mfma_f32_16x16x32_bf16 v[4:7], a[4:7], v[144:147], 0\n"
"s_lshl1_add_u32 s67, s55, s67\n"
"ds_read_b128 v[212:215], v249 offset:19456\n"
"s_bfe_u32 s55, %2, 0x20001\n"
"v_mfma_f32_16x16x32_bf16 v[44:47], a[4:7], v[148:151], 0\n"
"s_xor_b32 s54, s54, s55\n"
"ds_read_b128 v[216:219], v249 offset:32768\n"
"s_lshl2_add_u32 s54, s67, s54\n"
"v_mfma_f32_16x16x32_bf16 v[8:11], a[8:11], v[144:147], 0\n"
"s_lshl2_add_u32 s64, s54, 81846272\n"
"ds_write_b128 v127, v[184:187] offset:1024\n"
"v_mfma_f32_16x16x32_bf16 v[48:51], a[8:11], v[148:151], 0\n"
"s_sub_u32 s67, s54, s67\n"
"s_mul_i32 s67, s67, 2560\n"
"s_add_u32 s67, s67, 73397760\n"
"v_mfma_f32_16x16x32_bf16 v[12:15], a[12:15], v[144:147], 0\n"
"v_mfma_f32_16x16x32_bf16 v[52:55], a[12:15], v[148:151], 0\n"
"ds_write_b128 v127, v[188:191] offset:2048\n"
"v_mfma_f32_16x16x32_bf16 v[16:19], a[16:19], v[144:147], 0\n"
"v_mfma_f32_16x16x32_bf16 v[56:59], a[16:19], v[148:151], 0\n"
"v_mfma_f32_16x16x32_bf16 v[20:23], a[80:83], v[144:147], 0\n"
"ds_write_b128 v127, v[192:195] offset:3072\n"
"v_mfma_f32_16x16x32_bf16 v[60:63], a[80:83], v[148:151], 0\n"
"v_mfma_f32_16x16x32_bf16 v[24:27], a[84:87], v[144:147], 0\n"
"v_mfma_f32_16x16x32_bf16 v[64:67], a[84:87], v[148:151], 0\n"
"ds_write_b128 v127, v[196:199] offset:4096\n"
"v_mfma_f32_16x16x32_bf16 v[28:31], a[88:91], v[144:147], 0\n"
"v_mfma_f32_16x16x32_bf16 v[68:71], a[88:91], v[148:151], 0\n"
"v_mfma_f32_16x16x32_bf16 v[32:35], a[92:95], v[144:147], 0\n"
"ds_write_b128 v126, v[160:163] offset:0\n"
"v_mfma_f32_16x16x32_bf16 v[72:75], a[92:95], v[148:151], 0\n"
"v_mfma_f32_16x16x32_bf16 v[36:39], a[96:99], v[144:147], 0\n"
"v_mfma_f32_16x16x32_bf16 v[76:79], a[96:99], v[148:151], 0\n"
"s_waitcnt vmcnt(9)\n"
"buffer_load_dwordx4 v[144:147], v248, s[84:87], s18 offen offset:3072 sc1\n"
"v_mfma_f32_16x16x32_bf16 v[20:23], a[160:163], v[152:155], v[20:23]\n"
"ds_write_b128 v126, v[164:167] offset:1024\n"
"v_mfma_f32_16x16x32_bf16 v[60:63], a[160:163], v[156:159], v[60:63]\n"
"v_mfma_f32_16x16x32_bf16 v[24:27], a[164:167], v[152:155], v[24:27]\n"
"ds_write_b128 v126, v[168:171] offset:2048\n"
"v_mfma_f32_16x16x32_bf16 v[64:67], a[164:167], v[156:159], v[64:67]\n"
"buffer_load_dwordx4 v[148:151], v248, s[84:87], s20 offen offset:3072 sc1\n"
"v_mfma_f32_16x16x32_bf16 v[28:31], a[168:171], v[152:155], v[28:31]\n"
"v_mfma_f32_16x16x32_bf16 v[68:71], a[168:171], v[156:159], v[68:71]\n"
"ds_write_b128 v126, v[172:175] offset:3072\n"
"v_mfma_f32_16x16x32_bf16 v[32:35], a[172:175], v[152:155], v[32:35]\n"
"v_mfma_f32_16x16x32_bf16 v[72:75], a[172:175], v[156:159], v[72:75]\n"
"v_mfma_f32_16x16x32_bf16 v[36:39], a[176:179], v[152:155], v[36:39]\n"
"ds_write_b128 v126, v[176:179] offset:4096\n"
"v_mfma_f32_16x16x32_bf16 v[76:79], a[176:179], v[156:159], v[76:79]\n"
"s_waitcnt lgkmcnt(9)\n"
"v_cndmask_b32 v80, v80, v160, s[68:69]\n"
"v_cndmask_b32 v81, v81, v161, s[68:69]\n"
"v_mfma_f32_16x16x32_bf16 v[0:3], v[200:203], v[152:155], v[0:3]\n"
"v_cndmask_b32 v82, v82, v162, s[68:69]\n"
"v_cndmask_b32 v83, v83, v163, s[68:69]\n"
"v_mfma_f32_16x16x32_bf16 v[40:43], v[200:203], v[156:159], v[40:43]\n"
"ds_read_b128 v[200:203], v249 offset:33792\n"
"v_cndmask_b32 v84, v84, v164, s[68:69]\n"
"v_mfma_f32_16x16x32_bf16 v[4:7], v[204:207], v[152:155], v[4:7]\n"
"v_cndmask_b32 v85, v85, v165, s[68:69]\n"
"v_cndmask_b32 v86, v86, v166, s[68:69]\n"
"v_mfma_f32_16x16x32_bf16 v[44:47], v[204:207], v[156:159], v[44:47]\n"
"ds_read_b128 v[204:207], v249 offset:34816\n"
"v_cndmask_b32 v87, v87, v167, s[68:69]\n"
"v_mfma_f32_16x16x32_bf16 v[8:11], v[208:211], v[152:155], v[8:11]\n"
"v_cndmask_b32 v88, v88, v168, s[68:69]\n"
"v_cndmask_b32 v89, v89, v169, s[68:69]\n"
"v_mfma_f32_16x16x32_bf16 v[48:51], v[208:211], v[156:159], v[48:51]\n"
"s_waitcnt lgkmcnt(2)\n"
"v_cndmask_b32 v90, v90, v170, s[68:69]\n"
"v_cndmask_b32 v91, v91, v171, s[68:69]\n"
"v_mfma_f32_16x16x32_bf16 v[12:15], v[212:215], v[152:155], v[12:15]\n"
"s_barrier\n"
"ds_read_b128 v[208:211], v249 offset:35840\n"
"v_cndmask_b32 v92, v92, v172, s[68:69]\n"
"v_mfma_f32_16x16x32_bf16 v[52:55], v[212:215], v[156:159], v[52:55]\n"
"v_cndmask_b32 v93, v93, v173, s[68:69]\n"
"v_cndmask_b32 v94, v94, v174, s[68:69]\n"
"v_mfma_f32_16x16x32_bf16 v[16:19], v[216:219], v[152:155], v[16:19]\n"
"v_cndmask_b32 v95, v95, v175, s[68:69]\n"
"v_cndmask_b32 v96, v96, v176, s[68:69]\n"
"v_mfma_f32_16x16x32_bf16 v[56:59], v[216:219], v[156:159], v[56:59]\n"
"s_waitcnt vmcnt(9)\n"
"buffer_load_dwordx4 v[152:155], v248, s[84:87], s19 offen offset:3072 sc1\n"
"v_mfma_f32_16x16x32_bf16 v[0:3], a[20:23], a[240:243], v[0:3]\n"
"ds_read_b128 v[212:215], v249 offset:49152\n"
"v_cndmask_b32 v97, v97, v177, s[68:69]\n"
"v_mfma_f32_16x16x32_bf16 v[40:43], a[20:23], a[244:247], v[40:43]\n"
"v_cndmask_b32 v98, v98, v178, s[68:69]\n"
"v_cndmask_b32 v99, v99, v179, s[68:69]\n"
"v_mfma_f32_16x16x32_bf16 v[4:7], a[24:27], a[240:243], v[4:7]\n"
"ds_read_b128 v[216:219], v249 offset:50176\n"
"ds_read_b128 v[100:103], v123 offset:0\n"
"v_mfma_f32_16x16x32_bf16 v[44:47], a[24:27], a[244:247], v[44:47]\n"
"buffer_load_dwordx4 v[156:159], v248, s[84:87], s21 offen offset:3072 sc1\n"
"v_mfma_f32_16x16x32_bf16 v[8:11], a[28:31], a[240:243], v[8:11]\n"
"ds_read_b128 v[160:163], v123 offset:5120\n"
"v_mfma_f32_16x16x32_bf16 v[48:51], a[28:31], a[244:247], v[48:51]\n"
"ds_read_b128 v[180:183], v123 offset:10240\n"
"v_mfma_f32_16x16x32_bf16 v[12:15], a[32:35], a[240:243], v[12:15]\n"
"ds_read_b128 v[104:107], v123 offset:1024\n"
"v_mfma_f32_16x16x32_bf16 v[52:55], a[32:35], a[244:247], v[52:55]\n"
"ds_read_b128 v[164:167], v123 offset:6144\n"
"v_mfma_f32_16x16x32_bf16 v[16:19], a[36:39], a[240:243], v[16:19]\n"
"ds_read_b128 v[184:187], v123 offset:11264\n"
"v_mfma_f32_16x16x32_bf16 v[56:59], a[36:39], a[244:247], v[56:59]\n"
"ds_read_b128 v[108:111], v123 offset:2048\n"
"v_mfma_f32_16x16x32_bf16 v[20:23], a[100:103], a[240:243], v[20:23]\n"
"ds_read_b128 v[168:171], v123 offset:7168\n"
"v_mfma_f32_16x16x32_bf16 v[60:63], a[100:103], a[244:247], v[60:63]\n"
"ds_read_b128 v[188:191], v123 offset:12288\n"
"v_mfma_f32_16x16x32_bf16 v[24:27], a[104:107], a[240:243], v[24:27]\n"
"ds_read_b128 v[112:115], v123 offset:3072\n"
"v_mfma_f32_16x16x32_bf16 v[64:67], a[104:107], a[244:247], v[64:67]\n"
"ds_read_b128 v[172:175], v123 offset:8192\n"
"v_mfma_f32_16x16x32_bf16 v[28:31], a[108:111], a[240:243], v[28:31]\n"
"ds_read_b128 v[192:195], v123 offset:13312\n"
"v_mfma_f32_16x16x32_bf16 v[68:71], a[108:111], a[244:247], v[68:71]\n"
"s_bfe_u32 s74, s11, 0x30018\n"
"s_lshl_b32 s74, s74, 16\n"
"s_add_u32 s18, s74, s16\n"
"v_mfma_f32_16x16x32_bf16 v[32:35], a[112:115], a[240:243], v[32:35]\n"
"s_add_u32 s19, s18, s33\n"
"ds_read_b128 v[116:119], v123 offset:4096\n"
"v_mfma_f32_16x16x32_bf16 v[72:75], a[112:115], a[244:247], v[72:75]\n"
"ds_read_b128 v[176:179], v123 offset:9216\n"
"v_mfma_f32_16x16x32_bf16 v[36:39], a[116:119], a[240:243], v[36:39]\n"
"ds_read_b128 v[196:199], v123 offset:14336\n"
"v_mfma_f32_16x16x32_bf16 v[76:79], a[116:119], a[244:247], v[76:79]\n"
"s_waitcnt vmcnt(9)\n"
"buffer_load_dwordx4 a[240:243], v248, s[84:87], s18 offen offset:0 sc1\n"
"v_mfma_f32_16x16x32_bf16 v[20:23], a[180:183], a[248:251], v[20:23]\n"
"s_bfe_u32 s74, s11, 0x3001c\n"
"s_lshl_b32 s74, s74, 16\n"
"s_add_u32 s20, s74, s16\n"
"v_mfma_f32_16x16x32_bf16 v[60:63], a[180:183], a[252:255], v[60:63]\n"
"s_add_u32 s21, s20, s33\n"
"s_waitcnt lgkmcnt(12)\n"
"v_add_f32 v100, v100, v160\n"
"v_mfma_f32_16x16x32_bf16 v[24:27], a[184:187], a[248:251], v[24:27]\n"
"v_add_f32 v80, v80, v180\n"
"v_add_f32 v101, v101, v161\n"
"v_mfma_f32_16x16x32_bf16 v[64:67], a[184:187], a[252:255], v[64:67]\n"
"buffer_load_dwordx4 a[244:247], v248, s[84:87], s20 offen offset:0 sc1\n"
"v_mfma_f32_16x16x32_bf16 v[28:31], a[188:191], a[248:251], v[28:31]\n"
"v_add_f32 v81, v81, v181\n"
"v_add_f32 v102, v102, v162\n"
"v_mfma_f32_16x16x32_bf16 v[68:71], a[188:191], a[252:255], v[68:71]\n"
"v_add_f32 v82, v82, v182\n"
"v_add_f32 v103, v103, v163\n"
"v_mfma_f32_16x16x32_bf16 v[32:35], a[192:195], a[248:251], v[32:35]\n"
"v_add_f32 v83, v83, v183\n"
"v_add_f32 v80, v100, v80\n"
"v_mfma_f32_16x16x32_bf16 v[72:75], a[192:195], a[252:255], v[72:75]\n"
"buffer_load_dwordx4 v[220:223], v248, s[84:87], s13 offen offset:0 sc1\n"
"v_mfma_f32_16x16x32_bf16 v[36:39], a[196:199], a[248:251], v[36:39]\n"
"v_add_f32 v81, v101, v81\n"
"v_add_f32 v82, v102, v82\n"
"v_mfma_f32_16x16x32_bf16 v[76:79], a[196:199], a[252:255], v[76:79]\n"
"s_waitcnt lgkmcnt(15)\n"
"v_add_f32 v83, v103, v83\n"
"s_waitcnt lgkmcnt(9)\n"
"v_mfma_f32_16x16x32_bf16 v[0:3], v[200:203], a[248:251], v[0:3]\n"
"v_add_f32 v104, v104, v164\n"
"v_add_f32 v84, v84, v184\n"
"v_mfma_f32_16x16x32_bf16 v[40:43], v[200:203], a[252:255], v[40:43]\n"
"buffer_load_dwordx4 v[224:227], v248, s[84:87], s14 offen offset:0 sc1\n"
"v_mfma_f32_16x16x32_bf16 v[4:7], v[204:207], a[248:251], v[4:7]\n"
"ds_read_b128 v[200:203], v249 offset:51200\n"
"v_add_f32 v105, v105, v165\n"
"v_mfma_f32_16x16x32_bf16 v[44:47], v[204:207], a[252:255], v[44:47]\n"
"ds_read_b128 v[204:207], v249 offset:52224\n"
"v_add_f32 v85, v85, v185\n"
"v_mfma_f32_16x16x32_bf16 v[8:11], v[208:211], a[248:251], v[8:11]\n"
"v_add_f32 v106, v106, v166\n"
"v_add_f32 v86, v86, v186\n"
"v_mfma_f32_16x16x32_bf16 v[48:51], v[208:211], a[252:255], v[48:51]\n"
"buffer_load_dwordx4 v[228:231], v248, s[84:87], s15 offen offset:0 sc1\n"
"v_mfma_f32_16x16x32_bf16 v[12:15], v[212:215], a[248:251], v[12:15]\n"
"ds_read_b128 v[208:211], v250 offset:16384\n"
"v_add_f32 v107, v107, v167\n"
"v_mfma_f32_16x16x32_bf16 v[52:55], v[212:215], a[252:255], v[52:55]\n"
"v_add_f32 v87, v87, v187\n"
"v_add_f32 v84, v104, v84\n"
"v_mfma_f32_16x16x32_bf16 v[16:19], v[216:219], a[248:251], v[16:19]\n"
"v_add_f32 v85, v105, v85\n"
"v_add_f32 v86, v106, v86\n"
"v_mfma_f32_16x16x32_bf16 v[56:59], v[216:219], a[252:255], v[56:59]\n"
"s_waitcnt vmcnt(12)\n"
"ds_read_b128 v[212:215], v250 offset:17408\n"
"v_add_f32 v87, v107, v87\n"
"v_mfma_f32_16x16x32_bf16 v[0:3], a[40:43], v[128:131], v[0:3]\n"
"v_cvt_pk_bf16_f32 v80, v80, v81\n"
"v_cvt_pk_bf16_f32 v81, v82, v83\n"
"v_mfma_f32_16x16x32_bf16 v[40:43], a[40:43], v[132:135], v[40:43]\n"
"v_cvt_pk_bf16_f32 v82, v84, v85\n"
"v_cvt_pk_bf16_f32 v83, v86, v87\n"
"s_waitcnt lgkmcnt(10)\n"
"v_mfma_f32_16x16x32_bf16 v[4:7], a[44:47], v[128:131], v[4:7]\n"
"buffer_store_dwordx4 v[80:83], v248, s[84:87], s67 offen sc1\n"
"v_mfma_f32_16x16x32_bf16 v[44:47], a[44:47], v[132:135], v[44:47]\n"
"ds_read_b128 v[216:219], v250 offset:18432\n"
"v_add_f32 v108, v108, v168\n"
"v_mfma_f32_16x16x32_bf16 v[8:11], a[48:51], v[128:131], v[8:11]\n"
"v_add_f32 v88, v88, v188\n"
"v_add_f32 v109, v109, v169\n"
"v_mfma_f32_16x16x32_bf16 v[48:51], a[48:51], v[132:135], v[48:51]\n"
"v_add_f32 v89, v89, v189\n"
"v_add_f32 v110, v110, v170\n"
"v_mfma_f32_16x16x32_bf16 v[12:15], a[52:55], v[128:131], v[12:15]\n"
"v_add_f32 v90, v90, v190\n"
"v_add_f32 v111, v111, v171\n"
"v_mfma_f32_16x16x32_bf16 v[52:55], a[52:55], v[132:135], v[52:55]\n"
"v_add_f32 v91, v91, v191\n"
"v_add_f32 v88, v108, v88\n"
"v_mfma_f32_16x16x32_bf16 v[16:19], a[56:59], v[128:131], v[16:19]\n"
"v_add_f32 v89, v109, v89\n"
"v_add_f32 v90, v110, v90\n"
"v_mfma_f32_16x16x32_bf16 v[56:59], a[56:59], v[132:135], v[56:59]\n"
"v_add_f32 v91, v111, v91\n"
"s_waitcnt lgkmcnt(8)\n"
"v_add_f32 v112, v112, v172\n"
"v_mfma_f32_16x16x32_bf16 v[20:23], a[120:123], v[128:131], v[20:23]\n"
"v_add_f32 v92, v92, v192\n"
"v_add_f32 v113, v113, v173\n"
"v_mfma_f32_16x16x32_bf16 v[60:63], a[120:123], v[132:135], v[60:63]\n"
"v_add_f32 v93, v93, v193\n"
"v_add_f32 v114, v114, v174\n"
"v_mfma_f32_16x16x32_bf16 v[24:27], a[124:127], v[128:131], v[24:27]\n"
"buffer_load_dwordx4 a[248:251], v248, s[84:87], s19 offen offset:0 sc1\n"
"v_mfma_f32_16x16x32_bf16 v[64:67], a[124:127], v[132:135], v[64:67]\n"
"v_add_f32 v94, v94, v194\n"
"v_add_f32 v115, v115, v175\n"
"v_mfma_f32_16x16x32_bf16 v[28:31], a[128:131], v[128:131], v[28:31]\n"
"v_add_f32 v95, v95, v195\n"
"v_add_f32 v92, v112, v92\n"
"v_mfma_f32_16x16x32_bf16 v[68:71], a[128:131], v[132:135], v[68:71]\n"
"v_add_f32 v93, v113, v93\n"
"v_add_f32 v94, v114, v94\n"
"v_mfma_f32_16x16x32_bf16 v[32:35], a[132:135], v[128:131], v[32:35]\n"
"buffer_load_dwordx4 a[252:255], v248, s[84:87], s21 offen offset:0 sc1\n"
"v_mfma_f32_16x16x32_bf16 v[72:75], a[132:135], v[132:135], v[72:75]\n"
"v_add_f32 v95, v115, v95\n"
"v_cvt_pk_bf16_f32 v84, v88, v89\n"
"v_mfma_f32_16x16x32_bf16 v[36:39], a[136:139], v[128:131], v[36:39]\n"
"v_cvt_pk_bf16_f32 v85, v90, v91\n"
"v_cvt_pk_bf16_f32 v86, v92, v93\n"
"v_mfma_f32_16x16x32_bf16 v[76:79], a[136:139], v[132:135], v[76:79]\n"
"s_waitcnt vmcnt(12)\n"
"v_cvt_pk_bf16_f32 v87, v94, v95\n"
"s_waitcnt lgkmcnt(5)\n"
"v_mfma_f32_16x16x32_bf16 v[20:23], a[200:203], v[136:139], v[20:23]\n"
"buffer_load_dwordx4 v[232:235], v248, s[84:87], s12 offen offset:0 sc1\n"
"v_mfma_f32_16x16x32_bf16 v[60:63], a[200:203], v[140:143], v[60:63]\n"
"v_add_f32 v116, v116, v176\n"
"v_add_f32 v96, v96, v196\n"
"v_mfma_f32_16x16x32_bf16 v[24:27], a[204:207], v[136:139], v[24:27]\n"
"v_add_f32 v117, v117, v177\n"
"v_add_f32 v97, v97, v197\n"
"v_mfma_f32_16x16x32_bf16 v[64:67], a[204:207], v[140:143], v[64:67]\n"
"v_add_f32 v118, v118, v178\n"
"v_add_f32 v98, v98, v198\n"
"v_mfma_f32_16x16x32_bf16 v[28:31], a[208:211], v[136:139], v[28:31]\n"
"buffer_store_dwordx4 v[84:87], v248, s[84:87], s67 offen offset:1024 sc1\n"
"v_mfma_f32_16x16x32_bf16 v[68:71], a[208:211], v[140:143], v[68:71]\n"
"v_add_f32 v119, v119, v179\n"
"v_add_f32 v99, v99, v199\n"
"v_mfma_f32_16x16x32_bf16 v[32:35], a[212:215], v[136:139], v[32:35]\n"
"v_add_f32 v96, v116, v96\n"
"v_add_f32 v97, v117, v97\n"
"v_mfma_f32_16x16x32_bf16 v[72:75], a[212:215], v[140:143], v[72:75]\n"
"v_add_f32 v98, v118, v98\n"
"v_add_f32 v99, v119, v99\n"
"v_mfma_f32_16x16x32_bf16 v[36:39], a[216:219], v[136:139], v[36:39]\n"
"v_cvt_pk_bf16_f32 v88, v96, v97\n"
"v_cvt_pk_bf16_f32 v89, v98, v99\n"
"v_mfma_f32_16x16x32_bf16 v[76:79], a[216:219], v[140:143], v[76:79]\n"
"s_waitcnt lgkmcnt(0)\n"
"v_mfma_f32_16x16x32_bf16 v[0:3], v[200:203], v[136:139], v[0:3]\n"
"v_mfma_f32_16x16x32_bf16 v[40:43], v[200:203], v[140:143], v[40:43]\n"
"ds_read_b128 v[200:203], v250 offset:19456\n"
"v_mfma_f32_16x16x32_bf16 v[4:7], v[204:207], v[136:139], v[4:7]\n"
"v_mfma_f32_16x16x32_bf16 v[44:47], v[204:207], v[140:143], v[44:47]\n"
"ds_read_b128 v[204:207], v250 offset:32768\n"
"v_mfma_f32_16x16x32_bf16 v[8:11], v[208:211], v[136:139], v[8:11]\n"
"buffer_store_dwordx2 v[88:89], v252, s[84:87], s67 offen offset:2048 sc1\n"
".Lpad19_%=:\n"
"s_cbranch_execz .Lhopfin_%=\n"
"v_mfma_f32_16x16x32_bf16 v[48:51], v[208:211], v[140:143], v[48:51]\n"
"v_mfma_f32_16x16x32_bf16 v[12:15], v[212:215], v[136:139], v[12:15]\n"
"ds_read_b128 v[208:211], v250 offset:33792\n"
"v_mfma_f32_16x16x32_bf16 v[52:55], v[212:215], v[140:143], v[52:55]\n"
"v_mfma_f32_16x16x32_bf16 v[16:19], v[216:219], v[136:139], v[16:19]\n"
"v_mfma_f32_16x16x32_bf16 v[56:59], v[216:219], v[140:143], v[56:59]\n"
"s_waitcnt vmcnt(13)\n"
"buffer_load_dwordx4 v[128:131], v248, s[84:87], s18 offen offset:1024 sc1\n"
"v_mfma_f32_16x16x32_bf16 v[0:3], a[60:63], v[144:147], v[0:3]\n"
"ds_read_b128 v[212:215], v250 offset:34816\n"
"v_mfma_f32_16x16x32_bf16 v[40:43], a[60:63], v[148:151], v[40:43]\n"
"v_mfma_f32_16x16x32_bf16 v[4:7], a[64:67], v[144:147], v[4:7]\n"
"ds_read_b128 v[216:219], v250 offset:35840\n"
"v_mfma_f32_16x16x32_bf16 v[44:47], a[64:67], v[148:151], v[44:47]\n"
"buffer_load_dwordx4 v[132:135], v248, s[84:87], s20 offen offset:1024 sc1\n"
"v_mfma_f32_16x16x32_bf16 v[8:11], a[68:71], v[144:147], v[8:11]\n"
"v_mfma_f32_16x16x32_bf16 v[48:51], a[68:71], v[148:151], v[48:51]\n"
"v_mfma_f32_16x16x32_bf16 v[12:15], a[72:75], v[144:147], v[12:15]\n"
"v_mfma_f32_16x16x32_bf16 v[52:55], a[72:75], v[148:151], v[52:55]\n"
"buffer_load_dwordx4 v[136:139], v248, s[84:87], s19 offen offset:1024 sc1\n"
"v_mfma_f32_16x16x32_bf16 v[16:19], a[76:79], v[144:147], v[16:19]\n"
"v_mfma_f32_16x16x32_bf16 v[56:59], a[76:79], v[148:151], v[56:59]\n"
"v_mfma_f32_16x16x32_bf16 v[20:23], a[140:143], v[144:147], v[20:23]\n"
"s_barrier\n"
"v_mfma_f32_16x16x32_bf16 v[60:63], a[140:143], v[148:151], v[60:63]\n"
"buffer_load_dwordx4 v[140:143], v248, s[84:87], s21 offen offset:1024 sc1\n"
"v_mfma_f32_16x16x32_bf16 v[24:27], a[144:147], v[144:147], v[24:27]\n"
"v_mfma_f32_16x16x32_bf16 v[64:67], a[144:147], v[148:151], v[64:67]\n"
"v_mfma_f32_16x16x32_bf16 v[28:31], a[148:151], v[144:147], v[28:31]\n"
"v_mfma_f32_16x16x32_bf16 v[68:71], a[148:151], v[148:151], v[68:71]\n"
"buffer_load_dwordx4 v[240:243], v248, s[84:87], s17 offen offset:0 sc1\n"
"v_mfma_f32_16x16x32_bf16 v[32:35], a[152:155], v[144:147], v[32:35]\n"
"v_mfma_f32_16x16x32_bf16 v[72:75], a[152:155], v[148:151], v[72:75]\n"
"v_mfma_f32_16x16x32_bf16 v[36:39], a[156:159], v[144:147], v[36:39]\n"
"v_mfma_f32_16x16x32_bf16 v[76:79], a[156:159], v[148:151], v[76:79]\n"
"s_waitcnt vmcnt(16)\n"
"buffer_load_dwordx4 v[124:127], v248, s[84:87], s17 offen offset:1024 sc1\n"
"v_mfma_f32_16x16x32_bf16 v[20:23], a[220:223], v[152:155], v[20:23]\n"
"v_mfma_f32_16x16x32_bf16 v[60:63], a[220:223], v[156:159], v[60:63]\n"
"v_mfma_f32_16x16x32_bf16 v[24:27], a[224:227], v[152:155], v[24:27]\n"
"v_mfma_f32_16x16x32_bf16 v[64:67], a[224:227], v[156:159], v[64:67]\n"
"buffer_load_dwordx4 v[144:147], v248, s[84:87], s18 offen offset:2048 sc1\n"
"v_mfma_f32_16x16x32_bf16 v[28:31], a[228:231], v[152:155], v[28:31]\n"
"v_mfma_f32_16x16x32_bf16 v[68:71], a[228:231], v[156:159], v[68:71]\n"
"s_nop 1\n"
"ds_write_b128 v120, v[20:23] offset:0\n"
"v_mfma_f32_16x16x32_bf16 v[32:35], a[232:235], v[152:155], v[32:35]\n"
"v_mfma_f32_16x16x32_bf16 v[72:75], a[232:235], v[156:159], v[72:75]\n"
"buffer_load_dwordx4 v[148:151], v248, s[84:87], s20 offen offset:2048 sc1\n"
"v_mfma_f32_16x16x32_bf16 v[36:39], a[236:239], v[152:155], v[36:39]\n"
"ds_write_b128 v122, v[60:63] offset:0\n"
"v_mfma_f32_16x16x32_bf16 v[76:79], a[236:239], v[156:159], v[76:79]\n"
"s_waitcnt lgkmcnt(2)\n"
"v_mfma_f32_16x16x32_bf16 v[0:3], v[200:203], v[152:155], v[0:3]\n"
"s_waitcnt vmcnt(8)\n"
"buffer_store_dword v247, v246, s[84:87], s64 offen sc1\n"
"v_mfma_f32_16x16x32_bf16 v[40:43], v[200:203], v[156:159], v[40:43]\n"
"ds_write_b128 v120, v[24:27] offset:1024\n"
"v_mfma_f32_16x16x32_bf16 v[4:7], v[204:207], v[152:155], v[4:7]\n"
"ds_read_b128 v[200:203], v249 offset:16384\n"
"v_mfma_f32_16x16x32_bf16 v[44:47], v[204:207], v[156:159], v[44:47]\n"
"ds_write_b128 v122, v[64:67] offset:1024\n"
"v_mfma_f32_16x16x32_bf16 v[8:11], v[208:211], v[152:155], v[8:11]\n"
"ds_read_b128 v[204:207], v249 offset:17408\n"
"v_mfma_f32_16x16x32_bf16 v[48:51], v[208:211], v[156:159], v[48:51]\n"
"ds_write_b128 v120, v[28:31] offset:2048\n"
"v_mfma_f32_16x16x32_bf16 v[12:15], v[212:215], v[152:155], v[12:15]\n"
"ds_read_b128 v[208:211], v249 offset:18432\n"
"v_mfma_f32_16x16x32_bf16 v[52:55], v[212:215], v[156:159], v[52:55]\n"
"v_mfma_f32_16x16x32_bf16 v[16:19], v[216:219], v[152:155], v[16:19]\n"
"ds_write_b128 v122, v[68:71] offset:2048\n"
"v_mfma_f32_16x16x32_bf16 v[56:59], v[216:219], v[156:159], v[56:59]\n"
"s_waitcnt vmcnt(18)\n"
"buffer_load_dwordx4 v[152:155], v248, s[84:87], s19 offen offset:2048 sc1\n"
"v_mfma_f32_16x16x32_bf16 v[80:83], a[0:3], a[240:243], 0\n"
"s_bfe_u32 s54, s11, 0x20010\n"
"ds_read_b128 v[212:215], v249 offset:19456\n"
"s_lshl3_add_u32 s67, s54, s66\n"
"v_mfma_f32_16x16x32_bf16 v[160:163], a[0:3], a[244:247], 0\n"
"s_bfe_u32 s55, s11, 0x10012\n"
"ds_write_b128 v120, v[32:35] offset:3072\n"
"v_mfma_f32_16x16x32_bf16 v[84:87], a[4:7], a[240:243], 0\n"
"s_lshl1_add_u32 s67, s55, s67\n"
"ds_read_b128 v[216:219], v249 offset:32768\n"
"s_bfe_u32 s55, %2, 0x20001\n"
"v_mfma_f32_16x16x32_bf16 v[164:167], a[4:7], a[244:247], 0\n"
"s_xor_b32 s54, s54, s55\n"
"buffer_load_dwordx4 v[156:159], v248, s[84:87], s21 offen offset:2048 sc1\n"
"v_mfma_f32_16x16x32_bf16 v[88:91], a[8:11], a[240:243], 0\n"
"s_lshl2_add_u32 s54, s67, s54\n"
"ds_write_b128 v122, v[72:75] offset:3072\n"
"v_mfma_f32_16x16x32_bf16 v[168:171], a[8:11], a[244:247], 0\n"
"s_lshl2_add_u32 s65, s54, 81846272\n"
"s_sub_u32 s67, s54, s67\n"
"s_mul_i32 s67, s67, 2560\n"
"v_mfma_f32_16x16x32_bf16 v[92:95], a[12:15], a[240:243], 0\n"
"s_add_u32 s67, s67, 73397760\n"
"ds_write_b128 v120, v[36:39] offset:4096\n"
"v_mfma_f32_16x16x32_bf16 v[172:175], a[12:15], a[244:247], 0\n"
"buffer_load_dwordx4 a[0:3], v248, s[84:87], s17 offen offset:2048 sc1\n"
"v_mfma_f32_16x16x32_bf16 v[96:99], a[16:19], a[240:243], 0\n"
"v_mfma_f32_16x16x32_bf16 v[176:179], a[16:19], a[244:247], 0\n"
"ds_write_b128 v122, v[76:79] offset:4096\n"
"v_mfma_f32_16x16x32_bf16 v[100:103], a[80:83], a[240:243], 0\n"
"v_mfma_f32_16x16x32_bf16 v[180:183], a[80:83], a[244:247], 0\n"
"buffer_load_dwordx4 a[4:7], v248, s[84:87], s17 offen offset:3072 sc1\n"
"s_add_u32 s17, s17, 4096\n"
"v_mfma_f32_16x16x32_bf16 v[104:107], a[84:87], a[240:243], 0\n"
"ds_write_b128 v121, v[40:43] offset:0\n"
"v_mfma_f32_16x16x32_bf16 v[184:187], a[84:87], a[244:247], 0\n"
"v_mfma_f32_16x16x32_bf16 v[108:111], a[88:91], a[240:243], 0\n"
"v_mfma_f32_16x16x32_bf16 v[188:191], a[88:91], a[244:247], 0\n"
"buffer_load_dwordx4 a[8:11], v248, s[84:87], s17 offen offset:0 sc1\n"
"v_mfma_f32_16x16x32_bf16 v[112:115], a[92:95], a[240:243], 0\n"
"ds_write_b128 v121, v[44:47] offset:1024\n"
"v_mfma_f32_16x16x32_bf16 v[192:195], a[92:95], a[244:247], 0\n"
"ds_write_b128 v121, v[48:51] offset:2048\n"
"v_mfma_f32_16x16x32_bf16 v[116:119], a[96:99], a[240:243], 0\n"
"v_mfma_f32_16x16x32_bf16 v[196:199], a[96:99], a[244:247], 0\n"
"s_waitcnt vmcnt(17)\n"
"buffer_load_dwordx4 a[240:243], v248, s[84:87], s18 offen offset:3072 sc1\n"
"v_mfma_f32_16x16x32_bf16 v[100:103], a[160:163], a[248:251], v[100:103]\n"
"ds_write_b128 v121, v[52:55] offset:3072\n"
"v_mfma_f32_16x16x32_bf16 v[180:183], a[160:163], a[252:255], v[180:183]\n"
"v_mfma_f32_16x16x32_bf16 v[104:107], a[164:167], a[248:251], v[104:107]\n"
"v_mfma_f32_16x16x32_bf16 v[184:187], a[164:167], a[252:255], v[184:187]\n"
"buffer_load_dwordx4 a[244:247], v248, s[84:87], s20 offen offset:3072 sc1\n"
"v_mfma_f32_16x16x32_bf16 v[108:111], a[168:171], a[248:251], v[108:111]\n"
"ds_write_b128 v121, v[56:59] offset:4096\n"
"v_mfma_f32_16x16x32_bf16 v[188:191], a[168:171], a[252:255], v[188:191]\n"
"v_mfma_f32_16x16x32_bf16 v[112:115], a[172:175], a[248:251], v[112:115]\n"
"v_mfma_f32_16x16x32_bf16 v[192:195], a[172:175], a[252:255], v[192:195]\n"
"buffer_load_dwordx4 a[12:15], v248, s[84:87], s13 offen offset:1024 sc1\n"
"v_mfma_f32_16x16x32_bf16 v[116:119], a[176:179], a[248:251], v[116:119]\n"
"v_mfma_f32_16x16x32_bf16 v[196:199], a[176:179], a[252:255], v[196:199]\n"
"s_waitcnt lgkmcnt(0)\n"
"s_waitcnt lgkmcnt(8)\n"
"v_mfma_f32_16x16x32_bf16 v[80:83], v[200:203], a[248:251], v[80:83]\n"
"s_barrier\n"
"v_mfma_f32_16x16x32_bf16 v[160:163], v[200:203], a[252:255], v[160:163]\n"
"buffer_load_dwordx4 a[16:19], v248, s[84:87], s14 offen offset:1024 sc1\n"
"v_mfma_f32_16x16x32_bf16 v[84:87], v[204:207], a[248:251], v[84:87]\n"
"ds_read_b128 v[200:203], v249 offset:33792\n"
"ds_read_b128 v[20:23], v123 offset:0\n"
"v_mfma_f32_16x16x32_bf16 v[164:167], v[204:207], a[252:255], v[164:167]\n"
"ds_read_b128 v[204:207], v249 offset:34816\n"
"ds_read_b128 v[40:43], v123 offset:5120\n"
"v_mfma_f32_16x16x32_bf16 v[88:91], v[208:211], a[248:251], v[88:91]\n"
"ds_read_b128 v[60:63], v123 offset:10240\n"
"v_mfma_f32_16x16x32_bf16 v[168:171], v[208:211], a[252:255], v[168:171]\n"
"buffer_load_dwordx4 a[80:83], v248, s[84:87], s15 offen offset:1024 sc1\n"
"v_mfma_f32_16x16x32_bf16 v[92:95], v[212:215], a[248:251], v[92:95]\n"
"ds_read_b128 v[208:211], v249 offset:35840\n"
"ds_read_b128 v[24:27], v123 offset:1024\n"
"v_mfma_f32_16x16x32_bf16 v[172:175], v[212:215], a[252:255], v[172:175]\n"
"ds_read_b128 v[44:47], v123 offset:6144\n"
"v_mfma_f32_16x16x32_bf16 v[96:99], v[216:219], a[248:251], v[96:99]\n"
"ds_read_b128 v[64:67], v123 offset:11264\n"
"v_mfma_f32_16x16x32_bf16 v[176:179], v[216:219], a[252:255], v[176:179]\n"
"s_waitcnt vmcnt(17)\n"
"buffer_load_dwordx4 a[248:251], v248, s[84:87], s19 offen offset:3072 sc1\n"
"v_mfma_f32_16x16x32_bf16 v[80:83], a[20:23], v[128:131], v[80:83]\n"
"ds_read_b128 v[212:215], v249 offset:49152\n"
"ds_read_b128 v[28:31], v123 offset:2048\n"
"v_mfma_f32_16x16x32_bf16 v[160:163], a[20:23], v[132:135], v[160:163]\n"
"ds_read_b128 v[48:51], v123 offset:7168\n"
"v_mfma_f32_16x16x32_bf16 v[84:87], a[24:27], v[128:131], v[84:87]\n"
"ds_read_b128 v[216:219], v249 offset:50176\n"
"ds_read_b128 v[68:71], v123 offset:12288\n"
"v_mfma_f32_16x16x32_bf16 v[164:167], a[24:27], v[132:135], v[164:167]\n"
"buffer_load_dwordx4 a[252:255], v248, s[84:87], s21 offen offset:3072 sc1\n"
"v_mfma_f32_16x16x32_bf16 v[88:91], a[28:31], v[128:131], v[88:91]\n"
"ds_read_b128 v[32:35], v123 offset:3072\n"
"v_mfma_f32_16x16x32_bf16 v[168:171], a[28:31], v[132:135], v[168:171]\n"
"ds_read_b128 v[52:55], v123 offset:8192\n"
"v_mfma_f32_16x16x32_bf16 v[92:95], a[32:35], v[128:131], v[92:95]\n"
"ds_read_b128 v[72:75], v123 offset:13312\n"
"v_mfma_f32_16x16x32_bf16 v[172:175], a[32:35], v[132:135], v[172:175]\n"
"buffer_load_dwordx4 a[84:87], v248, s[84:87], s12 offen offset:1024 sc1\n"
"v_mfma_f32_16x16x32_bf16 v[96:99], a[36:39], v[128:131], v[96:99]\n"
"ds_read_b128 v[36:39], v123 offset:4096\n"
"v_mfma_f32_16x16x32_bf16 v[176:179], a[36:39], v[132:135], v[176:179]\n"
"ds_read_b128 v[56:59], v123 offset:9216\n"
"v_mfma_f32_16x16x32_bf16 v[100:103], a[100:103], v[128:131], v[100:103]\n"
"ds_read_b128 v[76:79], v123 offset:14336\n"
"v_mfma_f32_16x16x32_bf16 v[180:183], a[100:103], v[132:135], v[180:183]\n"
"buffer_load_dwordx4 a[88:91], v248, s[84:87], s17 offen offset:1024 sc1\n"
"v_mfma_f32_16x16x32_bf16 v[104:107], a[104:107], v[128:131], v[104:107]\n"
"v_mfma_f32_16x16x32_bf16 v[184:187], a[104:107], v[132:135], v[184:187]\n"
"s_waitcnt lgkmcnt(15)\n"
"v_add_f32 v20, v20, v40\n"
"v_add_f32 v0, v0, v60\n"
"v_mfma_f32_16x16x32_bf16 v[108:111], a[108:111], v[128:131], v[108:111]\n"
"v_add_f32 v21, v21, v41\n"
"v_add_f32 v1, v1, v61\n"
"v_mfma_f32_16x16x32_bf16 v[188:191], a[108:111], v[132:135], v[188:191]\n"
"buffer_load_dwordx4 a[92:95], v248, s[84:87], s17 offen offset:2048 sc1\n"
"v_mfma_f32_16x16x32_bf16 v[112:115], a[112:115], v[128:131], v[112:115]\n"
"v_add_f32 v22, v22, v42\n"
"v_add_f32 v2, v2, v62\n"
"v_mfma_f32_16x16x32_bf16 v[192:195], a[112:115], v[132:135], v[192:195]\n"
"v_add_f32 v23, v23, v43\n"
"v_add_f32 v3, v3, v63\n"
"v_mfma_f32_16x16x32_bf16 v[116:119], a[116:119], v[128:131], v[116:119]\n"
"v_add_f32 v0, v20, v0\n"
"v_add_f32 v1, v21, v1\n"
"v_mfma_f32_16x16x32_bf16 v[196:199], a[116:119], v[132:135], v[196:199]\n"
"s_waitcnt vmcnt(20)\n"
"buffer_load_dwordx4 a[96:99], v248, s[84:87], s17 offen offset:3072 sc1\n"
"v_mfma_f32_16x16x32_bf16 v[100:103], a[180:183], v[136:139], v[100:103]\n"
"s_add_u32 s17, s17, 4096\n"
"v_add_f32 v2, v22, v2\n"
"v_add_f32 v3, v23, v3\n"
"v_mfma_f32_16x16x32_bf16 v[180:183], a[180:183], v[140:143], v[180:183]\n"
"s_waitcnt lgkmcnt(11)\n"
"v_add_f32 v24, v24, v44\n"
"v_add_f32 v4, v4, v64\n"
"v_mfma_f32_16x16x32_bf16 v[104:107], a[184:187], v[136:139], v[104:107]\n"
"v_add_f32 v25, v25, v45\n"
"v_add_f32 v5, v5, v65\n"
"v_mfma_f32_16x16x32_bf16 v[184:187], a[184:187], v[140:143], v[184:187]\n"
"buffer_load_dwordx4 a[160:163], v248, s[84:87], s17 offen offset:0 sc1\n"
"v_mfma_f32_16x16x32_bf16 v[108:111], a[188:191], v[136:139], v[108:111]\n"
"v_add_f32 v26, v26, v46\n"
"v_add_f32 v6, v6, v66\n"
"v_mfma_f32_16x16x32_bf16 v[188:191], a[188:191], v[140:143], v[188:191]\n"
"v_add_f32 v27, v27, v47\n"
"v_add_f32 v7, v7, v67\n"
"v_mfma_f32_16x16x32_bf16 v[112:115], a[192:195], v[136:139], v[112:115]\n"
"v_add_f32 v4, v24, v4\n"
"v_add_f32 v5, v25, v5\n"
"v_mfma_f32_16x16x32_bf16 v[192:195], a[192:195], v[140:143], v[192:195]\n"
"buffer_load_dwordx4 a[164:167], v248, s[84:87], s17 offen offset:1024 sc1\n"
"v_mfma_f32_16x16x32_bf16 v[116:119], a[196:199], v[136:139], v[116:119]\n"
"v_add_f32 v6, v26, v6\n"
"v_add_f32 v7, v27, v7\n"
"v_mfma_f32_16x16x32_bf16 v[196:199], a[196:199], v[140:143], v[196:199]\n"
"s_waitcnt lgkmcnt(7)\n"
"v_cvt_pk_bf16_f32 v0, v0, v1\n"
"v_cvt_pk_bf16_f32 v1, v2, v3\n"
"v_mfma_f32_16x16x32_bf16 v[80:83], v[200:203], v[136:139], v[80:83]\n"
"v_cvt_pk_bf16_f32 v2, v4, v5\n"
"v_cvt_pk_bf16_f32 v3, v6, v7\n"
"s_waitcnt lgkmcnt(6)\n"
"v_mfma_f32_16x16x32_bf16 v[160:163], v[200:203], v[140:143], v[160:163]\n"
"buffer_load_dwordx4 a[168:171], v248, s[84:87], s13 offen offset:2048 sc1\n"
"v_mfma_f32_16x16x32_bf16 v[84:87], v[204:207], v[136:139], v[84:87]\n"
"ds_read_b128 v[200:203], v249 offset:51200\n"
"v_add_f32 v28, v28, v48\n"
"v_mfma_f32_16x16x32_bf16 v[164:167], v[204:207], v[140:143], v[164:167]\n"
"ds_read_b128 v[204:207], v249 offset:52224\n"
"v_add_f32 v8, v8, v68\n"
"v_mfma_f32_16x16x32_bf16 v[88:91], v[208:211], v[136:139], v[88:91]\n"
"v_add_f32 v29, v29, v49\n"
"v_add_f32 v9, v9, v69\n"
"v_mfma_f32_16x16x32_bf16 v[168:171], v[208:211], v[140:143], v[168:171]\n"
"buffer_store_dwordx4 v[0:3], v248, s[84:87], s67 offen sc1\n"
"v_mfma_f32_16x16x32_bf16 v[92:95], v[212:215], v[136:139], v[92:95]\n"
"ds_read_b128 v[208:211], v250 offset:16384\n"
"v_add_f32 v30, v30, v50\n"
"v_mfma_f32_16x16x32_bf16 v[172:175], v[212:215], v[140:143], v[172:175]\n"
"v_add_f32 v10, v10, v70\n"
"v_add_f32 v31, v31, v51\n"
"v_mfma_f32_16x16x32_bf16 v[96:99], v[216:219], v[136:139], v[96:99]\n"
"v_add_f32 v11, v11, v71\n"
"v_add_f32 v8, v28, v8\n"
"v_mfma_f32_16x16x32_bf16 v[176:179], v[216:219], v[140:143], v[176:179]\n"
"s_waitcnt vmcnt(21)\n"
"ds_read_b128 v[212:215], v250 offset:17408\n"
"v_add_f32 v9, v29, v9\n"
"v_mfma_f32_16x16x32_bf16 v[80:83], a[40:43], v[144:147], v[80:83]\n"
"v_add_f32 v10, v30, v10\n"
"v_add_f32 v11, v31, v11\n"
"s_waitcnt lgkmcnt(7)\n"
"v_mfma_f32_16x16x32_bf16 v[160:163], a[40:43], v[148:151], v[160:163]\n"
"v_add_f32 v32, v32, v52\n"
"v_add_f32 v12, v12, v72\n"
"v_mfma_f32_16x16x32_bf16 v[84:87], a[44:47], v[144:147], v[84:87]\n"
"ds_read_b128 v[216:219], v250 offset:18432\n"
"v_add_f32 v33, v33, v53\n"
"v_mfma_f32_16x16x32_bf16 v[164:167], a[44:47], v[148:151], v[164:167]\n"
"v_add_f32 v13, v13, v73\n"
"v_add_f32 v34, v34, v54\n"
"v_mfma_f32_16x16x32_bf16 v[88:91], a[48:51], v[144:147], v[88:91]\n"
"v_add_f32 v14, v14, v74\n"
"v_add_f32 v35, v35, v55\n"
"v_mfma_f32_16x16x32_bf16 v[168:171], a[48:51], v[148:151], v[168:171]\n"
"buffer_load_dwordx4 a[172:175], v248, s[84:87], s14 offen offset:2048 sc1\n"
"v_mfma_f32_16x16x32_bf16 v[92:95], a[52:55], v[144:147], v[92:95]\n"
"v_add_f32 v15, v15, v75\n"
"v_add_f32 v12, v32, v12\n"
"v_mfma_f32_16x16x32_bf16 v[172:175], a[52:55], v[148:151], v[172:175]\n"
"v_add_f32 v13, v33, v13\n"
"v_add_f32 v14, v34, v14\n"
"v_mfma_f32_16x16x32_bf16 v[96:99], a[56:59], v[144:147], v[96:99]\n"
"v_add_f32 v15, v35, v15\n"
"v_cvt_pk_bf16_f32 v4, v8, v9\n"
"v_mfma_f32_16x16x32_bf16 v[176:179], a[56:59], v[148:151], v[176:179]\n"
"buffer_load_dwordx4 a[176:179], v248, s[84:87], s15 offen offset:2048 sc1\n"
"v_mfma_f32_16x16x32_bf16 v[100:103], a[120:123], v[144:147], v[100:103]\n"
"v_cvt_pk_bf16_f32 v5, v10, v11\n"
"v_cvt_pk_bf16_f32 v6, v12, v13\n"
"v_mfma_f32_16x16x32_bf16 v[180:183], a[120:123], v[148:151], v[180:183]\n"
"v_cvt_pk_bf16_f32 v7, v14, v15\n"
"s_waitcnt lgkmcnt(5)\n"
"v_add_f32 v36, v36, v56\n"
"v_mfma_f32_16x16x32_bf16 v[104:107], a[124:127], v[144:147], v[104:107]\n"
"v_add_f32 v16, v16, v76\n"
"v_add_f32 v37, v37, v57\n"
"v_mfma_f32_16x16x32_bf16 v[184:187], a[124:127], v[148:151], v[184:187]\n"
"buffer_store_dwordx4 v[4:7], v248, s[84:87], s67 offen offset:1024 sc1\n"
"v_mfma_f32_16x16x32_bf16 v[108:111], a[128:131], v[144:147], v[108:111]\n"
"v_add_f32 v17, v17, v77\n"
"v_add_f32 v38, v38, v58\n"
"v_mfma_f32_16x16x32_bf16 v[188:191], a[128:131], v[148:151], v[188:191]\n"
"v_add_f32 v18, v18, v78\n"
"v_add_f32 v39, v39, v59\n"
"v_mfma_f32_16x16x32_bf16 v[112:115], a[132:135], v[144:147], v[112:115]\n"
"v_add_f32 v19, v19, v79\n"
"v_add_f32 v16, v36, v16\n"
"v_mfma_f32_16x16x32_bf16 v[192:195], a[132:135], v[148:151], v[192:195]\n"
"v_add_f32 v17, v37, v17\n"
"v_add_f32 v18, v38, v18\n"
"v_mfma_f32_16x16x32_bf16 v[116:119], a[136:139], v[144:147], v[116:119]\n"
"v_add_f32 v19, v39, v19\n"
"v_cvt_pk_bf16_f32 v8, v16, v17\n"
"v_mfma_f32_16x16x32_bf16 v[196:199], a[136:139], v[148:151], v[196:199]\n"
"s_waitcnt vmcnt(21)\n"
"v_cvt_pk_bf16_f32 v9, v18, v19\n"
"v_mfma_f32_16x16x32_bf16 v[100:103], a[200:203], v[152:155], v[100:103]\n"
"v_mfma_f32_16x16x32_bf16 v[180:183], a[200:203], v[156:159], v[180:183]\n"
"v_mfma_f32_16x16x32_bf16 v[104:107], a[204:207], v[152:155], v[104:107]\n"
"v_mfma_f32_16x16x32_bf16 v[184:187], a[204:207], v[156:159], v[184:187]\n"
"buffer_store_dwordx2 v[8:9], v252, s[84:87], s67 offen offset:2048 sc1\n"
"v_mfma_f32_16x16x32_bf16 v[108:111], a[208:211], v[152:155], v[108:111]\n"
"v_mfma_f32_16x16x32_bf16 v[188:191], a[208:211], v[156:159], v[188:191]\n"
"v_mfma_f32_16x16x32_bf16 v[112:115], a[212:215], v[152:155], v[112:115]\n"
"v_mfma_f32_16x16x32_bf16 v[192:195], a[212:215], v[156:159], v[192:195]\n"
"v_mfma_f32_16x16x32_bf16 v[116:119], a[216:219], v[152:155], v[116:119]\n"
"buffer_load_dwordx4 a[20:23], v248, s[84:87], s12 offen offset:2048 sc1\n"
"v_mfma_f32_16x16x32_bf16 v[196:199], a[216:219], v[156:159], v[196:199]\n"
"s_waitcnt lgkmcnt(0)\n"
"v_mfma_f32_16x16x32_bf16 v[80:83], v[200:203], v[152:155], v[80:83]\n"
"v_mfma_f32_16x16x32_bf16 v[160:163], v[200:203], v[156:159], v[160:163]\n"
"ds_read_b128 v[200:203], v250 offset:19456\n"
"v_mfma_f32_16x16x32_bf16 v[84:87], v[204:207], v[152:155], v[84:87]\n"
"buffer_load_dwordx4 a[24:27], v248, s[84:87], s17 offen offset:2048 sc1\n"
"v_mfma_f32_16x16x32_bf16 v[164:167], v[204:207], v[156:159], v[164:167]\n"
"ds_read_b128 v[204:207], v250 offset:32768\n"
"v_mfma_f32_16x16x32_bf16 v[88:91], v[208:211], v[152:155], v[88:91]\n"
"v_mfma_f32_16x16x32_bf16 v[168:171], v[208:211], v[156:159], v[168:171]\n"
"v_mfma_f32_16x16x32_bf16 v[92:95], v[212:215], v[152:155], v[92:95]\n"
"buffer_load_dwordx4 a[28:31], v248, s[84:87], s17 offen offset:3072 sc1\n"
"s_add_u32 s17, s17, 4096\n"
"v_mfma_f32_16x16x32_bf16 v[172:175], v[212:215], v[156:159], v[172:175]\n"
"ds_read_b128 v[208:211], v250 offset:33792\n"
"v_mfma_f32_16x16x32_bf16 v[96:99], v[216:219], v[152:155], v[96:99]\n"
"v_mfma_f32_16x16x32_bf16 v[176:179], v[216:219], v[156:159], v[176:179]\n"
"s_waitcnt vmcnt(20)\n"
"ds_read_b128 v[212:215], v250 offset:34816\n"
"v_mfma_f32_16x16x32_bf16 v[80:83], a[60:63], a[240:243], v[80:83]\n"
"buffer_load_dwordx4 a[32:35], v248, s[84:87], s17 offen offset:0 sc1\n"
"v_mfma_f32_16x16x32_bf16 v[160:163], a[60:63], a[244:247], v[160:163]\n"
"v_mfma_f32_16x16x32_bf16 v[84:87], a[64:67], a[240:243], v[84:87]\n"
"ds_read_b128 v[216:219], v250 offset:35840\n"
"v_mfma_f32_16x16x32_bf16 v[164:167], a[64:67], a[244:247], v[164:167]\n"
"v_mfma_f32_16x16x32_bf16 v[88:91], a[68:71], a[240:243], v[88:91]\n"
"buffer_load_dwordx4 a[36:39], v248, s[84:87], s17 offen offset:1024 sc1\n"
"v_mfma_f32_16x16x32_bf16 v[168:171], a[68:71], a[244:247], v[168:171]\n"
"v_mfma_f32_16x16x32_bf16 v[92:95], a[72:75], a[240:243], v[92:95]\n"
"v_mfma_f32_16x16x32_bf16 v[172:175], a[72:75], a[244:247], v[172:175]\n"
"v_mfma_f32_16x16x32_bf16 v[96:99], a[76:79], a[240:243], v[96:99]\n"
"buffer_load_dwordx4 a[100:103], v248, s[84:87], s17 offen offset:2048 sc1\n"
"v_mfma_f32_16x16x32_bf16 v[176:179], a[76:79], a[244:247], v[176:179]\n"
"v_mfma_f32_16x16x32_bf16 v[100:103], a[140:143], a[240:243], v[100:103]\n"
"s_barrier\n"
"v_mfma_f32_16x16x32_bf16 v[180:183], a[140:143], a[244:247], v[180:183]\n"
"v_mfma_f32_16x16x32_bf16 v[104:107], a[144:147], a[240:243], v[104:107]\n"
"buffer_load_dwordx4 a[104:107], v248, s[84:87], s13 offen offset:3072 sc1\n"
"v_mfma_f32_16x16x32_bf16 v[184:187], a[144:147], a[244:247], v[184:187]\n"
"v_mfma_f32_16x16x32_bf16 v[108:111], a[148:151], a[240:243], v[108:111]\n"
"v_mfma_f32_16x16x32_bf16 v[188:191], a[148:151], a[244:247], v[188:191]\n"
"v_mfma_f32_16x16x32_bf16 v[112:115], a[152:155], a[240:243], v[112:115]\n"
"buffer_load_dwordx4 a[108:111], v248, s[84:87], s14 offen offset:3072 sc1\n"
"v_mfma_f32_16x16x32_bf16 v[192:195], a[152:155], a[244:247], v[192:195]\n"
"v_mfma_f32_16x16x32_bf16 v[116:119], a[156:159], a[240:243], v[116:119]\n"
"v_mfma_f32_16x16x32_bf16 v[196:199], a[156:159], a[244:247], v[196:199]\n"
"s_waitcnt vmcnt(20)\n"
"v_mfma_f32_16x16x32_bf16 v[100:103], a[220:223], a[248:251], v[100:103]\n"
"buffer_load_dwordx4 a[112:115], v248, s[84:87], s15 offen offset:3072 sc1\n"
"v_mfma_f32_16x16x32_bf16 v[180:183], a[220:223], a[252:255], v[180:183]\n"
"v_mfma_f32_16x16x32_bf16 v[104:107], a[224:227], a[248:251], v[104:107]\n"
"v_mfma_f32_16x16x32_bf16 v[184:187], a[224:227], a[252:255], v[184:187]\n"
"v_mfma_f32_16x16x32_bf16 v[108:111], a[228:231], a[248:251], v[108:111]\n"
"buffer_load_dwordx4 a[116:119], v248, s[84:87], s12 offen offset:3072 sc1\n"
"v_mfma_f32_16x16x32_bf16 v[188:191], a[228:231], a[252:255], v[188:191]\n"
"s_nop 0\n"
"ds_write_b128 v120, v[100:103] offset:0\n"
"v_mfma_f32_16x16x32_bf16 v[112:115], a[232:235], a[248:251], v[112:115]\n"
"v_mfma_f32_16x16x32_bf16 v[192:195], a[232:235], a[252:255], v[192:195]\n"
"ds_write_b128 v122, v[180:183] offset:0\n"
"v_mfma_f32_16x16x32_bf16 v[116:119], a[236:239], a[248:251], v[116:119]\n"
"buffer_load_dwordx4 a[180:183], v248, s[84:87], s17 offen offset:3072 sc1\n"
"s_add_u32 s17, s17, 4096\n"
"v_mfma_f32_16x16x32_bf16 v[196:199], a[236:239], a[252:255], v[196:199]\n"
"s_waitcnt lgkmcnt(2)\n"
"ds_write_b128 v120, v[104:107] offset:1024\n"
"v_mfma_f32_16x16x32_bf16 v[80:83], v[200:203], a[248:251], v[80:83]\n"
"s_waitcnt vmcnt(11)\n"
"buffer_store_dword v247, v246, s[84:87], s65 offen sc1\n"
"v_mfma_f32_16x16x32_bf16 v[160:163], v[200:203], a[252:255], v[160:163]\n"
"ds_write_b128 v122, v[184:187] offset:1024\n"
"v_mfma_f32_16x16x32_bf16 v[84:87], v[204:207], a[248:251], v[84:87]\n"
"buffer_load_dwordx4 a[184:187], v248, s[84:87], s17 offen offset:0 sc1\n"
"v_mfma_f32_16x16x32_bf16 v[164:167], v[204:207], a[252:255], v[164:167]\n"
"ds_write_b128 v120, v[108:111] offset:2048\n"
"v_mfma_f32_16x16x32_bf16 v[88:91], v[208:211], a[248:251], v[88:91]\n"
"v_mfma_f32_16x16x32_bf16 v[168:171], v[208:211], a[252:255], v[168:171]\n"
"ds_write_b128 v122, v[188:191] offset:2048\n"
"v_mfma_f32_16x16x32_bf16 v[92:95], v[212:215], a[248:251], v[92:95]\n"
"buffer_load_dwordx4 a[188:191], v248, s[84:87], s17 offen offset:1024 sc1\n"
"v_mfma_f32_16x16x32_bf16 v[172:175], v[212:215], a[252:255], v[172:175]\n"
"ds_write_b128 v120, v[112:115] offset:3072\n"
"v_mfma_f32_16x16x32_bf16 v[96:99], v[216:219], a[248:251], v[96:99]\n"
"v_mfma_f32_16x16x32_bf16 v[176:179], v[216:219], a[252:255], v[176:179]\n"
"s_waitcnt vmcnt(42)\n"
"ds_write_b128 v122, v[192:195] offset:3072\n"
"v_mfma_f32_16x16x32_bf16 v[20:23], v[240:243], v[220:223], 0\n"
"buffer_load_dwordx4 a[192:195], v248, s[84:87], s17 offen offset:2048 sc1\n"
"v_mfma_f32_16x16x32_bf16 v[40:43], v[240:243], v[224:227], 0\n"
"ds_write_b128 v120, v[116:119] offset:4096\n"
"v_mfma_f32_16x16x32_bf16 v[60:63], v[240:243], v[228:231], 0\n"
"v_mfma_f32_16x16x32_bf16 v[0:3], v[240:243], v[232:235], 0\n"
"s_waitcnt vmcnt(42)\n"
"ds_write_b128 v122, v[196:199] offset:4096\n"
"v_mfma_f32_16x16x32_bf16 v[24:27], v[124:127], v[220:223], 0\n"
"buffer_load_dwordx4 a[196:199], v248, s[84:87], s17 offen offset:3072 sc1\n"
"s_add_u32 s13, s13, 4096\n"
"v_mfma_f32_16x16x32_bf16 v[44:47], v[124:127], v[224:227], 0\n"
"ds_write_b128 v121, v[160:163] offset:0\n"
"v_mfma_f32_16x16x32_bf16 v[64:67], v[124:127], v[228:231], 0\n"
"v_mfma_f32_16x16x32_bf16 v[4:7], v[124:127], v[232:235], 0\n"
"s_waitcnt vmcnt(37)\n"
"ds_write_b128 v121, v[164:167] offset:1024\n"
"v_mfma_f32_16x16x32_bf16 v[28:31], a[0:3], v[220:223], 0\n"
"buffer_load_dwordx4 a[40:43], v248, s[84:87], s13 offen offset:0 sc1\n"
"s_add_u32 s14, s14, 4096\n"
"v_mfma_f32_16x16x32_bf16 v[48:51], a[0:3], v[224:227], 0\n"
"ds_write_b128 v121, v[168:171] offset:2048\n"
"v_mfma_f32_16x16x32_bf16 v[68:71], a[0:3], v[228:231], 0\n"
"v_mfma_f32_16x16x32_bf16 v[8:11], a[0:3], v[232:235], 0\n"
"s_waitcnt vmcnt(37)\n"
"ds_write_b128 v121, v[172:175] offset:3072\n"
"v_mfma_f32_16x16x32_bf16 v[32:35], a[4:7], v[220:223], 0\n"
"buffer_load_dwordx4 a[44:47], v248, s[84:87], s14 offen offset:0 sc1\n"
"s_add_u32 s15, s15, 4096\n"
"v_mfma_f32_16x16x32_bf16 v[52:55], a[4:7], v[224:227], 0\n"
"ds_write_b128 v121, v[176:179] offset:4096\n"
"v_mfma_f32_16x16x32_bf16 v[72:75], a[4:7], v[228:231], 0\n"
"v_mfma_f32_16x16x32_bf16 v[12:15], a[4:7], v[232:235], 0\n"
"s_waitcnt vmcnt(37)\n"
"v_mfma_f32_16x16x32_bf16 v[36:39], a[8:11], v[220:223], 0\n"
"buffer_load_dwordx4 a[48:51], v248, s[84:87], s15 offen offset:0 sc1\n"
"s_add_u32 s12, s12, 4096\n"
"v_mfma_f32_16x16x32_bf16 v[56:59], a[8:11], v[224:227], 0\n"
"v_mfma_f32_16x16x32_bf16 v[76:79], a[8:11], v[228:231], 0\n"
"v_mfma_f32_16x16x32_bf16 v[16:19], a[8:11], v[232:235], 0\n"
"s_waitcnt lgkmcnt(0)\n"
"s_waitcnt vmcnt(29)\n"
"v_mfma_f32_16x16x32_bf16 v[20:23], a[88:91], a[12:15], v[20:23]\n"
"s_barrier\n"
"buffer_load_dword v237, v236, s[84:87], s73 offen sc1\n"
"v_mfma_f32_16x16x32_bf16 v[40:43], a[88:91], a[16:19], v[40:43]\n"
"buffer_load_dwordx4 a[52:55], v248, s[84:87], s12 offen offset:0 sc1\n"
"s_add_u32 s17, s17, 4096\n"
"v_mfma_f32_16x16x32_bf16 v[60:63], a[88:91], a[80:83], v[60:63]\n"
"ds_read_b128 v[100:103], v123 offset:0\n"
"v_mfma_f32_16x16x32_bf16 v[0:3], a[88:91], a[84:87], v[0:3]\n"
"s_waitcnt vmcnt(30)\n"
"ds_read_b128 v[160:163], v123 offset:5120\n"
"v_mfma_f32_16x16x32_bf16 v[24:27], a[92:95], a[12:15], v[24:27]\n"
"ds_read_b128 v[180:183], v123 offset:10240\n"
"v_mfma_f32_16x16x32_bf16 v[44:47], a[92:95], a[16:19], v[44:47]\n"
"buffer_load_dwordx4 a[56:59], v248, s[84:87], s17 offen offset:0 sc1\n"
"v_mfma_f32_16x16x32_bf16 v[64:67], a[92:95], a[80:83], v[64:67]\n"
"ds_read_b128 v[104:107], v123 offset:1024\n"
"v_mfma_f32_16x16x32_bf16 v[4:7], a[92:95], a[84:87], v[4:7]\n"
"s_waitcnt vmcnt(30)\n"
"ds_read_b128 v[164:167], v123 offset:6144\n"
"v_mfma_f32_16x16x32_bf16 v[28:31], a[96:99], a[12:15], v[28:31]\n"
"ds_read_b128 v[184:187], v123 offset:11264\n"
"v_mfma_f32_16x16x32_bf16 v[48:51], a[96:99], a[16:19], v[48:51]\n"
"buffer_load_dwordx4 a[120:123], v248, s[84:87], s17 offen offset:1024 sc1\n"
"v_mfma_f32_16x16x32_bf16 v[68:71], a[96:99], a[80:83], v[68:71]\n"
"ds_read_b128 v[108:111], v123 offset:2048\n"
"v_mfma_f32_16x16x32_bf16 v[8:11], a[96:99], a[84:87], v[8:11]\n"
"s_waitcnt vmcnt(30)\n"
"ds_read_b128 v[168:171], v123 offset:7168\n"
"v_mfma_f32_16x16x32_bf16 v[32:35], a[160:163], a[12:15], v[32:35]\n"
"ds_read_b128 v[188:191], v123 offset:12288\n"
"v_mfma_f32_16x16x32_bf16 v[52:55], a[160:163], a[16:19], v[52:55]\n"
"buffer_load_dwordx4 a[124:127], v248, s[84:87], s17 offen offset:2048 sc1\n"
"v_mfma_f32_16x16x32_bf16 v[72:75], a[160:163], a[80:83], v[72:75]\n"
"ds_read_b128 v[112:115], v123 offset:3072\n"
"v_mfma_f32_16x16x32_bf16 v[12:15], a[160:163], a[84:87], v[12:15]\n"
"s_waitcnt vmcnt(30)\n"
"ds_read_b128 v[172:175], v123 offset:8192\n"
"v_mfma_f32_16x16x32_bf16 v[36:39], a[164:167], a[12:15], v[36:39]\n"
"ds_read_b128 v[192:195], v123 offset:13312\n"
"v_mfma_f32_16x16x32_bf16 v[56:59], a[164:167], a[16:19], v[56:59]\n"
"buffer_load_dwordx4 a[128:131], v248, s[84:87], s17 offen offset:3072 sc1\n"
"s_add_u32 s17, s17, 4096\n"
"v_mfma_f32_16x16x32_bf16 v[76:79], a[164:167], a[80:83], v[76:79]\n"
"ds_read_b128 v[116:119], v123 offset:4096\n"
"v_mfma_f32_16x16x32_bf16 v[16:19], a[164:167], a[84:87], v[16:19]\n"
"s_waitcnt vmcnt(23)\n"
"ds_read_b128 v[176:179], v123 offset:9216\n"
"v_mfma_f32_16x16x32_bf16 v[20:23], a[24:27], a[168:171], v[20:23]\n"
"ds_read_b128 v[196:199], v123 offset:14336\n"
"v_mfma_f32_16x16x32_bf16 v[40:43], a[24:27], a[172:175], v[40:43]\n"
"buffer_load_dwordx4 a[132:135], v248, s[84:87], s17 offen offset:0 sc1\n"
"v_mfma_f32_16x16x32_bf16 v[60:63], a[24:27], a[176:179], v[60:63]\n"
"v_mfma_f32_16x16x32_bf16 v[0:3], a[24:27], a[20:23], v[0:3]\n"
"s_waitcnt vmcnt(23)\n"
"s_waitcnt lgkmcnt(12)\n"
"v_add_f32 v100, v100, v160\n"
"v_mfma_f32_16x16x32_bf16 v[24:27], a[28:31], a[168:171], v[24:27]\n"
"v_add_f32 v80, v80, v180\n"
"v_add_f32 v101, v101, v161\n"
"v_mfma_f32_16x16x32_bf16 v[44:47], a[28:31], a[172:175], v[44:47]\n"
"v_add_f32 v81, v81, v181\n"
"v_add_f32 v102, v102, v162\n"
"v_mfma_f32_16x16x32_bf16 v[64:67], a[28:31], a[176:179], v[64:67]\n"
"v_add_f32 v82, v82, v182\n"
"v_add_f32 v103, v103, v163\n"
"v_mfma_f32_16x16x32_bf16 v[4:7], a[28:31], a[20:23], v[4:7]\n"
"s_waitcnt vmcnt(22)\n"
"v_add_f32 v83, v83, v183\n"
"v_add_f32 v80, v100, v80\n"
"v_mfma_f32_16x16x32_bf16 v[28:31], a[32:35], a[168:171], v[28:31]\n"
"v_add_f32 v81, v101, v81\n"
"v_add_f32 v82, v102, v82\n"
"v_mfma_f32_16x16x32_bf16 v[48:51], a[32:35], a[172:175], v[48:51]\n"
"v_add_f32 v83, v103, v83\n"
"s_waitcnt lgkmcnt(9)\n"
"v_add_f32 v104, v104, v164\n"
"v_mfma_f32_16x16x32_bf16 v[68:71], a[32:35], a[176:179], v[68:71]\n"
"v_add_f32 v84, v84, v184\n"
"v_add_f32 v105, v105, v165\n"
"v_mfma_f32_16x16x32_bf16 v[8:11], a[32:35], a[20:23], v[8:11]\n"
"s_waitcnt vmcnt(21)\n"
"v_add_f32 v85, v85, v185\n"
"v_add_f32 v106, v106, v166\n"
"v_mfma_f32_16x16x32_bf16 v[32:35], a[36:39], a[168:171], v[32:35]\n"
"v_add_f32 v86, v86, v186\n"
"v_add_f32 v107, v107, v167\n"
"v_mfma_f32_16x16x32_bf16 v[52:55], a[36:39], a[172:175], v[52:55]\n"
"v_add_f32 v87, v87, v187\n"
"v_add_f32 v84, v104, v84\n"
"v_mfma_f32_16x16x32_bf16 v[72:75], a[36:39], a[176:179], v[72:75]\n"
"v_add_f32 v85, v105, v85\n"
"v_add_f32 v86, v106, v86\n"
"v_mfma_f32_16x16x32_bf16 v[12:15], a[36:39], a[20:23], v[12:15]\n"
"s_waitcnt vmcnt(20)\n"
"v_add_f32 v87, v107, v87\n"
"s_waitcnt lgkmcnt(6)\n"
"v_mfma_f32_16x16x32_bf16 v[36:39], a[100:103], a[168:171], v[36:39]\n"
"v_add_f32 v108, v108, v168\n"
"v_add_f32 v88, v88, v188\n"
"v_mfma_f32_16x16x32_bf16 v[56:59], a[100:103], a[172:175], v[56:59]\n"
"v_add_f32 v109, v109, v169\n"
"v_add_f32 v89, v89, v189\n"
"v_mfma_f32_16x16x32_bf16 v[76:79], a[100:103], a[176:179], v[76:79]\n"
"v_add_f32 v110, v110, v170\n"
"v_add_f32 v90, v90, v190\n"
"v_mfma_f32_16x16x32_bf16 v[16:19], a[100:103], a[20:23], v[16:19]\n"
"s_waitcnt vmcnt(15)\n"
"v_add_f32 v111, v111, v171\n"
"v_add_f32 v91, v91, v191\n"
"v_mfma_f32_16x16x32_bf16 v[20:23], a[180:183], a[104:107], v[20:23]\n"
"s_waitcnt vmcnt(6)\n"
"v_add_f32 v88, v108, v88\n"
"v_add_f32 v89, v109, v89\n"
"v_mfma_f32_16x16x32_bf16 v[40:43], a[180:183], a[108:111], v[40:43]\n"
"v_cmp_ne_u32 vcc, s4, v237\n"
"s_cbranch_vccz .Lback6_%=\n"
"s_mov_b32 s61, 0x8000\n"
".Lslowl6_%=:\n"
"buffer_load_dword v237, v236, s[84:87], s73 offen sc1\n"
"s_waitcnt vmcnt(0)\n"
"v_cmp_ne_u32 vcc, s4, v237\n"
"s_cbranch_vccz .Lback6_%=\n"
"s_sleep 2\n"
"s_sub_u32 s61, s61, 1\n"
"s_cbranch_scc0 .Lslowl6_%=\n"
".Lback6_%=:\n"
"v_mfma_f32_16x16x32_bf16 v[60:63], a[180:183], a[112:115], v[60:63]\n"
".Lpad20_%=:\n"
"s_cbranch_execz .Lhopfin_%=\n"
"buffer_load_dwordx4 v[200:203], v248, s[84:87], s72 offen sc1\n"
"v_mfma_f32_16x16x32_bf16 v[0:3], a[180:183], a[116:119], v[0:3]\n"
"s_waitcnt vmcnt(14)\n"
"v_add_f32 v90, v110, v90\n"
"v_add_f32 v91, v111, v91\n"
"v_mfma_f32_16x16x32_bf16 v[24:27], a[184:187], a[104:107], v[24:27]\n"
"s_waitcnt lgkmcnt(3)\n"
"v_add_f32 v112, v112, v172\n"
"v_add_f32 v92, v92, v192\n"
"v_mfma_f32_16x16x32_bf16 v[44:47], a[184:187], a[108:111], v[44:47]\n"
"v_add_f32 v113, v113, v173\n"
"v_add_f32 v93, v93, v193\n"
"v_mfma_f32_16x16x32_bf16 v[64:67], a[184:187], a[112:115], v[64:67]\n"
"buffer_load_dwordx4 v[204:207], v248, s[84:87], s72 offen offset:1024 sc1\n"
"v_mfma_f32_16x16x32_bf16 v[4:7], a[184:187], a[116:119], v[4:7]\n"
"s_waitcnt vmcnt(14)\n"
"v_add_f32 v114, v114, v174\n"
"v_add_f32 v94, v94, v194\n"
"v_mfma_f32_16x16x32_bf16 v[28:31], a[188:191], a[104:107], v[28:31]\n"
"v_add_f32 v115, v115, v175\n"
"v_add_f32 v95, v95, v195\n"
"v_mfma_f32_16x16x32_bf16 v[48:51], a[188:191], a[108:111], v[48:51]\n"
"v_add_f32 v92, v112, v92\n"
"v_add_f32 v93, v113, v93\n"
"v_mfma_f32_16x16x32_bf16 v[68:71], a[188:191], a[112:115], v[68:71]\n"
"buffer_load_dwordx2 v[208:209], v252, s[84:87], s72 offen offset:2048 sc1\n"
"s_add_u32 s72, s72, 2560\n"
"v_mfma_f32_16x16x32_bf16 v[8:11], a[188:191], a[116:119], v[8:11]\n"
"s_waitcnt vmcnt(14)\n"
"v_add_f32 v94, v114, v94\n"
"v_add_f32 v95, v115, v95\n"
"v_mfma_f32_16x16x32_bf16 v[32:35], a[192:195], a[104:107], v[32:35]\n"
"s_waitcnt lgkmcnt(0)\n"
"buffer_load_dwordx4 v[210:213], v248, s[84:87], s72 offen sc1\n"
"v_mfma_f32_16x16x32_bf16 v[52:55], a[192:195], a[108:111], v[52:55]\n"
"v_add_f32 v116, v116, v176\n"
"v_add_f32 v96, v96, v196\n"
"v_mfma_f32_16x16x32_bf16 v[72:75], a[192:195], a[112:115], v[72:75]\n"
"v_add_f32 v117, v117, v177\n"
"v_add_f32 v97, v97, v197\n"
"v_add_f32 v118, v118, v178\n"
"v_add_f32 v98, v98, v198\n"
"v_add_f32 v119, v119, v179\n"
"v_add_f32 v99, v99, v199\n"
"v_add_f32 v96, v116, v96\n"
"v_add_f32 v97, v117, v97\n"
"v_add_f32 v98, v118, v98\n"
"v_add_f32 v99, v119, v99\n"
"v_mfma_f32_16x16x32_bf16 v[12:15], a[192:195], a[116:119], v[12:15]\n"
"s_barrier\n"
"s_waitcnt vmcnt(14)\n"
"v_mfma_f32_16x16x32_bf16 v[36:39], a[196:199], a[104:107], v[36:39]\n"
"buffer_load_dwordx4 v[214:217], v248, s[84:87], s72 offen offset:1024 sc1\n"
"v_mfma_f32_16x16x32_bf16 v[56:59], a[196:199], a[108:111], v[56:59]\n"
"v_mfma_f32_16x16x32_bf16 v[76:79], a[196:199], a[112:115], v[76:79]\n"
"v_mfma_f32_16x16x32_bf16 v[16:19], a[196:199], a[116:119], v[16:19]\n"
"s_waitcnt vmcnt(9)\n"
"v_mfma_f32_16x16x32_bf16 v[20:23], a[56:59], a[40:43], v[20:23]\n"
"buffer_load_dwordx2 v[218:219], v252, s[84:87], s72 offen offset:2048 sc1\n"
"s_add_u32 s72, s72, 2560\n"
"v_mfma_f32_16x16x32_bf16 v[40:43], a[56:59], a[44:47], v[40:43]\n"
"v_mfma_f32_16x16x32_bf16 v[60:63], a[56:59], a[48:51], v[60:63]\n"
"buffer_load_dwordx4 v[220:223], v248, s[84:87], s72 offen sc1\n"
"v_mfma_f32_16x16x32_bf16 v[0:3], a[56:59], a[52:55], v[0:3]\n"
"s_waitcnt vmcnt(10)\n"
"v_mfma_f32_16x16x32_bf16 v[24:27], a[120:123], a[40:43], v[24:27]\n"
"ds_write_b128 v120, v[20:23] offset:0\n"
"v_mfma_f32_16x16x32_bf16 v[44:47], a[120:123], a[44:47], v[44:47]\n"
"s_nop 0\n"
"ds_write_b128 v121, v[40:43] offset:0\n"
"v_mfma_f32_16x16x32_bf16 v[64:67], a[120:123], a[48:51], v[64:67]\n"
"buffer_load_dwordx4 v[224:227], v248, s[84:87], s72 offen offset:1024 sc1\n"
"v_mfma_f32_16x16x32_bf16 v[4:7], a[120:123], a[52:55], v[4:7]\n"
"s_waitcnt vmcnt(10)\n"
"ds_write_b128 v122, v[60:63] offset:0\n"
"v_mfma_f32_16x16x32_bf16 v[28:31], a[124:127], a[40:43], v[28:31]\n"
"v_mfma_f32_16x16x32_bf16 v[48:51], a[124:127], a[44:47], v[48:51]\n"
"ds_write_b128 v120, v[24:27] offset:1024\n"
"v_mfma_f32_16x16x32_bf16 v[68:71], a[124:127], a[48:51], v[68:71]\n"
"buffer_load_dwordx2 v[228:229], v252, s[84:87], s72 offen offset:2048 sc1\n"
"v_mfma_f32_16x16x32_bf16 v[8:11], a[124:127], a[52:55], v[8:11]\n"
"s_waitcnt vmcnt(10)\n"
"ds_write_b128 v121, v[44:47] offset:1024\n"
"v_mfma_f32_16x16x32_bf16 v[32:35], a[128:131], a[40:43], v[32:35]\n"
"v_mfma_f32_16x16x32_bf16 v[52:55], a[128:131], a[44:47], v[52:55]\n"
"ds_write_b128 v122, v[64:67] offset:1024\n"
"v_mfma_f32_16x16x32_bf16 v[72:75], a[128:131], a[48:51], v[72:75]\n"
"v_mfma_f32_16x16x32_bf16 v[12:15], a[128:131], a[52:55], v[12:15]\n"
"s_waitcnt vmcnt(9)\n"
"ds_write_b128 v120, v[28:31] offset:2048\n"
"v_mfma_f32_16x16x32_bf16 v[36:39], a[132:135], a[40:43], v[36:39]\n"
"v_mfma_f32_16x16x32_bf16 v[56:59], a[132:135], a[44:47], v[56:59]\n"
"ds_write_b128 v121, v[48:51] offset:2048\n"
"v_mfma_f32_16x16x32_bf16 v[76:79], a[132:135], a[48:51], v[76:79]\n"
"v_mfma_f32_16x16x32_bf16 v[16:19], a[132:135], a[52:55], v[16:19]\n"
"ds_write_b128 v122, v[68:71] offset:2048\n"
"ds_write_b128 v120, v[32:35] offset:3072\n"
"ds_write_b128 v121, v[52:55] offset:3072\n"
"ds_write_b128 v122, v[72:75] offset:3072\n"
"ds_write_b128 v120, v[36:39] offset:4096\n"
"ds_write_b128 v121, v[56:59] offset:4096\n"
"s_nop 0\n"
"ds_write_b128 v122, v[76:79] offset:4096\n"
"s_waitcnt vmcnt(6)\n"
"v_lshlrev_b32 v100, 16, v200\n"
"v_and_b32 v101, 0xffff0000, v200\n"
"v_lshlrev_b32 v102, 16, v201\n"
"v_and_b32 v103, 0xffff0000, v201\n"
"v_lshlrev_b32 v104, 16, v202\n"
"v_and_b32 v105, 0xffff0000, v202\n"
"v_lshlrev_b32 v106, 16, v203\n"
"v_and_b32 v107, 0xffff0000, v203\n"
"v_lshlrev_b32 v108, 16, v204\n"
"v_and_b32 v109, 0xffff0000, v204\n"
"v_lshlrev_b32 v110, 16, v205\n"
"v_and_b32 v111, 0xffff0000, v205\n"
"v_lshlrev_b32 v112, 16, v206\n"
"v_and_b32 v113, 0xffff0000, v206\n"
"v_lshlrev_b32 v114, 16, v207\n"
"v_and_b32 v115, 0xffff0000, v207\n"
"v_lshlrev_b32 v116, 16, v208\n"
"v_and_b32 v117, 0xffff0000, v208\n"
"v_lshlrev_b32 v118, 16, v209\n"
"v_and_b32 v119, 0xffff0000, v209\n"
"v_pk_add_f32 v[80:81], v[80:81], v[100:101]\n"
"v_pk_add_f32 v[82:83], v[82:83], v[102:103]\n"
"v_pk_add_f32 v[84:85], v[84:85], v[104:105]\n"
"v_pk_add_f32 v[86:87], v[86:87], v[106:107]\n"
"v_pk_add_f32 v[88:89], v[88:89], v[108:109]\n"
"v_pk_add_f32 v[90:91], v[90:91], v[110:111]\n"
"v_pk_add_f32 v[92:93], v[92:93], v[112:113]\n"
"v_pk_add_f32 v[94:95], v[94:95], v[114:115]\n"
"v_pk_add_f32 v[96:97], v[96:97], v[116:117]\n"
"v_pk_add_f32 v[98:99], v[98:99], v[118:119]\n"
"s_waitcnt vmcnt(3)\n"
"v_lshlrev_b32 v100, 16, v210\n"
"v_and_b32 v101, 0xffff0000, v210\n"
"v_lshlrev_b32 v102, 16, v211\n"
"v_and_b32 v103, 0xffff0000, v211\n"
"v_lshlrev_b32 v104, 16, v212\n"
"v_and_b32 v105, 0xffff0000, v212\n"
"v_lshlrev_b32 v106, 16, v213\n"
"v_and_b32 v107, 0xffff0000, v213\n"
"v_lshlrev_b32 v108, 16, v214\n"
"v_and_b32 v109, 0xffff0000, v214\n"
"v_lshlrev_b32 v110, 16, v215\n"
"v_and_b32 v111, 0xffff0000, v215\n"
"v_lshlrev_b32 v112, 16, v216\n"
"v_and_b32 v113, 0xffff0000, v216\n"
"v_lshlrev_b32 v114, 16, v217\n"
"v_and_b32 v115, 0xffff0000, v217\n"
"v_lshlrev_b32 v116, 16, v218\n"
"v_and_b32 v117, 0xffff0000, v218\n"
"v_lshlrev_b32 v118, 16, v219\n"
"v_and_b32 v119, 0xffff0000, v219\n"
"v_pk_add_f32 v[80:81], v[80:81], v[100:101]\n"
"v_pk_add_f32 v[82:83], v[82:83], v[102:103]\n"
"v_pk_add_f32 v[84:85], v[84:85], v[104:105]\n"
"v_pk_add_f32 v[86:87], v[86:87], v[106:107]\n"
"v_pk_add_f32 v[88:89], v[88:89], v[108:109]\n"
"v_pk_add_f32 v[90:91], v[90:91], v[110:111]\n"
"v_pk_add_f32 v[92:93], v[92:93], v[112:113]\n"
"v_pk_add_f32 v[94:95], v[94:95], v[114:115]\n"
"v_pk_add_f32 v[96:97], v[96:97], v[116:117]\n"
"v_pk_add_f32 v[98:99], v[98:99], v[118:119]\n"
"s_waitcnt vmcnt(0)\n"
"v_lshlrev_b32 v100, 16, v220\n"
"v_and_b32 v101, 0xffff0000, v220\n"
"v_lshlrev_b32 v102, 16, v221\n"
"v_and_b32 v103, 0xffff0000, v221\n"
"v_lshlrev_b32 v104, 16, v222\n"
"v_and_b32 v105, 0xffff0000, v222\n"
"v_lshlrev_b32 v106, 16, v223\n"
"v_and_b32 v107, 0xffff0000, v223\n"
"v_lshlrev_b32 v108, 16, v224\n"
"v_and_b32 v109, 0xffff0000, v224\n"
"v_lshlrev_b32 v110, 16, v225\n"
"v_and_b32 v111, 0xffff0000, v225\n"
"v_lshlrev_b32 v112, 16, v226\n"
"v_and_b32 v113, 0xffff0000, v226\n"
"v_lshlrev_b32 v114, 16, v227\n"
"v_and_b32 v115, 0xffff0000, v227\n"
"v_lshlrev_b32 v116, 16, v228\n"
"v_and_b32 v117, 0xffff0000, v228\n"
"v_lshlrev_b32 v118, 16, v229\n"
"v_and_b32 v119, 0xffff0000, v229\n"
"v_pk_add_f32 v[80:81], v[80:81], v[100:101]\n"
"v_pk_add_f32 v[82:83], v[82:83], v[102:103]\n"
"v_pk_add_f32 v[84:85], v[84:85], v[104:105]\n"
"v_pk_add_f32 v[86:87], v[86:87], v[106:107]\n"
"v_pk_add_f32 v[88:89], v[88:89], v[108:109]\n"
"v_pk_add_f32 v[90:91], v[90:91], v[110:111]\n"
"v_pk_add_f32 v[92:93], v[92:93], v[112:113]\n"
"v_pk_add_f32 v[94:95], v[94:95], v[114:115]\n"
"v_pk_add_f32 v[96:97], v[96:97], v[116:117]\n"
"v_pk_add_f32 v[98:99], v[98:99], v[118:119]\n"
"v_cvt_pk_bf16_f32 v80, v80, v81\n"
"v_cvt_pk_bf16_f32 v81, v82, v83\n"
"v_cvt_pk_bf16_f32 v82, v84, v85\n"
"v_cvt_pk_bf16_f32 v83, v86, v87\n"
"v_cvt_pk_bf16_f32 v84, v88, v89\n"
"v_cvt_pk_bf16_f32 v85, v90, v91\n"
"v_cvt_pk_bf16_f32 v86, v92, v93\n"
"v_cvt_pk_bf16_f32 v87, v94, v95\n"
"v_cvt_pk_bf16_f32 v88, v96, v97\n"
"v_cvt_pk_bf16_f32 v89, v98, v99\n"
"buffer_store_dwordx4 v[80:83], v253, s[36:39], s71 offen offset:1280\n"
"buffer_store_dwordx4 v[84:87], v253, s[36:39], s71 offen offset:1344\n"
"buffer_store_dwordx2 v[88:89], v254, s[36:39], s71 offen offset:1408\n"
"s_waitcnt lgkmcnt(0)\n"
"s_barrier\n"
"ds_read_b128 v[20:23], v123 offset:0\n"
"ds_read_b128 v[40:43], v123 offset:5120\n"
"ds_read_b128 v[60:63], v123 offset:10240\n"
"ds_read_b128 v[24:27], v123 offset:1024\n"
"ds_read_b128 v[44:47], v123 offset:6144\n"
"ds_read_b128 v[64:67], v123 offset:11264\n"
"ds_read_b128 v[28:31], v123 offset:2048\n"
"ds_read_b128 v[48:51], v123 offset:7168\n"
"ds_read_b128 v[68:71], v123 offset:12288\n"
"ds_read_b128 v[32:35], v123 offset:3072\n"
"ds_read_b128 v[52:55], v123 offset:8192\n"
"ds_read_b128 v[72:75], v123 offset:13312\n"
"ds_read_b128 v[36:39], v123 offset:4096\n"
"ds_read_b128 v[56:59], v123 offset:9216\n"
"ds_read_b128 v[76:79], v123 offset:14336\n"
"s_waitcnt lgkmcnt(12)\n"
"v_pk_add_f32 v[20:21], v[20:21], v[40:41]\n"
"v_pk_add_f32 v[0:1], v[0:1], v[60:61]\n"
"v_pk_add_f32 v[22:23], v[22:23], v[42:43]\n"
"v_pk_add_f32 v[2:3], v[2:3], v[62:63]\n"
"v_pk_add_f32 v[0:1], v[20:21], v[0:1]\n"
"v_pk_add_f32 v[2:3], v[22:23], v[2:3]\n"
"s_waitcnt lgkmcnt(9)\n"
"v_pk_add_f32 v[24:25], v[24:25], v[44:45]\n"
"v_pk_add_f32 v[4:5], v[4:5], v[64:65]\n"
"v_pk_add_f32 v[26:27], v[26:27], v[46:47]\n"
"v_pk_add_f32 v[6:7], v[6:7], v[66:67]\n"
"v_pk_add_f32 v[4:5], v[24:25], v[4:5]\n"
"v_pk_add_f32 v[6:7], v[26:27], v[6:7]\n"
"s_waitcnt lgkmcnt(6)\n"
"v_pk_add_f32 v[28:29], v[28:29], v[48:49]\n"
"v_pk_add_f32 v[8:9], v[8:9], v[68:69]\n"
"v_pk_add_f32 v[30:31], v[30:31], v[50:51]\n"
"v_pk_add_f32 v[10:11], v[10:11], v[70:71]\n"
"v_pk_add_f32 v[8:9], v[28:29], v[8:9]\n"
"v_pk_add_f32 v[10:11], v[30:31], v[10:11]\n"
"s_waitcnt lgkmcnt(3)\n"
"v_pk_add_f32 v[32:33], v[32:33], v[52:53]\n"
"v_pk_add_f32 v[12:13], v[12:13], v[72:73]\n"
"v_pk_add_f32 v[34:35], v[34:35], v[54:55]\n"
"v_pk_add_f32 v[14:15], v[14:15], v[74:75]\n"
"v_pk_add_f32 v[12:13], v[32:33], v[12:13]\n"
"v_pk_add_f32 v[14:15], v[34:35], v[14:15]\n"
"s_waitcnt lgkmcnt(0)\n"
"v_pk_add_f32 v[36:37], v[36:37], v[56:57]\n"
"v_pk_add_f32 v[16:17], v[16:17], v[76:77]\n"
"v_pk_add_f32 v[38:39], v[38:39], v[58:59]\n"
"v_pk_add_f32 v[18:19], v[18:19], v[78:79]\n"
"v_pk_add_f32 v[16:17], v[36:37], v[16:17]\n"
"v_pk_add_f32 v[18:19], v[38:39], v[18:19]\n"
"v_cvt_pk_bf16_f32 v0, v0, v1\n"
"v_cvt_pk_bf16_f32 v1, v2, v3\n"
"v_cvt_pk_bf16_f32 v2, v4, v5\n"
"v_cvt_pk_bf16_f32 v3, v6, v7\n"
"v_cvt_pk_bf16_f32 v4, v8, v9\n"
"v_cvt_pk_bf16_f32 v5, v10, v11\n"
"v_cvt_pk_bf16_f32 v6, v12, v13\n"
"v_cvt_pk_bf16_f32 v7, v14, v15\n"
"v_cvt_pk_bf16_f32 v8, v16, v17\n"
"v_cvt_pk_bf16_f32 v9, v18, v19\n"
"buffer_store_dwordx4 v[0:3], v253, s[36:39], s70 offen offset:0\n"
"buffer_store_dwordx4 v[4:7], v253, s[36:39], s70 offen offset:64\n"
"buffer_store_dwordx2 v[8:9], v254, s[36:39], s70 offen offset:128\n"
"s_or_b32 s59, %2, s76\n"
"s_cmp_eq_u32 s59, 0\n"
"s_cselect_b32 s59, 81788928, 1073741824\n"
"s_xor_b32 s80, s88, 0x5bd1e995\n"
"s_mul_i32 s80, s80, 244002641\n"
"v_mbcnt_lo_u32_b32 v255, -1, 0\n"
"v_mbcnt_hi_u32_b32 v255, -1, v255\n"
"v_lshlrev_b32 v255, 21, v255\n"
"v_mov_b32 v254, s80\n"
"buffer_store_dword v254, v255, s[84:87], s59 offen sc1\n"
"s_endpgm\n"
".LstubV0_%=:\n"
"v_cmp_nge_f32 vcc, 0.5, v236\n"
"s_add_u32 s66, s62, .LbackV0_%=-.Lpc_%=\n"
"s_addc_u32 s67, s63, 0\n"
"s_cbranch_vccnz .LrescV1_%=\n"
"s_setpc_b64 s[60:61]\n"
".LstubV1_%=:\n"
"v_cmp_nge_f32 vcc, 0.5, v236\n"
"s_add_u32 s66, s62, .LbackV1_%=-.Lpc_%=\n"
"s_addc_u32 s67, s63, 0\n"
"s_cbranch_vccnz .LrescV0_%=\n"
"s_setpc_b64 s[60:61]\n"
".LstubV2_%=:\n"
"v_cmp_nge_f32 vcc, 0.5, v236\n"
"s_add_u32 s66, s62, .LbackV2_%=-.Lpc_%=\n"
"s_addc_u32 s67, s63, 0\n"
"s_cbranch_vccnz .LrescV1_%=\n"
"s_setpc_b64 s[60:61]\n"
".LstubV3_%=:\n"
"v_cmp_nge_f32 vcc, 0.5, v236\n"
"s_add_u32 s66, s62, .LbackV3_%=-.Lpc_%=\n"
"s_addc_u32 s67, s63, 0\n"
"s_cbranch_vccnz .LrescV0_%=\n"
"s_setpc_b64 s[60:61]\n"
".LstubG0_%=:\n"
"v_cmp_nge_f32 vcc, 0.5, v236\n"
"s_add_u32 s66, s66, 4\n"
"s_addc_u32 s67, s67, 0\n"
"s_cbranch_vccnz .LrescV0_%=\n"
"s_setpc_b64 s[60:61]\n"
".LstubG1_%=:\n"
"v_cmp_nge_f32 vcc, 0.5, v236\n"
"s_add_u32 s66, s66, 4\n"
"s_addc_u32 s67, s67, 0\n"
"s_cbranch_vccnz .LrescV1_%=\n"
"s_setpc_b64 s[60:61]\n"
".LevM1e_%=:\n"
"v_cndmask_b32 v128, v250, v128, s[68:69]\n"
"v_cndmask_b32 v129, v250, v129, s[70:71]\n"
"v_cndmask_b32 v130, v250, v130, s[72:73]\n"
"v_cndmask_b32 v131, v250, v131, s[74:75]\n"
"v_mov_b64 v[132:133], v[250:251]\n"
"v_mov_b64 v[134:135], v[250:251]\n"
"v_cndmask_b32 v140, v250, v140, s[68:69]\n"
"v_cndmask_b32 v141, v250, v141, s[70:71]\n"
"v_cndmask_b32 v142, v250, v142, s[72:73]\n"
"v_cndmask_b32 v143, v250, v143, s[74:75]\n"
"s_mov_b32 s64, 100\n"
"s_cmp_eq_u32 s52, 0\n"
"s_cbranch_scc1 .Linit2_%=\n"
"s_setpc_b64 s[66:67]\n"
".Linit_%=:\n"
".Lpadinit_%=:\n"
"s_cbranch_execz .Lhopi2_%=\n"
"s_mov_b32 s64, s65\n"
"s_add_u32 s60, s62, .LevM1e_%=-.Lpc_%=\n"
"s_addc_u32 s61, s63, 0\n"
".Linit2_%=:\n"
"v_max3_f32 v252, v128, v129, v130\n"
"v_max3_f32 v253, v136, v137, v138\n"
"v_max3_f32 v254, v144, v145, v146\n"
"v_max3_f32 v255, v152, v153, v154\n"
"v_max3_f32 v252, v252, v131, v132\n"
"v_max3_f32 v253, v253, v139, v140\n"
"v_max3_f32 v254, v254, v147, v148\n"
"v_max3_f32 v255, v255, v155, v156\n"
"v_max3_f32 v252, v252, v133, v134\n"
"v_max3_f32 v253, v253, v141, v142\n"
"v_max3_f32 v254, v254, v149, v150\n"
"v_max3_f32 v255, v255, v157, v158\n"
"v_max_f32 v252, v252, v135\n"
"v_max_f32 v253, v253, v143\n"
"v_max_f32 v254, v254, v151\n"
"v_max_f32 v255, v255, v159\n"
"v_mov_b32 v236, v252\n"
"v_mov_b32 v237, v253\n"
"v_mov_b32 v238, v254\n"
"v_mov_b32 v239, v255\n"
"v_permlane32_swap_b32 v252, v236\n"
"v_permlane32_swap_b32 v253, v237\n"
"v_permlane32_swap_b32 v254, v238\n"
"v_permlane32_swap_b32 v255, v239\n"
"v_max_f32 v252, v252, v236\n"
"v_max_f32 v253, v253, v237\n"
"v_max_f32 v254, v254, v238\n"
"v_max_f32 v255, v255, v239\n"
"v_mov_b32 v236, v252\n"
"v_mov_b32 v237, v253\n"
"v_mov_b32 v238, v254\n"
"v_mov_b32 v239, v255\n"
"v_permlane16_swap_b32 v252, v236\n"
"v_permlane16_swap_b32 v253, v237\n"
"v_permlane16_swap_b32 v254, v238\n"
"v_permlane16_swap_b32 v255, v239\n"
"v_max_f32 v252, v252, v236\n"
"v_max_f32 v253, v253, v237\n"
"v_max_f32 v254, v254, v238\n"
"v_max_f32 v255, v255, v239\n"
"v_sub_f32 v252, 0xc2c00000, v252\n"
"v_sub_f32 v253, 0xc2c00000, v253\n"
"v_sub_f32 v254, 0xc2c00000, v254\n"
"v_sub_f32 v255, 0xc2c00000, v255\n"
"v_floor_f32 v252, v252\n"
"v_floor_f32 v253, v253\n"
"v_floor_f32 v254, v254\n"
"v_floor_f32 v255, v255\n"
"v_add_u32 v249, 57344, v246\n"
"ds_write_b128 v249, v[252:255] offset:8192\n"
"v_pk_mov_b32 v[192:193], v[252:253], v[252:253] op_sel:[0,0]\n"
"v_pk_mov_b32 v[194:195], v[252:253], v[252:253] op_sel:[0,0]\n"
"v_pk_mov_b32 v[196:197], v[252:253], v[252:253] op_sel:[1,1]\n"
"v_pk_mov_b32 v[198:199], v[252:253], v[252:253] op_sel:[1,1]\n"
"v_pk_mov_b32 v[200:201], v[254:255], v[254:255] op_sel:[0,0]\n"
"v_pk_mov_b32 v[202:203], v[254:255], v[254:255] op_sel:[0,0]\n"
"v_pk_mov_b32 v[204:205], v[254:255], v[254:255] op_sel:[1,1]\n"
"v_pk_mov_b32 v[206:207], v[254:255], v[254:255] op_sel:[1,1]\n"
"v_pk_add_f32 v[128:129], v[128:129], v[192:193]\n"
"v_pk_add_f32 v[130:131], v[130:131], v[192:193]\n"
"v_pk_add_f32 v[132:133], v[132:133], v[192:193]\n"
"v_pk_add_f32 v[134:135], v[134:135], v[192:193]\n"
"v_pk_add_f32 v[136:137], v[136:137], v[196:197]\n"
"v_pk_add_f32 v[138:139], v[138:139], v[196:197]\n"
"v_pk_add_f32 v[140:141], v[140:141], v[196:197]\n"
"v_pk_add_f32 v[142:143], v[142:143], v[196:197]\n"
"v_pk_add_f32 v[144:145], v[144:145], v[200:201]\n"
"v_pk_add_f32 v[146:147], v[146:147], v[200:201]\n"
"v_pk_add_f32 v[148:149], v[148:149], v[200:201]\n"
"v_pk_add_f32 v[150:151], v[150:151], v[200:201]\n"
"v_pk_add_f32 v[152:153], v[152:153], v[204:205]\n"
"v_pk_add_f32 v[154:155], v[154:155], v[204:205]\n"
"v_pk_add_f32 v[156:157], v[156:157], v[204:205]\n"
"v_pk_add_f32 v[158:159], v[158:159], v[204:205]\n"
"s_waitcnt lgkmcnt(0)\n"
"s_barrier\n"
"ds_read_b128 v[232:235], v249\n"
"s_setpc_b64 s[66:67]\n"
".Lqp3_%=:\n"
".Lpadqp3_%=:\n"
"s_cbranch_execz .Lhopi2_%=\n"
"s_sub_u32 s49, s35, s57\n"
"s_add_u32 s48, s49, 0\n"
"buffer_load_dwordx4 a[0:3], v246, s[84:87], s48 offen offset:0 sc1\n"
"buffer_load_dwordx4 a[4:7], v246, s[84:87], s48 offen offset:1024 sc1\n"
"buffer_load_dwordx4 a[8:11], v246, s[84:87], s48 offen offset:2048 sc1\n"
"buffer_load_dwordx4 a[12:15], v246, s[84:87], s48 offen offset:3072 sc1\n"
"s_add_u32 s48, s48, 4096\n"
"buffer_load_dwordx4 a[16:19], v246, s[84:87], s48 offen offset:0 sc1\n"
"s_add_u32 s48, s49, 5120\n"
"buffer_load_dwordx4 a[20:23], v246, s[84:87], s48 offen offset:0 sc1\n"
"buffer_load_dwordx4 a[24:27], v246, s[84:87], s48 offen offset:1024 sc1\n"
"buffer_load_dwordx4 a[28:31], v246, s[84:87], s48 offen offset:2048 sc1\n"
"buffer_load_dwordx4 a[32:35], v246, s[84:87], s48 offen offset:3072 sc1\n"
"s_add_u32 s48, s48, 4096\n"
"buffer_load_dwordx4 a[36:39], v246, s[84:87], s48 offen offset:0 sc1\n"
"v_accvgpr_write_b32 a120, 0\n"
"v_accvgpr_write_b32 a121, 0\n"
"v_accvgpr_write_b32 a122, 0\n"
"v_accvgpr_write_b32 a123, 0\n"
"s_and_b32 s50, s76, 1\n"
"s_lshl_b32 s50, s50, 15\n"
"s_add_u32 s50, s50, s57\n"
"s_xor_b32 s51, s50, 32768\n"
"s_waitcnt lgkmcnt(0)\n"
"s_branch .Lmid3_%=\n"
".Lsw2_%=:\n"
"v_subrev_u32 v248, s57, v246\n"
"v_lshlrev_b32 v248, 17, v248\n"
"v_mov_b32 v249, s4\n"
"s_bitcmp1_b32 s76, 0\n"
"s_waitcnt vmcnt(8)\n"
"buffer_store_dword v249, v248, s[84:87], s5 offen sc1\n"
"s_mov_b32 s5, 1073741824\n"
"s_cbranch_scc1 .Lret2_%=\n"
"s_branch .Lret0_%=\n"
".Lsw_%=:\n"
"s_cmp_lg_u32 s5, 1073741824\n"
"s_cbranch_scc1 .Lsw2_%=\n"
"v_add_u32 v249, s82, v244\n"
"s_sub_u32 s59, s50, s57\n"
"v_add_u32 v245, s59, v245\n"
"v_mfma_f32_16x16x32_bf16 v[32:35], a[232:235], v[208:211], v[32:35]\n"
"s_mov_b32 s5, s7\n"
"v_cndmask_b32 v128, v250, v128, s[68:69]\n"
"v_cndmask_b32 v129, v250, v129, s[70:71]\n"
"v_cndmask_b32 v130, v250, v130, s[72:73]\n"
"v_cndmask_b32 v131, v250, v131, s[74:75]\n"
"v_cndmask_b32 v140, v250, v140, s[68:69]\n"
"v_mfma_f32_16x16x32_bf16 v[48:51], a[236:239], v[208:211], v[48:51]\n"
"s_add_u32 m0, s51, 16384\n"
"ds_read_b128 a[192:195], v245 offset:0\n"
"ds_read_b128 a[196:199], v245 offset:1024\n"
"v_cndmask_b32 v141, v250, v141, s[70:71]\n"
"v_cndmask_b32 v142, v250, v142, s[72:73]\n"
"v_cndmask_b32 v143, v250, v143, s[74:75]\n"
"v_mfma_f32_16x16x32_bf16 v[64:67], a[240:243], v[208:211], v[64:67]\n"
"s_add_u32 s83, s83, 4\n"
"buffer_load_dwordx4 v246, s[36:39], s54 offen offset:0 sc1 lds\n"
"ds_read_b128 a[200:203], v245 offset:2048\n"
"v_exp_f32 v160, v128 clamp\n"
"v_mfma_f32_16x16x32_bf16 v[80:83], a[244:247], v[208:211], v[80:83]\n"
"s_add_u32 s48, s49, 10240\n"
"ds_read_b128 a[204:207], v245 offset:3072\n"
"v_exp_f32 v161, v129 clamp\n"
"v_mfma_f32_16x16x32_bf16 v[96:99], a[248:251], v[208:211], v[96:99]\n"
"buffer_load_dwordx4 a[40:43], v246, s[84:87], s48 offen offset:0 sc1\n"
"ds_read_b128 a[208:211], v245 offset:4096\n"
"v_exp_f32 v162, v130 clamp\n"
"v_mfma_f32_16x16x32_bf16 v[112:115], a[252:255], v[208:211], v[112:115]\n"
"ds_read_b128 a[212:215], v245 offset:5120\n"
"v_cvt_pk_bf16_f32 v208, v160, v161\n"
"v_exp_f32 v163, v131 clamp\n"
"v_mfma_f32_16x16x32_bf16 v[4:7], a[224:227], v[212:215], v[4:7]\n"
"s_add_u32 s52, s52, 1\n"
"ds_read_b128 a[216:219], v245 offset:6144\n"
"v_cvt_pk_bf16_f32 v209, v162, v163\n"
"v_mov_b64 v[210:211], 0\n"
"v_exp_f32 v164, v136 clamp\n"
"v_mfma_f32_16x16x32_bf16 v[20:23], a[228:231], v[212:215], v[20:23]\n"
"s_sub_u32 s55, s55, s29\n"
"ds_read_b128 a[220:223], v245 offset:7168\n"
"buffer_load_dwordx4 a[44:47], v246, s[84:87], s48 offen offset:1024 sc1\n"
"v_exp_f32 v165, v137 clamp\n"
"v_mfma_f32_16x16x32_bf16 v[36:39], a[232:235], v[212:215], v[36:39]\n"
"buffer_load_dwordx4 v246, s[36:39], s54 offen offset:1024 sc1 lds\n"
"ds_read_b128 a[128:131], v249 offset:8192\n"
"v_mfma_f32_16x16x32_bf16 v[52:55], a[236:239], v[212:215], v[52:55]\n"
"ds_read_b128 a[132:135], v249 offset:9216\n"
"v_exp_f32 v166, v138 clamp\n"
"v_mfma_f32_16x16x32_bf16 v[68:71], a[240:243], v[212:215], v[68:71]\n"
"buffer_load_dwordx4 a[48:51], v246, s[84:87], s48 offen offset:2048 sc1\n"
"ds_read_b128 a[136:139], v249 offset:10240\n"
"v_exp_f32 v167, v139 clamp\n"
"v_mfma_f32_16x16x32_bf16 v[84:87], a[244:247], v[212:215], v[84:87]\n"
"s_add_u32 s59, s30, 12288\n"
"ds_read_b128 a[140:143], v249 offset:11264\n"
"s_add_u32 s56, s38, 8192\n"
"v_exp_f32 v168, v140 clamp\n"
"v_mfma_f32_16x16x32_bf16 v[100:103], a[248:251], v[212:215], v[100:103]\n"
"s_cmp_eq_u32 s52, 31\n"
"s_cselect_b32 s55, s59, s55\n"
"s_cselect_b32 s42, s56, s42\n"
"s_cselect_b32 s29, 0xfffff000, s29\n"
"ds_read_b128 a[144:147], v249 offset:12288\n"
"v_mfma_f32_16x16x32_bf16 v[116:119], a[252:255], v[212:215], v[116:119]\n"
"buffer_load_dwordx4 v246, s[36:39], s54 offen offset:2048 sc1 lds\n"
"ds_read_b128 a[148:151], v249 offset:13312\n"
"v_cvt_pk_bf16_f32 v212, v164, v165\n"
"v_cvt_pk_bf16_f32 v213, v166, v167\n"
"v_mfma_f32_16x16x32_bf16 v[8:11], a[224:227], v[216:219], v[8:11]\n"
"ds_read_b128 a[152:155], v249 offset:14336\n"
"buffer_load_dwordx4 a[52:55], v246, s[84:87], s48 offen offset:3072 sc1\n"
"s_add_u32 s48, s48, 4096\n"
"v_exp_f32 v169, v141 clamp\n"
"v_mfma_f32_16x16x32_bf16 v[24:27], a[228:231], v[216:219], v[24:27]\n"
"ds_read_b128 a[156:159], v249 offset:15360\n"
"v_cvt_pk_bf16_f32 v214, v168, v169\n"
"v_exp_f32 v170, v142 clamp\n"
"v_mfma_f32_16x16x32_bf16 v[40:43], a[232:235], v[216:219], v[40:43]\n"
"buffer_load_dwordx4 a[56:59], v246, s[84:87], s48 offen offset:0 sc1\n"
"s_add_u32 s48, s49, 15360\n"
"v_exp_f32 v171, v143 clamp\n"
"v_mfma_f32_16x16x32_bf16 v[56:59], a[236:239], v[216:219], v[56:59]\n"
"v_cvt_pk_bf16_f32 v215, v170, v171\n"
"buffer_load_dwordx4 a[60:63], v246, s[84:87], s48 offen offset:0 sc1\n"
"v_exp_f32 v172, v144 clamp\n"
"v_mfma_f32_16x16x32_bf16 v[72:75], a[240:243], v[216:219], v[72:75]\n"
"buffer_load_dwordx4 v246, s[36:39], s54 offen offset:3072 sc1 lds\n"
"v_exp_f32 v173, v145 clamp\n"
"v_mfma_f32_16x16x32_bf16 v[88:91], a[244:247], v[216:219], v[88:91]\n"
"v_exp_f32 v174, v146 clamp\n"
"v_mfma_f32_16x16x32_bf16 v[104:107], a[248:251], v[216:219], v[104:107]\n"
"buffer_load_dwordx4 a[64:67], v246, s[84:87], s48 offen offset:1024 sc1\n"
"v_mfma_f32_16x16x32_bf16 v[120:123], a[252:255], v[216:219], v[120:123]\n"
"v_cvt_pk_bf16_f32 v216, v172, v173\n"
"v_exp_f32 v175, v147 clamp\n"
"v_mfma_f32_16x16x32_bf16 v[12:15], a[224:227], v[220:223], v[12:15]\n"
"s_add_u32 s54, s54, s28\n"
"s_cmp_lt_u32 s55, 540672\n"
"s_cselect_b32 s56, 1073741824, s55\n"
"v_cvt_pk_bf16_f32 v217, v174, v175\n"
"s_add_u32 m0, s51, 81920\n"
"v_mfma_f32_16x16x32_bf16 v[28:31], a[228:231], v[220:223], v[28:31]\n"
"buffer_load_dwordx4 v246, s[40:43], s56 offen offset:0 sc1 lds\n"
"v_exp_f32 v160, v148 clamp\n"
"v_mfma_f32_16x16x32_bf16 v[44:47], a[232:235], v[220:223], v[44:47]\n"
"v_exp_f32 v161, v149 clamp\n"
"v_mfma_f32_16x16x32_bf16 v[60:63], a[236:239], v[220:223], v[60:63]\n"
"buffer_load_dwordx4 a[68:71], v246, s[84:87], s48 offen offset:2048 sc1\n"
"v_cvt_pk_bf16_f32 v218, v160, v161\n"
"v_exp_f32 v162, v150 clamp\n"
"v_mfma_f32_16x16x32_bf16 v[76:79], a[240:243], v[220:223], v[76:79]\n"
"buffer_load_dwordx4 a[72:75], v246, s[84:87], s48 offen offset:3072 sc1\n"
"v_exp_f32 v163, v151 clamp\n"
"v_mfma_f32_16x16x32_bf16 v[92:95], a[244:247], v[220:223], v[92:95]\n"
"s_add_u32 s48, s48, 4096\n"
"v_cvt_pk_bf16_f32 v219, v162, v163\n"
"v_mfma_f32_16x16x32_bf16 v[108:111], a[248:251], v[220:223], v[108:111]\n"
"buffer_load_dwordx4 v246, s[40:43], s56 offen offset:1024 sc1 lds\n"
"v_exp_f32 v164, v152 clamp\n"
"v_mfma_f32_16x16x32_bf16 v[124:127], a[252:255], v[220:223], v[124:127]\n"
"s_waitcnt lgkmcnt(0)\n"
"v_exp_f32 v165, v153 clamp\n"
"v_mfma_f32_16x16x32_bf16 v[176:179], a[192:195], a[160:163], v[200:203]\n"
"buffer_load_dwordx4 a[76:79], v246, s[84:87], s48 offen offset:0 sc1\n"
"s_add_u32 s48, s49, s34\n"
"ds_read_b128 a[224:227], v245 offset:8192\n"
"v_cvt_pk_bf16_f32 v220, v164, v165\n"
"v_mov_b32 v192, v234\n"
"v_exp_f32 v166, v154 clamp\n"
"v_mfma_f32_16x16x32_bf16 v[188:191], a[208:211], a[176:179], v[204:207]\n"
"buffer_load_dwordx4 a[80:83], v246, s[84:87], s48 offen offset:0 sc1\n"
"ds_read_b128 a[228:231], v245 offset:9216\n"
"v_mov_b32 v193, v234\n"
"v_mov_b64 v[194:195], v[192:193]\n"
"v_mfma_f32_16x16x32_bf16 v[184:187], a[192:195], a[176:179], v[204:207]\n"
"ds_read_b128 a[232:235], v245 offset:10240\n"
"v_mov_b32 v196, v235\n"
"v_mov_b32 v197, v235\n"
"v_exp_f32 v167, v155 clamp\n"
"v_mfma_f32_16x16x32_bf16 v[176:179], a[196:199], a[164:167], v[176:179]\n"
"ds_read_b128 a[236:239], v245 offset:11264\n"
"v_cvt_pk_bf16_f32 v221, v166, v167\n"
"v_mov_b64 v[198:199], v[196:197]\n"
"v_exp_f32 v168, v156 clamp\n"
"v_mfma_f32_16x16x32_bf16 v[188:191], a[212:215], a[180:183], v[188:191]\n"
"buffer_load_dwordx4 v246, s[40:43], s56 offen offset:2048 sc1 lds\n"
"ds_read_b128 a[240:243], v245 offset:12288\n"
"v_exp_f32 v169, v157 clamp\n"
"v_mfma_f32_16x16x32_bf16 v[184:187], a[196:199], a[180:183], v[184:187]\n"
"ds_read_b128 a[244:247], v245 offset:13312\n"
"v_cvt_pk_bf16_f32 v222, v168, v169\n"
"v_mov_b32 v200, v232\n"
"v_mfma_f32_16x16x32_bf16 v[176:179], a[200:203], a[168:171], v[176:179]\n"
"buffer_load_dwordx4 a[84:87], v246, s[84:87], s48 offen offset:1024 sc1\n"
"ds_read_b128 a[248:251], v245 offset:14336\n"
"v_exp_f32 v170, v158 clamp\n"
"v_mfma_f32_16x16x32_bf16 v[188:191], a[216:219], a[184:187], v[188:191]\n"
"buffer_load_dwordx4 a[88:91], v246, s[84:87], s48 offen offset:2048 sc1\n"
"ds_read_b128 a[252:255], v245 offset:15360\n"
"v_mov_b32 v201, v232\n"
"v_mov_b64 v[202:203], v[200:201]\n"
"v_exp_f32 v171, v159 clamp\n"
"v_mfma_f32_16x16x32_bf16 v[184:187], a[200:203], a[184:187], v[184:187]\n"
"v_add_u32 v244, 65536, v245\n"
"v_cvt_pk_bf16_f32 v223, v170, v171\n"
"v_mov_b32 v204, v233\n"
"v_mov_b32 v205, v233\n"
"v_mov_b64 v[206:207], v[204:205]\n"
"v_mfma_f32_16x16x32_bf16 v[176:179], a[204:207], a[172:175], v[176:179]\n"
"buffer_load_dwordx4 v246, s[40:43], s56 offen offset:3072 sc1 lds\n"
"v_mfma_f32_16x16x32_bf16 v[188:191], a[220:223], a[188:191], v[188:191]\n"
"buffer_load_dwordx4 a[92:95], v246, s[84:87], s48 offen offset:3072 sc1\n"
"v_mfma_f32_16x16x32_bf16 v[184:187], a[204:207], a[188:191], v[184:187]\n"
"s_add_u32 s48, s48, 4096\n"
"ds_read_b128 a[160:163], v249 offset:0\n"
"ds_read_b128 a[164:167], v249 offset:1024\n"
"ds_read_b128 a[168:171], v249 offset:2048\n"
"ds_read_b128 a[172:175], v249 offset:3072\n"
"ds_read_b128 a[176:179], v249 offset:4096\n"
"v_smfmac_f32_16x16x64_bf16 v[224:227], v[240:243], v[208:215], v247\n"
"buffer_load_dwordx4 a[96:99], v246, s[84:87], s48 offen offset:0 sc1\n"
"ds_read_b128 a[180:183], v249 offset:5120\n"
"ds_read_b128 a[184:187], v249 offset:6144\n"
"ds_read_b128 a[188:191], v249 offset:7168\n"
"v_smfmac_f32_16x16x64_bf16 v[228:231], v[240:243], v[216:223], v247\n"
"v_cndmask_b32 v176, v250, v176, s[68:69]\n"
"v_cndmask_b32 v177, v250, v177, s[70:71]\n"
"v_cndmask_b32 v178, v250, v178, s[72:73]\n"
"v_cndmask_b32 v179, v250, v179, s[74:75]\n"
"v_cndmask_b32 v188, v250, v188, s[68:69]\n"
"v_cndmask_b32 v189, v250, v189, s[70:71]\n"
"v_cndmask_b32 v190, v250, v190, s[72:73]\n"
"v_cndmask_b32 v191, v250, v191, s[74:75]\n"
"v_max3_f32 v236, v224, v225, v226\n"
"v_max3_f32 v236, v236, v227, v228\n"
"v_max3_f32 v236, v236, v229, v230\n"
"v_max_f32 v236, v236, v231\n"
"v_cmp_nge_f32 vcc, 0.5, v236\n"
"s_waitcnt lgkmcnt(8)\n"
"s_cbranch_vccnz .LstubS1_%=\n"
".LbackS1_%=:\n"
"v_mfma_f32_16x16x32_bf16 v[0:3], a[224:227], v[208:211], v[0:3]\n"
"s_waitcnt vmcnt(31)\n"
"s_barrier\n"
"v_add_f32 v232, v224, v225\n"
"v_add_f32 v233, v226, v227\n"
"v_rcp_f32 v232, v232\n"
"v_mfma_f32_16x16x32_bf16 v[16:19], a[228:231], v[208:211], v[16:19]\n"
"ds_read_b128 a[192:195], v244 offset:16384\n"
"v_rcp_f32 v233, v233\n"
"v_exp_f32 v160, v176 clamp\n"
"v_mfma_f32_16x16x32_bf16 v[32:35], a[232:235], v[208:211], v[32:35]\n"
"s_add_u32 m0, s50, 0\n"
"v_mfma_f32_16x16x32_bf16 v[48:51], a[236:239], v[208:211], v[48:51]\n"
"buffer_load_dwordx4 v246, s[36:39], s54 offen offset:0 sc1 lds\n"
"ds_read_b128 a[196:199], v244 offset:17408\n"
"v_exp_f32 v161, v177 clamp\n"
"v_mfma_f32_16x16x32_bf16 v[64:67], a[240:243], v[208:211], v[64:67]\n"
"v_mul_f32 v0, v0, v232\n"
"v_mfma_f32_16x16x32_bf16 v[80:83], a[244:247], v[208:211], v[80:83]\n"
"ds_read_b128 a[200:203], v244 offset:18432\n"
"v_mul_f32 v1, v1, v232\n"
"v_mul_f32 v2, v2, v232\n"
"v_mfma_f32_16x16x32_bf16 v[96:99], a[248:251], v[208:211], v[96:99]\n"
"v_mul_f32 v3, v3, v232\n"
"v_mul_f32 v16, v16, v232\n"
"v_mul_f32 v17, v17, v232\n"
"v_exp_f32 v162, v178 clamp\n"
"v_mfma_f32_16x16x32_bf16 v[112:115], a[252:255], v[208:211], v[112:115]\n"
"s_add_u32 s52, s52, 1\n"
"ds_read_b128 a[204:207], v244 offset:19456\n"
"s_sub_u32 s55, s55, s29\n"
"v_mul_f32 v18, v18, v232\n"
"v_mul_f32 v19, v19, v232\n"
"v_cvt_pk_bf16_f32 v0, v0, v1\n"
"v_mfma_f32_16x16x32_bf16 v[4:7], a[224:227], v[212:215], v[4:7]\n"
"buffer_load_dwordx4 v246, s[36:39], s54 offen offset:1024 sc1 lds\n"
"v_cvt_pk_bf16_f32 v1, v2, v3\n"
"v_cvt_pk_bf16_f32 v2, v16, v17\n"
"v_mfma_f32_16x16x32_bf16 v[20:23], a[228:231], v[212:215], v[20:23]\n"
"ds_read_b128 a[208:211], v244 offset:20480\n"
"v_cvt_pk_bf16_f32 v3, v18, v19\n"
"v_exp_f32 v163, v179 clamp\n"
"v_mfma_f32_16x16x32_bf16 v[36:39], a[232:235], v[212:215], v[36:39]\n"
"buffer_store_dwordx4 v[0:3], v246, s[84:87], s6 offen offset:0 sc1\n"
"ds_read_b128 a[212:215], v244 offset:21504\n"
"v_mul_f32 v32, v32, v232\n"
"v_mul_f32 v33, v33, v232\n"
"v_mfma_f32_16x16x32_bf16 v[52:55], a[236:239], v[212:215], v[52:55]\n"
"v_mul_f32 v34, v34, v232\n"
"v_mul_f32 v35, v35, v232\n"
"v_mul_f32 v48, v48, v232\n"
"v_exp_f32 v164, v184 clamp\n"
"v_mfma_f32_16x16x32_bf16 v[68:71], a[240:243], v[212:215], v[68:71]\n"
"ds_read_b128 a[216:219], v244 offset:22528\n"
"v_mul_f32 v49, v49, v232\n"
"v_mul_f32 v50, v50, v232\n"
"v_mul_f32 v51, v51, v232\n"
"v_mfma_f32_16x16x32_bf16 v[84:87], a[244:247], v[212:215], v[84:87]\n"
"buffer_load_dwordx4 v246, s[36:39], s54 offen offset:2048 sc1 lds\n"
"v_cvt_pk_bf16_f32 v32, v32, v33\n"
"v_cvt_pk_bf16_f32 v33, v34, v35\n"
"v_cvt_pk_bf16_f32 v34, v48, v49\n"
"v_mfma_f32_16x16x32_bf16 v[100:103], a[248:251], v[212:215], v[100:103]\n"
"ds_read_b128 a[220:223], v244 offset:23552\n"
"v_cvt_pk_bf16_f32 v35, v50, v51\n"
"v_exp_f32 v165, v185 clamp\n"
"v_mfma_f32_16x16x32_bf16 v[116:119], a[252:255], v[212:215], v[116:119]\n"
"buffer_store_dwordx4 v[32:35], v246, s[84:87], s6 offen offset:1024 sc1\n"
"v_mul_f32 v64, v64, v232\n"
"v_mul_f32 v65, v65, v232\n"
"v_mul_f32 v66, v66, v232\n"
"v_mfma_f32_16x16x32_bf16 v[8:11], a[224:227], v[216:219], v[8:11]\n"
"v_xor_b32 v244, 32768, v244\n"
"v_mul_f32 v67, v67, v232\n"
"v_mul_f32 v80, v80, v232\n"
"v_mfma_f32_16x16x32_bf16 v[24:27], a[228:231], v[216:219], v[24:27]\n"
"v_mul_f32 v81, v81, v232\n"
"v_mul_f32 v82, v82, v232\n"
"v_mul_f32 v83, v83, v232\n"
"v_exp_f32 v166, v186 clamp\n"
"v_mfma_f32_16x16x32_bf16 v[40:43], a[232:235], v[216:219], v[40:43]\n"
"v_cvt_pk_bf16_f32 v64, v64, v65\n"
"v_cvt_pk_bf16_f32 v65, v66, v67\n"
"v_cvt_pk_bf16_f32 v66, v80, v81\n"
"v_mfma_f32_16x16x32_bf16 v[56:59], a[236:239], v[216:219], v[56:59]\n"
"buffer_load_dwordx4 v246, s[36:39], s54 offen offset:3072 sc1 lds\n"
"v_cvt_pk_bf16_f32 v67, v82, v83\n"
"v_mfma_f32_16x16x32_bf16 v[72:75], a[240:243], v[216:219], v[72:75]\n"
"buffer_store_dwordx4 v[64:67], v246, s[84:87], s6 offen offset:2048 sc1\n"
"v_mul_f32 v96, v96, v232\n"
"v_mul_f32 v97, v97, v232\n"
"v_exp_f32 v167, v187 clamp\n"
"v_mfma_f32_16x16x32_bf16 v[88:91], a[244:247], v[216:219], v[88:91]\n"
"v_mul_f32 v98, v98, v232\n"
"v_mul_f32 v99, v99, v232\n"
"v_mul_f32 v112, v112, v232\n"
"v_mfma_f32_16x16x32_bf16 v[104:107], a[248:251], v[216:219], v[104:107]\n"
"v_mul_f32 v113, v113, v232\n"
"v_mul_f32 v114, v114, v232\n"
"v_mul_f32 v115, v115, v232\n"
"v_exp_f32 v168, v188 clamp\n"
"v_mfma_f32_16x16x32_bf16 v[120:123], a[252:255], v[216:219], v[120:123]\n"
"s_add_u32 s54, s54, s28\n"
"s_cmp_eq_u32 s52, 30\n"
"s_cselect_b32 s54, s30, s54\n"
"s_cselect_b32 s28, 0x1000, s28\n"
"v_cvt_pk_bf16_f32 v216, v160, v161\n"
"v_cvt_pk_bf16_f32 v217, v162, v163\n"
"v_mfma_f32_16x16x32_bf16 v[12:15], a[224:227], v[220:223], v[12:15]\n"
"v_mov_b64 v[218:219], 0\n"
"s_cmp_lt_u32 s55, 540672\n"
"s_cselect_b32 s56, 1073741824, s55\n"
"s_add_u32 m0, s50, 65536\n"
"v_cvt_pk_bf16_f32 v96, v96, v97\n"
"v_cvt_pk_bf16_f32 v97, v98, v99\n"
"v_mfma_f32_16x16x32_bf16 v[28:31], a[228:231], v[220:223], v[28:31]\n"
"buffer_load_dwordx4 v246, s[40:43], s56 offen offset:0 sc1 lds\n"
"v_cvt_pk_bf16_f32 v98, v112, v113\n"
"v_exp_f32 v169, v189 clamp\n"
"v_mfma_f32_16x16x32_bf16 v[44:47], a[232:235], v[220:223], v[44:47]\n"
"v_cvt_pk_bf16_f32 v99, v114, v115\n"
"v_mul_f32 v4, v4, v233\n"
"v_mul_f32 v5, v5, v233\n"
"v_mfma_f32_16x16x32_bf16 v[60:63], a[236:239], v[220:223], v[60:63]\n"
"buffer_store_dwordx4 v[96:99], v246, s[84:87], s6 offen offset:3072 sc1\n"
"v_mul_f32 v6, v6, v233\n"
"v_mul_f32 v7, v7, v233\n"
"v_mul_f32 v20, v20, v233\n"
"v_mul_f32 v21, v21, v233\n"
"v_mul_f32 v22, v22, v233\n"
"v_mfma_f32_16x16x32_bf16 v[76:79], a[240:243], v[220:223], v[76:79]\n"
"v_mul_f32 v23, v23, v233\n"
"s_add_u32 s6, s6, 4096\n"
"v_cvt_pk_bf16_f32 v4, v4, v5\n"
"v_cvt_pk_bf16_f32 v5, v6, v7\n"
"v_exp_f32 v170, v190 clamp\n"
"v_mfma_f32_16x16x32_bf16 v[92:95], a[244:247], v[220:223], v[92:95]\n"
"buffer_load_dwordx4 v246, s[40:43], s56 offen offset:1024 sc1 lds\n"
"v_cvt_pk_bf16_f32 v6, v20, v21\n"
"v_cvt_pk_bf16_f32 v7, v22, v23\n"
"v_mfma_f32_16x16x32_bf16 v[108:111], a[248:251], v[220:223], v[108:111]\n"
"buffer_store_dwordx4 v[4:7], v246, s[84:87], s6 offen offset:0 sc1\n"
"v_mul_f32 v36, v36, v233\n"
"v_mul_f32 v37, v37, v233\n"
"v_mfma_f32_16x16x32_bf16 v[124:127], a[252:255], v[220:223], v[124:127]\n"
"s_waitcnt lgkmcnt(0)\n"
"v_cvt_pk_bf16_f32 v220, v164, v165\n"
"v_cvt_pk_bf16_f32 v221, v166, v167\n"
"v_cvt_pk_bf16_f32 v222, v168, v169\n"
"v_exp_f32 v171, v191 clamp\n"
"v_mfma_f32_16x16x32_bf16 v[128:131], a[192:195], a[128:131], v[192:195]\n"
"ds_read_b128 a[224:227], v245 offset:24576\n"
"v_cvt_pk_bf16_f32 v223, v170, v171\n"
"v_mul_f32 v38, v38, v233\n"
"v_mul_f32 v39, v39, v233\n"
"v_mul_f32 v52, v52, v233\n"
"v_mfma_f32_16x16x32_bf16 v[140:143], a[208:211], a[144:147], v[196:199]\n"
"ds_read_b128 a[228:231], v245 offset:25600\n"
"v_mul_f32 v53, v53, v233\n"
"v_mul_f32 v54, v54, v233\n"
"v_mul_f32 v55, v55, v233\n"
"v_cvt_pk_bf16_f32 v36, v36, v37\n"
"v_mfma_f32_16x16x32_bf16 v[136:139], a[192:195], a[144:147], v[196:199]\n"
"ds_read_b128 a[232:235], v245 offset:26624\n"
"v_cvt_pk_bf16_f32 v37, v38, v39\n"
"v_cvt_pk_bf16_f32 v38, v52, v53\n"
"v_cvt_pk_bf16_f32 v39, v54, v55\n"
"v_mfma_f32_16x16x32_bf16 v[128:131], a[196:199], a[132:135], v[128:131]\n"
"buffer_load_dwordx4 v246, s[40:43], s56 offen offset:2048 sc1 lds\n"
"ds_read_b128 a[236:239], v245 offset:27648\n"
"v_mfma_f32_16x16x32_bf16 v[140:143], a[212:215], a[148:151], v[140:143]\n"
"buffer_store_dwordx4 v[36:39], v246, s[84:87], s6 offen offset:1024 sc1\n"
"ds_read_b128 a[240:243], v245 offset:28672\n"
"v_mul_f32 v68, v68, v233\n"
"v_mul_f32 v69, v69, v233\n"
"v_mul_f32 v70, v70, v233\n"
"v_mul_f32 v71, v71, v233\n"
"v_mfma_f32_16x16x32_bf16 v[136:139], a[196:199], a[148:151], v[136:139]\n"
"ds_read_b128 a[244:247], v245 offset:29696\n"
"v_mul_f32 v84, v84, v233\n"
"v_mul_f32 v85, v85, v233\n"
"v_mul_f32 v86, v86, v233\n"
"v_smfmac_f32_16x16x64_bf16 v[228:231], v[240:243], v[216:223], v247\n"
"ds_read_b128 a[248:251], v245 offset:30720\n"
"v_mul_f32 v87, v87, v233\n"
"v_cvt_pk_bf16_f32 v68, v68, v69\n"
"v_mfma_f32_16x16x32_bf16 v[128:131], a[200:203], a[136:139], v[128:131]\n"
"ds_read_b128 a[252:255], v245 offset:31744\n"
"v_cvt_pk_bf16_f32 v69, v70, v71\n"
"v_cvt_pk_bf16_f32 v70, v84, v85\n"
"v_cvt_pk_bf16_f32 v71, v86, v87\n"
"v_mfma_f32_16x16x32_bf16 v[140:143], a[216:219], a[152:155], v[140:143]\n"
"buffer_load_dwordx4 v246, s[40:43], s56 offen offset:3072 sc1 lds\n"
"v_and_b32 v245, 0xffff7fff, v244\n"
"v_mfma_f32_16x16x32_bf16 v[136:139], a[200:203], a[152:155], v[136:139]\n"
"buffer_store_dwordx4 v[68:71], v246, s[84:87], s6 offen offset:2048 sc1\n"
"v_mul_f32 v100, v100, v233\n"
"v_mul_f32 v101, v101, v233\n"
"v_mul_f32 v102, v102, v233\n"
"v_mul_f32 v103, v103, v233\n"
"v_mfma_f32_16x16x32_bf16 v[128:131], a[204:207], a[140:143], v[128:131]\n"
"v_mul_f32 v116, v116, v233\n"
"v_mul_f32 v117, v117, v233\n"
"v_mfma_f32_16x16x32_bf16 v[140:143], a[220:223], a[156:159], v[140:143]\n"
"v_mul_f32 v118, v118, v233\n"
"v_mul_f32 v119, v119, v233\n"
"v_cvt_pk_bf16_f32 v100, v100, v101\n"
"v_mfma_f32_16x16x32_bf16 v[136:139], a[204:207], a[156:159], v[136:139]\n"
"v_cvt_pk_bf16_f32 v101, v102, v103\n"
"v_cvt_pk_bf16_f32 v102, v116, v117\n"
"v_cvt_pk_bf16_f32 v103, v118, v119\n"
"buffer_store_dwordx4 v[100:103], v246, s[84:87], s6 offen offset:3072 sc1\n"
"v_cndmask_b32 v128, v250, v128, s[68:69]\n"
"v_cndmask_b32 v129, v250, v129, s[70:71]\n"
"v_cndmask_b32 v130, v250, v130, s[72:73]\n"
"v_cndmask_b32 v131, v250, v131, s[74:75]\n"
"v_cndmask_b32 v140, v250, v140, s[68:69]\n"
"v_cndmask_b32 v141, v250, v141, s[70:71]\n"
"v_cndmask_b32 v142, v250, v142, s[72:73]\n"
"v_cndmask_b32 v143, v250, v143, s[74:75]\n"
"v_max3_f32 v236, v228, v229, v230\n"
"v_max_f32 v236, v236, v231\n"
"v_cmp_nge_f32 vcc, 0.5, v236\n"
"s_waitcnt lgkmcnt(0)\n"
"s_cbranch_vccnz .LstubS2_%=\n"
".LbackS2_%=:\n"
"s_cmp_eq_u32 s52, 32\n"
"s_cbranch_scc1 .Lp3l_%=\n"
"v_mfma_f32_16x16x32_bf16 v[8:11], a[224:227], v[216:219], v[8:11]\n"
"s_waitcnt vmcnt(39)\n"
"s_barrier\n"
"v_add_f32 v234, v228, v229\n"
"v_add_f32 v235, v230, v231\n"
"v_rcp_f32 v234, v234\n"
"v_mfma_f32_16x16x32_bf16 v[24:27], a[228:231], v[216:219], v[24:27]\n"
"ds_read_b128 a[192:195], v244 offset:0\n"
"v_mov_b64 v[224:225], 0\n"
"v_mov_b64 v[226:227], 0\n"
"v_rcp_f32 v235, v235\n"
"v_mfma_f32_16x16x32_bf16 v[40:43], a[232:235], v[216:219], v[40:43]\n"
"s_add_u32 m0, s50, 16384\n"
"v_mov_b64 v[180:181], v[250:251]\n"
"v_mov_b64 v[182:183], v[250:251]\n"
"v_exp_f32 v144, v128 clamp\n"
"v_mfma_f32_16x16x32_bf16 v[56:59], a[236:239], v[216:219], v[56:59]\n"
"buffer_load_dwordx4 v246, s[36:39], s54 offen offset:0 sc1 lds\n"
"ds_read_b128 a[196:199], v244 offset:1024\n"
"v_exp_f32 v145, v129 clamp\n"
"v_mfma_f32_16x16x32_bf16 v[72:75], a[240:243], v[216:219], v[72:75]\n"
"s_add_u32 s48, s49, s34\n"
"ds_read_b128 a[200:203], v244 offset:2048\n"
"v_cvt_pk_bf16_f32 v208, v144, v145\n"
"s_add_u32 s48, s48, 5120\n"
"v_mfma_f32_16x16x32_bf16 v[88:91], a[244:247], v[216:219], v[88:91]\n"
"buffer_load_dwordx4 a[100:103], v246, s[84:87], s48 offen offset:0 sc1\n"
"ds_read_b128 a[204:207], v244 offset:3072\n"
"v_mul_f32 v8, v8, v234\n"
"v_mul_f32 v9, v9, v234\n"
"v_exp_f32 v146, v130 clamp\n"
"v_mfma_f32_16x16x32_bf16 v[104:107], a[248:251], v[216:219], v[104:107]\n"
"ds_read_b128 a[208:211], v244 offset:4096\n"
"v_mul_f32 v10, v10, v234\n"
"v_mul_f32 v11, v11, v234\n"
"v_mul_f32 v24, v24, v234\n"
"v_mul_f32 v25, v25, v234\n"
"v_mfma_f32_16x16x32_bf16 v[120:123], a[252:255], v[216:219], v[120:123]\n"
"s_add_u32 s52, s52, 1\n"
"s_sub_u32 s55, s55, s29\n"
"v_mul_f32 v26, v26, v234\n"
"v_mul_f32 v27, v27, v234\n"
"s_add_u32 s6, s6, 4096\n"
"v_cvt_pk_bf16_f32 v8, v8, v9\n"
"v_mfma_f32_16x16x32_bf16 v[12:15], a[224:227], v[220:223], v[12:15]\n"
"buffer_load_dwordx4 v246, s[36:39], s54 offen offset:1024 sc1 lds\n"
"ds_read_b128 a[212:215], v244 offset:5120\n"
"v_exp_f32 v147, v131 clamp\n"
"v_mfma_f32_16x16x32_bf16 v[28:31], a[228:231], v[220:223], v[28:31]\n"
"ds_read_b128 a[216:219], v244 offset:6144\n"
"v_cvt_pk_bf16_f32 v209, v146, v147\n"
"v_mov_b64 v[210:211], 0\n"
"v_cvt_pk_bf16_f32 v9, v10, v11\n"
"v_cvt_pk_bf16_f32 v10, v24, v25\n"
"v_mfma_f32_16x16x32_bf16 v[44:47], a[232:235], v[220:223], v[44:47]\n"
"ds_read_b128 a[220:223], v244 offset:7168\n"
"v_cvt_pk_bf16_f32 v11, v26, v27\n"
"v_exp_f32 v148, v136 clamp\n"
"v_mfma_f32_16x16x32_bf16 v[60:63], a[236:239], v[220:223], v[60:63]\n"
"buffer_store_dwordx4 v[8:11], v246, s[84:87], s6 offen offset:0 sc1\n"
"v_mul_f32 v40, v40, v234\n"
"v_mul_f32 v41, v41, v234\n"
"v_mul_f32 v42, v42, v234\n"
"v_mfma_f32_16x16x32_bf16 v[76:79], a[240:243], v[220:223], v[76:79]\n"
"s_add_u32 s59, s30, 12288\n"
"s_add_u32 s56, s38, 8192\n"
"s_cmp_eq_u32 s52, 31\n"
"s_cselect_b32 s55, s59, s55\n"
"s_cselect_b32 s42, s56, s42\n"
"s_cselect_b32 s29, 0xfffff000, s29\n"
"v_mfma_f32_16x16x32_bf16 v[92:95], a[244:247], v[220:223], v[92:95]\n"
"buffer_load_dwordx4 v246, s[36:39], s54 offen offset:2048 sc1 lds\n"
"v_mul_f32 v43, v43, v234\n"
"v_exp_f32 v149, v137 clamp\n"
"v_mfma_f32_16x16x32_bf16 v[108:111], a[248:251], v[220:223], v[108:111]\n"
"v_cvt_pk_bf16_f32 v212, v148, v149\n"
"v_mul_f32 v56, v56, v234\n"
"v_mul_f32 v57, v57, v234\n"
"v_mfma_f32_16x16x32_bf16 v[124:127], a[252:255], v[220:223], v[124:127]\n"
"buffer_load_dwordx4 a[104:107], v246, s[84:87], s48 offen offset:1024 sc1\n"
"s_waitcnt lgkmcnt(0)\n"
"v_mul_f32 v58, v58, v234\n"
"v_mul_f32 v59, v59, v234\n"
"v_cvt_pk_bf16_f32 v40, v40, v41\n"
"v_exp_f32 v150, v138 clamp\n"
"v_mfma_f32_16x16x32_bf16 v[176:179], a[192:195], a[160:163], v[200:203]\n"
"ds_read_b128 a[224:227], v244 offset:8192\n"
"v_cvt_pk_bf16_f32 v41, v42, v43\n"
"v_cvt_pk_bf16_f32 v42, v56, v57\n"
"v_cvt_pk_bf16_f32 v43, v58, v59\n"
"v_mfma_f32_16x16x32_bf16 v[160:163], a[192:195], a[128:131], v[192:195]\n"
"v_mul_f32 v72, v72, v234\n"
"v_exp_f32 v151, v139 clamp\n"
"v_mfma_f32_16x16x32_bf16 v[168:171], a[192:195], a[144:147], v[196:199]\n"
"buffer_store_dwordx4 v[40:43], v246, s[84:87], s6 offen offset:1024 sc1\n"
"v_cvt_pk_bf16_f32 v213, v150, v151\n"
"v_mul_f32 v73, v73, v234\n"
"v_mul_f32 v74, v74, v234\n"
"v_mul_f32 v75, v75, v234\n"
"v_mul_f32 v88, v88, v234\n"
"v_mfma_f32_16x16x32_bf16 v[184:187], a[192:195], a[176:179], v[204:207]\n"
"buffer_load_dwordx4 v246, s[36:39], s54 offen offset:3072 sc1 lds\n"
"ds_read_b128 a[228:231], v244 offset:9216\n"
"v_mul_f32 v89, v89, v234\n"
"v_mul_f32 v90, v90, v234\n"
"v_mfma_f32_16x16x32_bf16 v[176:179], a[196:199], a[164:167], v[176:179]\n"
"v_mul_f32 v91, v91, v234\n"
"v_cvt_pk_bf16_f32 v72, v72, v73\n"
"v_cvt_pk_bf16_f32 v73, v74, v75\n"
"v_exp_f32 v152, v140 clamp\n"
"v_mfma_f32_16x16x32_bf16 v[160:163], a[196:199], a[132:135], v[160:163]\n"
"buffer_load_dwordx4 a[108:111], v246, s[84:87], s48 offen offset:2048 sc1\n"
"v_cvt_pk_bf16_f32 v74, v88, v89\n"
"v_cvt_pk_bf16_f32 v75, v90, v91\n"
"v_mfma_f32_16x16x32_bf16 v[168:171], a[196:199], a[148:151], v[168:171]\n"
"ds_read_b128 a[232:235], v244 offset:10240\n"
"v_exp_f32 v153, v141 clamp\n"
"v_mfma_f32_16x16x32_bf16 v[184:187], a[196:199], a[180:183], v[184:187]\n"
"buffer_store_dwordx4 v[72:75], v246, s[84:87], s6 offen offset:2048 sc1\n"
"s_add_u32 s54, s54, s28\n"
"v_cvt_pk_bf16_f32 v214, v152, v153\n"
"s_cmp_lt_u32 s55, 540672\n"
"s_cselect_b32 s56, 1073741824, s55\n"
"s_add_u32 m0, s50, 81920\n"
"v_mul_f32 v104, v104, v234\n"
"v_mfma_f32_16x16x32_bf16 v[176:179], a[200:203], a[168:171], v[176:179]\n"
"buffer_load_dwordx4 v246, s[40:43], s56 offen offset:0 sc1 lds\n"
"v_mul_f32 v105, v105, v234\n"
"v_mul_f32 v106, v106, v234\n"
"v_mul_f32 v107, v107, v234\n"
"v_mfma_f32_16x16x32_bf16 v[160:163], a[200:203], a[136:139], v[160:163]\n"
"ds_read_b128 a[236:239], v244 offset:11264\n"
"v_mul_f32 v120, v120, v234\n"
"v_mul_f32 v121, v121, v234\n"
"v_exp_f32 v154, v142 clamp\n"
"v_mfma_f32_16x16x32_bf16 v[168:171], a[200:203], a[152:155], v[168:171]\n"
"v_mul_f32 v122, v122, v234\n"
"v_mul_f32 v123, v123, v234\n"
"v_cvt_pk_bf16_f32 v104, v104, v105\n"
"v_cvt_pk_bf16_f32 v105, v106, v107\n"
"v_cvt_pk_bf16_f32 v106, v120, v121\n"
"v_mfma_f32_16x16x32_bf16 v[184:187], a[200:203], a[184:187], v[184:187]\n"
"v_cvt_pk_bf16_f32 v107, v122, v123\n"
"v_exp_f32 v155, v143 clamp\n"
"v_mfma_f32_16x16x32_bf16 v[176:179], a[204:207], a[172:175], v[176:179]\n"
"buffer_store_dwordx4 v[104:107], v246, s[84:87], s6 offen offset:3072 sc1\n"
"ds_read_b128 a[240:243], v244 offset:12288\n"
"v_cvt_pk_bf16_f32 v215, v154, v155\n"
"v_mul_f32 v12, v12, v235\n"
"v_mul_f32 v13, v13, v235\n"
"v_mul_f32 v14, v14, v235\n"
"v_mfma_f32_16x16x32_bf16 v[160:163], a[204:207], a[140:143], v[160:163]\n"
"buffer_load_dwordx4 v246, s[40:43], s56 offen offset:1024 sc1 lds\n"
"v_mul_f32 v15, v15, v235\n"
"v_mfma_f32_16x16x32_bf16 v[168:171], a[204:207], a[156:159], v[168:171]\n"
"buffer_load_dwordx4 a[112:115], v246, s[84:87], s48 offen offset:3072 sc1\n"
"v_mul_f32 v28, v28, v235\n"
"v_mul_f32 v29, v29, v235\n"
"v_mul_f32 v30, v30, v235\n"
"v_mul_f32 v31, v31, v235\n"
"s_add_u32 s6, s6, 4096\n"
"v_cvt_pk_bf16_f32 v12, v12, v13\n"
"v_mfma_f32_16x16x32_bf16 v[184:187], a[204:207], a[188:191], v[184:187]\n"
"ds_read_b128 a[244:247], v244 offset:13312\n"
"v_cvt_pk_bf16_f32 v13, v14, v15\n"
"v_cvt_pk_bf16_f32 v14, v28, v29\n"
"v_cvt_pk_bf16_f32 v15, v30, v31\n"
"v_cndmask_b32 v176, v250, v176, s[68:69]\n"
"v_mfma_f32_16x16x32_bf16 v[188:191], a[208:211], a[176:179], v[204:207]\n"
"buffer_store_dwordx4 v[12:15], v246, s[84:87], s6 offen offset:0 sc1\n"
"v_mul_f32 v44, v44, v235\n"
"v_mul_f32 v45, v45, v235\n"
"v_mul_f32 v46, v46, v235\n"
"v_mfma_f32_16x16x32_bf16 v[164:167], a[208:211], a[128:131], v[192:195]\n"
"v_mul_f32 v47, v47, v235\n"
"v_mul_f32 v60, v60, v235\n"
"v_mul_f32 v61, v61, v235\n"
"v_mul_f32 v62, v62, v235\n"
"v_mul_f32 v63, v63, v235\n"
"v_smfmac_f32_16x16x64_bf16 v[224:227], v[240:243], v[208:215], v247\n"
"ds_read_b128 a[248:251], v244 offset:14336\n"
"v_cvt_pk_bf16_f32 v44, v44, v45\n"
"v_cvt_pk_bf16_f32 v45, v46, v47\n"
"v_cvt_pk_bf16_f32 v46, v60, v61\n"
"v_cvt_pk_bf16_f32 v47, v62, v63\n"
"v_mfma_f32_16x16x32_bf16 v[172:175], a[208:211], a[144:147], v[196:199]\n"
"buffer_load_dwordx4 v246, s[40:43], s56 offen offset:2048 sc1 lds\n"
"v_mul_f32 v76, v76, v235\n"
"v_mfma_f32_16x16x32_bf16 v[188:191], a[212:215], a[180:183], v[188:191]\n"
"buffer_store_dwordx4 v[44:47], v246, s[84:87], s6 offen offset:1024 sc1\n"
"s_add_u32 s48, s48, 4096\n"
"v_mul_f32 v77, v77, v235\n"
"v_mul_f32 v78, v78, v235\n"
"v_mul_f32 v79, v79, v235\n"
"v_cndmask_b32 v177, v250, v177, s[70:71]\n"
"v_cndmask_b32 v178, v250, v178, s[72:73]\n"
"v_mfma_f32_16x16x32_bf16 v[164:167], a[212:215], a[132:135], v[164:167]\n"
"buffer_load_dwordx4 a[116:119], v246, s[84:87], s48 offen offset:0 sc1\n"
"ds_read_b128 a[252:255], v244 offset:15360\n"
"v_mul_f32 v92, v92, v235\n"
"v_mul_f32 v93, v93, v235\n"
"v_mfma_f32_16x16x32_bf16 v[172:175], a[212:215], a[148:151], v[172:175]\n"
"v_mul_f32 v94, v94, v235\n"
"v_mul_f32 v95, v95, v235\n"
"v_cvt_pk_bf16_f32 v76, v76, v77\n"
"v_cndmask_b32 v179, v250, v179, s[74:75]\n"
"v_mfma_f32_16x16x32_bf16 v[188:191], a[216:219], a[184:187], v[188:191]\n"
"v_cvt_pk_bf16_f32 v77, v78, v79\n"
"v_cvt_pk_bf16_f32 v78, v92, v93\n"
"v_cvt_pk_bf16_f32 v79, v94, v95\n"
"v_mfma_f32_16x16x32_bf16 v[164:167], a[216:219], a[136:139], v[164:167]\n"
"buffer_load_dwordx4 v246, s[40:43], s56 offen offset:3072 sc1 lds\n"
"v_mul_f32 v108, v108, v235\n"
"v_mfma_f32_16x16x32_bf16 v[172:175], a[216:219], a[152:155], v[172:175]\n"
"buffer_store_dwordx4 v[76:79], v246, s[84:87], s6 offen offset:2048 sc1\n"
"v_mul_f32 v109, v109, v235\n"
"v_mul_f32 v110, v110, v235\n"
"v_mul_f32 v111, v111, v235\n"
"v_mfma_f32_16x16x32_bf16 v[188:191], a[220:223], a[188:191], v[188:191]\n"
"v_mul_f32 v124, v124, v235\n"
"v_mul_f32 v125, v125, v235\n"
"v_mfma_f32_16x16x32_bf16 v[164:167], a[220:223], a[140:143], v[164:167]\n"
"v_mul_f32 v126, v126, v235\n"
"v_mul_f32 v127, v127, v235\n"
"v_cvt_pk_bf16_f32 v108, v108, v109\n"
"v_mfma_f32_16x16x32_bf16 v[172:175], a[220:223], a[156:159], v[172:175]\n"
"v_cvt_pk_bf16_f32 v109, v110, v111\n"
"v_cvt_pk_bf16_f32 v110, v124, v125\n"
"v_cvt_pk_bf16_f32 v111, v126, v127\n"
"buffer_store_dwordx4 v[108:111], v246, s[84:87], s6 offen offset:3072 sc1\n"
"v_cndmask_b32 v188, v250, v188, s[68:69]\n"
"v_cndmask_b32 v189, v250, v189, s[70:71]\n"
"v_cndmask_b32 v190, v250, v190, s[72:73]\n"
"v_cndmask_b32 v191, v250, v191, s[74:75]\n"
"v_max3_f32 v236, v224, v225, v226\n"
"v_max_f32 v236, v236, v227\n"
"v_cmp_nge_f32 vcc, 0.5, v236\n"
"s_waitcnt lgkmcnt(0)\n"
"s_cbranch_vccnz .LstubS3_%=\n"
".LbackS3_%=:\n"
"v_mfma_f32_16x16x32_bf16 v[0:3], a[224:227], v[208:211], 0\n"
"s_waitcnt vmcnt(39)\n"
"v_exp_f32 v236, v160 clamp\n"
"v_mfma_f32_16x16x32_bf16 v[16:19], a[228:231], v[208:211], 0\n"
"s_barrier\n"
"v_mov_b64 v[228:229], 0\n"
"v_mov_b64 v[230:231], 0\n"
"v_mfma_f32_16x16x32_bf16 v[32:35], a[232:235], v[208:211], 0\n"
"s_add_u32 s52, s52, 1\n"
"v_exp_f32 v237, v161 clamp\n"
"v_mfma_f32_16x16x32_bf16 v[48:51], a[236:239], v[208:211], 0\n"
"s_mov_b32 m0, s51\n"
"ds_read_b128 a[192:195], v244 offset:16384\n"
"v_mfma_f32_16x16x32_bf16 v[64:67], a[240:243], v[208:211], 0\n"
"v_exp_f32 v238, v162 clamp\n"
"v_mfma_f32_16x16x32_bf16 v[80:83], a[244:247], v[208:211], 0\n"
"s_sub_u32 s55, s55, s29\n"
"v_exp_f32 v239, v163 clamp\n"
"v_mfma_f32_16x16x32_bf16 v[96:99], a[248:251], v[208:211], 0\n"
"s_mov_b32 s60, .Lmid3_%=-.Lpc_%=\n"
"buffer_load_dwordx4 v246, s[36:39], s54 offen offset:0 sc1 lds\n"
"v_mfma_f32_16x16x32_bf16 v[112:115], a[252:255], v[208:211], 0\n"
"s_bitcmp1_b32 s76, 0\n"
"ds_read_b128 a[196:199], v244 offset:17408\n"
"v_cvt_pk_bf16_f32 v208, v236, v237\n"
"v_mfma_f32_16x16x32_bf16 v[4:7], a[224:227], v[212:215], 0\n"
"s_cselect_b32 s60, .Lmid1_%=-.Lpc_%=, s60\n"
"ds_read_b128 a[200:203], v244 offset:18432\n"
"v_cvt_pk_bf16_f32 v209, v238, v239\n"
"v_mfma_f32_16x16x32_bf16 v[20:23], a[228:231], v[212:215], 0\n"
"s_add_u32 s60, s60, s62\n"
"v_exp_f32 v236, v164 clamp\n"
"v_mfma_f32_16x16x32_bf16 v[36:39], a[232:235], v[212:215], 0\n"
"s_addc_u32 s61, s63, 0\n"
"v_exp_f32 v237, v165 clamp\n"
"v_mfma_f32_16x16x32_bf16 v[52:55], a[236:239], v[212:215], 0\n"
"ds_read_b128 a[204:207], v244 offset:19456\n"
"v_cvt_pk_bf16_f32 v210, v236, v237\n"
"v_mfma_f32_16x16x32_bf16 v[68:71], a[240:243], v[212:215], 0\n"
"v_exp_f32 v238, v166 clamp\n"
"v_mfma_f32_16x16x32_bf16 v[84:87], a[244:247], v[212:215], 0\n"
"v_exp_f32 v239, v167 clamp\n"
"v_mfma_f32_16x16x32_bf16 v[100:103], a[248:251], v[212:215], 0\n"
"buffer_load_dwordx4 v246, s[36:39], s54 offen offset:1024 sc1 lds\n"
"v_mfma_f32_16x16x32_bf16 v[116:119], a[252:255], v[212:215], 0\n"
"ds_read_b128 a[208:211], v244 offset:20480\n"
"v_cvt_pk_bf16_f32 v211, v238, v239\n"
"v_mfma_f32_16x16x32_bf16 v[8:11], a[120:123], a[120:123], 0\n"
"v_exp_f32 v236, v168 clamp\n"
"v_mfma_f32_16x16x32_bf16 v[24:27], a[120:123], a[120:123], 0\n"
"v_exp_f32 v237, v169 clamp\n"
"v_mfma_f32_16x16x32_bf16 v[40:43], a[120:123], a[120:123], 0\n"
"ds_read_b128 a[212:215], v244 offset:21504\n"
"v_cvt_pk_bf16_f32 v212, v236, v237\n"
"v_mfma_f32_16x16x32_bf16 v[56:59], a[120:123], a[120:123], 0\n"
"v_exp_f32 v238, v170 clamp\n"
"v_mfma_f32_16x16x32_bf16 v[72:75], a[120:123], a[120:123], 0\n"
"v_exp_f32 v239, v171 clamp\n"
"v_mfma_f32_16x16x32_bf16 v[88:91], a[120:123], a[120:123], 0\n"
"buffer_load_dwordx4 v246, s[36:39], s54 offen offset:2048 sc1 lds\n"
"v_mfma_f32_16x16x32_bf16 v[104:107], a[120:123], a[120:123], 0\n"
"ds_read_b128 a[216:219], v244 offset:22528\n"
"v_cvt_pk_bf16_f32 v213, v238, v239\n"
"v_mfma_f32_16x16x32_bf16 v[120:123], a[120:123], a[120:123], 0\n"
"v_exp_f32 v236, v172 clamp\n"
"v_mfma_f32_16x16x32_bf16 v[12:15], a[120:123], a[120:123], 0\n"
"v_exp_f32 v237, v173 clamp\n"
"v_mfma_f32_16x16x32_bf16 v[28:31], a[120:123], a[120:123], 0\n"
"ds_read_b128 a[220:223], v244 offset:23552\n"
"v_cvt_pk_bf16_f32 v214, v236, v237\n"
"v_mfma_f32_16x16x32_bf16 v[44:47], a[120:123], a[120:123], 0\n"
"v_exp_f32 v238, v174 clamp\n"
"v_mfma_f32_16x16x32_bf16 v[60:63], a[120:123], a[120:123], 0\n"
"v_exp_f32 v239, v175 clamp\n"
"v_mfma_f32_16x16x32_bf16 v[76:79], a[120:123], a[120:123], 0\n"
"buffer_load_dwordx4 v246, s[36:39], s54 offen offset:3072 sc1 lds\n"
"v_mfma_f32_16x16x32_bf16 v[92:95], a[120:123], a[120:123], 0\n"
"s_add_u32 s54, s54, s28\n"
"v_mov_b32 v244, v245\n"
"v_cvt_pk_bf16_f32 v215, v238, v239\n"
"v_mfma_f32_16x16x32_bf16 v[108:111], a[120:123], a[120:123], 0\n"
"v_exp_f32 v236, v176 clamp\n"
"v_mfma_f32_16x16x32_bf16 v[124:127], a[120:123], a[120:123], 0\n"
"s_waitcnt lgkmcnt(0)\n"
"v_exp_f32 v237, v177 clamp\n"
"s_setpc_b64 s[60:61]\n"
".Lp3l_%=:\n"
"v_mfma_f32_16x16x32_bf16 v[8:11], a[224:227], v[216:219], v[8:11]\n"
"s_waitcnt vmcnt(39)\n"
"s_barrier\n"
"v_add_f32 v234, v228, v229\n"
"v_add_f32 v235, v230, v231\n"
"v_exp_f32 v144, v128 clamp\n"
"v_mfma_f32_16x16x32_bf16 v[24:27], a[228:231], v[216:219], v[24:27]\n"
"v_exp_f32 v145, v129 clamp\n"
"v_rcp_f32 v234, v234\n"
"v_rcp_f32 v235, v235\n"
"v_mfma_f32_16x16x32_bf16 v[40:43], a[232:235], v[216:219], v[40:43]\n"
"v_mov_b64 v[224:225], 0\n"
"v_mov_b64 v[226:227], 0\n"
"v_mov_b64 v[228:229], 0\n"
"v_mov_b64 v[230:231], 0\n"
"v_exp_f32 v146, v130 clamp\n"
"v_mfma_f32_16x16x32_bf16 v[56:59], a[236:239], v[216:219], v[56:59]\n"
"v_subrev_u32 v248, s57, v246\n"
"v_lshlrev_b32 v238, 5, v246\n"
"v_and_b32 v238, 0x1c00, v238\n"
"v_bfe_u32 v236, v246, 4, 1\n"
"v_exp_f32 v147, v131 clamp\n"
"v_mfma_f32_16x16x32_bf16 v[72:75], a[240:243], v[216:219], v[72:75]\n"
"ds_read_b128 v[160:163], v245 offset:8192\n"
"v_lshl_add_u32 v238, v236, 2, v238\n"
"v_min_u32 v236, 2052, v238\n"
"v_cvt_pk_bf16_f32 v208, v144, v145\n"
"v_exp_f32 v148, v136 clamp\n"
"v_mfma_f32_16x16x32_bf16 v[88:91], a[244:247], v[216:219], v[88:91]\n"
"v_cvt_pk_bf16_f32 v209, v146, v147\n"
"v_mov_b64 v[210:211], 0\n"
"s_add_u32 s58, s35, s34\n"
"v_exp_f32 v149, v137 clamp\n"
"v_mfma_f32_16x16x32_bf16 v[104:107], a[248:251], v[216:219], v[104:107]\n"
"buffer_load_dword a220, v236, s[84:87], s10 offen sc1\n"
"s_add_u32 s58, s58, s31\n"
"v_cvt_pk_bf16_f32 v212, v148, v149\n"
"v_mul_f32 v8, v8, v234\n"
"v_exp_f32 v150, v138 clamp\n"
"v_mfma_f32_16x16x32_bf16 v[120:123], a[252:255], v[216:219], v[120:123]\n"
"buffer_load_dwordx4 a[160:163], v248, s[84:87], s58 offen offset:0 sc1\n"
"ds_read_b128 v[164:167], v245 offset:9216\n"
"v_mul_f32 v9, v9, v234\n"
"v_exp_f32 v151, v139 clamp\n"
"v_mfma_f32_16x16x32_bf16 v[12:15], a[224:227], v[220:223], v[12:15]\n"
"buffer_load_dwordx4 a[164:167], v248, s[84:87], s58 offen offset:1024 sc1\n"
"v_cvt_pk_bf16_f32 v213, v150, v151\n"
"v_mul_f32 v10, v10, v234\n"
"v_exp_f32 v152, v140 clamp\n"
"v_mfma_f32_16x16x32_bf16 v[28:31], a[228:231], v[220:223], v[28:31]\n"
"buffer_load_dwordx4 a[168:171], v248, s[84:87], s58 offen offset:2048 sc1\n"
"v_mul_f32 v11, v11, v234\n"
"v_mul_f32 v24, v24, v234\n"
"v_exp_f32 v153, v141 clamp\n"
"v_mfma_f32_16x16x32_bf16 v[44:47], a[232:235], v[220:223], v[44:47]\n"
"buffer_load_dwordx4 a[172:175], v248, s[84:87], s58 offen offset:3072 sc1\n"
"s_add_u32 s58, s58, 4096\n"
"v_cvt_pk_bf16_f32 v214, v152, v153\n"
"v_mul_f32 v25, v25, v234\n"
"v_mul_f32 v26, v26, v234\n"
"v_mul_f32 v27, v27, v234\n"
"v_exp_f32 v154, v142 clamp\n"
"v_mfma_f32_16x16x32_bf16 v[60:63], a[236:239], v[220:223], v[60:63]\n"
"s_add_u32 s6, s6, 4096\n"
"ds_read_b128 v[168:171], v245 offset:10240\n"
"v_cvt_pk_bf16_f32 v8, v8, v9\n"
"v_exp_f32 v155, v143 clamp\n"
"v_mfma_f32_16x16x32_bf16 v[76:79], a[240:243], v[220:223], v[76:79]\n"
"buffer_load_dwordx4 a[176:179], v248, s[84:87], s58 offen offset:0 sc1\n"
"s_add_u32 s58, s35, s34\n"
"v_cvt_pk_bf16_f32 v215, v154, v155\n"
"s_add_u32 m0, s57, 49152\n"
"s_add_u32 s52, s52, 1\n"
"v_cvt_pk_bf16_f32 v9, v10, v11\n"
"v_cvt_pk_bf16_f32 v10, v24, v25\n"
"v_cvt_pk_bf16_f32 v11, v26, v27\n"
"v_mfma_f32_16x16x32_bf16 v[92:95], a[244:247], v[220:223], v[92:95]\n"
"buffer_load_dwordx4 v246, s[36:39], s54 offen offset:0 sc1 lds\n"
"v_mul_f32 v40, v40, v234\n"
"v_mul_f32 v41, v41, v234\n"
"v_mfma_f32_16x16x32_bf16 v[108:111], a[248:251], v[220:223], v[108:111]\n"
"buffer_store_dwordx4 v[8:11], v246, s[84:87], s6 offen offset:0 sc1\n"
"v_mul_f32 v42, v42, v234\n"
"v_mul_f32 v43, v43, v234\n"
"v_mul_f32 v56, v56, v234\n"
"v_mul_f32 v57, v57, v234\n"
"v_mul_f32 v58, v58, v234\n"
"v_mul_f32 v59, v59, v234\n"
"v_mfma_f32_16x16x32_bf16 v[124:127], a[252:255], v[220:223], v[124:127]\n"
"buffer_load_dwordx4 v246, s[36:39], s54 offen offset:1024 sc1 lds\n"
"ds_read_b128 v[172:175], v245 offset:11264\n"
"v_cvt_pk_bf16_f32 v40, v40, v41\n"
"v_cvt_pk_bf16_f32 v41, v42, v43\n"
"v_cvt_pk_bf16_f32 v42, v56, v57\n"
"v_smfmac_f32_16x16x64_bf16 v[224:227], v[240:243], v[208:215], v247\n"
"buffer_load_dwordx4 v246, s[36:39], s54 offen offset:2048 sc1 lds\n"
"v_mov_b64 v[216:217], 0\n"
"v_mov_b64 v[218:219], 0\n"
"v_mov_b64 v[220:221], 0\n"
"v_mov_b64 v[222:223], 0\n"
"v_cvt_pk_bf16_f32 v43, v58, v59\n"
"buffer_store_dwordx4 v[40:43], v246, s[84:87], s6 offen offset:1024 sc1\n"
"v_mul_f32 v72, v72, v234\n"
"v_mul_f32 v73, v73, v234\n"
"v_mul_f32 v74, v74, v234\n"
"buffer_load_dwordx4 v246, s[36:39], s54 offen offset:3072 sc1 lds\n"
"v_mul_f32 v75, v75, v234\n"
"v_mul_f32 v88, v88, v234\n"
"v_mul_f32 v89, v89, v234\n"
"v_mul_f32 v90, v90, v234\n"
"s_add_u32 s58, s58, 10240\n"
"ds_read_b128 v[176:179], v245 offset:12288\n"
"v_mul_f32 v91, v91, v234\n"
"v_cvt_pk_bf16_f32 v72, v72, v73\n"
"v_cvt_pk_bf16_f32 v73, v74, v75\n"
"v_cvt_pk_bf16_f32 v74, v88, v89\n"
"v_cvt_pk_bf16_f32 v75, v90, v91\n"
"buffer_load_dwordx4 a[120:123], v248, s[84:87], s58 offen offset:0 sc1\n"
"buffer_store_dwordx4 v[72:75], v246, s[84:87], s6 offen offset:2048 sc1\n"
"v_mul_f32 v104, v104, v234\n"
"v_mul_f32 v105, v105, v234\n"
"buffer_load_dwordx4 a[124:127], v248, s[84:87], s58 offen offset:1024 sc1\n"
"v_mul_f32 v106, v106, v234\n"
"v_mul_f32 v107, v107, v234\n"
"v_mul_f32 v120, v120, v234\n"
"v_mul_f32 v121, v121, v234\n"
"v_mul_f32 v122, v122, v234\n"
"v_mul_f32 v123, v123, v234\n"
"v_cvt_pk_bf16_f32 v104, v104, v105\n"
"v_cvt_pk_bf16_f32 v105, v106, v107\n"
"v_cvt_pk_bf16_f32 v106, v120, v121\n"
"v_cvt_pk_bf16_f32 v107, v122, v123\n"
"buffer_load_dwordx4 a[128:131], v248, s[84:87], s58 offen offset:2048 sc1\n"
"buffer_store_dwordx4 v[104:107], v246, s[84:87], s6 offen offset:3072 sc1\n"
"ds_read_b128 v[180:183], v245 offset:13312\n"
"v_mul_f32 v12, v12, v235\n"
"v_mul_f32 v13, v13, v235\n"
"v_mul_f32 v14, v14, v235\n"
"v_mul_f32 v15, v15, v235\n"
"v_mul_f32 v28, v28, v235\n"
"v_mul_f32 v29, v29, v235\n"
"v_mul_f32 v30, v30, v235\n"
"buffer_load_dwordx4 a[132:135], v248, s[84:87], s58 offen offset:3072 sc1\n"
"v_mul_f32 v31, v31, v235\n"
"s_add_u32 s6, s6, 4096\n"
"v_cvt_pk_bf16_f32 v12, v12, v13\n"
"v_cvt_pk_bf16_f32 v13, v14, v15\n"
"v_cvt_pk_bf16_f32 v14, v28, v29\n"
"s_add_u32 s58, s58, 4096\n"
"v_cvt_pk_bf16_f32 v15, v30, v31\n"
"v_mfma_f32_32x32x16_bf16 v[16:31], v[216:219], v[216:219], 0\n"
"buffer_store_dwordx4 v[12:15], v246, s[84:87], s6 offen offset:0 sc1\n"
"s_nop 0\n"
"v_mfma_f32_32x32x16_bf16 v[0:15], v[216:219], v[216:219], 0\n"
"buffer_load_dwordx4 a[136:139], v248, s[84:87], s58 offen offset:0 sc1\n"
"s_add_u32 s58, s35, s34\n"
"s_add_u32 s58, s58, s31\n"
"s_add_u32 s58, s58, 5120\n"
"v_mul_f32 v44, v44, v235\n"
"v_mul_f32 v45, v45, v235\n"
"buffer_load_dwordx4 a[180:183], v248, s[84:87], s58 offen offset:0 sc1\n"
"ds_read_b128 v[184:187], v245 offset:14336\n"
"v_mul_f32 v46, v46, v235\n"
"v_mul_f32 v47, v47, v235\n"
"v_mul_f32 v60, v60, v235\n"
"v_mul_f32 v61, v61, v235\n"
"v_mul_f32 v62, v62, v235\n"
"v_mul_f32 v63, v63, v235\n"
"buffer_load_dwordx4 a[184:187], v248, s[84:87], s58 offen offset:1024 sc1\n"
"v_cvt_pk_bf16_f32 v44, v44, v45\n"
"v_cvt_pk_bf16_f32 v45, v46, v47\n"
"v_cvt_pk_bf16_f32 v46, v60, v61\n"
"v_cvt_pk_bf16_f32 v47, v62, v63\n"
"buffer_store_dwordx4 v[44:47], v246, s[84:87], s6 offen offset:1024 sc1\n"
"ds_read_b128 v[188:191], v245 offset:15360\n"
"v_mfma_f32_32x32x16_bf16 v[48:63], v[216:219], v[216:219], 0\n"
"buffer_load_dwordx4 a[188:191], v248, s[84:87], s58 offen offset:2048 sc1\n"
"v_mfma_f32_32x32x16_bf16 v[32:47], v[216:219], v[216:219], 0\n"
"buffer_load_dwordx4 a[192:195], v248, s[84:87], s58 offen offset:3072 sc1\n"
"v_mul_f32 v76, v76, v235\n"
"v_mul_f32 v77, v77, v235\n"
"s_add_u32 s58, s58, 4096\n"
"v_mul_f32 v78, v78, v235\n"
"v_mul_f32 v79, v79, v235\n"
"v_mul_f32 v92, v92, v235\n"
"buffer_load_dwordx4 a[196:199], v248, s[84:87], s58 offen offset:0 sc1\n"
"v_mul_f32 v93, v93, v235\n"
"v_mul_f32 v94, v94, v235\n"
"s_add_u32 s48, s49, s34\n"
"s_add_u32 s48, s48, 5120\n"
"v_mul_f32 v95, v95, v235\n"
"v_cvt_pk_bf16_f32 v76, v76, v77\n"
"v_cvt_pk_bf16_f32 v77, v78, v79\n"
"buffer_load_dwordx4 a[100:103], v246, s[84:87], s48 offen offset:0 sc1\n"
"v_cvt_pk_bf16_f32 v78, v92, v93\n"
"v_cvt_pk_bf16_f32 v79, v94, v95\n"
"buffer_store_dwordx4 v[76:79], v246, s[84:87], s6 offen offset:2048 sc1\n"
"v_mfma_f32_32x32x16_bf16 v[80:95], v[216:219], v[216:219], 0\n"
"buffer_load_dwordx4 a[104:107], v246, s[84:87], s48 offen offset:1024 sc1\n"
"v_mfma_f32_32x32x16_bf16 v[64:79], v[216:219], v[216:219], 0\n"
"v_mul_f32 v108, v108, v235\n"
"buffer_load_dwordx4 a[108:111], v246, s[84:87], s48 offen offset:2048 sc1\n"
"v_mul_f32 v109, v109, v235\n"
"v_mul_f32 v110, v110, v235\n"
"v_mul_f32 v111, v111, v235\n"
"v_mul_f32 v124, v124, v235\n"
"v_mul_f32 v125, v125, v235\n"
"v_mul_f32 v126, v126, v235\n"
"buffer_load_dwordx4 a[112:115], v246, s[84:87], s48 offen offset:3072 sc1\n"
"v_mul_f32 v127, v127, v235\n"
"v_cvt_pk_bf16_f32 v108, v108, v109\n"
"v_cvt_pk_bf16_f32 v109, v110, v111\n"
"s_add_u32 s48, s48, 4096\n"
"v_cvt_pk_bf16_f32 v110, v124, v125\n"
"v_cvt_pk_bf16_f32 v111, v126, v127\n"
"buffer_store_dwordx4 v[108:111], v246, s[84:87], s6 offen offset:3072 sc1\n"
"v_mfma_f32_32x32x16_bf16 v[112:127], v[216:219], v[216:219], 0\n"
"buffer_load_dwordx4 a[116:119], v246, s[84:87], s48 offen offset:0 sc1\n"
"v_mfma_f32_32x32x16_bf16 v[96:111], v[216:219], v[216:219], 0\n"
"v_max3_f32 v249, v224, v225, v226\n"
"v_max_f32 v249, v249, v227\n"
"v_cmp_nge_f32 vcc, 0.5, v249\n"
"s_waitcnt lgkmcnt(0)\n"
"s_cbranch_vccnz .LstubSL_%=\n"
".LbackSL_%=:\n"
"s_branch .Lend_%=\n"
".Lcodeend_%=:\n"
".Lslow1_%=:\n"
"s_mov_b32 s53, 0x8000\n"
".Lslowl1_%=:\n"
"buffer_load_dword v239, v238, s[84:87], s82 offen sc1\n"
"s_waitcnt vmcnt(0)\n"
"v_cmp_ne_u32 vcc, s88, v239\n"
"s_cbranch_vccz .Lback1_%=\n"
"s_sleep 2\n"
"s_sub_u32 s53, s53, 1\n"
"s_cbranch_scc0 .Lslowl1_%=\n"
"s_branch .Lback1_%=\n"
".Lslow3_%=:\n"
"s_mov_b32 s65, 0x8000\n"
".Lslowl3_%=:\n"
"buffer_load_dword v45, v44, s[84:87], s82 offen sc1\n"
"s_waitcnt vmcnt(0)\n"
"v_cmp_ne_u32 vcc, s88, v45\n"
"s_cbranch_vccz .Lback3_%=\n"
"s_sleep 2\n"
"s_sub_u32 s65, s65, 1\n"
"s_cbranch_scc0 .Lslowl3_%=\n"
"s_branch .Lback3_%=\n"
".Lslow2_%=:\n"
"s_mov_b32 s47, 0x8000\n"
".Lslowl2_%=:\n"
"buffer_load_dword v45, v44, s[84:87], s82 offen sc1\n"
"buffer_load_dwordx4 v[60:63], v46, s[84:87], s53 offen sc1\n"
"s_waitcnt vmcnt(0)\n"
"v_xor_b32 v60, s88, v60\n"
"v_xor_b32 v61, s88, v61\n"
"v_xor_b32 v62, s88, v62\n"
"v_xor_b32 v63, s88, v63\n"
"v_or_b32 v60, v60, v61\n"
"v_or_b32 v60, v60, v62\n"
"v_or_b32 v60, v60, v63\n"
"v_xor_b32 v45, s88, v45\n"
"v_or_b32 v45, v45, v60\n"
"v_cmp_ne_u32 vcc, 0, v45\n"
"s_cbranch_vccz .Lback2_%=\n"
"s_sleep 2\n"
"s_sub_u32 s47, s47, 1\n"
"s_cbranch_scc0 .Lslowl2_%=\n"
"s_branch .Lback2_%=\n"
".Lslow5_%=:\n"
"s_mov_b32 s47, 0x8000\n"
".Lslowl5_%=:\n"
"buffer_load_dwordx4 a[120:123], v246, s[84:87], s44 offen sc1\n"
"buffer_load_dword a124, v253, s[84:87], s46 offen sc1\n"
"s_waitcnt vmcnt(0)\n"
"v_accvgpr_read_b32 v236, a120\n"
"v_accvgpr_read_b32 v237, a121\n"
"v_accvgpr_read_b32 v238, a122\n"
"v_accvgpr_read_b32 v239, a123\n"
"v_accvgpr_read_b32 v254, a124\n"
"s_waitcnt vmcnt(0)\n"
"v_xor_b32 v236, s88, v236\n"
"v_xor_b32 v237, s88, v237\n"
"v_xor_b32 v238, s88, v238\n"
"v_xor_b32 v239, s88, v239\n"
"v_or_b32 v236, v236, v237\n"
"v_or_b32 v236, v236, v238\n"
"v_or_b32 v236, v236, v239\n"
"v_xor_b32 v254, s88, v254\n"
"v_or_b32 v254, v254, v236\n"
"v_cmp_ne_u32 vcc, 0, v254\n"
"s_cbranch_vccz .Lback5_%=\n"
"s_sleep 2\n"
"s_sub_u32 s47, s47, 1\n"
"s_cbranch_scc0 .Lslowl5_%=\n"
"s_branch .Lback5_%=\n"
".LstubSF_%=:\n"
"s_add_u32 s66, s62, .LfixSF_%=-.Lpc_%=\n"
"s_addc_u32 s67, s63, 0\n"
"s_branch .LrescV1_%=\n"
".LfixSF_%=:\n"
"ds_read_b128 v[160:163], v245 offset:8192\n"
"ds_read_b128 v[164:167], v245 offset:9216\n"
"ds_read_b128 v[168:171], v245 offset:10240\n"
"ds_read_b128 v[172:175], v245 offset:11264\n"
"ds_read_b128 v[176:179], v245 offset:12288\n"
"ds_read_b128 v[180:183], v245 offset:13312\n"
"ds_read_b128 v[184:187], v245 offset:14336\n"
"ds_read_b128 v[188:191], v245 offset:15360\n"
"v_subrev_u32 v248, s57, v246\n"
"v_lshlrev_b32 v238, 5, v246\n"
"v_and_b32 v238, 0x1c00, v238\n"
"v_bfe_u32 v236, v246, 4, 1\n"
"v_lshl_add_u32 v238, v236, 2, v238\n"
"v_min_u32 v236, 2052, v238\n"
"s_waitcnt lgkmcnt(0)\n"
"s_branch .LbackSF_%=\n"
".Lslow4_%=:\n"
"s_mov_b32 s61, 0x8000\n"
".Lslowl4_%=:\n"
"buffer_load_dword v237, v236, s[84:87], s10 offen sc1\n"
"s_waitcnt vmcnt(0)\n"
"v_cmp_ne_u32 vcc, s4, v237\n"
"s_cbranch_vccz .Lback4_%=\n"
"s_sleep 1\n"
"s_sub_u32 s61, s61, 1\n"
"s_cbranch_scc0 .Lslowl4_%=\n"
"s_branch .Lback4_%=\n"
".LrescV0_%=:\n"
"s_nop 7\n"
"v_xor_b32 v208, 0x80008000, v208\n"
"v_xor_b32 v209, 0x80008000, v209\n"
"v_xor_b32 v210, 0x80008000, v210\n"
"v_xor_b32 v211, 0x80008000, v211\n"
"v_xor_b32 v212, 0x80008000, v212\n"
"v_xor_b32 v213, 0x80008000, v213\n"
"v_xor_b32 v214, 0x80008000, v214\n"
"v_xor_b32 v215, 0x80008000, v215\n"
"v_xor_b32 v216, 0x80008000, v216\n"
"v_xor_b32 v217, 0x80008000, v217\n"
"v_xor_b32 v218, 0x80008000, v218\n"
"v_xor_b32 v219, 0x80008000, v219\n"
"v_xor_b32 v220, 0x80008000, v220\n"
"v_xor_b32 v221, 0x80008000, v221\n"
"v_xor_b32 v222, 0x80008000, v222\n"
"v_xor_b32 v223, 0x80008000, v223\n"
"s_nop 1\n"
"v_smfmac_f32_16x16x64_bf16 v[224:227], v[240:243], v[208:215], v247\n"
"v_smfmac_f32_16x16x64_bf16 v[228:231], v[240:243], v[216:223], v247\n"
"s_nop 7\n"
"v_max3_f32 v248, v160, v161, v162\n"
"v_max3_f32 v248, v248, v163, v164\n"
"v_max3_f32 v248, v248, v165, v166\n"
"v_max_f32 v248, v248, v167\n"
"v_mov_b32 v249, v248\n"
"s_nop 1\n"
"v_permlane32_swap_b32 v248, v249\n"
"v_max_f32 v248, v248, v249\n"
"v_mov_b32 v249, v248\n"
"s_nop 1\n"
"v_permlane16_swap_b32 v248, v249\n"
"v_max_f32 v248, v248, v249\n"
"v_add_f32 v248, 0x42c00000, v248\n"
"v_ceil_f32 v248, v248\n"
"v_max_f32 v248, 0, v248\n"
"v_cvt_i32_f32 v252, -v248\n"
"s_nop 0\n"
"v_ldexp_f32 v0, v0, v252\n"
"v_ldexp_f32 v1, v1, v252\n"
"v_ldexp_f32 v2, v2, v252\n"
"v_ldexp_f32 v3, v3, v252\n"
"v_ldexp_f32 v16, v16, v252\n"
"v_ldexp_f32 v17, v17, v252\n"
"v_ldexp_f32 v18, v18, v252\n"
"v_ldexp_f32 v19, v19, v252\n"
"v_ldexp_f32 v32, v32, v252\n"
"v_ldexp_f32 v33, v33, v252\n"
"v_ldexp_f32 v34, v34, v252\n"
"v_ldexp_f32 v35, v35, v252\n"
"v_ldexp_f32 v48, v48, v252\n"
"v_ldexp_f32 v49, v49, v252\n"
"v_ldexp_f32 v50, v50, v252\n"
"v_ldexp_f32 v51, v51, v252\n"
"v_ldexp_f32 v64, v64, v252\n"
"v_ldexp_f32 v65, v65, v252\n"
"v_ldexp_f32 v66, v66, v252\n"
"v_ldexp_f32 v67, v67, v252\n"
"v_ldexp_f32 v80, v80, v252\n"
"v_ldexp_f32 v81, v81, v252\n"
"v_ldexp_f32 v82, v82, v252\n"
"v_ldexp_f32 v83, v83, v252\n"
"v_ldexp_f32 v96, v96, v252\n"
"v_ldexp_f32 v97, v97, v252\n"
"v_ldexp_f32 v98, v98, v252\n"
"v_ldexp_f32 v99, v99, v252\n"
"v_ldexp_f32 v112, v112, v252\n"
"v_ldexp_f32 v113, v113, v252\n"
"v_ldexp_f32 v114, v114, v252\n"
"v_ldexp_f32 v115, v115, v252\n"
"v_ldexp_f32 v224, v224, v252\n"
"v_ldexp_f32 v225, v225, v252\n"
"v_sub_f32 v236, v160, v248\n"
"v_sub_f32 v237, v161, v248\n"
"v_sub_f32 v238, v162, v248\n"
"v_sub_f32 v239, v163, v248\n"
"v_exp_f32 v236, v236\n"
"v_exp_f32 v237, v237\n"
"v_exp_f32 v238, v238\n"
"v_exp_f32 v239, v239\n"
"s_nop 0\n"
"v_cvt_pk_bf16_f32 v208, v236, v237\n"
"v_cvt_pk_bf16_f32 v209, v238, v239\n"
"v_sub_f32 v236, v164, v248\n"
"v_sub_f32 v237, v165, v248\n"
"v_sub_f32 v238, v166, v248\n"
"v_sub_f32 v239, v167, v248\n"
"v_exp_f32 v236, v236\n"
"v_exp_f32 v237, v237\n"
"v_exp_f32 v238, v238\n"
"v_exp_f32 v239, v239\n"
"s_nop 0\n"
"v_cvt_pk_bf16_f32 v210, v236, v237\n"
"v_cvt_pk_bf16_f32 v211, v238, v239\n"
"s_cmp_eq_u32 s52, s53\n"
"s_cbranch_scc1 .LhV0r0_%=\n"
"v_sub_f32 v128, v128, v248\n"
"v_sub_f32 v129, v129, v248\n"
"v_sub_f32 v130, v130, v248\n"
"v_sub_f32 v131, v131, v248\n"
"v_sub_f32 v132, v132, v248\n"
"v_sub_f32 v133, v133, v248\n"
"v_sub_f32 v134, v134, v248\n"
"v_sub_f32 v135, v135, v248\n"
"v_sub_f32 v192, v192, v248\n"
"v_sub_f32 v193, v193, v248\n"
"v_sub_f32 v194, v194, v248\n"
"v_sub_f32 v195, v195, v248\n"
".LhV0r0_%=:\n"
"v_max3_f32 v248, v168, v169, v170\n"
"v_max3_f32 v248, v248, v171, v172\n"
"v_max3_f32 v248, v248, v173, v174\n"
"v_max_f32 v248, v248, v175\n"
"v_mov_b32 v249, v248\n"
"s_nop 1\n"
"v_permlane32_swap_b32 v248, v249\n"
"v_max_f32 v248, v248, v249\n"
"v_mov_b32 v249, v248\n"
"s_nop 1\n"
"v_permlane16_swap_b32 v248, v249\n"
"v_max_f32 v248, v248, v249\n"
"v_add_f32 v248, 0x42c00000, v248\n"
"v_ceil_f32 v248, v248\n"
"v_max_f32 v248, 0, v248\n"
"v_cvt_i32_f32 v252, -v248\n"
"s_nop 0\n"
"v_ldexp_f32 v4, v4, v252\n"
"v_ldexp_f32 v5, v5, v252\n"
"v_ldexp_f32 v6, v6, v252\n"
"v_ldexp_f32 v7, v7, v252\n"
"v_ldexp_f32 v20, v20, v252\n"
"v_ldexp_f32 v21, v21, v252\n"
"v_ldexp_f32 v22, v22, v252\n"
"v_ldexp_f32 v23, v23, v252\n"
"v_ldexp_f32 v36, v36, v252\n"
"v_ldexp_f32 v37, v37, v252\n"
"v_ldexp_f32 v38, v38, v252\n"
"v_ldexp_f32 v39, v39, v252\n"
"v_ldexp_f32 v52, v52, v252\n"
"v_ldexp_f32 v53, v53, v252\n"
"v_ldexp_f32 v54, v54, v252\n"
"v_ldexp_f32 v55, v55, v252\n"
"v_ldexp_f32 v68, v68, v252\n"
"v_ldexp_f32 v69, v69, v252\n"
"v_ldexp_f32 v70, v70, v252\n"
"v_ldexp_f32 v71, v71, v252\n"
"v_ldexp_f32 v84, v84, v252\n"
"v_ldexp_f32 v85, v85, v252\n"
"v_ldexp_f32 v86, v86, v252\n"
"v_ldexp_f32 v87, v87, v252\n"
"v_ldexp_f32 v100, v100, v252\n"
"v_ldexp_f32 v101, v101, v252\n"
"v_ldexp_f32 v102, v102, v252\n"
"v_ldexp_f32 v103, v103, v252\n"
"v_ldexp_f32 v116, v116, v252\n"
"v_ldexp_f32 v117, v117, v252\n"
"v_ldexp_f32 v118, v118, v252\n"
"v_ldexp_f32 v119, v119, v252\n"
"v_ldexp_f32 v226, v226, v252\n"
"v_ldexp_f32 v227, v227, v252\n"
"v_sub_f32 v236, v168, v248\n"
"v_sub_f32 v237, v169, v248\n"
"v_sub_f32 v238, v170, v248\n"
"v_sub_f32 v239, v171, v248\n"
"v_exp_f32 v236, v236\n"
"v_exp_f32 v237, v237\n"
"v_exp_f32 v238, v238\n"
"v_exp_f32 v239, v239\n"
"s_nop 0\n"
"v_cvt_pk_bf16_f32 v212, v236, v237\n"
"v_cvt_pk_bf16_f32 v213, v238, v239\n"
"v_sub_f32 v236, v172, v248\n"
"v_sub_f32 v237, v173, v248\n"
"v_sub_f32 v238, v174, v248\n"
"v_sub_f32 v239, v175, v248\n"
"v_exp_f32 v236, v236\n"
"v_exp_f32 v237, v237\n"
"v_exp_f32 v238, v238\n"
"v_exp_f32 v239, v239\n"
"s_nop 0\n"
"v_cvt_pk_bf16_f32 v214, v236, v237\n"
"v_cvt_pk_bf16_f32 v215, v238, v239\n"
"s_cmp_eq_u32 s52, s53\n"
"s_cbranch_scc1 .LhV0r1_%=\n"
"v_sub_f32 v136, v136, v248\n"
"v_sub_f32 v137, v137, v248\n"
"v_sub_f32 v138, v138, v248\n"
"v_sub_f32 v139, v139, v248\n"
"v_sub_f32 v140, v140, v248\n"
"v_sub_f32 v141, v141, v248\n"
"v_sub_f32 v142, v142, v248\n"
"v_sub_f32 v143, v143, v248\n"
"v_sub_f32 v196, v196, v248\n"
"v_sub_f32 v197, v197, v248\n"
"v_sub_f32 v198, v198, v248\n"
"v_sub_f32 v199, v199, v248\n"
".LhV0r1_%=:\n"
"v_max3_f32 v248, v176, v177, v178\n"
"v_max3_f32 v248, v248, v179, v180\n"
"v_max3_f32 v248, v248, v181, v182\n"
"v_max_f32 v248, v248, v183\n"
"v_mov_b32 v249, v248\n"
"s_nop 1\n"
"v_permlane32_swap_b32 v248, v249\n"
"v_max_f32 v248, v248, v249\n"
"v_mov_b32 v249, v248\n"
"s_nop 1\n"
"v_permlane16_swap_b32 v248, v249\n"
"v_max_f32 v248, v248, v249\n"
"v_add_f32 v248, 0x42c00000, v248\n"
"v_ceil_f32 v248, v248\n"
"v_max_f32 v248, 0, v248\n"
"v_cvt_i32_f32 v252, -v248\n"
"s_nop 0\n"
"v_ldexp_f32 v8, v8, v252\n"
"v_ldexp_f32 v9, v9, v252\n"
"v_ldexp_f32 v10, v10, v252\n"
"v_ldexp_f32 v11, v11, v252\n"
"v_ldexp_f32 v24, v24, v252\n"
"v_ldexp_f32 v25, v25, v252\n"
"v_ldexp_f32 v26, v26, v252\n"
"v_ldexp_f32 v27, v27, v252\n"
"v_ldexp_f32 v40, v40, v252\n"
"v_ldexp_f32 v41, v41, v252\n"
"v_ldexp_f32 v42, v42, v252\n"
"v_ldexp_f32 v43, v43, v252\n"
"v_ldexp_f32 v56, v56, v252\n"
"v_ldexp_f32 v57, v57, v252\n"
"v_ldexp_f32 v58, v58, v252\n"
"v_ldexp_f32 v59, v59, v252\n"
"v_ldexp_f32 v72, v72, v252\n"
"v_ldexp_f32 v73, v73, v252\n"
"v_ldexp_f32 v74, v74, v252\n"
"v_ldexp_f32 v75, v75, v252\n"
"v_ldexp_f32 v88, v88, v252\n"
"v_ldexp_f32 v89, v89, v252\n"
"v_ldexp_f32 v90, v90, v252\n"
"v_ldexp_f32 v91, v91, v252\n"
"v_ldexp_f32 v104, v104, v252\n"
"v_ldexp_f32 v105, v105, v252\n"
"v_ldexp_f32 v106, v106, v252\n"
"v_ldexp_f32 v107, v107, v252\n"
"v_ldexp_f32 v120, v120, v252\n"
"v_ldexp_f32 v121, v121, v252\n"
"v_ldexp_f32 v122, v122, v252\n"
"v_ldexp_f32 v123, v123, v252\n"
"v_ldexp_f32 v228, v228, v252\n"
"v_ldexp_f32 v229, v229, v252\n"
"v_sub_f32 v236, v176, v248\n"
"v_sub_f32 v237, v177, v248\n"
"v_sub_f32 v238, v178, v248\n"
"v_sub_f32 v239, v179, v248\n"
"v_exp_f32 v236, v236\n"
"v_exp_f32 v237, v237\n"
"v_exp_f32 v238, v238\n"
"v_exp_f32 v239, v239\n"
"s_nop 0\n"
"v_cvt_pk_bf16_f32 v216, v236, v237\n"
"v_cvt_pk_bf16_f32 v217, v238, v239\n"
"v_sub_f32 v236, v180, v248\n"
"v_sub_f32 v237, v181, v248\n"
"v_sub_f32 v238, v182, v248\n"
"v_sub_f32 v239, v183, v248\n"
"v_exp_f32 v236, v236\n"
"v_exp_f32 v237, v237\n"
"v_exp_f32 v238, v238\n"
"v_exp_f32 v239, v239\n"
"s_nop 0\n"
"v_cvt_pk_bf16_f32 v218, v236, v237\n"
"v_cvt_pk_bf16_f32 v219, v238, v239\n"
"s_cmp_eq_u32 s52, s53\n"
"s_cbranch_scc1 .LhV0r2_%=\n"
"v_sub_f32 v144, v144, v248\n"
"v_sub_f32 v145, v145, v248\n"
"v_sub_f32 v146, v146, v248\n"
"v_sub_f32 v147, v147, v248\n"
"v_sub_f32 v148, v148, v248\n"
"v_sub_f32 v149, v149, v248\n"
"v_sub_f32 v150, v150, v248\n"
"v_sub_f32 v151, v151, v248\n"
"v_sub_f32 v200, v200, v248\n"
"v_sub_f32 v201, v201, v248\n"
"v_sub_f32 v202, v202, v248\n"
"v_sub_f32 v203, v203, v248\n"
".LhV0r2_%=:\n"
"v_max3_f32 v248, v184, v185, v186\n"
"v_max3_f32 v248, v248, v187, v188\n"
"v_max3_f32 v248, v248, v189, v190\n"
"v_max_f32 v248, v248, v191\n"
"v_mov_b32 v249, v248\n"
"s_nop 1\n"
"v_permlane32_swap_b32 v248, v249\n"
"v_max_f32 v248, v248, v249\n"
"v_mov_b32 v249, v248\n"
"s_nop 1\n"
"v_permlane16_swap_b32 v248, v249\n"
"v_max_f32 v248, v248, v249\n"
"v_add_f32 v248, 0x42c00000, v248\n"
"v_ceil_f32 v248, v248\n"
"v_max_f32 v248, 0, v248\n"
"v_cvt_i32_f32 v252, -v248\n"
"s_nop 0\n"
"v_ldexp_f32 v12, v12, v252\n"
"v_ldexp_f32 v13, v13, v252\n"
"v_ldexp_f32 v14, v14, v252\n"
"v_ldexp_f32 v15, v15, v252\n"
"v_ldexp_f32 v28, v28, v252\n"
"v_ldexp_f32 v29, v29, v252\n"
"v_ldexp_f32 v30, v30, v252\n"
"v_ldexp_f32 v31, v31, v252\n"
"v_ldexp_f32 v44, v44, v252\n"
"v_ldexp_f32 v45, v45, v252\n"
"v_ldexp_f32 v46, v46, v252\n"
"v_ldexp_f32 v47, v47, v252\n"
"v_ldexp_f32 v60, v60, v252\n"
"v_ldexp_f32 v61, v61, v252\n"
"v_ldexp_f32 v62, v62, v252\n"
"v_ldexp_f32 v63, v63, v252\n"
"v_ldexp_f32 v76, v76, v252\n"
"v_ldexp_f32 v77, v77, v252\n"
"v_ldexp_f32 v78, v78, v252\n"
"v_ldexp_f32 v79, v79, v252\n"
"v_ldexp_f32 v92, v92, v252\n"
"v_ldexp_f32 v93, v93, v252\n"
"v_ldexp_f32 v94, v94, v252\n"
"v_ldexp_f32 v95, v95, v252\n"
"v_ldexp_f32 v108, v108, v252\n"
"v_ldexp_f32 v109, v109, v252\n"
"v_ldexp_f32 v110, v110, v252\n"
"v_ldexp_f32 v111, v111, v252\n"
"v_ldexp_f32 v124, v124, v252\n"
"v_ldexp_f32 v125, v125, v252\n"
"v_ldexp_f32 v126, v126, v252\n"
"v_ldexp_f32 v127, v127, v252\n"
"v_ldexp_f32 v230, v230, v252\n"
"v_ldexp_f32 v231, v231, v252\n"
"v_sub_f32 v236, v184, v248\n"
"v_sub_f32 v237, v185, v248\n"
"v_sub_f32 v238, v186, v248\n"
"v_sub_f32 v239, v187, v248\n"
"v_exp_f32 v236, v236\n"
"v_exp_f32 v237, v237\n"
"v_exp_f32 v238, v238\n"
"v_exp_f32 v239, v239\n"
"s_nop 0\n"
"v_cvt_pk_bf16_f32 v220, v236, v237\n"
"v_cvt_pk_bf16_f32 v221, v238, v239\n"
"v_sub_f32 v236, v188, v248\n"
"v_sub_f32 v237, v189, v248\n"
"v_sub_f32 v238, v190, v248\n"
"v_sub_f32 v239, v191, v248\n"
"v_exp_f32 v236, v236\n"
"v_exp_f32 v237, v237\n"
"v_exp_f32 v238, v238\n"
"v_exp_f32 v239, v239\n"
"s_nop 0\n"
"v_cvt_pk_bf16_f32 v222, v236, v237\n"
"v_cvt_pk_bf16_f32 v223, v238, v239\n"
"s_cmp_eq_u32 s52, s53\n"
"s_cbranch_scc1 .LhV0r3_%=\n"
"v_sub_f32 v152, v152, v248\n"
"v_sub_f32 v153, v153, v248\n"
"v_sub_f32 v154, v154, v248\n"
"v_sub_f32 v155, v155, v248\n"
"v_sub_f32 v156, v156, v248\n"
"v_sub_f32 v157, v157, v248\n"
"v_sub_f32 v158, v158, v248\n"
"v_sub_f32 v159, v159, v248\n"
"v_sub_f32 v204, v204, v248\n"
"v_sub_f32 v205, v205, v248\n"
"v_sub_f32 v206, v206, v248\n"
"v_sub_f32 v207, v207, v248\n"
".LhV0r3_%=:\n"
"s_nop 1\n"
"v_smfmac_f32_16x16x64_bf16 v[224:227], v[240:243], v[208:215], v247\n"
"v_smfmac_f32_16x16x64_bf16 v[228:231], v[240:243], v[216:223], v247\n"
"s_nop 7\n"
"s_cmp_eq_u32 s52, s64\n"
"s_cselect_b64 s[80:81], s[60:61], s[66:67]\n"
"s_setpc_b64 s[80:81]\n"
".LrescV1_%=:\n"
"s_nop 7\n"
"v_xor_b32 v208, 0x80008000, v208\n"
"v_xor_b32 v209, 0x80008000, v209\n"
"v_xor_b32 v210, 0x80008000, v210\n"
"v_xor_b32 v211, 0x80008000, v211\n"
"v_xor_b32 v212, 0x80008000, v212\n"
"v_xor_b32 v213, 0x80008000, v213\n"
"v_xor_b32 v214, 0x80008000, v214\n"
"v_xor_b32 v215, 0x80008000, v215\n"
"v_xor_b32 v216, 0x80008000, v216\n"
"v_xor_b32 v217, 0x80008000, v217\n"
"v_xor_b32 v218, 0x80008000, v218\n"
"v_xor_b32 v219, 0x80008000, v219\n"
"v_xor_b32 v220, 0x80008000, v220\n"
"v_xor_b32 v221, 0x80008000, v221\n"
"v_xor_b32 v222, 0x80008000, v222\n"
"v_xor_b32 v223, 0x80008000, v223\n"
"s_nop 1\n"
"v_smfmac_f32_16x16x64_bf16 v[224:227], v[240:243], v[208:215], v247\n"
"v_smfmac_f32_16x16x64_bf16 v[228:231], v[240:243], v[216:223], v247\n"
"s_nop 7\n"
"v_max3_f32 v248, v128, v129, v130\n"
"v_max3_f32 v248, v248, v131, v132\n"
"v_max3_f32 v248, v248, v133, v134\n"
"v_max_f32 v248, v248, v135\n"
"v_mov_b32 v249, v248\n"
"s_nop 1\n"
"v_permlane32_swap_b32 v248, v249\n"
"v_max_f32 v248, v248, v249\n"
"v_mov_b32 v249, v248\n"
"s_nop 1\n"
"v_permlane16_swap_b32 v248, v249\n"
"v_max_f32 v248, v248, v249\n"
"v_add_f32 v248, 0x42c00000, v248\n"
"v_ceil_f32 v248, v248\n"
"v_max_f32 v248, 0, v248\n"
"v_cvt_i32_f32 v252, -v248\n"
"s_nop 0\n"
"v_ldexp_f32 v0, v0, v252\n"
"v_ldexp_f32 v1, v1, v252\n"
"v_ldexp_f32 v2, v2, v252\n"
"v_ldexp_f32 v3, v3, v252\n"
"v_ldexp_f32 v16, v16, v252\n"
"v_ldexp_f32 v17, v17, v252\n"
"v_ldexp_f32 v18, v18, v252\n"
"v_ldexp_f32 v19, v19, v252\n"
"v_ldexp_f32 v32, v32, v252\n"
"v_ldexp_f32 v33, v33, v252\n"
"v_ldexp_f32 v34, v34, v252\n"
"v_ldexp_f32 v35, v35, v252\n"
"v_ldexp_f32 v48, v48, v252\n"
"v_ldexp_f32 v49, v49, v252\n"
"v_ldexp_f32 v50, v50, v252\n"
"v_ldexp_f32 v51, v51, v252\n"
"v_ldexp_f32 v64, v64, v252\n"
"v_ldexp_f32 v65, v65, v252\n"
"v_ldexp_f32 v66, v66, v252\n"
"v_ldexp_f32 v67, v67, v252\n"
"v_ldexp_f32 v80, v80, v252\n"
"v_ldexp_f32 v81, v81, v252\n"
"v_ldexp_f32 v82, v82, v252\n"
"v_ldexp_f32 v83, v83, v252\n"
"v_ldexp_f32 v96, v96, v252\n"
"v_ldexp_f32 v97, v97, v252\n"
"v_ldexp_f32 v98, v98, v252\n"
"v_ldexp_f32 v99, v99, v252\n"
"v_ldexp_f32 v112, v112, v252\n"
"v_ldexp_f32 v113, v113, v252\n"
"v_ldexp_f32 v114, v114, v252\n"
"v_ldexp_f32 v115, v115, v252\n"
"v_ldexp_f32 v224, v224, v252\n"
"v_ldexp_f32 v225, v225, v252\n"
"v_sub_f32 v236, v128, v248\n"
"v_sub_f32 v237, v129, v248\n"
"v_sub_f32 v238, v130, v248\n"
"v_sub_f32 v239, v131, v248\n"
"v_exp_f32 v236, v236\n"
"v_exp_f32 v237, v237\n"
"v_exp_f32 v238, v238\n"
"v_exp_f32 v239, v239\n"
"s_nop 0\n"
"v_cvt_pk_bf16_f32 v208, v236, v237\n"
"v_cvt_pk_bf16_f32 v209, v238, v239\n"
"v_sub_f32 v236, v132, v248\n"
"v_sub_f32 v237, v133, v248\n"
"v_sub_f32 v238, v134, v248\n"
"v_sub_f32 v239, v135, v248\n"
"v_exp_f32 v236, v236\n"
"v_exp_f32 v237, v237\n"
"v_exp_f32 v238, v238\n"
"v_exp_f32 v239, v239\n"
"s_nop 0\n"
"v_cvt_pk_bf16_f32 v210, v236, v237\n"
"v_cvt_pk_bf16_f32 v211, v238, v239\n"
"s_cmp_eq_u32 s52, s53\n"
"s_cbranch_scc1 .LhV1r0_%=\n"
"v_sub_f32 v160, v160, v248\n"
"v_sub_f32 v161, v161, v248\n"
"v_sub_f32 v162, v162, v248\n"
"v_sub_f32 v163, v163, v248\n"
"v_sub_f32 v164, v164, v248\n"
"v_sub_f32 v165, v165, v248\n"
"v_sub_f32 v166, v166, v248\n"
"v_sub_f32 v167, v167, v248\n"
"v_sub_f32 v192, v192, v248\n"
"v_sub_f32 v193, v193, v248\n"
"v_sub_f32 v194, v194, v248\n"
"v_sub_f32 v195, v195, v248\n"
".LhV1r0_%=:\n"
"v_max3_f32 v248, v136, v137, v138\n"
"v_max3_f32 v248, v248, v139, v140\n"
"v_max3_f32 v248, v248, v141, v142\n"
"v_max_f32 v248, v248, v143\n"
"v_mov_b32 v249, v248\n"
"s_nop 1\n"
"v_permlane32_swap_b32 v248, v249\n"
"v_max_f32 v248, v248, v249\n"
"v_mov_b32 v249, v248\n"
"s_nop 1\n"
"v_permlane16_swap_b32 v248, v249\n"
"v_max_f32 v248, v248, v249\n"
"v_add_f32 v248, 0x42c00000, v248\n"
"v_ceil_f32 v248, v248\n"
"v_max_f32 v248, 0, v248\n"
"v_cvt_i32_f32 v252, -v248\n"
"s_nop 0\n"
"v_ldexp_f32 v4, v4, v252\n"
"v_ldexp_f32 v5, v5, v252\n"
"v_ldexp_f32 v6, v6, v252\n"
"v_ldexp_f32 v7, v7, v252\n"
"v_ldexp_f32 v20, v20, v252\n"
"v_ldexp_f32 v21, v21, v252\n"
"v_ldexp_f32 v22, v22, v252\n"
"v_ldexp_f32 v23, v23, v252\n"
"v_ldexp_f32 v36, v36, v252\n"
"v_ldexp_f32 v37, v37, v252\n"
"v_ldexp_f32 v38, v38, v252\n"
"v_ldexp_f32 v39, v39, v252\n"
"v_ldexp_f32 v52, v52, v252\n"
"v_ldexp_f32 v53, v53, v252\n"
"v_ldexp_f32 v54, v54, v252\n"
"v_ldexp_f32 v55, v55, v252\n"
"v_ldexp_f32 v68, v68, v252\n"
"v_ldexp_f32 v69, v69, v252\n"
"v_ldexp_f32 v70, v70, v252\n"
"v_ldexp_f32 v71, v71, v252\n"
"v_ldexp_f32 v84, v84, v252\n"
"v_ldexp_f32 v85, v85, v252\n"
"v_ldexp_f32 v86, v86, v252\n"
"v_ldexp_f32 v87, v87, v252\n"
"v_ldexp_f32 v100, v100, v252\n"
"v_ldexp_f32 v101, v101, v252\n"
"v_ldexp_f32 v102, v102, v252\n"
"v_ldexp_f32 v103, v103, v252\n"
"v_ldexp_f32 v116, v116, v252\n"
"v_ldexp_f32 v117, v117, v252\n"
"v_ldexp_f32 v118, v118, v252\n"
"v_ldexp_f32 v119, v119, v252\n"
"v_ldexp_f32 v226, v226, v252\n"
"v_ldexp_f32 v227, v227, v252\n"
"v_sub_f32 v236, v136, v248\n"
"v_sub_f32 v237, v137, v248\n"
"v_sub_f32 v238, v138, v248\n"
"v_sub_f32 v239, v139, v248\n"
"v_exp_f32 v236, v236\n"
"v_exp_f32 v237, v237\n"
"v_exp_f32 v238, v238\n"
"v_exp_f32 v239, v239\n"
"s_nop 0\n"
"v_cvt_pk_bf16_f32 v212, v236, v237\n"
"v_cvt_pk_bf16_f32 v213, v238, v239\n"
"v_sub_f32 v236, v140, v248\n"
"v_sub_f32 v237, v141, v248\n"
"v_sub_f32 v238, v142, v248\n"
"v_sub_f32 v239, v143, v248\n"
"v_exp_f32 v236, v236\n"
"v_exp_f32 v237, v237\n"
"v_exp_f32 v238, v238\n"
"v_exp_f32 v239, v239\n"
"s_nop 0\n"
"v_cvt_pk_bf16_f32 v214, v236, v237\n"
"v_cvt_pk_bf16_f32 v215, v238, v239\n"
"s_cmp_eq_u32 s52, s53\n"
"s_cbranch_scc1 .LhV1r1_%=\n"
"v_sub_f32 v168, v168, v248\n"
"v_sub_f32 v169, v169, v248\n"
"v_sub_f32 v170, v170, v248\n"
"v_sub_f32 v171, v171, v248\n"
"v_sub_f32 v172, v172, v248\n"
"v_sub_f32 v173, v173, v248\n"
"v_sub_f32 v174, v174, v248\n"
"v_sub_f32 v175, v175, v248\n"
"v_sub_f32 v196, v196, v248\n"
"v_sub_f32 v197, v197, v248\n"
"v_sub_f32 v198, v198, v248\n"
"v_sub_f32 v199, v199, v248\n"
".LhV1r1_%=:\n"
"v_max3_f32 v248, v144, v145, v146\n"
"v_max3_f32 v248, v248, v147, v148\n"
"v_max3_f32 v248, v248, v149, v150\n"
"v_max_f32 v248, v248, v151\n"
"v_mov_b32 v249, v248\n"
"s_nop 1\n"
"v_permlane32_swap_b32 v248, v249\n"
"v_max_f32 v248, v248, v249\n"
"v_mov_b32 v249, v248\n"
"s_nop 1\n"
"v_permlane16_swap_b32 v248, v249\n"
"v_max_f32 v248, v248, v249\n"
"v_add_f32 v248, 0x42c00000, v248\n"
"v_ceil_f32 v248, v248\n"
"v_max_f32 v248, 0, v248\n"
"v_cvt_i32_f32 v252, -v248\n"
"s_nop 0\n"
"v_ldexp_f32 v8, v8, v252\n"
"v_ldexp_f32 v9, v9, v252\n"
"v_ldexp_f32 v10, v10, v252\n"
"v_ldexp_f32 v11, v11, v252\n"
"v_ldexp_f32 v24, v24, v252\n"
"v_ldexp_f32 v25, v25, v252\n"
"v_ldexp_f32 v26, v26, v252\n"
"v_ldexp_f32 v27, v27, v252\n"
"v_ldexp_f32 v40, v40, v252\n"
"v_ldexp_f32 v41, v41, v252\n"
"v_ldexp_f32 v42, v42, v252\n"
"v_ldexp_f32 v43, v43, v252\n"
"v_ldexp_f32 v56, v56, v252\n"
"v_ldexp_f32 v57, v57, v252\n"
"v_ldexp_f32 v58, v58, v252\n"
"v_ldexp_f32 v59, v59, v252\n"
"v_ldexp_f32 v72, v72, v252\n"
"v_ldexp_f32 v73, v73, v252\n"
"v_ldexp_f32 v74, v74, v252\n"
"v_ldexp_f32 v75, v75, v252\n"
"v_ldexp_f32 v88, v88, v252\n"
"v_ldexp_f32 v89, v89, v252\n"
"v_ldexp_f32 v90, v90, v252\n"
"v_ldexp_f32 v91, v91, v252\n"
"v_ldexp_f32 v104, v104, v252\n"
"v_ldexp_f32 v105, v105, v252\n"
"v_ldexp_f32 v106, v106, v252\n"
"v_ldexp_f32 v107, v107, v252\n"
"v_ldexp_f32 v120, v120, v252\n"
"v_ldexp_f32 v121, v121, v252\n"
"v_ldexp_f32 v122, v122, v252\n"
"v_ldexp_f32 v123, v123, v252\n"
"v_ldexp_f32 v228, v228, v252\n"
"v_ldexp_f32 v229, v229, v252\n"
"v_sub_f32 v236, v144, v248\n"
"v_sub_f32 v237, v145, v248\n"
"v_sub_f32 v238, v146, v248\n"
"v_sub_f32 v239, v147, v248\n"
"v_exp_f32 v236, v236\n"
"v_exp_f32 v237, v237\n"
"v_exp_f32 v238, v238\n"
"v_exp_f32 v239, v239\n"
"s_nop 0\n"
"v_cvt_pk_bf16_f32 v216, v236, v237\n"
"v_cvt_pk_bf16_f32 v217, v238, v239\n"
"v_sub_f32 v236, v148, v248\n"
"v_sub_f32 v237, v149, v248\n"
"v_sub_f32 v238, v150, v248\n"
"v_sub_f32 v239, v151, v248\n"
"v_exp_f32 v236, v236\n"
"v_exp_f32 v237, v237\n"
"v_exp_f32 v238, v238\n"
"v_exp_f32 v239, v239\n"
"s_nop 0\n"
"v_cvt_pk_bf16_f32 v218, v236, v237\n"
"v_cvt_pk_bf16_f32 v219, v238, v239\n"
"s_cmp_eq_u32 s52, s53\n"
"s_cbranch_scc1 .LhV1r2_%=\n"
"v_sub_f32 v176, v176, v248\n"
"v_sub_f32 v177, v177, v248\n"
"v_sub_f32 v178, v178, v248\n"
"v_sub_f32 v179, v179, v248\n"
"v_sub_f32 v180, v180, v248\n"
"v_sub_f32 v181, v181, v248\n"
"v_sub_f32 v182, v182, v248\n"
"v_sub_f32 v183, v183, v248\n"
"v_sub_f32 v200, v200, v248\n"
"v_sub_f32 v201, v201, v248\n"
"v_sub_f32 v202, v202, v248\n"
"v_sub_f32 v203, v203, v248\n"
".LhV1r2_%=:\n"
"v_max3_f32 v248, v152, v153, v154\n"
"v_max3_f32 v248, v248, v155, v156\n"
"v_max3_f32 v248, v248, v157, v158\n"
"v_max_f32 v248, v248, v159\n"
"v_mov_b32 v249, v248\n"
"s_nop 1\n"
"v_permlane32_swap_b32 v248, v249\n"
"v_max_f32 v248, v248, v249\n"
"v_mov_b32 v249, v248\n"
"s_nop 1\n"
"v_permlane16_swap_b32 v248, v249\n"
"v_max_f32 v248, v248, v249\n"
"v_add_f32 v248, 0x42c00000, v248\n"
"v_ceil_f32 v248, v248\n"
"v_max_f32 v248, 0, v248\n"
"v_cvt_i32_f32 v252, -v248\n"
"s_nop 0\n"
"v_ldexp_f32 v12, v12, v252\n"
"v_ldexp_f32 v13, v13, v252\n"
"v_ldexp_f32 v14, v14, v252\n"
"v_ldexp_f32 v15, v15, v252\n"
"v_ldexp_f32 v28, v28, v252\n"
"v_ldexp_f32 v29, v29, v252\n"
"v_ldexp_f32 v30, v30, v252\n"
"v_ldexp_f32 v31, v31, v252\n"
"v_ldexp_f32 v44, v44, v252\n"
"v_ldexp_f32 v45, v45, v252\n"
"v_ldexp_f32 v46, v46, v252\n"
"v_ldexp_f32 v47, v47, v252\n"
"v_ldexp_f32 v60, v60, v252\n"
"v_ldexp_f32 v61, v61, v252\n"
"v_ldexp_f32 v62, v62, v252\n"
"v_ldexp_f32 v63, v63, v252\n"
"v_ldexp_f32 v76, v76, v252\n"
"v_ldexp_f32 v77, v77, v252\n"
"v_ldexp_f32 v78, v78, v252\n"
"v_ldexp_f32 v79, v79, v252\n"
"v_ldexp_f32 v92, v92, v252\n"
"v_ldexp_f32 v93, v93, v252\n"
"v_ldexp_f32 v94, v94, v252\n"
"v_ldexp_f32 v95, v95, v252\n"
"v_ldexp_f32 v108, v108, v252\n"
"v_ldexp_f32 v109, v109, v252\n"
"v_ldexp_f32 v110, v110, v252\n"
"v_ldexp_f32 v111, v111, v252\n"
"v_ldexp_f32 v124, v124, v252\n"
"v_ldexp_f32 v125, v125, v252\n"
"v_ldexp_f32 v126, v126, v252\n"
"v_ldexp_f32 v127, v127, v252\n"
"v_ldexp_f32 v230, v230, v252\n"
"v_ldexp_f32 v231, v231, v252\n"
"v_sub_f32 v236, v152, v248\n"
"v_sub_f32 v237, v153, v248\n"
"v_sub_f32 v238, v154, v248\n"
"v_sub_f32 v239, v155, v248\n"
"v_exp_f32 v236, v236\n"
"v_exp_f32 v237, v237\n"
"v_exp_f32 v238, v238\n"
"v_exp_f32 v239, v239\n"
"s_nop 0\n"
"v_cvt_pk_bf16_f32 v220, v236, v237\n"
"v_cvt_pk_bf16_f32 v221, v238, v239\n"
"v_sub_f32 v236, v156, v248\n"
"v_sub_f32 v237, v157, v248\n"
"v_sub_f32 v238, v158, v248\n"
"v_sub_f32 v239, v159, v248\n"
"v_exp_f32 v236, v236\n"
"v_exp_f32 v237, v237\n"
"v_exp_f32 v238, v238\n"
"v_exp_f32 v239, v239\n"
"s_nop 0\n"
"v_cvt_pk_bf16_f32 v222, v236, v237\n"
"v_cvt_pk_bf16_f32 v223, v238, v239\n"
"s_cmp_eq_u32 s52, s53\n"
"s_cbranch_scc1 .LhV1r3_%=\n"
"v_sub_f32 v184, v184, v248\n"
"v_sub_f32 v185, v185, v248\n"
"v_sub_f32 v186, v186, v248\n"
"v_sub_f32 v187, v187, v248\n"
"v_sub_f32 v188, v188, v248\n"
"v_sub_f32 v189, v189, v248\n"
"v_sub_f32 v190, v190, v248\n"
"v_sub_f32 v191, v191, v248\n"
"v_sub_f32 v204, v204, v248\n"
"v_sub_f32 v205, v205, v248\n"
"v_sub_f32 v206, v206, v248\n"
"v_sub_f32 v207, v207, v248\n"
".LhV1r3_%=:\n"
"s_nop 1\n"
"v_smfmac_f32_16x16x64_bf16 v[224:227], v[240:243], v[208:215], v247\n"
"v_smfmac_f32_16x16x64_bf16 v[228:231], v[240:243], v[216:223], v247\n"
"s_nop 7\n"
"s_cmp_eq_u32 s52, s64\n"
"s_cselect_b64 s[80:81], s[60:61], s[66:67]\n"
"s_setpc_b64 s[80:81]\n"
".LstubS1_%=:\n"
"v_mov_b64 v[132:133], v[250:251]\n"
"v_mov_b64 v[134:135], v[250:251]\n"
"s_add_u32 s66, s62, .LfixS1_%=-.Lpc_%=\n"
"s_addc_u32 s67, s63, 0\n"
"s_branch .LrescV1_%=\n"
".LfixS1_%=:\n"
"v_mov_b32 v192, v234\n"
"v_mov_b32 v193, v234\n"
"v_mov_b64 v[194:195], v[192:193]\n"
"v_mov_b32 v196, v235\n"
"v_mov_b32 v197, v235\n"
"v_mov_b64 v[198:199], v[196:197]\n"
"v_mov_b32 v200, v232\n"
"v_mov_b32 v201, v232\n"
"v_mov_b64 v[202:203], v[200:201]\n"
"v_mov_b32 v204, v233\n"
"v_mov_b32 v205, v233\n"
"v_mov_b64 v[206:207], v[204:205]\n"
"s_branch .LbackS1_%=\n"
".LstubS2_%=:\n"
"v_mov_b64 v[160:161], v[250:251]\n"
"v_mov_b64 v[162:163], v[250:251]\n"
"v_mov_b64 v[164:165], v[250:251]\n"
"v_mov_b64 v[166:167], v[250:251]\n"
"v_mov_b64 v[168:169], v[250:251]\n"
"v_mov_b64 v[170:171], v[250:251]\n"
"v_mov_b64 v[172:173], v[250:251]\n"
"v_mov_b64 v[174:175], v[250:251]\n"
"v_mov_b64 v[180:181], v[250:251]\n"
"v_mov_b64 v[182:183], v[250:251]\n"
"s_add_u32 s66, s62, .LbackS2_%=-.Lpc_%=\n"
"s_addc_u32 s67, s63, 0\n"
"s_branch .LrescV0_%=\n"
".LstubSL_%=:\n"
"v_mov_b64 v[132:133], v[250:251]\n"
"v_mov_b64 v[134:135], v[250:251]\n"
"v_mov_b64 v[144:145], v[250:251]\n"
"v_mov_b64 v[146:147], v[250:251]\n"
"v_mov_b64 v[148:149], v[250:251]\n"
"v_mov_b64 v[150:151], v[250:251]\n"
"v_mov_b64 v[152:153], v[250:251]\n"
"v_mov_b64 v[154:155], v[250:251]\n"
"v_mov_b64 v[156:157], v[250:251]\n"
"v_mov_b64 v[158:159], v[250:251]\n"
"s_add_u32 s66, s62, .LfixSL_%=-.Lpc_%=\n"
"s_addc_u32 s67, s63, 0\n"
"s_branch .LrescV1_%=\n"
".LfixSL_%=:\n"
"ds_read_b128 v[160:163], v245 offset:8192\n"
"ds_read_b128 v[164:167], v245 offset:9216\n"
"ds_read_b128 v[168:171], v245 offset:10240\n"
"ds_read_b128 v[172:175], v245 offset:11264\n"
"ds_read_b128 v[176:179], v245 offset:12288\n"
"ds_read_b128 v[180:183], v245 offset:13312\n"
"ds_read_b128 v[184:187], v245 offset:14336\n"
"ds_read_b128 v[188:191], v245 offset:15360\n"
"v_subrev_u32 v248, s57, v246\n"
"v_lshlrev_b32 v238, 5, v246\n"
"v_and_b32 v238, 0x1c00, v238\n"
"v_bfe_u32 v236, v246, 4, 1\n"
"v_lshl_add_u32 v238, v236, 2, v238\n"
"v_min_u32 v236, 2052, v238\n"
"s_waitcnt lgkmcnt(0)\n"
"s_branch .LbackSL_%=\n"
".LstubS3_%=:\n"
"v_mov_b64 v[132:133], v[250:251]\n"
"v_mov_b64 v[134:135], v[250:251]\n"
"v_mov_b64 v[144:145], v[250:251]\n"
"v_mov_b64 v[146:147], v[250:251]\n"
"v_mov_b64 v[148:149], v[250:251]\n"
"v_mov_b64 v[150:151], v[250:251]\n"
"v_mov_b64 v[152:153], v[250:251]\n"
"v_mov_b64 v[154:155], v[250:251]\n"
"v_mov_b64 v[156:157], v[250:251]\n"
"v_mov_b64 v[158:159], v[250:251]\n"
"s_add_u32 s66, s62, .LbackS3_%=-.Lpc_%=\n"
"s_addc_u32 s67, s63, 0\n"
"s_branch .LrescV1_%=\n"
".Lexit_%=:\n"
    : "+{v0}"(tid) : "s"(__builtin_amdgcn_kernarg_segment_ptr()), "s"(bid)
    : "v1","v2","v3","v4","v5","v6","v7","v8","v9","v10","v11","v12","v13","v14","v15","v16","v17","v18","v19","v20","v21","v22","v23","v24","v25","v26","v27","v28","v29","v30","v31","v32","v33","v34","v35","v36","v37","v38","v39","v40","v41","v42","v43","v44","v45","v46","v47","v48","v49","v50","v51","v52","v53","v54","v55","v56","v57","v58","v59","v60","v61","v62","v63","v64","v65","v66","v67","v68","v69","v70","v71","v72","v73","v74","v75","v76","v77","v78","v79","v80","v81","v82","v83","v84","v85","v86","v87","v88","v89","v90","v91","v92","v93","v94","v95","v96","v97","v98","v99","v100","v101","v102","v103","v104","v105","v106","v107","v108","v109","v110","v111","v112","v113","v114","v115","v116","v117","v118","v119","v120","v121","v122","v123","v124","v125","v126","v127","v128","v129","v130","v131","v132","v133","v134","v135","v136","v137","v138","v139","v140","v141","v142","v143","v144","v145","v146","v147","v148","v149","v150","v151","v152","v153","v154","v155","v156","v157","v158","v159","v160","v161","v162","v163","v164","v165","v166","v167","v168","v169","v170","v171","v172","v173","v174","v175","v176","v177","v178","v179","v180","v181","v182","v183","v184","v185","v186","v187","v188","v189","v190","v191","v192","v193","v194","v195","v196","v197","v198","v199","v200","v201","v202","v203","v204","v205","v206","v207","v208","v209","v210","v211","v212","v213","v214","v215","v216","v217","v218","v219","v220","v221","v222","v223","v224","v225","v226","v227","v228","v229","v230","v231","v232","v233","v234","v235","v236","v237","v238","v239","v240","v241","v242","v243","v244","v245","v246","v247","v248","v249","v250","v251","v252","v253","v254","v255","a0","a1","a2","a3","a4","a5","a6","a7","a8","a9","a10","a11","a12","a13","a14","a15","a16","a17","a18","a19","a20","a21","a22","a23","a24","a25","a26","a27","a28","a29","a30","a31","a32","a33","a34","a35","a36","a37","a38","a39","a40","a41","a42","a43","a44","a45","a46","a47","a48","a49","a50","a51","a52","a53","a54","a55","a56","a57","a58","a59","a60","a61","a62","a63","a64","a65","a66","a67","a68","a69","a70","a71","a72","a73","a74","a75","a76","a77","a78","a79","a80","a81","a82","a83","a84","a85","a86","a87","a88","a89","a90","a91","a92","a93","a94","a95","a96","a97","a98","a99","a100","a101","a102","a103","a104","a105","a106","a107","a108","a109","a110","a111","a112","a113","a114","a115","a116","a117","a118","a119","a120","a121","a122","a123","a124","a125","a126","a127","a128","a129","a130","a131","a132","a133","a134","a135","a136","a137","a138","a139","a140","a141","a142","a143","a144","a145","a146","a147","a148","a149","a150","a151","a152","a153","a154","a155","a156","a157","a158","a159","a160","a161","a162","a163","a164","a165","a166","a167","a168","a169","a170","a171","a172","a173","a174","a175","a176","a177","a178","a179","a180","a181","a182","a183","a184","a185","a186","a187","a188","a189","a190","a191","a192","a193","a194","a195","a196","a197","a198","a199","a200","a201","a202","a203","a204","a205","a206","a207","a208","a209","a210","a211","a212","a213","a214","a215","a216","a217","a218","a219","a220","a221","a222","a223","a224","a225","a226","a227","a228","a229","a230","a231","a232","a233","a234","a235","a236","a237","a238","a239","a240","a241","a242","a243","a244","a245","a246","a247","a248","a249","a250","a251","a252","a253","a254","a255","s4","s5","s6","s7","s8","s9","s10","s11","s12","s13","s14","s15","s16","s17","s18","s19","s20","s21","s22","s23","s24","s25","s26","s27","s28","s29","s30","s31","s32","s33","s34","s35","s36","s37","s38","s39","s40","s41","s42","s43","s44","s45","s46","s47","s48","s49","s50","s51","s52","s53","s54","s55","s56","s57","s58","s59","s60","s61","s62","s63","s64","s65","s66","s67","s68","s69","s70","s71","s72","s73","s74","s75","s76","s77","s78","s79","s80","s81","s82","s83","s84","s85","s86","s87","s88","s89","vcc","m0","scc","memory");
}

extern "C" void kernel_launch(void* const* d_in, const int* in_sizes, int n_in, void* d_out, int out_size, void* d_ws, size_t ws_size, hipStream_t stream){
  (void)hipFuncSetAttribute((const void*)fused, hipFuncAttributeMaxDynamicSharedMemorySize, 163840);
  fused<<<256,256,163840,stream>>>(d_in[0],d_in[1],d_in[2],d_in[3],d_in[4],d_in[5],d_in[6],d_in[7],d_in[8],d_in[9],d_out,d_ws);
}
```
